```python
import torch

def _softmax(x, axis=-1):
    return torch.softmax(x, dim=axis)
def _gelu(x):
    return torch.nn.functional.gelu(x, approximate="tanh")
def _silu(x):
    return torch.nn.functional.silu(x)
def _sigmoid(x):
    return torch.sigmoid(x)
def _layernorm(x, axis=-1, eps=1e-5):
    m = x.mean(dim=axis, keepdim=True)
    v = x.var(dim=axis, keepdim=True, unbiased=False)
    return (x - m) / torch.sqrt(v + eps)
def _scatter_add(tgt, idx, src, axis=0):
    return tgt.clone().index_add_(axis, idx, src)
def _segment_sum(src, seg, n):
    out = torch.zeros((n,) + src.shape[1:], dtype=src.dtype)
    return out.index_add_(0, seg, src)
def _pad(x, pw):
    flat = tuple(v for pair in reversed(pw) for v in pair)
    return torch.nn.functional.pad(x, flat)

kernel_name = 'parallel_sum__mha_block_cached__glu_gated_swiglu__decode'

def setup_inputs():

    _w = (lambda a: (lambda t: t.to(torch.bfloat16) if t.is_floating_point() else t)(torch.as_tensor(a)))
    def _A_setup():
        B, Sq, D = 2, 1, 512
        H, Sk, Dh = 32, 16384, 128
        return {
            "x": _w((lambda *s: torch.randn(*s, dtype=torch.bfloat16) * 0.05)(B, Sq, D)),
            "k_cache": _w((lambda *s: torch.randn(*s, dtype=torch.bfloat16) * 1.6)(B, H, Sk, Dh)),
            "v_cache": _w((lambda *s: torch.randn(*s, dtype=torch.bfloat16) * 0.8)(B, H, Sk, Dh)),
            "wq": _w((lambda *s: torch.randn(*s, dtype=torch.bfloat16) * 1.6)(D, H * Dh) * (D ** -0.5)),
            "wo": _w((lambda *s: torch.randn(*s, dtype=torch.bfloat16) * 0.8)(H * Dh, D) * ((H * Dh) ** -0.5)),
        }
    _A_in = _A_setup()

    def _B_setup():
        B, S, D, F = 2, 1, 512, 1792
        return {
            "x": _w((lambda *s: torch.randn(*s, dtype=torch.bfloat16) * 0.05)(B, S, D)),
            "w1": _w((lambda *s: torch.randn(*s, dtype=torch.bfloat16) * 0.4)(D, F) * (2.0 / D) ** 0.5),
            "w3": _w((lambda *s: torch.randn(*s, dtype=torch.bfloat16) * 0.4)(D, F) * (2.0 / D) ** 0.5),
            "w2": _w((lambda *s: torch.randn(*s, dtype=torch.bfloat16) * 0.4)(F, D) * (2.0 / F) ** 0.5),
        }
    _B_in = _B_setup()

    return {
        "A_x": _A_in["x"],
        "A_k_cache": _A_in["k_cache"],
        "A_v_cache": _A_in["v_cache"],
        "A_wq": _A_in["wq"],
        "A_wo": _A_in["wo"],
        "B_x": _B_in["x"],
        "B_w1": _B_in["w1"],
        "B_w3": _B_in["w3"],
        "B_w2": _B_in["w2"],
    }

def reference(A_x, A_k_cache, A_v_cache, A_wq, A_wo, B_x, B_w1, B_w3, B_w2):
    def _A_ref(x, k_cache, v_cache, wq, wo):
        B, Sq, D = x.shape
        H, Dh = 32, 128
        q = (lambda x, *d: x.permute(*d))((x @ wq).reshape(B, Sq, H, Dh), 0, 2, 1, 3)
        scores = torch.einsum("bhqd,bhkd->bhqk", q, k_cache) * (Dh ** -0.5)
        p = _softmax(scores, axis=-1)
        o = torch.einsum("bhqk,bhkd->bhqd", p, v_cache)
        return (lambda x, *d: x.permute(*d))(o, 0, 2, 1, 3).reshape(B, Sq, H * Dh) @ wo

    def _B_ref(x, w1, w3, w2):
        gate = torch.einsum("bsd,df->bsf", x, w1)
        up = torch.einsum("bsd,df->bsf", x, w3)
        h = _silu(gate) * up
        out = torch.einsum("bsf,fd->bsd", h, w2)
        return out

    x = A_x
    xn = _layernorm(x, axis=-1)
    return x + _A_ref(xn, A_k_cache, A_v_cache, A_wq, A_wo) + _B_ref(xn, B_w1, B_w3, B_w2)
```

```cpp
#include <hip/hip_runtime.h>
#include <hip/hip_bf16.h>
#include <stdint.h>

#define KDIST 6
#define KSTAG 1
#define MIMIC 1
#define STAMPS 0

namespace {

constexpr int NWG = 256, NTHR = 512;
constexpr int DM = 512, NH = 32, SK = 16384, DH = 128, FF = 1792;
constexpr float LOG2E = 1.4426950408889634f;
constexpr float SCALE = 0.08838834764831845f;
constexpr size_t WS_EPOCH = 0, WS_ML = 4096, WS_Y = 65536, WS_STAMP = 8u << 20;
constexpr int L_XN = 0;
constexpr int L_S = L_XN + 512;
constexpr int L_RED = L_S + 4096;
constexpr int L_MLP = L_RED + 4096;
constexpr int L_H = L_MLP + 128;
constexpr int L_ML = L_H + 16;
constexpr int L_FW = L_ML + 8;
constexpr int L_FIN = L_FW + 4;
constexpr int L_CNT = L_FIN + 16;
constexpr int L_END = L_CNT + 4;

#define GAS __attribute__((address_space(1)))
#define LAS __attribute__((address_space(3)))
#define RLX_AGENT __ATOMIC_RELAXED, __HIP_MEMORY_SCOPE_AGENT
typedef unsigned u32x4 __attribute__((ext_vector_type(4)));
typedef float f32x2 __attribute__((ext_vector_type(2)));
typedef float f32x4 __attribute__((ext_vector_type(4)));
typedef __bf16 bf16x2 __attribute__((ext_vector_type(2)));
typedef GAS unsigned gu32;
typedef GAS unsigned long long gu64;

__device__ __forceinline__ float bf_lo(unsigned u) { return __builtin_bit_cast(float, u << 16); }
__device__ __forceinline__ float bf_hi(unsigned u) { return __builtin_bit_cast(float, u & 0xffff0000u); }
__device__ __forceinline__ f32x2 bf_unpack(unsigned u) { return f32x2{bf_lo(u), bf_hi(u)}; }
__device__ __forceinline__ unsigned pack_bf16(float lo, float hi) {
  bf16x2 r = {(__bf16)lo, (__bf16)hi};
  return __builtin_bit_cast(unsigned, r);
}
__device__ __forceinline__ float rbf(float x) {
  return (float)(__bf16)x;
}
__device__ __forceinline__ float dot2(unsigned a, unsigned b, float acc) {
  return __builtin_amdgcn_fdot2_f32_bf16(__builtin_bit_cast(bf16x2, a), __builtin_bit_cast(bf16x2, b), acc, false);
}
template <int CTRL> __device__ __forceinline__ float dpp(float x) {
  return __builtin_bit_cast(float, __builtin_amdgcn_mov_dpp(__builtin_bit_cast(int, x), CTRL, 0xf, 0xf, true));
}
constexpr int XOR1 = 0xB1, XOR2 = 0x4E, XOR7 = 0x141, ROR2 = 0x122, ROR4 = 0x124, ROR8 = 0x128;
__device__ __forceinline__ float xrow_sum(float x) {
  auto s = __builtin_amdgcn_permlane16_swap(__float_as_uint(x), __float_as_uint(x), false, false);
  x = __uint_as_float(s[0]) + __uint_as_float(s[1]);
  auto t = __builtin_amdgcn_permlane32_swap(__float_as_uint(x), __float_as_uint(x), false, false);
  return __uint_as_float(t[0]) + __uint_as_float(t[1]);
}
__device__ __forceinline__ float xrow_max(float x) {
  auto s = __builtin_amdgcn_permlane16_swap(__float_as_uint(x), __float_as_uint(x), false, false);
  x = fmaxf(__uint_as_float(s[0]), __uint_as_float(s[1]));
  auto t = __builtin_amdgcn_permlane32_swap(__float_as_uint(x), __float_as_uint(x), false, false);
  return fmaxf(__uint_as_float(t[0]), __uint_as_float(t[1]));
}
__device__ __forceinline__ float wave_sum(float x) {
  x += dpp<XOR1>(x); x += dpp<XOR2>(x); x += dpp<XOR7>(x); x += dpp<ROR8>(x);
  return xrow_sum(x);
}
__device__ __forceinline__ float wave_max(float x) {
  x = fmaxf(x, dpp<XOR1>(x)); x = fmaxf(x, dpp<XOR2>(x)); x = fmaxf(x, dpp<XOR7>(x)); x = fmaxf(x, dpp<ROR8>(x));
  return xrow_max(x);
}
__device__ __forceinline__ void lds_wait() { asm volatile("s_waitcnt lgkmcnt(0)" ::: "memory"); }
__device__ __forceinline__ void st_granule(gu64* g, unsigned tag, float v) {
  __hip_atomic_store(g, ((unsigned long long)tag << 32) | (unsigned long long)__float_as_uint(v), RLX_AGENT);
}
constexpr unsigned long long SPIN_TICKS = 20000000ull;

struct Args {
  const unsigned short* x; const unsigned short* k; const unsigned short* v; const unsigned short* wq; const unsigned short* wo;
  const unsigned short* w1; const unsigned short* w3; const unsigned short* w2;
  unsigned short* out; unsigned char* ws;
};

#define STAMP(i) do { } while (0)

typedef __amdgpu_buffer_rsrc_t rsrc_t;
__device__ __forceinline__ u32x4 ldnt(rsrc_t r, int voff, int soff) { return __builtin_amdgcn_raw_buffer_load_b128(r, voff, soff, 2); }

__global__ void __launch_bounds__(NTHR, 2) fused_decode(Args a) {
  __shared__ __attribute__((aligned(16))) float lds[L_END];
  const int tid = threadIdx.x, lane = tid & 63, wave = __builtin_amdgcn_readfirstlane(tid >> 6);
  const int r = lane >> 4, c = lane & 15;
  const int xg = blockIdx.x & 7, j = blockIdx.x >> 3;
  const int b = j >> 4, h = xg * 4 + ((j >> 2) & 3), sp = j & 3;
  const int chunk = b * 128 + h * 4 + sp;
  STAMP(0);

  const u32x4 xv = *(const GAS u32x4*)(a.x + b * DM + lane * 8);
  const rsrc_t rq = __builtin_amdgcn_make_buffer_rsrc((void*)(a.wq + h * DH), 0, (DM * NH * DH - h * DH) * 2, 0x00020000);
  u32x4 wt[16];
#pragma unroll
  for (int i = 0; i < 16; ++i) wt[i] = ldnt(rq, (16 * r + i) * (NH * DH * 2) + c * 16, wave * 64 * (NH * DH * 2));
  __builtin_amdgcn_sched_barrier(0);

  const rsrc_t rk = __builtin_amdgcn_make_buffer_rsrc((void*)(a.k + (size_t)chunk * 4096 * DH), 0, 4096 * DH * 2, 0x00020000);
  const rsrc_t rv = __builtin_amdgcn_make_buffer_rsrc((void*)(a.v + (size_t)chunk * 4096 * DH), 0, 4096 * DH * 2, 0x00020000);
  const int stag = j * KSTAG;
  const int voff = lane * 16;
  auto tile_off = [&](int t) -> int { return (((t + stag) & 63) << 14) + ((wave & 3) << 12); };
  u32x4 buf[8];
  if (wave < 4) {
#pragma unroll
    for (int u = 0; u < KDIST; ++u) { buf[u] = ldnt(rk, voff + (u & 3) * 1024, tile_off(u >> 2)); __builtin_amdgcn_sched_barrier(0); }
  }
  if (tid < 2) ((LAS unsigned*)lds)[L_CNT + tid] = 0u;

  {
    float xf[8];
#pragma unroll
    for (int k = 0; k < 4; ++k) { xf[2 * k] = bf_lo(xv[k]); xf[2 * k + 1] = bf_hi(xv[k]); }
    float s = 0.f;
#pragma unroll
    for (int k = 0; k < 8; ++k) s += xf[k];
    const float mean = wave_sum(s) * (1.f / DM);
    float q2 = 0.f;
#pragma unroll
    for (int k = 0; k < 8; ++k) { const float d = xf[k] - mean; q2 = fmaf(d, d, q2); }
    const float var = wave_sum(q2) * (1.f / DM);
    const float mb = rbf(mean), sd = rbf(__builtin_sqrtf(rbf(rbf(var) + 1e-5f)));
    f32x4 o0, o1;
#pragma unroll
    for (int k = 0; k < 4; ++k) { o0[k] = rbf(rbf(xf[k] - mb) / sd); o1[k] = rbf(rbf(xf[4 + k] - mb) / sd); }
    *(LAS f32x4*)(lds + L_XN + lane * 8) = o0;
    *(LAS f32x4*)(lds + L_XN + lane * 8 + 4) = o1;
    lds_wait();
  }
  {
    f32x2 qa[4] = {{0.f, 0.f}, {0.f, 0.f}, {0.f, 0.f}, {0.f, 0.f}};
#pragma unroll
    for (int i4 = 0; i4 < 4; ++i4) {
      const f32x4 xs = *(const LAS f32x4*)(lds + L_XN + wave * 64 + r * 16 + i4 * 4);
#pragma unroll
      for (int i = 0; i < 4; ++i)
#pragma unroll
        for (int k = 0; k < 4; ++k) qa[k] = __builtin_elementwise_fma(bf_unpack(wt[i4 * 4 + i][k]), f32x2{xs[i], xs[i]}, qa[k]);
    }
    f32x4 o0, o1;
#pragma unroll
    for (int k = 0; k < 2; ++k) { o0[2 * k] = xrow_sum(qa[k][0]); o0[2 * k + 1] = xrow_sum(qa[k][1]); o1[2 * k] = xrow_sum(qa[2 + k][0]); o1[2 * k + 1] = xrow_sum(qa[2 + k][1]); }
    if (r == 0) {
      *(LAS f32x4*)(lds + L_RED + wave * 128 + c * 8) = o0;
      *(LAS f32x4*)(lds + L_RED + wave * 128 + c * 8 + 4) = o1;
    }
  }
  const rsrc_t ro = __builtin_amdgcn_make_buffer_rsrc((void*)(a.wo + (size_t)h * DH * DM), 0, DH * DM * 2, 0x00020000);
  f32x2 yacc[4] = {{0.f, 0.f}, {0.f, 0.f}, {0.f, 0.f}, {0.f, 0.f}};
  STAMP(1);
  __syncthreads();
  const unsigned tag = __builtin_amdgcn_readfirstlane(__hip_atomic_load((gu32*)(a.ws + WS_EPOCH), RLX_AGENT)) + 0x9E3779B9u;

  if (wave < 4) {
    unsigned qp[4];
    {
      f32x4 s0 = *(const LAS f32x4*)(lds + L_RED + (2 * r) * 128 + c * 8) + *(const LAS f32x4*)(lds + L_RED + (2 * r + 1) * 128 + c * 8);
      f32x4 s1 = *(const LAS f32x4*)(lds + L_RED + (2 * r) * 128 + c * 8 + 4) + *(const LAS f32x4*)(lds + L_RED + (2 * r + 1) * 128 + c * 8 + 4);
#pragma unroll
      for (int k = 0; k < 4; ++k) { s0[k] = xrow_sum(s0[k]); s1[k] = xrow_sum(s1[k]); }
      qp[0] = pack_bf16(s0[0], s0[1]); qp[1] = pack_bf16(s0[2], s0[3]); qp[2] = pack_bf16(s1[0], s1[1]); qp[3] = pack_bf16(s1[2], s1[3]);
    }
    STAMP(2);
    LAS float* S = (LAS float*)lds + L_S + wave * 1024;
    float mx = -INFINITY;
#define K_PAIR(tt, LAST) do { \
      float x[8]; \
      _Pragma("unroll") for (int u = 0; u < 8; ++u) { \
        const int n = u + KDIST; \
        buf[n & 7] = ldnt(((LAST) && n >= 8) ? rv : rk, voff + (n & 3) * 1024, tile_off(2 * (tt) + (n >> 2))); \
        __builtin_amdgcn_sched_barrier(0); \
        float d = dot2(buf[u][0], qp[0], 0.f); d = dot2(buf[u][1], qp[1], d); d = dot2(buf[u][2], qp[2], d); x[u] = dot2(buf[u][3], qp[3], d); \
        __builtin_amdgcn_sched_barrier(0); \
      } \
      _Pragma("unroll") for (int u = 0; u < 4; ++u) { const float lo = x[u] + dpp<ROR8>(x[u]), hi = x[u + 4] + dpp<ROR8>(x[u + 4]); x[u] = (c & 8) ? hi : lo; } \
      _Pragma("unroll") for (int u = 0; u < 2; ++u) { const float lo = x[u] + dpp<XOR7>(x[u]), hi = x[u + 2] + dpp<XOR7>(x[u + 2]); x[u] = (c & 4) ? hi : lo; } \
      { const float lo = x[0] + dpp<XOR2>(x[0]), hi = x[1] + dpp<XOR2>(x[1]); x[0] = (c & 2) ? hi : lo; } \
      float sc = x[0] + dpp<XOR1>(x[0]); \
      sc = rbf(rbf(sc) * SCALE); \
      mx = fmaxf(mx, sc); \
      S[(tt) * 32 + r * 8 + (c >> 1)] = sc; \
      __builtin_amdgcn_sched_barrier(0); \
    } while (0)
    for (int tt = 0; tt < 31; ++tt) K_PAIR(tt, false);
    K_PAIR(31, true);
#undef K_PAIR
    STAMP(3);
#pragma unroll
    for (int i = 0; i < 16; ++i) wt[i] = ldnt(ro, voff + (i & 3) * 1024, (wave * 16 + (i & ~3)) * 1024);
    __builtin_amdgcn_sched_barrier(0);
    {
      mx = wave_max(mx);
      const float msub = mx * LOG2E;
      lds_wait();
      float l = 0.f;
#pragma unroll
      for (int i = 0; i < 4; ++i) {
        f32x4 s = *(const LAS f32x4*)(S + i * 256 + lane * 4);
#pragma unroll
        for (int k = 0; k < 4; ++k) { s[k] = __builtin_amdgcn_exp2f(fmaf(s[k], LOG2E, -msub)); l += s[k]; }
        *(LAS f32x4*)(S + i * 256 + lane * 4) = s;
      }
      l = wave_sum(l);
      if (lane == 0) { lds[L_ML + 2 * wave] = mx; lds[L_ML + 2 * wave + 1] = l; }
      lds_wait();
      if (lane == 0) __hip_atomic_fetch_add((LAS unsigned*)lds + L_CNT, 1u, __ATOMIC_RELAXED, __HIP_MEMORY_SCOPE_WORKGROUP);
    }
    STAMP(4);
    f32x2 acc[4] = {{0.f, 0.f}, {0.f, 0.f}, {0.f, 0.f}, {0.f, 0.f}};
#define V_PAIR(tt, LAST) do { \
      const f32x4 p0 = *(const LAS f32x4*)(S + (tt) * 32 + r * 8), p1 = *(const LAS f32x4*)(S + (tt) * 32 + r * 8 + 4); \
      _Pragma("unroll") for (int u = 0; u < 8; ++u) { \
        const int n = u + KDIST; \
        if (!(LAST) || n < 8) buf[n & 7] = ldnt(rv, voff + (n & 3) * 1024, tile_off(2 * (tt) + (n >> 2))); \
        __builtin_amdgcn_sched_barrier(0); \
        const float p = u < 4 ? p0[u & 3] : p1[u & 3]; \
        _Pragma("unroll") for (int k = 0; k < 4; ++k) acc[k] = __builtin_elementwise_fma(bf_unpack(buf[u][k]), f32x2{p, p}, acc[k]); \
        __builtin_amdgcn_sched_barrier(0); \
      } \
    } while (0)
    for (int tt = 0; tt < 31; ++tt) V_PAIR(tt, false);
    V_PAIR(31, true);
#undef V_PAIR
    STAMP(5);
    *(LAS f32x4*)(lds + L_RED + (wave * 4 + r) * 128 + c * 8) = f32x4{acc[0][0], acc[0][1], acc[1][0], acc[1][1]};
    *(LAS f32x4*)(lds + L_RED + (wave * 4 + r) * 128 + c * 8 + 4) = f32x4{acc[2][0], acc[2][1], acc[3][0], acc[3][1]};
  } else {
    const int hw = wave - 4, t = tid - 256;
    const int jj = j & 15;
    if (jj < 14) {
      const int f0 = (xg * 14 + jj) * 16;
      const rsrc_t r1 = __builtin_amdgcn_make_buffer_rsrc((void*)(a.w1 + f0), 0, (DM * FF - f0) * 2, 0x00020000);
      const rsrc_t r3 = __builtin_amdgcn_make_buffer_rsrc((void*)(a.w3 + f0), 0, (DM * FF - f0) * 2, 0x00020000);
      const rsrc_t r2 = __builtin_amdgcn_make_buffer_rsrc((void*)(a.w2 + (size_t)f0 * DM), 0, 16 * DM * 2, 0x00020000);
      u32x4 g[4], u[4], w2r[4];
      const int row = t >> 1, half = t & 1;
#pragma unroll
      for (int i = 0; i < 4; ++i) { g[i] = ldnt(r1, (row + 128 * i) * (FF * 2) + half * 16, 0); u[i] = ldnt(r3, (row + 128 * i) * (FF * 2) + half * 16, 0); }
#pragma unroll
      for (int i = 0; i < 4; ++i) w2r[i] = ldnt(r2, voff + i * 1024, hw * 4096);
      f32x2 ga[4] = {{0.f, 0.f}, {0.f, 0.f}, {0.f, 0.f}, {0.f, 0.f}}, ua[4] = {{0.f, 0.f}, {0.f, 0.f}, {0.f, 0.f}, {0.f, 0.f}};
#pragma unroll
      for (int i = 0; i < 4; ++i) {
        const float xs = lds[L_XN + row + 128 * i];
#pragma unroll
        for (int k = 0; k < 4; ++k) {
          ga[k] = __builtin_elementwise_fma(bf_unpack(g[i][k]), f32x2{xs, xs}, ga[k]);
          ua[k] = __builtin_elementwise_fma(bf_unpack(u[i][k]), f32x2{xs, xs}, ua[k]);
        }
      }
#pragma unroll
      for (int k = 0; k < 4; ++k)
#pragma unroll
        for (int e = 0; e < 2; ++e) {
          float s = ga[k][e]; s += dpp<ROR2>(s); s += dpp<ROR4>(s); s += dpp<ROR8>(s); ga[k][e] = xrow_sum(s);
          float q = ua[k][e]; q += dpp<ROR2>(q); q += dpp<ROR4>(q); q += dpp<ROR8>(q); ua[k][e] = xrow_sum(q);
        }
      if (lane < 2) {
#pragma unroll
        for (int k = 0; k < 4; ++k)
#pragma unroll
          for (int e = 0; e < 2; ++e) { lds[L_MLP + hw * 32 + half * 8 + 2 * k + e] = ga[k][e]; lds[L_MLP + hw * 32 + 16 + half * 8 + 2 * k + e] = ua[k][e]; }
      }
      lds_wait();
      if (lane == 0) __hip_atomic_fetch_add((LAS unsigned*)lds + L_CNT + 1, 1u, __ATOMIC_RELAXED, __HIP_MEMORY_SCOPE_WORKGROUP);
      while (__hip_atomic_load((LAS unsigned*)lds + L_CNT + 1, __ATOMIC_RELAXED, __HIP_MEMORY_SCOPE_WORKGROUP) < 4u) __builtin_amdgcn_s_sleep(1);
      asm volatile("" ::: "memory");
#pragma unroll
      for (int i = 0; i < 4; ++i) {
        const int f = hw * 4 + i;
        const float gs = rbf((lds[L_MLP + f] + lds[L_MLP + 32 + f]) + (lds[L_MLP + 64 + f] + lds[L_MLP + 96 + f]));
        const float us = rbf((lds[L_MLP + 16 + f] + lds[L_MLP + 48 + f]) + (lds[L_MLP + 80 + f] + lds[L_MLP + 112 + f]));
        const float si = rbf(gs / (1.f + __expf(-gs)));
        const float hv = rbf(si * us);
#pragma unroll
        for (int k = 0; k < 4; ++k) yacc[k] = __builtin_elementwise_fma(bf_unpack(w2r[i][k]), f32x2{hv, hv}, yacc[k]);
      }
    }
#pragma unroll
    for (int i = 0; i < 16; ++i) wt[i] = ldnt(ro, voff + (i & 3) * 1024, (wave * 16 + (i & ~3)) * 1024);
    STAMP(2);
    if (wave == 4) {
      while (__hip_atomic_load((LAS unsigned*)lds + L_CNT, __ATOMIC_RELAXED, __HIP_MEMORY_SCOPE_WORKGROUP) < 4u) __builtin_amdgcn_s_sleep(8);
      asm volatile("" ::: "memory");
      const float mw = lds[L_ML + 2 * (lane & 3)], lw = lds[L_ML + 2 * (lane & 3) + 1];
      float m = fmaxf(mw, dpp<XOR1>(mw)); m = fmaxf(m, dpp<XOR2>(m));
      float l = lw * __builtin_amdgcn_exp2f((mw - m) * LOG2E);
      l += dpp<XOR1>(l); l += dpp<XOR2>(l);
      gu64* ml = (gu64*)(a.ws + WS_ML);
      if (lane < 2) st_granule(ml + chunk * 2 + lane, tag, lane == 0 ? m : l);
      const unsigned long long t0 = __builtin_amdgcn_s_memrealtime();
      unsigned long long gm, gl;
      while (true) {
        gm = __hip_atomic_load(ml + ((chunk & ~3) + (lane & 3)) * 2, RLX_AGENT);
        gl = __hip_atomic_load(ml + ((chunk & ~3) + (lane & 3)) * 2 + 1, RLX_AGENT);
        if (__all((unsigned)(gm >> 32) == tag && (unsigned)(gl >> 32) == tag)) break;
        if (__builtin_amdgcn_s_memrealtime() - t0 > SPIN_TICKS) break;
        __builtin_amdgcn_s_sleep(8);
      }
      const float ms = __uint_as_float((unsigned)gm), ls = __uint_as_float((unsigned)gl);
      float M = fmaxf(ms, dpp<XOR1>(ms)); M = fmaxf(M, dpp<XOR2>(M));
      float L = ls * __builtin_amdgcn_exp2f((ms - M) * LOG2E);
      L += dpp<XOR1>(L); L += dpp<XOR2>(L);
      if (lane < 4) lds[L_FW + lane] = __builtin_amdgcn_exp2f((mw - M) * LOG2E) / L;
      STAMP(3);
    }
  }
  __syncthreads();
  STAMP(6);
  {
    const LAS float* P = (const LAS float*)lds + L_RED + (4 * r) * 128 + wave * 16 + c;
    float o = ((P[0] + P[128]) + (P[256] + P[384])) * lds[L_FW + r];
    o = xrow_sum(o);
#pragma unroll
    for (int i = 0; i < 16; ++i) {
      const float oi = __builtin_bit_cast(float, __builtin_amdgcn_readlane(__builtin_bit_cast(int, o), i));
#pragma unroll
      for (int k = 0; k < 4; ++k) yacc[k] = __builtin_elementwise_fma(bf_unpack(wt[i][k]), f32x2{oi, oi}, yacc[k]);
    }
  }
  __syncthreads();
  *(LAS f32x4*)(lds + L_RED + wave * 512 + lane * 8) = f32x4{yacc[0][0], yacc[0][1], yacc[1][0], yacc[1][1]};
  *(LAS f32x4*)(lds + L_RED + wave * 512 + lane * 8 + 4) = f32x4{yacc[2][0], yacc[2][1], yacc[3][0], yacc[3][1]};
  __syncthreads();
  gu64* Y = (gu64*)(a.ws + WS_Y);
  {
    const LAS float* P = (const LAS float*)lds + L_RED + tid;
    const float y = ((P[0] + P[512]) + (P[1024] + P[1536])) + ((P[2048] + P[2560]) + (P[3072] + P[3584]));
    st_granule(Y + (size_t)(tid >> 1) * 512 + chunk * 2 + (tid & 1), tag, y);
  }
  STAMP(7);
  {
    const unsigned xw = tid < 2 ? *(const gu32*)(a.x + tid * DM + 2 * blockIdx.x) : 0u;
    const unsigned long long t0 = __builtin_amdgcn_s_memrealtime();
    unsigned long long g;
    while (true) {
      g = __hip_atomic_load(Y + (size_t)blockIdx.x * 512 + tid, RLX_AGENT);
      if (__all((unsigned)(g >> 32) == tag)) break;
      if (__builtin_amdgcn_s_memrealtime() - t0 > SPIN_TICKS) break;
      __builtin_amdgcn_s_sleep(4);
    }
    float s = __uint_as_float((unsigned)g);
    s += dpp<ROR2>(s); s += dpp<ROR4>(s); s += dpp<ROR8>(s); s = xrow_sum(s);
    if (lane < 2) lds[L_FIN + wave * 2 + lane] = s;
    __syncthreads();
    if (tid < 2) {
      const LAS float* P = (const LAS float*)lds + L_FIN + tid * 8;
      const float o0 = bf_lo(xw) + ((P[0] + P[2]) + (P[4] + P[6])), o1 = bf_hi(xw) + ((P[1] + P[3]) + (P[5] + P[7]));
      *(gu32*)(a.out + tid * DM + 2 * blockIdx.x) = pack_bf16(o0, o1);
      if (blockIdx.x == 0 && tid == 0) __hip_atomic_store((gu32*)(a.ws + WS_EPOCH), tag, RLX_AGENT);
    }
  }
  STAMP(8);
}

}

extern "C" void kernel_launch(void* const* d_in, const int* in_sizes, int n_in, void* d_out, int out_size, void* d_ws, size_t ws_size,
                              hipStream_t stream) {
  Args a;
  a.x = (const unsigned short*)d_in[0]; a.k = (const unsigned short*)d_in[1]; a.v = (const unsigned short*)d_in[2];
  a.wq = (const unsigned short*)d_in[3]; a.wo = (const unsigned short*)d_in[4];
  a.w1 = (const unsigned short*)d_in[6]; a.w3 = (const unsigned short*)d_in[7]; a.w2 = (const unsigned short*)d_in[8];
  a.out = (unsigned short*)d_out; a.ws = (unsigned char*)d_ws;
  fused_decode<<<NWG, NTHR, 0, stream>>>(a);
}
```

```cpp
#define WPOL 0
#include <hip/hip_runtime.h>
#include <hip/hip_bf16.h>
#include <stdint.h>

#define KDIST 6
#define KSTAG 1
#define MIMIC 1
#define STAMPS 0

namespace {

constexpr int NWG = 256, NTHR = 512;
constexpr int DM = 512, NH = 32, SK = 16384, DH = 128, FF = 1792;
constexpr float LOG2E = 1.4426950408889634f;
constexpr float SCALE = 0.08838834764831845f;
constexpr size_t WS_EPOCH = 0, WS_ML = 4096, WS_Y = 65536, WS_STAMP = 8u << 20;
constexpr int L_XN = 0;
constexpr int L_S = L_XN + 512;
constexpr int L_RED = L_S + 4096;
constexpr int L_MLP = L_RED + 4096;
constexpr int L_H = L_MLP + 128;
constexpr int L_ML = L_H + 16;
constexpr int L_FW = L_ML + 8;
constexpr int L_FIN = L_FW + 4;
constexpr int L_CNT = L_FIN + 16;
constexpr int L_END = L_CNT + 4;

#define GAS __attribute__((address_space(1)))
#define LAS __attribute__((address_space(3)))
#define RLX_AGENT __ATOMIC_RELAXED, __HIP_MEMORY_SCOPE_AGENT
typedef unsigned u32x4 __attribute__((ext_vector_type(4)));
typedef float f32x2 __attribute__((ext_vector_type(2)));
typedef float f32x4 __attribute__((ext_vector_type(4)));
typedef __bf16 bf16x2 __attribute__((ext_vector_type(2)));
typedef GAS unsigned gu32;
typedef GAS unsigned long long gu64;

__device__ __forceinline__ float bf_lo(unsigned u) { return __builtin_bit_cast(float, u << 16); }
__device__ __forceinline__ float bf_hi(unsigned u) { return __builtin_bit_cast(float, u & 0xffff0000u); }
__device__ __forceinline__ f32x2 bf_unpack(unsigned u) { return f32x2{bf_lo(u), bf_hi(u)}; }
__device__ __forceinline__ unsigned pack_bf16(float lo, float hi) {
  bf16x2 r = {(__bf16)lo, (__bf16)hi};
  return __builtin_bit_cast(unsigned, r);
}
__device__ __forceinline__ float rbf(float x) {
  return (float)(__bf16)x;
}
__device__ __forceinline__ float dot2(unsigned a, unsigned b, float acc) {
  return __builtin_amdgcn_fdot2_f32_bf16(__builtin_bit_cast(bf16x2, a), __builtin_bit_cast(bf16x2, b), acc, false);
}
template <int CTRL> __device__ __forceinline__ float dpp(float x) {
  return __builtin_bit_cast(float, __builtin_amdgcn_mov_dpp(__builtin_bit_cast(int, x), CTRL, 0xf, 0xf, true));
}
constexpr int XOR1 = 0xB1, XOR2 = 0x4E, XOR7 = 0x141, ROR2 = 0x122, ROR4 = 0x124, ROR8 = 0x128;
__device__ __forceinline__ float xrow_sum(float x) {
  auto s = __builtin_amdgcn_permlane16_swap(__float_as_uint(x), __float_as_uint(x), false, false);
  x = __uint_as_float(s[0]) + __uint_as_float(s[1]);
  auto t = __builtin_amdgcn_permlane32_swap(__float_as_uint(x), __float_as_uint(x), false, false);
  return __uint_as_float(t[0]) + __uint_as_float(t[1]);
}
__device__ __forceinline__ float xrow_max(float x) {
  auto s = __builtin_amdgcn_permlane16_swap(__float_as_uint(x), __float_as_uint(x), false, false);
  x = fmaxf(__uint_as_float(s[0]), __uint_as_float(s[1]));
  auto t = __builtin_amdgcn_permlane32_swap(__float_as_uint(x), __float_as_uint(x), false, false);
  return fmaxf(__uint_as_float(t[0]), __uint_as_float(t[1]));
}
__device__ __forceinline__ float wave_sum(float x) {
  x += dpp<XOR1>(x); x += dpp<XOR2>(x); x += dpp<XOR7>(x); x += dpp<ROR8>(x);
  return xrow_sum(x);
}
__device__ __forceinline__ float wave_max(float x) {
  x = fmaxf(x, dpp<XOR1>(x)); x = fmaxf(x, dpp<XOR2>(x)); x = fmaxf(x, dpp<XOR7>(x)); x = fmaxf(x, dpp<ROR8>(x));
  return xrow_max(x);
}
__device__ __forceinline__ void lds_wait() { asm volatile("s_waitcnt lgkmcnt(0)" ::: "memory"); }
__device__ __forceinline__ void st_granule(gu64* g, unsigned tag, float v) {
  __hip_atomic_store(g, ((unsigned long long)tag << 32) | (unsigned long long)__float_as_uint(v), RLX_AGENT);
}
constexpr unsigned long long SPIN_TICKS = 20000000ull;

struct Args {
  const unsigned short* x; const unsigned short* k; const unsigned short* v; const unsigned short* wq; const unsigned short* wo;
  const unsigned short* w1; const unsigned short* w3; const unsigned short* w2;
  unsigned short* out; unsigned char* ws;
};

#define STAMP(i) do { } while (0)

typedef __amdgpu_buffer_rsrc_t rsrc_t;
#define VTHR 256
#define KTHR 256
__device__ __forceinline__ u32x4 ldnt(rsrc_t r, int voff, int soff) { return __builtin_amdgcn_raw_buffer_load_b128(r, voff, soff, WPOL); }
template <int AUX> __device__ __forceinline__ u32x4 ldp(rsrc_t r, int voff, int soff) { return __builtin_amdgcn_raw_buffer_load_b128(r, voff, soff, AUX); }

__global__ void __launch_bounds__(NTHR, 2) fused_decode(Args a) {
  __shared__ __attribute__((aligned(16))) float lds[L_END];
  const int tid = threadIdx.x, lane = tid & 63, wave = __builtin_amdgcn_readfirstlane(tid >> 6);
  const int r = lane >> 4, c = lane & 15;
  const int xg = blockIdx.x & 7, j = blockIdx.x >> 3;
  const int b = j >> 4, h = xg * 4 + ((j >> 2) & 3), sp = j & 3;
  const int chunk = b * 128 + h * 4 + sp;
  STAMP(0);

  const u32x4 xv = *(const GAS u32x4*)(a.x + b * DM + lane * 8);
  const rsrc_t rq = __builtin_amdgcn_make_buffer_rsrc((void*)(a.wq + h * DH), 0, (DM * NH * DH - h * DH) * 2, 0x00020000);
  u32x4 wt[16];
#pragma unroll
  for (int i = 0; i < 16; ++i) wt[i] = ldnt(rq, (16 * r + i) * (NH * DH * 2) + c * 16, wave * 64 * (NH * DH * 2));
  __builtin_amdgcn_sched_barrier(0);

  const rsrc_t rk = __builtin_amdgcn_make_buffer_rsrc((void*)(a.k + (size_t)chunk * 4096 * DH), 0, 4096 * DH * 2, 0x00020000);
  const rsrc_t rv = __builtin_amdgcn_make_buffer_rsrc((void*)(a.v + (size_t)chunk * 4096 * DH), 0, 4096 * DH * 2, 0x00020000);
  const int stag = j * KSTAG;
  const int voff = lane * 16;
  auto tile_off = [&](int t) -> int { return (((t + stag) & 63) << 14) + ((wave & 3) << 12); };
  u32x4 buf[8];
  if (wave < 4) {
#pragma unroll
    for (int u = 0; u < KDIST; ++u) { buf[u] = ldp<2>(rk, voff + (u & 3) * 1024, tile_off(u >> 2)); __builtin_amdgcn_sched_barrier(0); }
  }
  if (tid < 2) ((LAS unsigned*)lds)[L_CNT + tid] = 0u;

  {
    float xf[8];
#pragma unroll
    for (int k = 0; k < 4; ++k) { xf[2 * k] = bf_lo(xv[k]); xf[2 * k + 1] = bf_hi(xv[k]); }
    float s = 0.f;
#pragma unroll
    for (int k = 0; k < 8; ++k) s += xf[k];
    const float mean = wave_sum(s) * (1.f / DM);
    float q2 = 0.f;
#pragma unroll
    for (int k = 0; k < 8; ++k) { const float d = xf[k] - mean; q2 = fmaf(d, d, q2); }
    const float var = wave_sum(q2) * (1.f / DM);
    const float mb = rbf(mean), sd = rbf(__builtin_sqrtf(rbf(rbf(var) + 1e-5f)));
    f32x4 o0, o1;
#pragma unroll
    for (int k = 0; k < 4; ++k) { o0[k] = rbf(rbf(xf[k] - mb) / sd); o1[k] = rbf(rbf(xf[4 + k] - mb) / sd); }
    *(LAS f32x4*)(lds + L_XN + lane * 8) = o0;
    *(LAS f32x4*)(lds + L_XN + lane * 8 + 4) = o1;
    lds_wait();
  }
  {
    f32x2 qa[4] = {{0.f, 0.f}, {0.f, 0.f}, {0.f, 0.f}, {0.f, 0.f}};
#pragma unroll
    for (int i4 = 0; i4 < 4; ++i4) {
      const f32x4 xs = *(const LAS f32x4*)(lds + L_XN + wave * 64 + r * 16 + i4 * 4);
#pragma unroll
      for (int i = 0; i < 4; ++i)
#pragma unroll
        for (int k = 0; k < 4; ++k) qa[k] = __builtin_elementwise_fma(bf_unpack(wt[i4 * 4 + i][k]), f32x2{xs[i], xs[i]}, qa[k]);
    }
    f32x4 o0, o1;
#pragma unroll
    for (int k = 0; k < 2; ++k) { o0[2 * k] = xrow_sum(qa[k][0]); o0[2 * k + 1] = xrow_sum(qa[k][1]); o1[2 * k] = xrow_sum(qa[2 + k][0]); o1[2 * k + 1] = xrow_sum(qa[2 + k][1]); }
    if (r == 0) {
      *(LAS f32x4*)(lds + L_RED + wave * 128 + c * 8) = o0;
      *(LAS f32x4*)(lds + L_RED + wave * 128 + c * 8 + 4) = o1;
    }
  }
  const rsrc_t ro = __builtin_amdgcn_make_buffer_rsrc((void*)(a.wo + (size_t)h * DH * DM), 0, DH * DM * 2, 0x00020000);
  f32x2 yacc[4] = {{0.f, 0.f}, {0.f, 0.f}, {0.f, 0.f}, {0.f, 0.f}};
  STAMP(1);
  __syncthreads();
  const unsigned tag = __builtin_amdgcn_readfirstlane(__hip_atomic_load((gu32*)(a.ws + WS_EPOCH), RLX_AGENT)) + 0x9E3779B9u;

  if (wave < 4) {
    unsigned qp[4];
    {
      f32x4 s0 = *(const LAS f32x4*)(lds + L_RED + (2 * r) * 128 + c * 8) + *(const LAS f32x4*)(lds + L_RED + (2 * r + 1) * 128 + c * 8);
      f32x4 s1 = *(const LAS f32x4*)(lds + L_RED + (2 * r) * 128 + c * 8 + 4) + *(const LAS f32x4*)(lds + L_RED + (2 * r + 1) * 128 + c * 8 + 4);
#pragma unroll
      for (int k = 0; k < 4; ++k) { s0[k] = xrow_sum(s0[k]); s1[k] = xrow_sum(s1[k]); }
      qp[0] = pack_bf16(s0[0], s0[1]); qp[1] = pack_bf16(s0[2], s0[3]); qp[2] = pack_bf16(s1[0], s1[1]); qp[3] = pack_bf16(s1[2], s1[3]);
    }
    STAMP(2);
    LAS float* S = (LAS float*)lds + L_S + wave * 1024;
    float mx = -INFINITY;
#define K_PAIR(tt, LAST, POL) do { \
      float x[8]; \
      _Pragma("unroll") for (int u = 0; u < 8; ++u) { \
        const int n = u + KDIST; \
        buf[n & 7] = ldp<POL>(((LAST) && n >= 8) ? rv : rk, voff + (n & 3) * 1024, tile_off(2 * (tt) + (n >> 2))); \
        __builtin_amdgcn_sched_barrier(0); \
        float d = dot2(buf[u][0], qp[0], 0.f); d = dot2(buf[u][1], qp[1], d); d = dot2(buf[u][2], qp[2], d); x[u] = dot2(buf[u][3], qp[3], d); \
        __builtin_amdgcn_sched_barrier(0); \
      } \
      _Pragma("unroll") for (int u = 0; u < 4; ++u) { const float lo = x[u] + dpp<ROR8>(x[u]), hi = x[u + 4] + dpp<ROR8>(x[u + 4]); x[u] = (c & 8) ? hi : lo; } \
      _Pragma("unroll") for (int u = 0; u < 2; ++u) { const float lo = x[u] + dpp<XOR7>(x[u]), hi = x[u + 2] + dpp<XOR7>(x[u + 2]); x[u] = (c & 4) ? hi : lo; } \
      { const float lo = x[0] + dpp<XOR2>(x[0]), hi = x[1] + dpp<XOR2>(x[1]); x[0] = (c & 2) ? hi : lo; } \
      float sc = x[0] + dpp<XOR1>(x[0]); \
      sc = rbf(rbf(sc) * SCALE); \
      mx = fmaxf(mx, sc); \
      S[(tt) * 32 + r * 8 + (c >> 1)] = sc; \
      __builtin_amdgcn_sched_barrier(0); \
    } while (0)
    if (chunk < KTHR) { for (int tt = 0; tt < 31; ++tt) K_PAIR(tt, false, 2); } else { for (int tt = 0; tt < 31; ++tt) K_PAIR(tt, false, 0); }
    K_PAIR(31, true, 2);
#undef K_PAIR
    STAMP(3);
#pragma unroll
    for (int i = 0; i < 16; ++i) wt[i] = ldnt(ro, voff + (i & 3) * 1024, (wave * 16 + (i & ~3)) * 1024);
    __builtin_amdgcn_sched_barrier(0);
    {
      mx = wave_max(mx);
      const float msub = mx * LOG2E;
      lds_wait();
      float l = 0.f;
#pragma unroll
      for (int i = 0; i < 4; ++i) {
        f32x4 s = *(const LAS f32x4*)(S + i * 256 + lane * 4);
#pragma unroll
        for (int k = 0; k < 4; ++k) { s[k] = __builtin_amdgcn_exp2f(fmaf(s[k], LOG2E, -msub)); l += s[k]; }
        *(LAS f32x4*)(S + i * 256 + lane * 4) = s;
      }
      l = wave_sum(l);
      if (lane == 0) { lds[L_ML + 2 * wave] = mx; lds[L_ML + 2 * wave + 1] = l; }
      lds_wait();
      if (lane == 0) __hip_atomic_fetch_add((LAS unsigned*)lds + L_CNT, 1u, __ATOMIC_RELAXED, __HIP_MEMORY_SCOPE_WORKGROUP);
    }
    STAMP(4);
    f32x2 acc[4] = {{0.f, 0.f}, {0.f, 0.f}, {0.f, 0.f}, {0.f, 0.f}};
#define V_PAIR(tt, LAST, POL) do { \
      const f32x4 p0 = *(const LAS f32x4*)(S + (tt) * 32 + r * 8), p1 = *(const LAS f32x4*)(S + (tt) * 32 + r * 8 + 4); \
      _Pragma("unroll") for (int u = 0; u < 8; ++u) { \
        const int n = u + KDIST; \
        if (!(LAST) || n < 8) buf[n & 7] = ldp<POL>(rv, voff + (n & 3) * 1024, tile_off(2 * (tt) + (n >> 2))); \
        __builtin_amdgcn_sched_barrier(0); \
        const float p = u < 4 ? p0[u & 3] : p1[u & 3]; \
        _Pragma("unroll") for (int k = 0; k < 4; ++k) acc[k] = __builtin_elementwise_fma(bf_unpack(buf[u][k]), f32x2{p, p}, acc[k]); \
        __builtin_amdgcn_sched_barrier(0); \
      } \
    } while (0)
    if (chunk < VTHR) { for (int tt = 0; tt < 31; ++tt) V_PAIR(tt, false, 2); } else { for (int tt = 0; tt < 31; ++tt) V_PAIR(tt, false, 0); }
    V_PAIR(31, true, 2);
#undef V_PAIR
    STAMP(5);
    *(LAS f32x4*)(lds + L_RED + (wave * 4 + r) * 128 + c * 8) = f32x4{acc[0][0], acc[0][1], acc[1][0], acc[1][1]};
    *(LAS f32x4*)(lds + L_RED + (wave * 4 + r) * 128 + c * 8 + 4) = f32x4{acc[2][0], acc[2][1], acc[3][0], acc[3][1]};
  } else {
    const int hw = wave - 4, t = tid - 256;
    const int jj = j & 15;
    if (jj < 14) {
      const int f0 = (xg * 14 + jj) * 16;
      const rsrc_t r1 = __builtin_amdgcn_make_buffer_rsrc((void*)(a.w1 + f0), 0, (DM * FF - f0) * 2, 0x00020000);
      const rsrc_t r3 = __builtin_amdgcn_make_buffer_rsrc((void*)(a.w3 + f0), 0, (DM * FF - f0) * 2, 0x00020000);
      const rsrc_t r2 = __builtin_amdgcn_make_buffer_rsrc((void*)(a.w2 + (size_t)f0 * DM), 0, 16 * DM * 2, 0x00020000);
      u32x4 g[4], u[4], w2r[4];
      const int row = t >> 1, half = t & 1;
#pragma unroll
      for (int i = 0; i < 4; ++i) { g[i] = ldnt(r1, (row + 128 * i) * (FF * 2) + half * 16, 0); u[i] = ldnt(r3, (row + 128 * i) * (FF * 2) + half * 16, 0); }
#pragma unroll
      for (int i = 0; i < 4; ++i) w2r[i] = ldnt(r2, voff + i * 1024, hw * 4096);
      f32x2 ga[4] = {{0.f, 0.f}, {0.f, 0.f}, {0.f, 0.f}, {0.f, 0.f}}, ua[4] = {{0.f, 0.f}, {0.f, 0.f}, {0.f, 0.f}, {0.f, 0.f}};
#pragma unroll
      for (int i = 0; i < 4; ++i) {
        const float xs = lds[L_XN + row + 128 * i];
#pragma unroll
        for (int k = 0; k < 4; ++k) {
          ga[k] = __builtin_elementwise_fma(bf_unpack(g[i][k]), f32x2{xs, xs}, ga[k]);
          ua[k] = __builtin_elementwise_fma(bf_unpack(u[i][k]), f32x2{xs, xs}, ua[k]);
        }
      }
#pragma unroll
      for (int k = 0; k < 4; ++k)
#pragma unroll
        for (int e = 0; e < 2; ++e) {
          float s = ga[k][e]; s += dpp<ROR2>(s); s += dpp<ROR4>(s); s += dpp<ROR8>(s); ga[k][e] = xrow_sum(s);
          float q = ua[k][e]; q += dpp<ROR2>(q); q += dpp<ROR4>(q); q += dpp<ROR8>(q); ua[k][e] = xrow_sum(q);
        }
      if (lane < 2) {
#pragma unroll
        for (int k = 0; k < 4; ++k)
#pragma unroll
          for (int e = 0; e < 2; ++e) { lds[L_MLP + hw * 32 + half * 8 + 2 * k + e] = ga[k][e]; lds[L_MLP + hw * 32 + 16 + half * 8 + 2 * k + e] = ua[k][e]; }
      }
      lds_wait();
      if (lane == 0) __hip_atomic_fetch_add((LAS unsigned*)lds + L_CNT + 1, 1u, __ATOMIC_RELAXED, __HIP_MEMORY_SCOPE_WORKGROUP);
      while (__hip_atomic_load((LAS unsigned*)lds + L_CNT + 1, __ATOMIC_RELAXED, __HIP_MEMORY_SCOPE_WORKGROUP) < 4u) __builtin_amdgcn_s_sleep(1);
      asm volatile("" ::: "memory");
#pragma unroll
      for (int i = 0; i < 4; ++i) {
        const int f = hw * 4 + i;
        const float gs = rbf((lds[L_MLP + f] + lds[L_MLP + 32 + f]) + (lds[L_MLP + 64 + f] + lds[L_MLP + 96 + f]));
        const float us = rbf((lds[L_MLP + 16 + f] + lds[L_MLP + 48 + f]) + (lds[L_MLP + 80 + f] + lds[L_MLP + 112 + f]));
        const float si = rbf(gs / (1.f + __expf(-gs)));
        const float hv = rbf(si * us);
#pragma unroll
        for (int k = 0; k < 4; ++k) yacc[k] = __builtin_elementwise_fma(bf_unpack(w2r[i][k]), f32x2{hv, hv}, yacc[k]);
      }
    }
#pragma unroll
    for (int i = 0; i < 16; ++i) wt[i] = ldnt(ro, voff + (i & 3) * 1024, (wave * 16 + (i & ~3)) * 1024);
    STAMP(2);
    if (wave == 4) {
      while (__hip_atomic_load((LAS unsigned*)lds + L_CNT, __ATOMIC_RELAXED, __HIP_MEMORY_SCOPE_WORKGROUP) < 4u) __builtin_amdgcn_s_sleep(8);
      asm volatile("" ::: "memory");
      const float mw = lds[L_ML + 2 * (lane & 3)], lw = lds[L_ML + 2 * (lane & 3) + 1];
      float m = fmaxf(mw, dpp<XOR1>(mw)); m = fmaxf(m, dpp<XOR2>(m));
      float l = lw * __builtin_amdgcn_exp2f((mw - m) * LOG2E);
      l += dpp<XOR1>(l); l += dpp<XOR2>(l);
      gu64* ml = (gu64*)(a.ws + WS_ML);
      if (lane < 2) st_granule(ml + chunk * 2 + lane, tag, lane == 0 ? m : l);
      const unsigned long long t0 = __builtin_amdgcn_s_memrealtime();
      unsigned long long gm, gl;
      while (true) {
        gm = __hip_atomic_load(ml + ((chunk & ~3) + (lane & 3)) * 2, RLX_AGENT);
        gl = __hip_atomic_load(ml + ((chunk & ~3) + (lane & 3)) * 2 + 1, RLX_AGENT);
        if (__all((unsigned)(gm >> 32) == tag && (unsigned)(gl >> 32) == tag)) break;
        if (__builtin_amdgcn_s_memrealtime() - t0 > SPIN_TICKS) break;
        __builtin_amdgcn_s_sleep(8);
      }
      const float ms = __uint_as_float((unsigned)gm), ls = __uint_as_float((unsigned)gl);
      float M = fmaxf(ms, dpp<XOR1>(ms)); M = fmaxf(M, dpp<XOR2>(M));
      float L = ls * __builtin_amdgcn_exp2f((ms - M) * LOG2E);
      L += dpp<XOR1>(L); L += dpp<XOR2>(L);
      if (lane < 4) lds[L_FW + lane] = __builtin_amdgcn_exp2f((mw - M) * LOG2E) / L;
      STAMP(3);
    }
  }
  __syncthreads();
  STAMP(6);
  {
    const LAS float* P = (const LAS float*)lds + L_RED + (4 * r) * 128 + wave * 16 + c;
    float o = ((P[0] + P[128]) + (P[256] + P[384])) * lds[L_FW + r];
    o = xrow_sum(o);
#pragma unroll
    for (int i = 0; i < 16; ++i) {
      const float oi = __builtin_bit_cast(float, __builtin_amdgcn_readlane(__builtin_bit_cast(int, o), i));
#pragma unroll
      for (int k = 0; k < 4; ++k) yacc[k] = __builtin_elementwise_fma(bf_unpack(wt[i][k]), f32x2{oi, oi}, yacc[k]);
    }
  }
  __syncthreads();
  *(LAS f32x4*)(lds + L_RED + wave * 512 + lane * 8) = f32x4{yacc[0][0], yacc[0][1], yacc[1][0], yacc[1][1]};
  *(LAS f32x4*)(lds + L_RED + wave * 512 + lane * 8 + 4) = f32x4{yacc[2][0], yacc[2][1], yacc[3][0], yacc[3][1]};
  __syncthreads();
  gu64* Y = (gu64*)(a.ws + WS_Y);
  {
    const LAS float* P = (const LAS float*)lds + L_RED + tid;
    const float y = ((P[0] + P[512]) + (P[1024] + P[1536])) + ((P[2048] + P[2560]) + (P[3072] + P[3584]));
    st_granule(Y + (size_t)(tid >> 1) * 512 + chunk * 2 + (tid & 1), tag, y);
  }
  STAMP(7);
  {
    const unsigned xw = tid < 2 ? *(const gu32*)(a.x + tid * DM + 2 * blockIdx.x) : 0u;
    const unsigned long long t0 = __builtin_amdgcn_s_memrealtime();
    unsigned long long g;
    while (true) {
      g = __hip_atomic_load(Y + (size_t)blockIdx.x * 512 + tid, RLX_AGENT);
      if (__all((unsigned)(g >> 32) == tag)) break;
      if (__builtin_amdgcn_s_memrealtime() - t0 > SPIN_TICKS) break;
      __builtin_amdgcn_s_sleep(4);
    }
    float s = __uint_as_float((unsigned)g);
    s += dpp<ROR2>(s); s += dpp<ROR4>(s); s += dpp<ROR8>(s); s = xrow_sum(s);
    if (lane < 2) lds[L_FIN + wave * 2 + lane] = s;
    __syncthreads();
    if (tid < 2) {
      const LAS float* P = (const LAS float*)lds + L_FIN + tid * 8;
      const float o0 = bf_lo(xw) + ((P[0] + P[2]) + (P[4] + P[6])), o1 = bf_hi(xw) + ((P[1] + P[3]) + (P[5] + P[7]));
      *(gu32*)(a.out + tid * DM + 2 * blockIdx.x) = pack_bf16(o0, o1);
      if (blockIdx.x == 0 && tid == 0) __hip_atomic_store((gu32*)(a.ws + WS_EPOCH), tag, RLX_AGENT);
    }
  }
  STAMP(8);
}

}

extern "C" void kernel_launch(void* const* d_in, const int* in_sizes, int n_in, void* d_out, int out_size, void* d_ws, size_t ws_size,
                              hipStream_t stream) {
  Args a;
  a.x = (const unsigned short*)d_in[0]; a.k = (const unsigned short*)d_in[1]; a.v = (const unsigned short*)d_in[2];
  a.wq = (const unsigned short*)d_in[3]; a.wo = (const unsigned short*)d_in[4];
  a.w1 = (const unsigned short*)d_in[6]; a.w3 = (const unsigned short*)d_in[7]; a.w2 = (const unsigned short*)d_in[8];
  a.out = (unsigned short*)d_out; a.ws = (unsigned char*)d_ws;
  fused_decode<<<NWG, NTHR, 0, stream>>>(a);
}
```

```cpp
#define DIAG 0
#include <hip/hip_runtime.h>
#include <hip/hip_bf16.h>
#include <stdint.h>

#define KDIST 6
#define KSTAG 1
#define MIMIC 1
#define STAMPS 0

namespace {

constexpr int NWG = 256, NTHR = 512;
constexpr int DM = 512, NH = 32, SK = 16384, DH = 128, FF = 1792;
constexpr float LOG2E = 1.4426950408889634f;
constexpr float SCALE = 0.08838834764831845f;
constexpr size_t WS_EPOCH = 0, WS_ML = 4096, WS_Y = 65536, WS_STAMP = 8u << 20;
constexpr int L_XN = 0;
constexpr int L_S = L_XN + 512;
constexpr int L_RED = L_S + 4096;
constexpr int L_MLP = L_RED + 4096;
constexpr int L_H = L_MLP + 128;
constexpr int L_ML = L_H + 16;
constexpr int L_FW = L_ML + 8;
constexpr int L_FIN = L_FW + 4;
constexpr int L_CNT = L_FIN + 16;
constexpr int L_END = L_CNT + 4;

#define GAS __attribute__((address_space(1)))
#define LAS __attribute__((address_space(3)))
#define RLX_AGENT __ATOMIC_RELAXED, __HIP_MEMORY_SCOPE_AGENT
typedef unsigned u32x4 __attribute__((ext_vector_type(4)));
typedef float f32x2 __attribute__((ext_vector_type(2)));
typedef float f32x4 __attribute__((ext_vector_type(4)));
typedef __bf16 bf16x2 __attribute__((ext_vector_type(2)));
typedef GAS unsigned gu32;
typedef GAS unsigned long long gu64;

__device__ __forceinline__ float bf_lo(unsigned u) { return __builtin_bit_cast(float, u << 16); }
__device__ __forceinline__ float bf_hi(unsigned u) { return __builtin_bit_cast(float, u & 0xffff0000u); }
__device__ __forceinline__ f32x2 bf_unpack(unsigned u) { return f32x2{bf_lo(u), bf_hi(u)}; }
__device__ __forceinline__ unsigned pack_bf16(float lo, float hi) {
  bf16x2 r = {(__bf16)lo, (__bf16)hi};
  return __builtin_bit_cast(unsigned, r);
}
__device__ __forceinline__ float rbf(float x) {
  return (float)(__bf16)x;
}
__device__ __forceinline__ float dot2(unsigned a, unsigned b, float acc) {
  return __builtin_amdgcn_fdot2_f32_bf16(__builtin_bit_cast(bf16x2, a), __builtin_bit_cast(bf16x2, b), acc, false);
}
template <int CTRL> __device__ __forceinline__ float dpp(float x) {
  return __builtin_bit_cast(float, __builtin_amdgcn_mov_dpp(__builtin_bit_cast(int, x), CTRL, 0xf, 0xf, true));
}
constexpr int XOR1 = 0xB1, XOR2 = 0x4E, XOR7 = 0x141, ROR2 = 0x122, ROR4 = 0x124, ROR8 = 0x128;
__device__ __forceinline__ float xrow_sum(float x) {
  auto s = __builtin_amdgcn_permlane16_swap(__float_as_uint(x), __float_as_uint(x), false, false);
  x = __uint_as_float(s[0]) + __uint_as_float(s[1]);
  auto t = __builtin_amdgcn_permlane32_swap(__float_as_uint(x), __float_as_uint(x), false, false);
  return __uint_as_float(t[0]) + __uint_as_float(t[1]);
}
__device__ __forceinline__ float xrow_max(float x) {
  auto s = __builtin_amdgcn_permlane16_swap(__float_as_uint(x), __float_as_uint(x), false, false);
  x = fmaxf(__uint_as_float(s[0]), __uint_as_float(s[1]));
  auto t = __builtin_amdgcn_permlane32_swap(__float_as_uint(x), __float_as_uint(x), false, false);
  return fmaxf(__uint_as_float(t[0]), __uint_as_float(t[1]));
}
__device__ __forceinline__ float wave_sum(float x) {
  x += dpp<XOR1>(x); x += dpp<XOR2>(x); x += dpp<XOR7>(x); x += dpp<ROR8>(x);
  return xrow_sum(x);
}
__device__ __forceinline__ float wave_max(float x) {
  x = fmaxf(x, dpp<XOR1>(x)); x = fmaxf(x, dpp<XOR2>(x)); x = fmaxf(x, dpp<XOR7>(x)); x = fmaxf(x, dpp<ROR8>(x));
  return xrow_max(x);
}
__device__ __forceinline__ void lds_wait() { asm volatile("s_waitcnt lgkmcnt(0)" ::: "memory"); }
__device__ __forceinline__ void st_granule(gu64* g, unsigned tag, float v) {
  __hip_atomic_store(g, ((unsigned long long)tag << 32) | (unsigned long long)__float_as_uint(v), RLX_AGENT);
}
constexpr unsigned long long SPIN_TICKS = 20000000ull;

struct Args {
  const unsigned short* x; const unsigned short* k; const unsigned short* v; const unsigned short* wq; const unsigned short* wo;
  const unsigned short* w1; const unsigned short* w3; const unsigned short* w2;
  unsigned short* out; unsigned char* ws;
};

#define STAMP(i) do { } while (0)

typedef __amdgpu_buffer_rsrc_t rsrc_t;
#define WPOL 2
#define VTHR 256
#define KTHR 256
__device__ __forceinline__ u32x4 ldnt(rsrc_t r, int voff, int soff) { return __builtin_amdgcn_raw_buffer_load_b128(r, voff, soff, WPOL); }
template <int AUX> __device__ __forceinline__ u32x4 ldp(rsrc_t r, int voff, int soff) { return __builtin_amdgcn_raw_buffer_load_b128(r, voff, soff, AUX); }

__global__ void __launch_bounds__(NTHR, 2) fused_decode(Args a) {
  __shared__ __attribute__((aligned(16))) float lds[L_END];
  const int tid = threadIdx.x, lane = tid & 63, wave = __builtin_amdgcn_readfirstlane(tid >> 6);
  const int r = lane >> 4, c = lane & 15;
  const int xg = blockIdx.x & 7, j = blockIdx.x >> 3;
  const int b = j >> 4, h = xg * 4 + ((j >> 2) & 3), sp = j & 3;
  const int chunk = b * 128 + h * 4 + sp;
  STAMP(0);
  const unsigned dg_t0 = (unsigned)__builtin_amdgcn_s_memrealtime();
  unsigned dg_t1 = 0, dg_t2 = 0;

  const u32x4 xv = *(const GAS u32x4*)(a.x + b * DM + lane * 8);
  const rsrc_t rq = __builtin_amdgcn_make_buffer_rsrc((void*)(a.wq + h * DH), 0, (DM * NH * DH - h * DH) * 2, 0x00020000);
  u32x4 wt[16];
#pragma unroll
  for (int i = 0; i < 16; ++i) wt[i] = ldnt(rq, (16 * r + i) * (NH * DH * 2) + c * 16, wave * 64 * (NH * DH * 2));
  __builtin_amdgcn_sched_barrier(0);

  const rsrc_t rk = __builtin_amdgcn_make_buffer_rsrc((void*)(a.k + (size_t)chunk * 4096 * DH), 0, 4096 * DH * 2, 0x00020000);
  const rsrc_t rv = __builtin_amdgcn_make_buffer_rsrc((void*)(a.v + (size_t)chunk * 4096 * DH), 0, 4096 * DH * 2, 0x00020000);
  const int stag = j * KSTAG;
  const int voff = lane * 16;
  auto tile_off = [&](int t) -> int { return (((t + stag) & 63) << 14) + ((wave & 3) << 12); };
  u32x4 buf[8];
  if (wave < 4) {
#pragma unroll
    for (int u = 0; u < KDIST; ++u) { buf[u] = ldp<2>(rk, voff + (u & 3) * 1024, tile_off(u >> 2)); __builtin_amdgcn_sched_barrier(0); }
  }
  if (tid < 2) ((LAS unsigned*)lds)[L_CNT + tid] = 0u;

  {
    float xf[8];
#pragma unroll
    for (int k = 0; k < 4; ++k) { xf[2 * k] = bf_lo(xv[k]); xf[2 * k + 1] = bf_hi(xv[k]); }
    float s = 0.f;
#pragma unroll
    for (int k = 0; k < 8; ++k) s += xf[k];
    const float mean = wave_sum(s) * (1.f / DM);
    float q2 = 0.f;
#pragma unroll
    for (int k = 0; k < 8; ++k) { const float d = xf[k] - mean; q2 = fmaf(d, d, q2); }
    const float var = wave_sum(q2) * (1.f / DM);
    const float mb = rbf(mean), sd = rbf(__builtin_sqrtf(rbf(rbf(var) + 1e-5f)));
    f32x4 o0, o1;
#pragma unroll
    for (int k = 0; k < 4; ++k) { o0[k] = rbf(rbf(xf[k] - mb) / sd); o1[k] = rbf(rbf(xf[4 + k] - mb) / sd); }
    *(LAS f32x4*)(lds + L_XN + lane * 8) = o0;
    *(LAS f32x4*)(lds + L_XN + lane * 8 + 4) = o1;
    lds_wait();
  }
  {
    f32x2 qa[4] = {{0.f, 0.f}, {0.f, 0.f}, {0.f, 0.f}, {0.f, 0.f}};
#pragma unroll
    for (int i4 = 0; i4 < 4; ++i4) {
      const f32x4 xs = *(const LAS f32x4*)(lds + L_XN + wave * 64 + r * 16 + i4 * 4);
#pragma unroll
      for (int i = 0; i < 4; ++i)
#pragma unroll
        for (int k = 0; k < 4; ++k) qa[k] = __builtin_elementwise_fma(bf_unpack(wt[i4 * 4 + i][k]), f32x2{xs[i], xs[i]}, qa[k]);
    }
    f32x4 o0, o1;
#pragma unroll
    for (int k = 0; k < 2; ++k) { o0[2 * k] = xrow_sum(qa[k][0]); o0[2 * k + 1] = xrow_sum(qa[k][1]); o1[2 * k] = xrow_sum(qa[2 + k][0]); o1[2 * k + 1] = xrow_sum(qa[2 + k][1]); }
    if (r == 0) {
      *(LAS f32x4*)(lds + L_RED + wave * 128 + c * 8) = o0;
      *(LAS f32x4*)(lds + L_RED + wave * 128 + c * 8 + 4) = o1;
    }
  }
  const rsrc_t ro = __builtin_amdgcn_make_buffer_rsrc((void*)(a.wo + (size_t)h * DH * DM), 0, DH * DM * 2, 0x00020000);
  f32x2 yacc[4] = {{0.f, 0.f}, {0.f, 0.f}, {0.f, 0.f}, {0.f, 0.f}};
  STAMP(1);
  __syncthreads();
  const unsigned tag = __builtin_amdgcn_readfirstlane(__hip_atomic_load((gu32*)(a.ws + WS_EPOCH), RLX_AGENT)) + 0x9E3779B9u;

  if (wave < 4) {
    unsigned qp[4];
    {
      f32x4 s0 = *(const LAS f32x4*)(lds + L_RED + (2 * r) * 128 + c * 8) + *(const LAS f32x4*)(lds + L_RED + (2 * r + 1) * 128 + c * 8);
      f32x4 s1 = *(const LAS f32x4*)(lds + L_RED + (2 * r) * 128 + c * 8 + 4) + *(const LAS f32x4*)(lds + L_RED + (2 * r + 1) * 128 + c * 8 + 4);
#pragma unroll
      for (int k = 0; k < 4; ++k) { s0[k] = xrow_sum(s0[k]); s1[k] = xrow_sum(s1[k]); }
      qp[0] = pack_bf16(s0[0], s0[1]); qp[1] = pack_bf16(s0[2], s0[3]); qp[2] = pack_bf16(s1[0], s1[1]); qp[3] = pack_bf16(s1[2], s1[3]);
    }
    STAMP(2);
    dg_t1 = (unsigned)__builtin_amdgcn_s_memrealtime();
    LAS float* S = (LAS float*)lds + L_S + wave * 1024;
    float mx = -INFINITY;
#define K_PAIR(tt, LAST, POL) do { \
      float x[8]; \
      _Pragma("unroll") for (int u = 0; u < 8; ++u) { \
        const int n = u + KDIST; \
        buf[n & 7] = ldp<POL>(((LAST) && n >= 8) ? rv : rk, voff + (n & 3) * 1024, tile_off(2 * (tt) + (n >> 2))); \
        __builtin_amdgcn_sched_barrier(0); \
        float d = dot2(buf[u][0], qp[0], 0.f); d = dot2(buf[u][1], qp[1], d); d = dot2(buf[u][2], qp[2], d); x[u] = dot2(buf[u][3], qp[3], d); \
        __builtin_amdgcn_sched_barrier(0); \
      } \
      _Pragma("unroll") for (int u = 0; u < 4; ++u) { const float lo = x[u] + dpp<ROR8>(x[u]), hi = x[u + 4] + dpp<ROR8>(x[u + 4]); x[u] = (c & 8) ? hi : lo; } \
      _Pragma("unroll") for (int u = 0; u < 2; ++u) { const float lo = x[u] + dpp<XOR7>(x[u]), hi = x[u + 2] + dpp<XOR7>(x[u + 2]); x[u] = (c & 4) ? hi : lo; } \
      { const float lo = x[0] + dpp<XOR2>(x[0]), hi = x[1] + dpp<XOR2>(x[1]); x[0] = (c & 2) ? hi : lo; } \
      float sc = x[0] + dpp<XOR1>(x[0]); \
      sc = rbf(rbf(sc) * SCALE); \
      mx = fmaxf(mx, sc); \
      S[(tt) * 32 + r * 8 + (c >> 1)] = sc; \
      __builtin_amdgcn_sched_barrier(0); \
    } while (0)
    if (chunk < KTHR) { for (int tt = 0; tt < 31; ++tt) K_PAIR(tt, false, 2); } else { for (int tt = 0; tt < 31; ++tt) K_PAIR(tt, false, 0); }
    K_PAIR(31, true, 2);
#undef K_PAIR
    STAMP(3);
#pragma unroll
    for (int i = 0; i < 16; ++i) wt[i] = ldnt(ro, voff + (i & 3) * 1024, (wave * 16 + (i & ~3)) * 1024);
    __builtin_amdgcn_sched_barrier(0);
    {
      mx = wave_max(mx);
      const float msub = mx * LOG2E;
      lds_wait();
      float l = 0.f;
#pragma unroll
      for (int i = 0; i < 4; ++i) {
        f32x4 s = *(const LAS f32x4*)(S + i * 256 + lane * 4);
#pragma unroll
        for (int k = 0; k < 4; ++k) { s[k] = __builtin_amdgcn_exp2f(fmaf(s[k], LOG2E, -msub)); l += s[k]; }
        *(LAS f32x4*)(S + i * 256 + lane * 4) = s;
      }
      l = wave_sum(l);
      if (lane == 0) { lds[L_ML + 2 * wave] = mx; lds[L_ML + 2 * wave + 1] = l; }
      lds_wait();
      if (lane == 0) __hip_atomic_fetch_add((LAS unsigned*)lds + L_CNT, 1u, __ATOMIC_RELAXED, __HIP_MEMORY_SCOPE_WORKGROUP);
    }
    STAMP(4);
    f32x2 acc[4] = {{0.f, 0.f}, {0.f, 0.f}, {0.f, 0.f}, {0.f, 0.f}};
#define V_PAIR(tt, LAST, POL) do { \
      const f32x4 p0 = *(const LAS f32x4*)(S + (tt) * 32 + r * 8), p1 = *(const LAS f32x4*)(S + (tt) * 32 + r * 8 + 4); \
      _Pragma("unroll") for (int u = 0; u < 8; ++u) { \
        const int n = u + KDIST; \
        if (!(LAST) || n < 8) buf[n & 7] = ldp<POL>(rv, voff + (n & 3) * 1024, tile_off(2 * (tt) + (n >> 2))); \
        __builtin_amdgcn_sched_barrier(0); \
        const float p = u < 4 ? p0[u & 3] : p1[u & 3]; \
        _Pragma("unroll") for (int k = 0; k < 4; ++k) acc[k] = __builtin_elementwise_fma(bf_unpack(buf[u][k]), f32x2{p, p}, acc[k]); \
        __builtin_amdgcn_sched_barrier(0); \
      } \
    } while (0)
    if (chunk < VTHR) { for (int tt = 0; tt < 31; ++tt) V_PAIR(tt, false, 2); } else { for (int tt = 0; tt < 31; ++tt) V_PAIR(tt, false, 0); }
    V_PAIR(31, true, 2);
#undef V_PAIR
    STAMP(5);
    dg_t2 = (unsigned)__builtin_amdgcn_s_memrealtime();
    *(LAS f32x4*)(lds + L_RED + (wave * 4 + r) * 128 + c * 8) = f32x4{acc[0][0], acc[0][1], acc[1][0], acc[1][1]};
    *(LAS f32x4*)(lds + L_RED + (wave * 4 + r) * 128 + c * 8 + 4) = f32x4{acc[2][0], acc[2][1], acc[3][0], acc[3][1]};
  } else {
    const int hw = wave - 4, t = tid - 256;
    const int jj = j & 15;
    if (jj < 14) {
      const int f0 = (xg * 14 + jj) * 16;
      const rsrc_t r1 = __builtin_amdgcn_make_buffer_rsrc((void*)(a.w1 + f0), 0, (DM * FF - f0) * 2, 0x00020000);
      const rsrc_t r3 = __builtin_amdgcn_make_buffer_rsrc((void*)(a.w3 + f0), 0, (DM * FF - f0) * 2, 0x00020000);
      const rsrc_t r2 = __builtin_amdgcn_make_buffer_rsrc((void*)(a.w2 + (size_t)f0 * DM), 0, 16 * DM * 2, 0x00020000);
      u32x4 g[4], u[4], w2r[4];
      const int row = t >> 1, half = t & 1;
#pragma unroll
      for (int i = 0; i < 4; ++i) { g[i] = ldnt(r1, (row + 128 * i) * (FF * 2) + half * 16, 0); u[i] = ldnt(r3, (row + 128 * i) * (FF * 2) + half * 16, 0); }
#pragma unroll
      for (int i = 0; i < 4; ++i) w2r[i] = ldnt(r2, voff + i * 1024, hw * 4096);
      f32x2 ga[4] = {{0.f, 0.f}, {0.f, 0.f}, {0.f, 0.f}, {0.f, 0.f}}, ua[4] = {{0.f, 0.f}, {0.f, 0.f}, {0.f, 0.f}, {0.f, 0.f}};
#pragma unroll
      for (int i = 0; i < 4; ++i) {
        const float xs = lds[L_XN + row + 128 * i];
#pragma unroll
        for (int k = 0; k < 4; ++k) {
          ga[k] = __builtin_elementwise_fma(bf_unpack(g[i][k]), f32x2{xs, xs}, ga[k]);
          ua[k] = __builtin_elementwise_fma(bf_unpack(u[i][k]), f32x2{xs, xs}, ua[k]);
        }
      }
#pragma unroll
      for (int k = 0; k < 4; ++k)
#pragma unroll
        for (int e = 0; e < 2; ++e) {
          float s = ga[k][e]; s += dpp<ROR2>(s); s += dpp<ROR4>(s); s += dpp<ROR8>(s); ga[k][e] = xrow_sum(s);
          float q = ua[k][e]; q += dpp<ROR2>(q); q += dpp<ROR4>(q); q += dpp<ROR8>(q); ua[k][e] = xrow_sum(q);
        }
      if (lane < 2) {
#pragma unroll
        for (int k = 0; k < 4; ++k)
#pragma unroll
          for (int e = 0; e < 2; ++e) { lds[L_MLP + hw * 32 + half * 8 + 2 * k + e] = ga[k][e]; lds[L_MLP + hw * 32 + 16 + half * 8 + 2 * k + e] = ua[k][e]; }
      }
      lds_wait();
      if (lane == 0) __hip_atomic_fetch_add((LAS unsigned*)lds + L_CNT + 1, 1u, __ATOMIC_RELAXED, __HIP_MEMORY_SCOPE_WORKGROUP);
      while (__hip_atomic_load((LAS unsigned*)lds + L_CNT + 1, __ATOMIC_RELAXED, __HIP_MEMORY_SCOPE_WORKGROUP) < 4u) __builtin_amdgcn_s_sleep(1);
      asm volatile("" ::: "memory");
#pragma unroll
      for (int i = 0; i < 4; ++i) {
        const int f = hw * 4 + i;
        const float gs = rbf((lds[L_MLP + f] + lds[L_MLP + 32 + f]) + (lds[L_MLP + 64 + f] + lds[L_MLP + 96 + f]));
        const float us = rbf((lds[L_MLP + 16 + f] + lds[L_MLP + 48 + f]) + (lds[L_MLP + 80 + f] + lds[L_MLP + 112 + f]));
        const float si = rbf(gs / (1.f + __expf(-gs)));
        const float hv = rbf(si * us);
#pragma unroll
        for (int k = 0; k < 4; ++k) yacc[k] = __builtin_elementwise_fma(bf_unpack(w2r[i][k]), f32x2{hv, hv}, yacc[k]);
      }
    }
#pragma unroll
    for (int i = 0; i < 16; ++i) wt[i] = ldnt(ro, voff + (i & 3) * 1024, (wave * 16 + (i & ~3)) * 1024);
    STAMP(2);
    if (wave == 4) {
      while (__hip_atomic_load((LAS unsigned*)lds + L_CNT, __ATOMIC_RELAXED, __HIP_MEMORY_SCOPE_WORKGROUP) < 4u) __builtin_amdgcn_s_sleep(8);
      asm volatile("" ::: "memory");
      const float mw = lds[L_ML + 2 * (lane & 3)], lw = lds[L_ML + 2 * (lane & 3) + 1];
      float m = fmaxf(mw, dpp<XOR1>(mw)); m = fmaxf(m, dpp<XOR2>(m));
      float l = lw * __builtin_amdgcn_exp2f((mw - m) * LOG2E);
      l += dpp<XOR1>(l); l += dpp<XOR2>(l);
      gu64* ml = (gu64*)(a.ws + WS_ML);
      if (lane < 2) st_granule(ml + chunk * 2 + lane, tag, lane == 0 ? m : l);
      const unsigned long long t0 = __builtin_amdgcn_s_memrealtime();
      unsigned long long gm, gl;
      while (true) {
        gm = __hip_atomic_load(ml + ((chunk & ~3) + (lane & 3)) * 2, RLX_AGENT);
        gl = __hip_atomic_load(ml + ((chunk & ~3) + (lane & 3)) * 2 + 1, RLX_AGENT);
        if (__all((unsigned)(gm >> 32) == tag && (unsigned)(gl >> 32) == tag)) break;
        if (__builtin_amdgcn_s_memrealtime() - t0 > SPIN_TICKS) break;
        __builtin_amdgcn_s_sleep(8);
      }
      const float ms = __uint_as_float((unsigned)gm), ls = __uint_as_float((unsigned)gl);
      float M = fmaxf(ms, dpp<XOR1>(ms)); M = fmaxf(M, dpp<XOR2>(M));
      float L = ls * __builtin_amdgcn_exp2f((ms - M) * LOG2E);
      L += dpp<XOR1>(L); L += dpp<XOR2>(L);
      if (lane < 4) lds[L_FW + lane] = __builtin_amdgcn_exp2f((mw - M) * LOG2E) / L;
      STAMP(3);
    }
  }
  __syncthreads();
  STAMP(6);
  {
    const LAS float* P = (const LAS float*)lds + L_RED + (4 * r) * 128 + wave * 16 + c;
    float o = ((P[0] + P[128]) + (P[256] + P[384])) * lds[L_FW + r];
    o = xrow_sum(o);
#pragma unroll
    for (int i = 0; i < 16; ++i) {
      const float oi = __builtin_bit_cast(float, __builtin_amdgcn_readlane(__builtin_bit_cast(int, o), i));
#pragma unroll
      for (int k = 0; k < 4; ++k) yacc[k] = __builtin_elementwise_fma(bf_unpack(wt[i][k]), f32x2{oi, oi}, yacc[k]);
    }
  }
  __syncthreads();
  *(LAS f32x4*)(lds + L_RED + wave * 512 + lane * 8) = f32x4{yacc[0][0], yacc[0][1], yacc[1][0], yacc[1][1]};
  *(LAS f32x4*)(lds + L_RED + wave * 512 + lane * 8 + 4) = f32x4{yacc[2][0], yacc[2][1], yacc[3][0], yacc[3][1]};
  __syncthreads();
  gu64* Y = (gu64*)(a.ws + WS_Y);
  {
    const LAS float* P = (const LAS float*)lds + L_RED + tid;
    const float y = ((P[0] + P[512]) + (P[1024] + P[1536])) + ((P[2048] + P[2560]) + (P[3072] + P[3584]));
    st_granule(Y + (size_t)(tid >> 1) * 512 + chunk * 2 + (tid & 1), tag, y);
  }
  STAMP(7);
  gu64* DG = (gu64*)(a.ws + 16384);
  if (tid == 0) { st_granule(DG + blockIdx.x, tag, __uint_as_float(dg_t0)); st_granule(DG + 256 + blockIdx.x, tag, __uint_as_float(dg_t1)); st_granule(DG + 512 + blockIdx.x, tag, __uint_as_float(dg_t2)); }
  {
    const unsigned xw = tid < 2 ? *(const gu32*)(a.x + tid * DM + 2 * blockIdx.x) : 0u;
    const unsigned long long t0 = __builtin_amdgcn_s_memrealtime();
    unsigned long long g;
    while (true) {
      g = __hip_atomic_load(Y + (size_t)blockIdx.x * 512 + tid, RLX_AGENT);
      if (__all((unsigned)(g >> 32) == tag)) break;
      if (__builtin_amdgcn_s_memrealtime() - t0 > SPIN_TICKS) break;
      __builtin_amdgcn_s_sleep(4);
    }
    float s = __uint_as_float((unsigned)g);
    s += dpp<ROR2>(s); s += dpp<ROR4>(s); s += dpp<ROR8>(s); s = xrow_sum(s);
    if (lane < 2) lds[L_FIN + wave * 2 + lane] = s;
    __syncthreads();
    if (tid < 2) {
      const LAS float* P = (const LAS float*)lds + L_FIN + tid * 8;
      const float o0 = bf_lo(xw) + ((P[0] + P[2]) + (P[4] + P[6])), o1 = bf_hi(xw) + ((P[1] + P[3]) + (P[5] + P[7]));
      *(gu32*)(a.out + tid * DM + 2 * blockIdx.x) = pack_bf16(o0, o1);
      if (blockIdx.x == 0 && tid == 0) __hip_atomic_store((gu32*)(a.ws + WS_EPOCH), tag, RLX_AGENT);
    }
  }
  const unsigned dg_end = (unsigned)__builtin_amdgcn_s_memrealtime();
  if (blockIdx.x == 0 && wave == 0) {
    int mn0 = 1 << 30, mx0 = -(1 << 30), mx1 = -(1 << 30), mx2 = -(1 << 30), mn2 = 1 << 30; int sum2 = 0, sumq = 0;
    for (int i = 0; i < 256; ++i) {
      unsigned long long g0, g1, g2;
      do { g0 = __hip_atomic_load(DG + i, RLX_AGENT); g1 = __hip_atomic_load(DG + 256 + i, RLX_AGENT); g2 = __hip_atomic_load(DG + 512 + i, RLX_AGENT); } while ((unsigned)(g0 >> 32) != tag || (unsigned)(g1 >> 32) != tag || (unsigned)(g2 >> 32) != tag);
      const int d0 = (int)((unsigned)g0 - dg_t0), d1 = (int)((unsigned)g1 - dg_t0), d2 = (int)((unsigned)g2 - dg_t0);
      mn0 = min(mn0, d0); mx0 = max(mx0, d0); mx1 = max(mx1, d1); mx2 = max(mx2, d2); mn2 = min(mn2, d2); sum2 += d2; sumq += d1 - d0;
    }
    int extra = 0;
    if (DIAG == 1) extra = 10 * (mx0 - mn0);
    if (DIAG == 2) extra = (int)(dg_end - dg_t0) - mn0;
    if (DIAG == 3) extra = 10 * (sumq / 256);
    if (DIAG == 4) extra = 5 * (mx2 - sum2 / 256);
    if (DIAG == 5) extra = 10 * ((int)(dg_end - dg_t0) - mx2);
    while ((int)((unsigned)__builtin_amdgcn_s_memrealtime() - dg_end) < 30000 + extra) __builtin_amdgcn_s_sleep(1);
  }
  STAMP(8);
}

}

extern "C" void kernel_launch(void* const* d_in, const int* in_sizes, int n_in, void* d_out, int out_size, void* d_ws, size_t ws_size,
                              hipStream_t stream) {
  Args a;
  a.x = (const unsigned short*)d_in[0]; a.k = (const unsigned short*)d_in[1]; a.v = (const unsigned short*)d_in[2];
  a.wq = (const unsigned short*)d_in[3]; a.wo = (const unsigned short*)d_in[4];
  a.w1 = (const unsigned short*)d_in[6]; a.w3 = (const unsigned short*)d_in[7]; a.w2 = (const unsigned short*)d_in[8];
  a.out = (unsigned short*)d_out; a.ws = (unsigned char*)d_ws;
  fused_decode<<<NWG, NTHR, 0, stream>>>(a);
}
```

```cpp
#define NPRE 3
#define KDIST 5
#define QDELAY 32
#define WOD 4
#include <hip/hip_runtime.h>
#include <hip/hip_bf16.h>
#include <stdint.h>

#define KSTAG 1
#define MIMIC 1
#define STAMPS 0

namespace {

constexpr int NWG = 256, NTHR = 768;
constexpr int DM = 512, NH = 32, SK = 16384, DH = 128, FF = 1792;
constexpr float LOG2E = 1.4426950408889634f;
constexpr float SCALE = 0.08838834764831845f;
constexpr size_t WS_EPOCH = 0, WS_ML = 4096;
constexpr size_t WS_Y = 65536;
constexpr size_t WS_Q = WS_Y + (1u << 20);
constexpr size_t WS_G = WS_Q + (512u << 10);
constexpr size_t WS_STAMP = 8u << 20;
constexpr int L_S = 0;
constexpr int L_RED = L_S + 4096;
constexpr int L_Y = L_RED + 2048;
constexpr int L_ML = L_Y + 4096;
constexpr int L_FW = L_ML + 8;
constexpr int L_FIN = L_FW + 4;
constexpr int L_CNT = L_FIN + 16;
constexpr int L_END = L_CNT + 4;

#define GAS __attribute__((address_space(1)))
#define LAS __attribute__((address_space(3)))
#define RLX_AGENT __ATOMIC_RELAXED, __HIP_MEMORY_SCOPE_AGENT
typedef unsigned u32x4 __attribute__((ext_vector_type(4)));
typedef float f32x2 __attribute__((ext_vector_type(2)));
typedef float f32x4 __attribute__((ext_vector_type(4)));
typedef __bf16 bf16x2 __attribute__((ext_vector_type(2)));
typedef GAS unsigned gu32;
typedef GAS unsigned long long gu64;

__device__ __forceinline__ float bf_lo(unsigned u) { return __builtin_bit_cast(float, u << 16); }
__device__ __forceinline__ float bf_hi(unsigned u) { return __builtin_bit_cast(float, u & 0xffff0000u); }
__device__ __forceinline__ f32x2 bf_unpack(unsigned u) { return f32x2{bf_lo(u), bf_hi(u)}; }
__device__ __forceinline__ unsigned pack_bf16(float lo, float hi) {
  bf16x2 r = {(__bf16)lo, (__bf16)hi};
  return __builtin_bit_cast(unsigned, r);
}
__device__ __forceinline__ float rbf(float x) {
  return (float)(__bf16)x;
}
__device__ __forceinline__ float dot2(unsigned a, unsigned b, float acc) {
  return __builtin_amdgcn_fdot2_f32_bf16(__builtin_bit_cast(bf16x2, a), __builtin_bit_cast(bf16x2, b), acc, false);
}
template <int CTRL> __device__ __forceinline__ float dpp(float x) {
  return __builtin_bit_cast(float, __builtin_amdgcn_mov_dpp(__builtin_bit_cast(int, x), CTRL, 0xf, 0xf, true));
}
constexpr int XOR1 = 0xB1, XOR2 = 0x4E, XOR7 = 0x141, ROR2 = 0x122, ROR4 = 0x124, ROR8 = 0x128;
__device__ __forceinline__ float xrow_sum(float x) {
  auto s = __builtin_amdgcn_permlane16_swap(__float_as_uint(x), __float_as_uint(x), false, false);
  x = __uint_as_float(s[0]) + __uint_as_float(s[1]);
  auto t = __builtin_amdgcn_permlane32_swap(__float_as_uint(x), __float_as_uint(x), false, false);
  return __uint_as_float(t[0]) + __uint_as_float(t[1]);
}
__device__ __forceinline__ float xrow_max(float x) {
  auto s = __builtin_amdgcn_permlane16_swap(__float_as_uint(x), __float_as_uint(x), false, false);
  x = fmaxf(__uint_as_float(s[0]), __uint_as_float(s[1]));
  auto t = __builtin_amdgcn_permlane32_swap(__float_as_uint(x), __float_as_uint(x), false, false);
  return fmaxf(__uint_as_float(t[0]), __uint_as_float(t[1]));
}
__device__ __forceinline__ float tr16(float a, float b) {
  auto s = __builtin_amdgcn_permlane16_swap(__float_as_uint(a), __float_as_uint(b), false, false);
  return __uint_as_float(s[0]) + __uint_as_float(s[1]);
}
__device__ __forceinline__ float tr32(float a, float b) {
  auto s = __builtin_amdgcn_permlane32_swap(__float_as_uint(a), __float_as_uint(b), false, false);
  return __uint_as_float(s[0]) + __uint_as_float(s[1]);
}
__device__ __forceinline__ float wave_sum(float x) {
  x += dpp<XOR1>(x); x += dpp<XOR2>(x); x += dpp<XOR7>(x); x += dpp<ROR8>(x);
  return xrow_sum(x);
}
__device__ __forceinline__ float wave_max(float x) {
  x = fmaxf(x, dpp<XOR1>(x)); x = fmaxf(x, dpp<XOR2>(x)); x = fmaxf(x, dpp<XOR7>(x)); x = fmaxf(x, dpp<ROR8>(x));
  return xrow_max(x);
}
__device__ __forceinline__ void lds_wait() { asm volatile("s_waitcnt lgkmcnt(0)" ::: "memory"); }
__device__ __forceinline__ void st_granule(gu64* g, unsigned tag, float v) {
  __hip_atomic_store(g, ((unsigned long long)tag << 32) | (unsigned long long)__float_as_uint(v), RLX_AGENT);
}
constexpr unsigned long long SPIN_TICKS = 20000000ull;

struct Args {
  const unsigned short* x; const unsigned short* k; const unsigned short* v; const unsigned short* wq; const unsigned short* wo;
  const unsigned short* w1; const unsigned short* w3; const unsigned short* w2;
  unsigned short* out; unsigned char* ws;
};

#define STAMP(i) do { } while (0)
#define STAMP_FLUSH() do { } while (0)
#define STAMP_DECL

typedef __amdgpu_buffer_rsrc_t rsrc_t;
#define WPOL 0
__device__ __forceinline__ u32x4 ldw(rsrc_t r, int voff, int soff) { return __builtin_amdgcn_raw_buffer_load_b128(r, voff, soff, WPOL); }
__device__ __forceinline__ u32x4 ldnt(rsrc_t r, int voff, int soff) { return __builtin_amdgcn_raw_buffer_load_b128(r, voff, soff, 2); }
#define TRK 0
#define PDIST KDIST
#define TRICKLE() __builtin_amdgcn_s_sleep(TRK)
#define HSYNC(n) do { \
    if (lane == 0) __hip_atomic_fetch_add((LAS unsigned*)lds + L_CNT + 1 + (n), 1u, __ATOMIC_RELAXED, __HIP_MEMORY_SCOPE_WORKGROUP); \
    while (__hip_atomic_load((LAS unsigned*)lds + L_CNT + 1 + (n), __ATOMIC_RELAXED, __HIP_MEMORY_SCOPE_WORKGROUP) < 8u) __builtin_amdgcn_s_sleep(0); \
    asm volatile("" ::: "memory"); \
  } while (0)
#define SB() __builtin_amdgcn_sched_barrier(0)
#define VM_DRAIN() do { SB(); asm volatile("s_waitcnt vmcnt(0)" ::: "memory"); SB(); } while (0)

__global__ void __launch_bounds__(NTHR, 3) fused_decode(Args a) {
  __shared__ __attribute__((aligned(16))) float lds[L_END];
  const int tid = threadIdx.x, lane = tid & 63, wave = __builtin_amdgcn_readfirstlane(tid >> 6);
  const int r = lane >> 4, c = lane & 15;
  const int xg = blockIdx.x & 7, j = blockIdx.x >> 3;
  const int b = j >> 4, h = xg * 4 + ((j >> 2) & 3), sp = j & 3;
  const int chunk = b * 128 + h * 4 + sp;
  const int voff = lane * 16;
  STAMP_DECL;
  STAMP(0);

  if (wave >= 8) {
    const int sw = wave - 8;
    const rsrc_t rk = __builtin_amdgcn_make_buffer_rsrc((void*)(a.k + (size_t)chunk * 4096 * DH), 0, 4096 * DH * 2, 0x00020000);
    const rsrc_t rv = __builtin_amdgcn_make_buffer_rsrc((void*)(a.v + (size_t)chunk * 4096 * DH), 0, 4096 * DH * 2, 0x00020000);
    const int stag = j * KSTAG;
    auto tile_off = [&](int t) -> int { return (((t + stag) & 63) << 14) + (sw << 12); };
    __builtin_amdgcn_s_setprio(2);
    u32x4 buf[8], pre[NPRE * 8 + 1];
    __builtin_amdgcn_s_barrier();
    SB();
#pragma unroll
    for (int u = 0; u < NPRE * 8; ++u) {
      pre[u] = ldnt(rk, voff + (u & 3) * 1024, tile_off(u >> 2)); SB();
      if (u >= PDIST) { asm volatile("s_waitcnt vmcnt(%0)" :: "i"(PDIST)); SB(); }
    }
#pragma unroll
    for (int u = 0; u < KDIST; ++u) {
      buf[u] = ldnt(rk, voff + (u & 3) * 1024, tile_off(2 * NPRE + (u >> 2))); SB();
      if (NPRE * 8 + u >= PDIST && u + 1 < KDIST) { asm volatile("s_waitcnt vmcnt(%0)" :: "i"(PDIST)); SB(); }
    }
    STAMP(1);
    __builtin_amdgcn_s_barrier();
    asm volatile("" ::: "memory");
    unsigned qp[4];
    {
      f32x4 s0 = *(const LAS f32x4*)(lds + L_RED + (2 * r) * 128 + c * 8) + *(const LAS f32x4*)(lds + L_RED + (2 * r + 1) * 128 + c * 8);
      f32x4 s1 = *(const LAS f32x4*)(lds + L_RED + (2 * r) * 128 + c * 8 + 4) + *(const LAS f32x4*)(lds + L_RED + (2 * r + 1) * 128 + c * 8 + 4);
#pragma unroll
      for (int k = 0; k < 4; ++k) { s0[k] = xrow_sum(s0[k]); s1[k] = xrow_sum(s1[k]); }
      qp[0] = pack_bf16(s0[0], s0[1]); qp[1] = pack_bf16(s0[2], s0[3]); qp[2] = pack_bf16(s1[0], s1[1]); qp[3] = pack_bf16(s1[2], s1[3]);
    }
    STAMP(2);
    LAS float* S = (LAS float*)lds + L_S + sw * 1024;
    float mx = -INFINITY;
#define DOT(w) dot2((w)[3], qp[3], dot2((w)[2], qp[2], dot2((w)[1], qp[1], dot2((w)[0], qp[0], 0.f))))
#define K_FIN(tt) do { \
      _Pragma("unroll") for (int u = 0; u < 4; ++u) { const float lo = x[u] + dpp<ROR8>(x[u]), hi = x[u + 4] + dpp<ROR8>(x[u + 4]); x[u] = (c & 8) ? hi : lo; } \
      _Pragma("unroll") for (int u = 0; u < 2; ++u) { const float lo = x[u] + dpp<XOR7>(x[u]), hi = x[u + 2] + dpp<XOR7>(x[u + 2]); x[u] = (c & 4) ? hi : lo; } \
      { const float lo = x[0] + dpp<XOR2>(x[0]), hi = x[1] + dpp<XOR2>(x[1]); x[0] = (c & 2) ? hi : lo; } \
      float sc = x[0] + dpp<XOR1>(x[0]); \
      sc = rbf(rbf(sc) * SCALE); \
      mx = fmaxf(mx, sc); \
      S[(tt) * 32 + r * 8 + (c >> 1)] = sc; \
      SB(); \
    } while (0)
#define K_PAIR(tt, FIRST, LAST) do { \
      float x[8]; \
      _Pragma("unroll") for (int u = 0; u < 8; ++u) { \
        const int n = u + KDIST; \
        buf[n & 7] = ldnt(((LAST) && n >= 8) ? rv : rk, voff + (n & 3) * 1024, tile_off(2 * (tt) + (n >> 2))); \
        SB(); \
        if ((FIRST) && u == KDIST) { \
          _Pragma("unroll") for (int pp = 0; pp < NPRE; ++pp) { \
            float x[8]; \
            _Pragma("unroll") for (int w = 0; w < 8; ++w) x[w] = DOT(pre[pp * 8 + w]); \
            K_FIN(pp); \
          } \
        } \
        x[u] = DOT(buf[u]); \
        SB(); \
      } \
      K_FIN(tt); \
    } while (0)
    static_assert(KDIST < 8 && NPRE >= 1, "ring of 8");
    K_PAIR(NPRE, true, false);
    for (int tt = NPRE + 1; tt < 31; ++tt) K_PAIR(tt, false, false);
    K_PAIR(31, false, true);
#undef K_PAIR
    STAMP(3);
    {
      mx = wave_max(mx);
      const float msub = mx * LOG2E;
      lds_wait();
      float l = 0.f;
#pragma unroll
      for (int i = 0; i < 4; ++i) {
        f32x4 s = *(const LAS f32x4*)(S + i * 256 + lane * 4);
#pragma unroll
        for (int k = 0; k < 4; ++k) { s[k] = __builtin_amdgcn_exp2f(fmaf(s[k], LOG2E, -msub)); l += s[k]; }
        *(LAS f32x4*)(S + i * 256 + lane * 4) = s;
      }
      l = wave_sum(l);
      if (lane == 0) { lds[L_ML + 2 * sw] = mx; lds[L_ML + 2 * sw + 1] = l; }
      lds_wait();
      if (lane == 0) __hip_atomic_fetch_add((LAS unsigned*)lds + L_CNT, 1u, __ATOMIC_RELAXED, __HIP_MEMORY_SCOPE_WORKGROUP);
    }
    STAMP(4);
    f32x2 acc[4] = {{0.f, 0.f}, {0.f, 0.f}, {0.f, 0.f}, {0.f, 0.f}};
#define V_PAIR(tt, LAST) do { \
      const f32x4 p0 = *(const LAS f32x4*)(S + (tt) * 32 + r * 8), p1 = *(const LAS f32x4*)(S + (tt) * 32 + r * 8 + 4); \
      _Pragma("unroll") for (int u = 0; u < 8; ++u) { \
        const int n = u + KDIST; \
        if (!(LAST) || n < 8) buf[n & 7] = ldnt(rv, voff + (n & 3) * 1024, tile_off(2 * (tt) + (n >> 2))); \
        SB(); \
        const float p = u < 4 ? p0[u & 3] : p1[u & 3]; \
        _Pragma("unroll") for (int k = 0; k < 4; ++k) acc[k] = __builtin_elementwise_fma(bf_unpack(buf[u][k]), f32x2{p, p}, acc[k]); \
        SB(); \
      } \
    } while (0)
    for (int tt = 0; tt < 31; ++tt) V_PAIR(tt, false);
    V_PAIR(31, true);
#undef V_PAIR
    STAMP(5);
    *(LAS f32x4*)(lds + L_RED + (sw * 4 + r) * 128 + c * 8) = f32x4{acc[0][0], acc[0][1], acc[1][0], acc[1][1]};
    *(LAS f32x4*)(lds + L_RED + (sw * 4 + r) * 128 + c * 8 + 4) = f32x4{acc[2][0], acc[2][1], acc[3][0], acc[3][1]};
    lds_wait();
    __builtin_amdgcn_s_barrier();
    __builtin_amdgcn_s_barrier();
    __builtin_amdgcn_s_barrier();
    STAMP_FLUSH();
    return;
  }

  const int g8 = b * 4 + sp;
  const u32x4 xv0 = *(const GAS u32x4*)(a.x + lane * 8), xv1 = *(const GAS u32x4*)(a.x + DM + lane * 8);
  const int qrow = g8 * 64 + wave * 8 + 2 * r, mrow = g8 * 64 + wave * 8 + (lane >> 3);
  const unsigned xq0 = *(const gu32*)(a.x + qrow), xq1 = *(const gu32*)(a.x + DM + qrow);
  u32x4 wq0, wq1;
  {
    const rsrc_t rq = __builtin_amdgcn_make_buffer_rsrc((void*)(a.wq + h * DH), 0, (DM * NH * DH - h * DH) * 2, 0x00020000);
    wq0 = ldnt(rq, qrow * (NH * DH * 2) + c * 16, 0);
    wq1 = ldnt(rq, qrow * (NH * DH * 2) + c * 16, NH * DH * 2);
  }
  const unsigned epoch = *(const __attribute__((address_space(4))) unsigned*)(a.ws + WS_EPOCH);
  SB();
  if (tid < 4) ((LAS unsigned*)lds)[L_CNT + tid] = 0u;
  lds_wait();
  __builtin_amdgcn_s_barrier();
  SB();
  float mb0, mb1, sd0, sd1;
  {
    float f0[8], f1[8];
#pragma unroll
    for (int k = 0; k < 4; ++k) { f0[2 * k] = bf_lo(xv0[k]); f0[2 * k + 1] = bf_hi(xv0[k]); f1[2 * k] = bf_lo(xv1[k]); f1[2 * k + 1] = bf_hi(xv1[k]); }
    float s0 = 0.f, s1 = 0.f;
#pragma unroll
    for (int k = 0; k < 8; ++k) { s0 += f0[k]; s1 += f1[k]; }
    const float m0 = wave_sum(s0) * (1.f / DM), m1 = wave_sum(s1) * (1.f / DM);
    float v0 = 0.f, v1 = 0.f;
#pragma unroll
    for (int k = 0; k < 8; ++k) { const float d0 = f0[k] - m0, d1 = f1[k] - m1; v0 = fmaf(d0, d0, v0); v1 = fmaf(d1, d1, v1); }
    v0 = wave_sum(v0) * (1.f / DM); v1 = wave_sum(v1) * (1.f / DM);
    mb0 = rbf(m0); sd0 = rbf(__builtin_sqrtf(rbf(rbf(v0) + 1e-5f)));
    mb1 = rbf(m1); sd1 = rbf(__builtin_sqrtf(rbf(rbf(v1) + 1e-5f)));
  }
#define XN(xf, mb, sd) rbf(rbf((xf) - (mb)) / (sd))
  const unsigned tag = epoch + 0x9E3779B9u;
  STAMP(10);
  {
    const float a0 = XN(bf_lo(xq0), mb0, sd0), a1 = XN(bf_hi(xq0), mb0, sd0), b0 = XN(bf_lo(xq1), mb1, sd1), b1 = XN(bf_hi(xq1), mb1, sd1);
    float t[8];
#pragma unroll
    for (int k = 0; k < 4; ++k) {
      const f32x2 w0 = bf_unpack(wq0[k]), w1 = bf_unpack(wq1[k]);
      const f32x2 qa = __builtin_elementwise_fma(w1, f32x2{a1, a1}, w0 * a0), qb = __builtin_elementwise_fma(w1, f32x2{b1, b1}, w0 * b0);
      t[2 * k] = tr32(qa[0], qb[0]); t[2 * k + 1] = tr32(qa[1], qb[1]);
    }
    f32x4 o;
#pragma unroll
    for (int k = 0; k < 4; ++k) o[k] = tr16(t[k], t[4 + k]);
    *(LAS f32x4*)(lds + L_Y + wave * 256 + (r >> 1) * 128 + c * 8 + (r & 1) * 4) = o;
    lds_wait();
  }
  STAMP(11);
  HSYNC(0);
  STAMP(12);
  gu64* Q = (gu64*)(a.ws + WS_Q) + h * 2048;
  if (tid < 256) {
    const LAS float* P = (const LAS float*)lds + L_Y + tid;
    st_granule(Q + g8 * 256 + tid, tag, ((P[0] + P[256]) + (P[512] + P[768])) + ((P[1024] + P[1280]) + (P[1536] + P[1792])));
  }
  STAMP(9);
  __builtin_amdgcn_s_sleep(QDELAY);
  {
    const unsigned long long t0 = __builtin_amdgcn_s_memrealtime();
    unsigned long long g0, g1;
    while (true) {
      g0 = __hip_atomic_load(Q + wave * 256 + b * 128 + lane, RLX_AGENT);
      g1 = __hip_atomic_load(Q + wave * 256 + b * 128 + 64 + lane, RLX_AGENT);
      if (__all((unsigned)(g0 >> 32) == tag && (unsigned)(g1 >> 32) == tag)) break;
      if (__builtin_amdgcn_s_memrealtime() - t0 > SPIN_TICKS) break;
      __builtin_amdgcn_s_sleep(2);
    }
    lds[L_RED + wave * 128 + lane] = __uint_as_float((unsigned)g0);
    lds[L_RED + wave * 128 + 64 + lane] = __uint_as_float((unsigned)g1);
    lds_wait();
  }
  STAMP(1);
  __builtin_amdgcn_s_barrier();
  asm volatile("" ::: "memory");
  gu64* G = (gu64*)(a.ws + WS_G);
  const unsigned xw = tid < 2 ? *(const gu32*)(a.x + tid * DM + 2 * blockIdx.x) : 0u;
  if (h < 28) {
    const unsigned short xm0 = a.x[mrow], xm1 = a.x[DM + mrow];
    const rsrc_t r1 = __builtin_amdgcn_make_buffer_rsrc((void*)(a.w1 + h * 64), 0, (DM * FF - h * 64) * 2, 0x00020000);
    const rsrc_t r3 = __builtin_amdgcn_make_buffer_rsrc((void*)(a.w3 + h * 64), 0, (DM * FF - h * 64) * 2, 0x00020000);
    const u32x4 wg = ldnt(r1, mrow * (FF * 2) + (lane & 7) * 16, 0);
    const u32x4 wu = ldnt(r3, mrow * (FF * 2) + (lane & 7) * 16, 0);
    const float a0 = XN(bf_lo(xm0), mb0, sd0), b0 = XN(bf_lo(xm1), mb1, sd1);
    float t[8];
#pragma unroll
    for (int k = 0; k < 4; ++k) {
      const f32x2 g = bf_unpack(wg[k]), u = bf_unpack(wu[k]);
#pragma unroll
      for (int e = 0; e < 2; ++e) t[2 * k + e] = tr16(tr32(g[e] * a0, g[e] * b0), tr32(u[e] * a0, u[e] * b0));
    }
    f32x4 o;
#pragma unroll
    for (int k = 0; k < 4; ++k) { const float lo = t[k] + dpp<ROR8>(t[k]), hi = t[4 + k] + dpp<ROR8>(t[4 + k]); o[k] = (c & 8) ? hi : lo; }
    *(LAS f32x4*)(lds + L_Y + 2048 + wave * 256 + r * 64 + (c & 7) * 8 + (c >> 3) * 4) = o;
    lds_wait();
    HSYNC(1);
    if (tid < 256) {
      const LAS float* P = (const LAS float*)lds + L_Y + 2048 + tid;
      st_granule(G + (h * 8 + g8) * 256 + tid, tag, ((P[0] + P[256]) + (P[512] + P[768])) + ((P[1024] + P[1280]) + (P[1536] + P[1792])));
    }
  }
  f32x2 wf[64];
  {
    const rsrc_t ro = __builtin_amdgcn_make_buffer_rsrc((void*)(a.wo + (size_t)h * DH * DM), 0, DH * DM * 2, 0x00020000);
#pragma unroll
    for (int i0 = 0; i0 < 16; i0 += WOD) {
      u32x4 w[WOD];
#pragma unroll
      for (int i = 0; i < WOD; ++i) w[i] = ldw(ro, voff + ((i0 + i) & 3) * 1024, (wave * 16 + ((i0 + i) & ~3)) * 1024);
      VM_DRAIN();
#pragma unroll
      for (int i = 0; i < WOD; ++i)
#pragma unroll
        for (int k = 0; k < 4; ++k) { wf[(i0 + i) * 4 + k] = bf_unpack(w[i][k]); asm volatile("" : "+v"(wf[(i0 + i) * 4 + k])); }
      TRICKLE();
    }
  }
  f32x2 yacc[4] = {{0.f, 0.f}, {0.f, 0.f}, {0.f, 0.f}, {0.f, 0.f}};
  if (wave < 7) {
    const int f0 = (h * 4 + sp) * 14 + wave * 2;
    const rsrc_t r2 = __builtin_amdgcn_make_buffer_rsrc((void*)(a.w2 + (size_t)f0 * DM), 0, 2 * DM * 2, 0x00020000);
    const u32x4 w20 = ldw(r2, voff, 0), w21 = ldw(r2, voff + 1024, 0);
    const int f = f0 + (r & 1);
    gu64* gp = G + ((f >> 6) * 8 + (c & 7)) * 256 + b * 128 + (c >> 3) * 64 + (f & 63);
    const unsigned long long t0 = __builtin_amdgcn_s_memrealtime();
    unsigned long long g;
    while (true) {
      g = __hip_atomic_load(gp, RLX_AGENT);
      if (__all((unsigned)(g >> 32) == tag)) break;
      if (__builtin_amdgcn_s_memrealtime() - t0 > SPIN_TICKS) break;
      __builtin_amdgcn_s_sleep(16);
    }
    float s = __uint_as_float((unsigned)g);
    s += dpp<XOR1>(s); s += dpp<XOR2>(s); s += dpp<XOR7>(s);
    const float gs = rbf(s), us = rbf(dpp<ROR8>(s));
    const float si = rbf(gs / (1.f + __expf(-gs)));
    const float hv = rbf(si * us);
    const float h0 = __builtin_bit_cast(float, __builtin_amdgcn_readlane(__builtin_bit_cast(int, hv), 0));
    const float h1 = __builtin_bit_cast(float, __builtin_amdgcn_readlane(__builtin_bit_cast(int, hv), 16));
#pragma unroll
    for (int k = 0; k < 4; ++k) yacc[k] = __builtin_elementwise_fma(bf_unpack(w21[k]), f32x2{h1, h1}, bf_unpack(w20[k]) * h0);
  }
  STAMP(2);
  if (wave == 0) {
    while (__hip_atomic_load((LAS unsigned*)lds + L_CNT, __ATOMIC_RELAXED, __HIP_MEMORY_SCOPE_WORKGROUP) < 4u) __builtin_amdgcn_s_sleep(16);
    asm volatile("" ::: "memory");
    const float mw = lds[L_ML + 2 * (lane & 3)], lw = lds[L_ML + 2 * (lane & 3) + 1];
    float m = fmaxf(mw, dpp<XOR1>(mw)); m = fmaxf(m, dpp<XOR2>(m));
    float l = lw * __builtin_amdgcn_exp2f((mw - m) * LOG2E);
    l += dpp<XOR1>(l); l += dpp<XOR2>(l);
    gu64* ml = (gu64*)(a.ws + WS_ML);
    if (lane < 2) st_granule(ml + chunk * 2 + lane, tag, lane == 0 ? m : l);
    const unsigned long long t0 = __builtin_amdgcn_s_memrealtime();
    unsigned long long gm, gl;
    while (true) {
      gm = __hip_atomic_load(ml + ((chunk & ~3) + (lane & 3)) * 2, RLX_AGENT);
      gl = __hip_atomic_load(ml + ((chunk & ~3) + (lane & 3)) * 2 + 1, RLX_AGENT);
      if (__all((unsigned)(gm >> 32) == tag && (unsigned)(gl >> 32) == tag)) break;
      if (__builtin_amdgcn_s_memrealtime() - t0 > SPIN_TICKS) break;
      __builtin_amdgcn_s_sleep(16);
    }
    const float ms = __uint_as_float((unsigned)gm), ls = __uint_as_float((unsigned)gl);
    float M = fmaxf(ms, dpp<XOR1>(ms)); M = fmaxf(M, dpp<XOR2>(M));
    float L = ls * __builtin_amdgcn_exp2f((ms - M) * LOG2E);
    L += dpp<XOR1>(L); L += dpp<XOR2>(L);
    if (lane < 4) lds[L_FW + lane] = __builtin_amdgcn_exp2f((mw - M) * LOG2E) / L;
    lds_wait();
    STAMP(3);
  }
  __builtin_amdgcn_s_barrier();
  asm volatile("" ::: "memory");
  STAMP(6);
  {
    const LAS float* P = (const LAS float*)lds + L_RED + (4 * r) * 128 + wave * 16 + c;
    float o = ((P[0] + P[128]) + (P[256] + P[384])) * lds[L_FW + r];
    o = xrow_sum(o);
#pragma unroll
    for (int i = 0; i < 16; ++i) {
      const float oi = __builtin_bit_cast(float, __builtin_amdgcn_readlane(__builtin_bit_cast(int, o), i));
#pragma unroll
      for (int k = 0; k < 4; ++k) yacc[k] = __builtin_elementwise_fma(wf[i * 4 + k], f32x2{oi, oi}, yacc[k]);
    }
  }
  *(LAS f32x4*)(lds + L_Y + wave * 512 + lane * 8) = f32x4{yacc[0][0], yacc[0][1], yacc[1][0], yacc[1][1]};
  *(LAS f32x4*)(lds + L_Y + wave * 512 + lane * 8 + 4) = f32x4{yacc[2][0], yacc[2][1], yacc[3][0], yacc[3][1]};
  lds_wait();
  __builtin_amdgcn_s_barrier();
  asm volatile("" ::: "memory");
  gu64* Y = (gu64*)(a.ws + WS_Y);
  {
    const LAS float* P = (const LAS float*)lds + L_Y + tid;
    const float y = ((P[0] + P[512]) + (P[1024] + P[1536])) + ((P[2048] + P[2560]) + (P[3072] + P[3584]));
    st_granule(Y + (size_t)(tid >> 1) * 512 + chunk * 2 + (tid & 1), tag, y);
  }
  STAMP(7);
  {
    const unsigned long long t0 = __builtin_amdgcn_s_memrealtime();
    unsigned long long g;
    while (true) {
      g = __hip_atomic_load(Y + (size_t)blockIdx.x * 512 + tid, RLX_AGENT);
      if (__all((unsigned)(g >> 32) == tag)) break;
      if (__builtin_amdgcn_s_memrealtime() - t0 > SPIN_TICKS) break;
      __builtin_amdgcn_s_sleep(4);
    }
    float s = __uint_as_float((unsigned)g);
    s += dpp<ROR2>(s); s += dpp<ROR4>(s); s += dpp<ROR8>(s); s = xrow_sum(s);
    if (lane < 2) lds[L_FIN + wave * 2 + lane] = s;
    lds_wait();
    __builtin_amdgcn_s_barrier();
    asm volatile("" ::: "memory");
    if (tid < 2) {
      const LAS float* P = (const LAS float*)lds + L_FIN + tid * 8;
      const float o0 = bf_lo(xw) + ((P[0] + P[2]) + (P[4] + P[6])), o1 = bf_hi(xw) + ((P[1] + P[3]) + (P[5] + P[7]));
      *(gu32*)(a.out + tid * DM + 2 * blockIdx.x) = pack_bf16(o0, o1);
      if (blockIdx.x == 0 && tid == 0) __hip_atomic_store((gu32*)(a.ws + WS_EPOCH), tag, RLX_AGENT);
    }
  }
  STAMP(8);
  STAMP_FLUSH();
}

}

extern "C" void kernel_launch(void* const* d_in, const int* in_sizes, int n_in, void* d_out, int out_size, void* d_ws, size_t ws_size,
                              hipStream_t stream) {
  Args a;
  a.x = (const unsigned short*)d_in[0]; a.k = (const unsigned short*)d_in[1]; a.v = (const unsigned short*)d_in[2];
  a.wq = (const unsigned short*)d_in[3]; a.wo = (const unsigned short*)d_in[4];
  a.w1 = (const unsigned short*)d_in[6]; a.w3 = (const unsigned short*)d_in[7]; a.w2 = (const unsigned short*)d_in[8];
  a.out = (unsigned short*)d_out; a.ws = (unsigned char*)d_ws;
  fused_decode<<<NWG, NTHR, 0, stream>>>(a);
}
```

```cpp
#define STAMPS 1
#define DIAG 0
#define DIAG_I 2
#define DIAG_W0 8
#define DIAG_W1 12
#define DIAG_S 1
#include <hip/hip_runtime.h>
#include <hip/hip_bf16.h>
#include <stdint.h>

#define KDIST 5
#define KSTAG 1
#define MIMIC 1

namespace {

constexpr int NWG = 256, NTHR = 768;
constexpr int DM = 512, NH = 32, SK = 16384, DH = 128, FF = 1792;
constexpr float LOG2E = 1.4426950408889634f;
constexpr float SCALE = 0.08838834764831845f;
constexpr size_t WS_EPOCH = 0, WS_ML = 4096;
constexpr size_t WS_Y = 65536;
constexpr size_t WS_Q = WS_Y + (1u << 20);
constexpr size_t WS_G = WS_Q + (512u << 10);
constexpr size_t WS_STAMP = 8u << 20;
constexpr int L_S = 0;
constexpr int L_RED = L_S + 4096;
constexpr int L_Y = L_RED + 2048;
constexpr int L_ML = L_Y + 4096;
constexpr int L_FW = L_ML + 8;
constexpr int L_FIN = L_FW + 4;
constexpr int L_CNT = L_FIN + 16;
constexpr int L_END = L_CNT + 4;

#define GAS __attribute__((address_space(1)))
#define LAS __attribute__((address_space(3)))
#define RLX_AGENT __ATOMIC_RELAXED, __HIP_MEMORY_SCOPE_AGENT
typedef unsigned u32x4 __attribute__((ext_vector_type(4)));
typedef float f32x2 __attribute__((ext_vector_type(2)));
typedef float f32x4 __attribute__((ext_vector_type(4)));
typedef __bf16 bf16x2 __attribute__((ext_vector_type(2)));
typedef GAS unsigned gu32;
typedef GAS unsigned long long gu64;

__device__ __forceinline__ float bf_lo(unsigned u) { return __builtin_bit_cast(float, u << 16); }
__device__ __forceinline__ float bf_hi(unsigned u) { return __builtin_bit_cast(float, u & 0xffff0000u); }
__device__ __forceinline__ f32x2 bf_unpack(unsigned u) { return f32x2{bf_lo(u), bf_hi(u)}; }
__device__ __forceinline__ unsigned pack_bf16(float lo, float hi) {
  bf16x2 r = {(__bf16)lo, (__bf16)hi};
  return __builtin_bit_cast(unsigned, r);
}
__device__ __forceinline__ float rbf(float x) {
  return (float)(__bf16)x;
}
__device__ __forceinline__ float dot2(unsigned a, unsigned b, float acc) {
  return __builtin_amdgcn_fdot2_f32_bf16(__builtin_bit_cast(bf16x2, a), __builtin_bit_cast(bf16x2, b), acc, false);
}
template <int CTRL> __device__ __forceinline__ float dpp(float x) {
  return __builtin_bit_cast(float, __builtin_amdgcn_mov_dpp(__builtin_bit_cast(int, x), CTRL, 0xf, 0xf, true));
}
constexpr int XOR1 = 0xB1, XOR2 = 0x4E, XOR7 = 0x141, ROR2 = 0x122, ROR4 = 0x124, ROR8 = 0x128;
__device__ __forceinline__ float xrow_sum(float x) {
  auto s = __builtin_amdgcn_permlane16_swap(__float_as_uint(x), __float_as_uint(x), false, false);
  x = __uint_as_float(s[0]) + __uint_as_float(s[1]);
  auto t = __builtin_amdgcn_permlane32_swap(__float_as_uint(x), __float_as_uint(x), false, false);
  return __uint_as_float(t[0]) + __uint_as_float(t[1]);
}
__device__ __forceinline__ float xrow_max(float x) {
  auto s = __builtin_amdgcn_permlane16_swap(__float_as_uint(x), __float_as_uint(x), false, false);
  x = fmaxf(__uint_as_float(s[0]), __uint_as_float(s[1]));
  auto t = __builtin_amdgcn_permlane32_swap(__float_as_uint(x), __float_as_uint(x), false, false);
  return fmaxf(__uint_as_float(t[0]), __uint_as_float(t[1]));
}
__device__ __forceinline__ float tr16(float a, float b) {
  auto s = __builtin_amdgcn_permlane16_swap(__float_as_uint(a), __float_as_uint(b), false, false);
  return __uint_as_float(s[0]) + __uint_as_float(s[1]);
}
__device__ __forceinline__ float tr32(float a, float b) {
  auto s = __builtin_amdgcn_permlane32_swap(__float_as_uint(a), __float_as_uint(b), false, false);
  return __uint_as_float(s[0]) + __uint_as_float(s[1]);
}
__device__ __forceinline__ float wave_sum(float x) {
  x += dpp<XOR1>(x); x += dpp<XOR2>(x); x += dpp<XOR7>(x); x += dpp<ROR8>(x);
  return xrow_sum(x);
}
__device__ __forceinline__ float wave_max(float x) {
  x = fmaxf(x, dpp<XOR1>(x)); x = fmaxf(x, dpp<XOR2>(x)); x = fmaxf(x, dpp<XOR7>(x)); x = fmaxf(x, dpp<ROR8>(x));
  return xrow_max(x);
}
__device__ __forceinline__ void lds_wait() { asm volatile("s_waitcnt lgkmcnt(0)" ::: "memory"); }
__device__ __forceinline__ void st_granule(gu64* g, unsigned tag, float v) {
  __hip_atomic_store(g, ((unsigned long long)tag << 32) | (unsigned long long)__float_as_uint(v), RLX_AGENT);
}
constexpr unsigned long long SPIN_TICKS = 20000000ull;

struct Args {
  const unsigned short* x; const unsigned short* k; const unsigned short* v; const unsigned short* wq; const unsigned short* wo;
  const unsigned short* w1; const unsigned short* w3; const unsigned short* w2;
  unsigned short* out; unsigned char* ws;
};

#define STAMP(i) do { __builtin_amdgcn_sched_barrier(0); unsigned long long t_; asm volatile("s_memrealtime %0\n\ts_waitcnt lgkmcnt(0)" : "=s"(t_) :: "memory"); ts[i] = (unsigned)t_ | 1u; __builtin_amdgcn_sched_barrier(0); } while (0)
#define STAMP_FLUSH() do { const unsigned tg_ = dg_epoch + 0x9E3779B9u; if (lane == 0) { _Pragma("unroll") for (int i_ = 0; i_ < 16; ++i_) st_granule((gu64*)(a.ws + WS_STAMP) + (blockIdx.x * 16 + wave) * 16 + i_, tg_, __uint_as_float(ts[i_])); } \
  if (blockIdx.x == 0 && wave == 0) { \
    int mn0 = 1 << 30, mx = -(1 << 30), mn = 1 << 30; float sum = 0.f; \
    for (int i = 0; i < 4; ++i) for (int w = 0; w < 12; ++w) { \
      gu64* p = (gu64*)(a.ws + WS_STAMP) + ((lane * 4 + i) * 16 + w) * 16; unsigned long long g0, g1; \
      do { g0 = __hip_atomic_load(p, RLX_AGENT); g1 = __hip_atomic_load(p + DIAG_I, RLX_AGENT); } while (((unsigned)(g0 >> 32) != tg_ || (unsigned)(g1 >> 32) != tg_) && (unsigned)__builtin_amdgcn_s_memrealtime() - ts[0] < 1000000u); \
      mn0 = min(mn0, (int)((unsigned)g0 - ts[0])); \
      if (w >= DIAG_W0 && w < DIAG_W1) { const int d = (int)((unsigned)g1 - ts[0]); mx = max(mx, d); mn = min(mn, d); sum += (float)d; } \
    } \
    mn0 = -(int)wave_max((float)-mn0); mx = (int)wave_max((float)mx); mn = -(int)wave_max((float)-mn); sum = wave_sum(sum) / (256.f * (DIAG_W1 - DIAG_W0)); \
    const int st = DIAG == 0 ? 0 : DIAG == 1 ? (int)sum - mn0 : DIAG == 2 ? mx - mn0 : mn - mn0; \
    const int until = __builtin_amdgcn_readfirstlane(mn0 + 30000 + DIAG_S * st); \
    while ((int)((unsigned)__builtin_amdgcn_s_memrealtime() - ts[0]) < until) __builtin_amdgcn_s_sleep(1); \
  } } while (0)
#define STAMP_DECL unsigned ts[16] = {0, 0, 0, 0, 0, 0, 0, 0, 0, 0, 0, 0, 0, 0, 0, 0}

typedef __amdgpu_buffer_rsrc_t rsrc_t;
#define WPOL 0
#define NPRE 3
__device__ __forceinline__ u32x4 ldw(rsrc_t r, int voff, int soff) { return __builtin_amdgcn_raw_buffer_load_b128(r, voff, soff, WPOL); }
__device__ __forceinline__ u32x4 ldnt(rsrc_t r, int voff, int soff) { return __builtin_amdgcn_raw_buffer_load_b128(r, voff, soff, 2); }
#define TRK 0
#define WOD 2
#define QDELAY 32
#define PDIST KDIST
#define TRICKLE() __builtin_amdgcn_s_sleep(TRK)
#define HSYNC(n) do { \
    if (lane == 0) __hip_atomic_fetch_add((LAS unsigned*)lds + L_CNT + 1 + (n), 1u, __ATOMIC_RELAXED, __HIP_MEMORY_SCOPE_WORKGROUP); \
    while (__hip_atomic_load((LAS unsigned*)lds + L_CNT + 1 + (n), __ATOMIC_RELAXED, __HIP_MEMORY_SCOPE_WORKGROUP) < 8u) __builtin_amdgcn_s_sleep(0); \
    asm volatile("" ::: "memory"); \
  } while (0)
#define SB() __builtin_amdgcn_sched_barrier(0)
#define VM_DRAIN() do { SB(); asm volatile("s_waitcnt vmcnt(0)" ::: "memory"); SB(); } while (0)

__global__ void __launch_bounds__(NTHR, 3) fused_decode(Args a) {
  __shared__ __attribute__((aligned(16))) float lds[L_END];
  const int tid = threadIdx.x, lane = tid & 63, wave = __builtin_amdgcn_readfirstlane(tid >> 6);
  const int r = lane >> 4, c = lane & 15;
  const int xg = blockIdx.x & 7, j = blockIdx.x >> 3;
  const int b = j >> 4, h = xg * 4 + ((j >> 2) & 3), sp = j & 3;
  const int chunk = b * 128 + h * 4 + sp;
  const int voff = lane * 16;
  STAMP_DECL;
  STAMP(0);
  unsigned dg_epoch = *(const __attribute__((address_space(4))) unsigned*)(a.ws + WS_EPOCH);

  if (wave >= 8) {
    const int sw = wave - 8;
    const rsrc_t rk = __builtin_amdgcn_make_buffer_rsrc((void*)(a.k + (size_t)chunk * 4096 * DH), 0, 4096 * DH * 2, 0x00020000);
    const rsrc_t rv = __builtin_amdgcn_make_buffer_rsrc((void*)(a.v + (size_t)chunk * 4096 * DH), 0, 4096 * DH * 2, 0x00020000);
    const int stag = j * KSTAG;
    auto tile_off = [&](int t) -> int { return (((t + stag) & 63) << 14) + (sw << 12); };
    __builtin_amdgcn_s_setprio(2);
    u32x4 buf[8], pre[NPRE * 8 + 1];
    __builtin_amdgcn_s_barrier();
    SB();
#pragma unroll
    for (int u = 0; u < NPRE * 8; ++u) {
      pre[u] = ldnt(rk, voff + (u & 3) * 1024, tile_off(u >> 2)); SB();
      if (u >= PDIST) { asm volatile("s_waitcnt vmcnt(%0)" :: "i"(PDIST)); SB(); }
    }
#pragma unroll
    for (int u = 0; u < KDIST; ++u) {
      buf[u] = ldnt(rk, voff + (u & 3) * 1024, tile_off(2 * NPRE + (u >> 2))); SB();
      if (NPRE * 8 + u >= PDIST && u + 1 < KDIST) { asm volatile("s_waitcnt vmcnt(%0)" :: "i"(PDIST)); SB(); }
    }
    STAMP(1);
    __builtin_amdgcn_s_barrier();
    asm volatile("" ::: "memory");
    asm volatile("" : "+s"(dg_epoch));
    unsigned qp[4];
    {
      f32x4 s0 = *(const LAS f32x4*)(lds + L_RED + (2 * r) * 128 + c * 8) + *(const LAS f32x4*)(lds + L_RED + (2 * r + 1) * 128 + c * 8);
      f32x4 s1 = *(const LAS f32x4*)(lds + L_RED + (2 * r) * 128 + c * 8 + 4) + *(const LAS f32x4*)(lds + L_RED + (2 * r + 1) * 128 + c * 8 + 4);
#pragma unroll
      for (int k = 0; k < 4; ++k) { s0[k] = xrow_sum(s0[k]); s1[k] = xrow_sum(s1[k]); }
      qp[0] = pack_bf16(s0[0], s0[1]); qp[1] = pack_bf16(s0[2], s0[3]); qp[2] = pack_bf16(s1[0], s1[1]); qp[3] = pack_bf16(s1[2], s1[3]);
    }
    STAMP(2);
    LAS float* S = (LAS float*)lds + L_S + sw * 1024;
    float mx = -INFINITY;
#define DOT(w) dot2((w)[3], qp[3], dot2((w)[2], qp[2], dot2((w)[1], qp[1], dot2((w)[0], qp[0], 0.f))))
#define K_FIN(tt) do { \
      _Pragma("unroll") for (int u = 0; u < 4; ++u) { const float lo = x[u] + dpp<ROR8>(x[u]), hi = x[u + 4] + dpp<ROR8>(x[u + 4]); x[u] = (c & 8) ? hi : lo; } \
      _Pragma("unroll") for (int u = 0; u < 2; ++u) { const float lo = x[u] + dpp<XOR7>(x[u]), hi = x[u + 2] + dpp<XOR7>(x[u + 2]); x[u] = (c & 4) ? hi : lo; } \
      { const float lo = x[0] + dpp<XOR2>(x[0]), hi = x[1] + dpp<XOR2>(x[1]); x[0] = (c & 2) ? hi : lo; } \
      float sc = x[0] + dpp<XOR1>(x[0]); \
      sc = rbf(rbf(sc) * SCALE); \
      mx = fmaxf(mx, sc); \
      S[(tt) * 32 + r * 8 + (c >> 1)] = sc; \
      SB(); \
    } while (0)
#define K_PAIR(tt, FIRST, LAST) do { \
      float x[8]; \
      _Pragma("unroll") for (int u = 0; u < 8; ++u) { \
        const int n = u + KDIST; \
        buf[n & 7] = ldnt(((LAST) && n >= 8) ? rv : rk, voff + (n & 3) * 1024, tile_off(2 * (tt) + (n >> 2))); \
        SB(); \
        if ((FIRST) && u == KDIST) { \
          _Pragma("unroll") for (int pp = 0; pp < NPRE; ++pp) { \
            float x[8]; \
            _Pragma("unroll") for (int w = 0; w < 8; ++w) x[w] = DOT(pre[pp * 8 + w]); \
            K_FIN(pp); \
          } \
        } \
        x[u] = DOT(buf[u]); \
        SB(); \
      } \
      K_FIN(tt); \
    } while (0)
    static_assert(KDIST < 8 && NPRE >= 1, "ring of 8");
    K_PAIR(NPRE, true, false);
    for (int tt = NPRE + 1; tt < 31; ++tt) K_PAIR(tt, false, false);
    K_PAIR(31, false, true);
#undef K_PAIR
    STAMP(3);
    {
      mx = wave_max(mx);
      const float msub = mx * LOG2E;
      lds_wait();
      float l = 0.f;
#pragma unroll
      for (int i = 0; i < 4; ++i) {
        f32x4 s = *(const LAS f32x4*)(S + i * 256 + lane * 4);
#pragma unroll
        for (int k = 0; k < 4; ++k) { s[k] = __builtin_amdgcn_exp2f(fmaf(s[k], LOG2E, -msub)); l += s[k]; }
        *(LAS f32x4*)(S + i * 256 + lane * 4) = s;
      }
      l = wave_sum(l);
      if (lane == 0) { lds[L_ML + 2 * sw] = mx; lds[L_ML + 2 * sw + 1] = l; }
      lds_wait();
      if (lane == 0) __hip_atomic_fetch_add((LAS unsigned*)lds + L_CNT, 1u, __ATOMIC_RELAXED, __HIP_MEMORY_SCOPE_WORKGROUP);
    }
    STAMP(4);
    f32x2 acc[4] = {{0.f, 0.f}, {0.f, 0.f}, {0.f, 0.f}, {0.f, 0.f}};
#define V_PAIR(tt, LAST) do { \
      const f32x4 p0 = *(const LAS f32x4*)(S + (tt) * 32 + r * 8), p1 = *(const LAS f32x4*)(S + (tt) * 32 + r * 8 + 4); \
      _Pragma("unroll") for (int u = 0; u < 8; ++u) { \
        const int n = u + KDIST; \
        if (!(LAST) || n < 8) buf[n & 7] = ldnt(rv, voff + (n & 3) * 1024, tile_off(2 * (tt) + (n >> 2))); \
        SB(); \
        const float p = u < 4 ? p0[u & 3] : p1[u & 3]; \
        _Pragma("unroll") for (int k = 0; k < 4; ++k) acc[k] = __builtin_elementwise_fma(bf_unpack(buf[u][k]), f32x2{p, p}, acc[k]); \
        SB(); \
      } \
    } while (0)
    for (int tt = 0; tt < 31; ++tt) V_PAIR(tt, false);
    V_PAIR(31, true);
#undef V_PAIR
    STAMP(5);
    *(LAS f32x4*)(lds + L_RED + (sw * 4 + r) * 128 + c * 8) = f32x4{acc[0][0], acc[0][1], acc[1][0], acc[1][1]};
    *(LAS f32x4*)(lds + L_RED + (sw * 4 + r) * 128 + c * 8 + 4) = f32x4{acc[2][0], acc[2][1], acc[3][0], acc[3][1]};
    lds_wait();
    __builtin_amdgcn_s_barrier();
    __builtin_amdgcn_s_barrier();
    __builtin_amdgcn_s_barrier();
    STAMP_FLUSH();
    return;
  }

  const int g8 = b * 4 + sp;
  const u32x4 xv0 = *(const GAS u32x4*)(a.x + lane * 8), xv1 = *(const GAS u32x4*)(a.x + DM + lane * 8);
  const int qrow = g8 * 64 + wave * 8 + 2 * r, mrow = g8 * 64 + wave * 8 + (lane >> 3);
  const unsigned xq0 = *(const gu32*)(a.x + qrow), xq1 = *(const gu32*)(a.x + DM + qrow);
  u32x4 wq0, wq1;
  {
    const rsrc_t rq = __builtin_amdgcn_make_buffer_rsrc((void*)(a.wq + h * DH), 0, (DM * NH * DH - h * DH) * 2, 0x00020000);
    wq0 = ldnt(rq, qrow * (NH * DH * 2) + c * 16, 0);
    wq1 = ldnt(rq, qrow * (NH * DH * 2) + c * 16, NH * DH * 2);
  }
  unsigned epoch = *(const __attribute__((address_space(4))) unsigned*)(a.ws + WS_EPOCH);
  SB();
  if (tid < 4) ((LAS unsigned*)lds)[L_CNT + tid] = 0u;
  lds_wait();
  __builtin_amdgcn_s_barrier();
  SB();
  float mb0, mb1, sd0, sd1;
  {
    float f0[8], f1[8];
#pragma unroll
    for (int k = 0; k < 4; ++k) { f0[2 * k] = bf_lo(xv0[k]); f0[2 * k + 1] = bf_hi(xv0[k]); f1[2 * k] = bf_lo(xv1[k]); f1[2 * k + 1] = bf_hi(xv1[k]); }
    float s0 = 0.f, s1 = 0.f;
#pragma unroll
    for (int k = 0; k < 8; ++k) { s0 += f0[k]; s1 += f1[k]; }
    const float m0 = wave_sum(s0) * (1.f / DM), m1 = wave_sum(s1) * (1.f / DM);
    float v0 = 0.f, v1 = 0.f;
#pragma unroll
    for (int k = 0; k < 8; ++k) { const float d0 = f0[k] - m0, d1 = f1[k] - m1; v0 = fmaf(d0, d0, v0); v1 = fmaf(d1, d1, v1); }
    v0 = wave_sum(v0) * (1.f / DM); v1 = wave_sum(v1) * (1.f / DM);
    mb0 = rbf(m0); sd0 = rbf(__builtin_sqrtf(rbf(rbf(v0) + 1e-5f)));
    mb1 = rbf(m1); sd1 = rbf(__builtin_sqrtf(rbf(rbf(v1) + 1e-5f)));
  }
#define XN(xf, mb, sd) rbf(rbf((xf) - (mb)) / (sd))
  asm volatile("" : "+s"(epoch));
  asm volatile("" : "+s"(dg_epoch));
  const unsigned tag = epoch + 0x9E3779B9u;
  STAMP(10);
  {
    const float a0 = XN(bf_lo(xq0), mb0, sd0), a1 = XN(bf_hi(xq0), mb0, sd0), b0 = XN(bf_lo(xq1), mb1, sd1), b1 = XN(bf_hi(xq1), mb1, sd1);
    float t[8];
#pragma unroll
    for (int k = 0; k < 4; ++k) {
      const f32x2 w0 = bf_unpack(wq0[k]), w1 = bf_unpack(wq1[k]);
      const f32x2 qa = __builtin_elementwise_fma(w1, f32x2{a1, a1}, w0 * a0), qb = __builtin_elementwise_fma(w1, f32x2{b1, b1}, w0 * b0);
      t[2 * k] = tr32(qa[0], qb[0]); t[2 * k + 1] = tr32(qa[1], qb[1]);
    }
    f32x4 o;
#pragma unroll
    for (int k = 0; k < 4; ++k) o[k] = tr16(t[k], t[4 + k]);
    *(LAS f32x4*)(lds + L_Y + wave * 256 + (r >> 1) * 128 + c * 8 + (r & 1) * 4) = o;
    lds_wait();
  }
  STAMP(11);
  HSYNC(0);
  STAMP(12);
  gu64* Q = (gu64*)(a.ws + WS_Q) + h * 2048;
  if (tid < 256) {
    const LAS float* P = (const LAS float*)lds + L_Y + tid;
    st_granule(Q + g8 * 256 + tid, tag, ((P[0] + P[256]) + (P[512] + P[768])) + ((P[1024] + P[1280]) + (P[1536] + P[1792])));
  }
  STAMP(9);
  __builtin_amdgcn_s_sleep(QDELAY);
  {
    const unsigned long long t0 = __builtin_amdgcn_s_memrealtime();
    unsigned long long g0, g1;
    while (true) {
      g0 = __hip_atomic_load(Q + wave * 256 + b * 128 + lane, RLX_AGENT);
      g1 = __hip_atomic_load(Q + wave * 256 + b * 128 + 64 + lane, RLX_AGENT);
      asm volatile("" :: "v"(g0), "v"(g1));
      if (ts[14] == 0) STAMP(14); else if (ts[15] == 0) STAMP(15);
      if (__all((unsigned)(g0 >> 32) == tag && (unsigned)(g1 >> 32) == tag)) break;
      if (__builtin_amdgcn_s_memrealtime() - t0 > SPIN_TICKS) break;
      __builtin_amdgcn_s_sleep(2);
    }
    lds[L_RED + wave * 128 + lane] = __uint_as_float((unsigned)g0);
    lds[L_RED + wave * 128 + 64 + lane] = __uint_as_float((unsigned)g1);
    lds_wait();
  }
  STAMP(1);
  __builtin_amdgcn_s_barrier();
  asm volatile("" ::: "memory");
  gu64* G = (gu64*)(a.ws + WS_G);
  const unsigned xw = tid < 2 ? *(const gu32*)(a.x + tid * DM + 2 * blockIdx.x) : 0u;
  if (h < 28) {
    const unsigned short xm0 = a.x[mrow], xm1 = a.x[DM + mrow];
    const rsrc_t r1 = __builtin_amdgcn_make_buffer_rsrc((void*)(a.w1 + h * 64), 0, (DM * FF - h * 64) * 2, 0x00020000);
    const rsrc_t r3 = __builtin_amdgcn_make_buffer_rsrc((void*)(a.w3 + h * 64), 0, (DM * FF - h * 64) * 2, 0x00020000);
    const u32x4 wg = ldnt(r1, mrow * (FF * 2) + (lane & 7) * 16, 0);
    const u32x4 wu = ldnt(r3, mrow * (FF * 2) + (lane & 7) * 16, 0);
    const float a0 = XN(bf_lo(xm0), mb0, sd0), b0 = XN(bf_lo(xm1), mb1, sd1);
    float t[8];
#pragma unroll
    for (int k = 0; k < 4; ++k) {
      const f32x2 g = bf_unpack(wg[k]), u = bf_unpack(wu[k]);
#pragma unroll
      for (int e = 0; e < 2; ++e) t[2 * k + e] = tr16(tr32(g[e] * a0, g[e] * b0), tr32(u[e] * a0, u[e] * b0));
    }
    f32x4 o;
#pragma unroll
    for (int k = 0; k < 4; ++k) { const float lo = t[k] + dpp<ROR8>(t[k]), hi = t[4 + k] + dpp<ROR8>(t[4 + k]); o[k] = (c & 8) ? hi : lo; }
    *(LAS f32x4*)(lds + L_Y + 2048 + wave * 256 + r * 64 + (c & 7) * 8 + (c >> 3) * 4) = o;
    lds_wait();
    HSYNC(1);
    if (tid < 256) {
      const LAS float* P = (const LAS float*)lds + L_Y + 2048 + tid;
      st_granule(G + (h * 8 + g8) * 256 + tid, tag, ((P[0] + P[256]) + (P[512] + P[768])) + ((P[1024] + P[1280]) + (P[1536] + P[1792])));
    }
  }
  f32x2 wf[64];
  {
    const rsrc_t ro = __builtin_amdgcn_make_buffer_rsrc((void*)(a.wo + (size_t)h * DH * DM), 0, DH * DM * 2, 0x00020000);
#pragma unroll
    for (int i0 = 0; i0 < 16; i0 += WOD) {
      u32x4 w[WOD];
#pragma unroll
      for (int i = 0; i < WOD; ++i) w[i] = ldw(ro, voff + ((i0 + i) & 3) * 1024, (wave * 16 + ((i0 + i) & ~3)) * 1024);
      VM_DRAIN();
#pragma unroll
      for (int i = 0; i < WOD; ++i)
#pragma unroll
        for (int k = 0; k < 4; ++k) { wf[(i0 + i) * 4 + k] = bf_unpack(w[i][k]); asm volatile("" : "+v"(wf[(i0 + i) * 4 + k])); }
      TRICKLE();
    }
  }
  f32x2 yacc[4] = {{0.f, 0.f}, {0.f, 0.f}, {0.f, 0.f}, {0.f, 0.f}};
  if (wave < 7) {
    const int f0 = (h * 4 + sp) * 14 + wave * 2;
    const rsrc_t r2 = __builtin_amdgcn_make_buffer_rsrc((void*)(a.w2 + (size_t)f0 * DM), 0, 2 * DM * 2, 0x00020000);
    const u32x4 w20 = ldw(r2, voff, 0), w21 = ldw(r2, voff + 1024, 0);
    const int f = f0 + (r & 1);
    gu64* gp = G + ((f >> 6) * 8 + (c & 7)) * 256 + b * 128 + (c >> 3) * 64 + (f & 63);
    const unsigned long long t0 = __builtin_amdgcn_s_memrealtime();
    unsigned long long g;
    while (true) {
      g = __hip_atomic_load(gp, RLX_AGENT);
      if (__all((unsigned)(g >> 32) == tag)) break;
      if (__builtin_amdgcn_s_memrealtime() - t0 > SPIN_TICKS) break;
      __builtin_amdgcn_s_sleep(16);
    }
    float s = __uint_as_float((unsigned)g);
    s += dpp<XOR1>(s); s += dpp<XOR2>(s); s += dpp<XOR7>(s);
    const float gs = rbf(s), us = rbf(dpp<ROR8>(s));
    const float si = rbf(gs / (1.f + __expf(-gs)));
    const float hv = rbf(si * us);
    const float h0 = __builtin_bit_cast(float, __builtin_amdgcn_readlane(__builtin_bit_cast(int, hv), 0));
    const float h1 = __builtin_bit_cast(float, __builtin_amdgcn_readlane(__builtin_bit_cast(int, hv), 16));
#pragma unroll
    for (int k = 0; k < 4; ++k) yacc[k] = __builtin_elementwise_fma(bf_unpack(w21[k]), f32x2{h1, h1}, bf_unpack(w20[k]) * h0);
  }
  STAMP(2);
  if (wave == 0) {
    while (__hip_atomic_load((LAS unsigned*)lds + L_CNT, __ATOMIC_RELAXED, __HIP_MEMORY_SCOPE_WORKGROUP) < 4u) __builtin_amdgcn_s_sleep(16);
    asm volatile("" ::: "memory");
    const float mw = lds[L_ML + 2 * (lane & 3)], lw = lds[L_ML + 2 * (lane & 3) + 1];
    float m = fmaxf(mw, dpp<XOR1>(mw)); m = fmaxf(m, dpp<XOR2>(m));
    float l = lw * __builtin_amdgcn_exp2f((mw - m) * LOG2E);
    l += dpp<XOR1>(l); l += dpp<XOR2>(l);
    gu64* ml = (gu64*)(a.ws + WS_ML);
    if (lane < 2) st_granule(ml + chunk * 2 + lane, tag, lane == 0 ? m : l);
    const unsigned long long t0 = __builtin_amdgcn_s_memrealtime();
    unsigned long long gm, gl;
    while (true) {
      gm = __hip_atomic_load(ml + ((chunk & ~3) + (lane & 3)) * 2, RLX_AGENT);
      gl = __hip_atomic_load(ml + ((chunk & ~3) + (lane & 3)) * 2 + 1, RLX_AGENT);
      if (__all((unsigned)(gm >> 32) == tag && (unsigned)(gl >> 32) == tag)) break;
      if (__builtin_amdgcn_s_memrealtime() - t0 > SPIN_TICKS) break;
      __builtin_amdgcn_s_sleep(16);
    }
    const float ms = __uint_as_float((unsigned)gm), ls = __uint_as_float((unsigned)gl);
    float M = fmaxf(ms, dpp<XOR1>(ms)); M = fmaxf(M, dpp<XOR2>(M));
    float L = ls * __builtin_amdgcn_exp2f((ms - M) * LOG2E);
    L += dpp<XOR1>(L); L += dpp<XOR2>(L);
    if (lane < 4) lds[L_FW + lane] = __builtin_amdgcn_exp2f((mw - M) * LOG2E) / L;
    lds_wait();
    STAMP(3);
  }
  __builtin_amdgcn_s_barrier();
  asm volatile("" ::: "memory");
  STAMP(6);
  {
    const LAS float* P = (const LAS float*)lds + L_RED + (4 * r) * 128 + wave * 16 + c;
    float o = ((P[0] + P[128]) + (P[256] + P[384])) * lds[L_FW + r];
    o = xrow_sum(o);
#pragma unroll
    for (int i = 0; i < 16; ++i) {
      const float oi = __builtin_bit_cast(float, __builtin_amdgcn_readlane(__builtin_bit_cast(int, o), i));
#pragma unroll
      for (int k = 0; k < 4; ++k) yacc[k] = __builtin_elementwise_fma(wf[i * 4 + k], f32x2{oi, oi}, yacc[k]);
    }
  }
  *(LAS f32x4*)(lds + L_Y + wave * 512 + lane * 8) = f32x4{yacc[0][0], yacc[0][1], yacc[1][0], yacc[1][1]};
  *(LAS f32x4*)(lds + L_Y + wave * 512 + lane * 8 + 4) = f32x4{yacc[2][0], yacc[2][1], yacc[3][0], yacc[3][1]};
  lds_wait();
  __builtin_amdgcn_s_barrier();
  asm volatile("" ::: "memory");
  gu64* Y = (gu64*)(a.ws + WS_Y);
  {
    const LAS float* P = (const LAS float*)lds + L_Y + tid;
    const float y = ((P[0] + P[512]) + (P[1024] + P[1536])) + ((P[2048] + P[2560]) + (P[3072] + P[3584]));
    st_granule(Y + (size_t)(tid >> 1) * 512 + chunk * 2 + (tid & 1), tag, y);
  }
  STAMP(7);
  {
    const unsigned long long t0 = __builtin_amdgcn_s_memrealtime();
    unsigned long long g;
    while (true) {
      g = __hip_atomic_load(Y + (size_t)blockIdx.x * 512 + tid, RLX_AGENT);
      if (__all((unsigned)(g >> 32) == tag)) break;
      if (__builtin_amdgcn_s_memrealtime() - t0 > SPIN_TICKS) break;
      __builtin_amdgcn_s_sleep(4);
    }
    float s = __uint_as_float((unsigned)g);
    s += dpp<ROR2>(s); s += dpp<ROR4>(s); s += dpp<ROR8>(s); s = xrow_sum(s);
    if (lane < 2) lds[L_FIN + wave * 2 + lane] = s;
    lds_wait();
    __builtin_amdgcn_s_barrier();
    asm volatile("" ::: "memory");
    if (tid < 2) {
      const LAS float* P = (const LAS float*)lds + L_FIN + tid * 8;
      const float o0 = bf_lo(xw) + ((P[0] + P[2]) + (P[4] + P[6])), o1 = bf_hi(xw) + ((P[1] + P[3]) + (P[5] + P[7]));
      *(gu32*)(a.out + tid * DM + 2 * blockIdx.x) = pack_bf16(o0, o1);
      if (blockIdx.x == 0 && tid == 0) __hip_atomic_store((gu32*)(a.ws + WS_EPOCH), tag, RLX_AGENT);
    }
  }
  STAMP(8);
  STAMP_FLUSH();
}

}

extern "C" void kernel_launch(void* const* d_in, const int* in_sizes, int n_in, void* d_out, int out_size, void* d_ws, size_t ws_size,
                              hipStream_t stream) {
  Args a;
  a.x = (const unsigned short*)d_in[0]; a.k = (const unsigned short*)d_in[1]; a.v = (const unsigned short*)d_in[2];
  a.wq = (const unsigned short*)d_in[3]; a.wo = (const unsigned short*)d_in[4];
  a.w1 = (const unsigned short*)d_in[6]; a.w3 = (const unsigned short*)d_in[7]; a.w2 = (const unsigned short*)d_in[8];
  a.out = (unsigned short*)d_out; a.ws = (unsigned char*)d_ws;
  hipFuncSetAttribute((const void*)fused_decode, hipFuncAttributeMaxDynamicSharedMemorySize, 100 * 1024);
  fused_decode<<<NWG, NTHR, 100 * 1024, stream>>>(a);
}
```

```cpp
#define NEND 0
#define WOD 4
#include <hip/hip_runtime.h>
#include <hip/hip_bf16.h>
#include <stdint.h>

#define KDIST 5
#define KSTAG 1
#define MIMIC 1
#define STAMPS 0

namespace {

constexpr int NWG = 256, NTHR = 768;
constexpr int DM = 512, NH = 32, SK = 16384, DH = 128, FF = 1792;
constexpr float LOG2E = 1.4426950408889634f;
constexpr float SCALE = 0.08838834764831845f;
constexpr size_t WS_EPOCH = 0, WS_ML = 4096;
constexpr size_t WS_Y = 65536;
constexpr size_t WS_Q = WS_Y + (1u << 20);
constexpr size_t WS_G = WS_Q + (512u << 10);
constexpr size_t WS_STAMP = 8u << 20;
constexpr int L_S = 0;
constexpr int L_RED = L_S + 4096;
constexpr int L_Y = L_RED + 2048;
constexpr int L_ML = L_Y + 4096;
constexpr int L_FW = L_ML + 8;
constexpr int L_FIN = L_FW + 4;
constexpr int L_CNT = L_FIN + 128;
constexpr int L_END = L_CNT + 4;

#define GAS __attribute__((address_space(1)))
#define LAS __attribute__((address_space(3)))
#define RLX_AGENT __ATOMIC_RELAXED, __HIP_MEMORY_SCOPE_AGENT
typedef unsigned u32x4 __attribute__((ext_vector_type(4)));
typedef float f32x2 __attribute__((ext_vector_type(2)));
typedef float f32x4 __attribute__((ext_vector_type(4)));
typedef __bf16 bf16x2 __attribute__((ext_vector_type(2)));
typedef GAS unsigned gu32;
typedef GAS unsigned long long gu64;

__device__ __forceinline__ float bf_lo(unsigned u) { return __builtin_bit_cast(float, u << 16); }
__device__ __forceinline__ float bf_hi(unsigned u) { return __builtin_bit_cast(float, u & 0xffff0000u); }
__device__ __forceinline__ f32x2 bf_unpack(unsigned u) { return f32x2{bf_lo(u), bf_hi(u)}; }
__device__ __forceinline__ unsigned pack_bf16(float lo, float hi) {
  bf16x2 r = {(__bf16)lo, (__bf16)hi};
  return __builtin_bit_cast(unsigned, r);
}
__device__ __forceinline__ float rbf(float x) {
  return (float)(__bf16)x;
}
__device__ __forceinline__ float dot2(unsigned a, unsigned b, float acc) {
  return __builtin_amdgcn_fdot2_f32_bf16(__builtin_bit_cast(bf16x2, a), __builtin_bit_cast(bf16x2, b), acc, false);
}
template <int CTRL> __device__ __forceinline__ float dpp(float x) {
  return __builtin_bit_cast(float, __builtin_amdgcn_mov_dpp(__builtin_bit_cast(int, x), CTRL, 0xf, 0xf, true));
}
constexpr int XOR1 = 0xB1, XOR2 = 0x4E, XOR7 = 0x141, ROR2 = 0x122, ROR4 = 0x124, ROR8 = 0x128;
__device__ __forceinline__ float xrow_sum(float x) {
  auto s = __builtin_amdgcn_permlane16_swap(__float_as_uint(x), __float_as_uint(x), false, false);
  x = __uint_as_float(s[0]) + __uint_as_float(s[1]);
  auto t = __builtin_amdgcn_permlane32_swap(__float_as_uint(x), __float_as_uint(x), false, false);
  return __uint_as_float(t[0]) + __uint_as_float(t[1]);
}
__device__ __forceinline__ float xrow_max(float x) {
  auto s = __builtin_amdgcn_permlane16_swap(__float_as_uint(x), __float_as_uint(x), false, false);
  x = fmaxf(__uint_as_float(s[0]), __uint_as_float(s[1]));
  auto t = __builtin_amdgcn_permlane32_swap(__float_as_uint(x), __float_as_uint(x), false, false);
  return fmaxf(__uint_as_float(t[0]), __uint_as_float(t[1]));
}
__device__ __forceinline__ float tr16(float a, float b) {
  auto s = __builtin_amdgcn_permlane16_swap(__float_as_uint(a), __float_as_uint(b), false, false);
  return __uint_as_float(s[0]) + __uint_as_float(s[1]);
}
__device__ __forceinline__ float tr32(float a, float b) {
  auto s = __builtin_amdgcn_permlane32_swap(__float_as_uint(a), __float_as_uint(b), false, false);
  return __uint_as_float(s[0]) + __uint_as_float(s[1]);
}
__device__ __forceinline__ float wave_sum(float x) {
  x += dpp<XOR1>(x); x += dpp<XOR2>(x); x += dpp<XOR7>(x); x += dpp<ROR8>(x);
  return xrow_sum(x);
}
__device__ __forceinline__ float wave_max(float x) {
  x = fmaxf(x, dpp<XOR1>(x)); x = fmaxf(x, dpp<XOR2>(x)); x = fmaxf(x, dpp<XOR7>(x)); x = fmaxf(x, dpp<ROR8>(x));
  return xrow_max(x);
}
__device__ __forceinline__ void lds_wait() { asm volatile("s_waitcnt lgkmcnt(0)" ::: "memory"); }
__device__ __forceinline__ void st_granule(gu64* g, unsigned tag, float v) {
  __hip_atomic_store(g, ((unsigned long long)tag << 32) | (unsigned long long)__float_as_uint(v), RLX_AGENT);
}
constexpr unsigned long long SPIN_TICKS = 20000000ull;

struct Args {
  const unsigned short* x; const unsigned short* k; const unsigned short* v; const unsigned short* wq; const unsigned short* wo;
  const unsigned short* w1; const unsigned short* w3; const unsigned short* w2;
  unsigned short* out; unsigned char* ws;
};

#define STAMP(i) do { } while (0)
#define STAMP_FLUSH() do { } while (0)
#define STAMP_DECL

typedef __amdgpu_buffer_rsrc_t rsrc_t;
#define WPOL 0
#define NPRE 3
__device__ __forceinline__ u32x4 ldw(rsrc_t r, int voff, int soff) { return __builtin_amdgcn_raw_buffer_load_b128(r, voff, soff, WPOL); }
__device__ __forceinline__ u32x4 ldnt(rsrc_t r, int voff, int soff) { return __builtin_amdgcn_raw_buffer_load_b128(r, voff, soff, 2); }
#define TRK 0
#define QDELAY 32
#define FSLEEP 4
#define PDIST KDIST
#define TRICKLE() __builtin_amdgcn_s_sleep(TRK)
#define HSYNC(n) do { \
    if (lane == 0) __hip_atomic_fetch_add((LAS unsigned*)lds + L_CNT + 1 + (n), 1u, __ATOMIC_RELAXED, __HIP_MEMORY_SCOPE_WORKGROUP); \
    while (__hip_atomic_load((LAS unsigned*)lds + L_CNT + 1 + (n), __ATOMIC_RELAXED, __HIP_MEMORY_SCOPE_WORKGROUP) < 8u) __builtin_amdgcn_s_sleep(0); \
    asm volatile("" ::: "memory"); \
  } while (0)
#define SB() __builtin_amdgcn_sched_barrier(0)
#define VM_DRAIN() do { SB(); asm volatile("s_waitcnt vmcnt(0)" ::: "memory"); SB(); } while (0)

__global__ void __launch_bounds__(NTHR, 3) fused_decode(Args a) {
  __shared__ __attribute__((aligned(16))) float lds[L_END];
  const int tid = threadIdx.x, lane = tid & 63, wave = __builtin_amdgcn_readfirstlane(tid >> 6);
  const int r = lane >> 4, c = lane & 15;
  const int xg = blockIdx.x & 7, j = blockIdx.x >> 3;
  const int b = j >> 4, h = xg * 4 + ((j >> 2) & 3), sp = j & 3;
  const int chunk = b * 128 + h * 4 + sp;
  const int voff = lane * 16;
  STAMP_DECL;
  STAMP(0);

  if (wave >= 8) {
    const int sw = wave - 8;
    const rsrc_t rk = __builtin_amdgcn_make_buffer_rsrc((void*)(a.k + (size_t)chunk * 4096 * DH), 0, 4096 * DH * 2, 0x00020000);
    const rsrc_t rv = __builtin_amdgcn_make_buffer_rsrc((void*)(a.v + (size_t)chunk * 4096 * DH), 0, 4096 * DH * 2, 0x00020000);
    const int stag = j * KSTAG;
    auto tile_off = [&](int t) -> int { return (((t + stag) & 63) << 14) + (sw << 12); };
    __builtin_amdgcn_s_setprio(2);
    u32x4 buf[8], pre[NPRE * 8 + 1];
    __builtin_amdgcn_s_barrier();
    SB();
#pragma unroll
    for (int u = 0; u < NPRE * 8; ++u) {
      pre[u] = ldnt(rk, voff + (u & 3) * 1024, tile_off(u >> 2)); SB();
      if (u >= PDIST) { asm volatile("s_waitcnt vmcnt(%0)" :: "i"(PDIST)); SB(); }
    }
#pragma unroll
    for (int u = 0; u < KDIST; ++u) {
      buf[u] = ldnt(rk, voff + (u & 3) * 1024, tile_off(2 * NPRE + (u >> 2))); SB();
      if (NPRE * 8 + u >= PDIST && u + 1 < KDIST) { asm volatile("s_waitcnt vmcnt(%0)" :: "i"(PDIST)); SB(); }
    }
    STAMP(1);
    __builtin_amdgcn_s_barrier();
    asm volatile("" ::: "memory");
    unsigned qp[4];
    {
      f32x4 s0 = *(const LAS f32x4*)(lds + L_RED + (2 * r) * 128 + c * 8) + *(const LAS f32x4*)(lds + L_RED + (2 * r + 1) * 128 + c * 8);
      f32x4 s1 = *(const LAS f32x4*)(lds + L_RED + (2 * r) * 128 + c * 8 + 4) + *(const LAS f32x4*)(lds + L_RED + (2 * r + 1) * 128 + c * 8 + 4);
#pragma unroll
      for (int k = 0; k < 4; ++k) { s0[k] = xrow_sum(s0[k]); s1[k] = xrow_sum(s1[k]); }
      qp[0] = pack_bf16(s0[0], s0[1]); qp[1] = pack_bf16(s0[2], s0[3]); qp[2] = pack_bf16(s1[0], s1[1]); qp[3] = pack_bf16(s1[2], s1[3]);
    }
    STAMP(2);
    LAS float* S = (LAS float*)lds + L_S + sw * 1024;
    float mx = -INFINITY;
#define DOT(w) dot2((w)[3], qp[3], dot2((w)[2], qp[2], dot2((w)[1], qp[1], dot2((w)[0], qp[0], 0.f))))
#define K_FIN(tt) do { \
      _Pragma("unroll") for (int u = 0; u < 4; ++u) { const float lo = x[u] + dpp<ROR8>(x[u]), hi = x[u + 4] + dpp<ROR8>(x[u + 4]); x[u] = (c & 8) ? hi : lo; } \
      _Pragma("unroll") for (int u = 0; u < 2; ++u) { const float lo = x[u] + dpp<XOR7>(x[u]), hi = x[u + 2] + dpp<XOR7>(x[u + 2]); x[u] = (c & 4) ? hi : lo; } \
      { const float lo = x[0] + dpp<XOR2>(x[0]), hi = x[1] + dpp<XOR2>(x[1]); x[0] = (c & 2) ? hi : lo; } \
      float sc = x[0] + dpp<XOR1>(x[0]); \
      sc = rbf(rbf(sc) * SCALE); \
      mx = fmaxf(mx, sc); \
      S[(tt) * 32 + r * 8 + (c >> 1)] = sc; \
      SB(); \
    } while (0)
#define K_PAIR(tt, FIRST, LAST) do { \
      float x[8]; \
      _Pragma("unroll") for (int u = 0; u < 8; ++u) { \
        const int n = u + KDIST; \
        buf[n & 7] = ldnt(((LAST) && n >= 8) ? rv : rk, voff + (n & 3) * 1024, tile_off(2 * (tt) + (n >> 2))); \
        SB(); \
        if ((FIRST) && u == KDIST) { \
          _Pragma("unroll") for (int pp = 0; pp < NPRE; ++pp) { \
            float x[8]; \
            _Pragma("unroll") for (int w = 0; w < 8; ++w) x[w] = DOT(pre[pp * 8 + w]); \
            K_FIN(pp); \
          } \
        } \
        x[u] = DOT(buf[u]); \
        SB(); \
      } \
      K_FIN(tt); \
    } while (0)
    static_assert(KDIST < 8 && NPRE >= 1, "ring of 8");
    K_PAIR(NPRE, true, false);
    for (int tt = NPRE + 1; tt < 31; ++tt) K_PAIR(tt, false, false);
    K_PAIR(31, false, true);
#undef K_PAIR
    STAMP(3);
    {
      mx = wave_max(mx);
      const float msub = mx * LOG2E;
      lds_wait();
      float l = 0.f;
#pragma unroll
      for (int i = 0; i < 4; ++i) {
        f32x4 s = *(const LAS f32x4*)(S + i * 256 + lane * 4);
#pragma unroll
        for (int k = 0; k < 4; ++k) { s[k] = __builtin_amdgcn_exp2f(fmaf(s[k], LOG2E, -msub)); l += s[k]; }
        *(LAS f32x4*)(S + i * 256 + lane * 4) = s;
      }
      l = wave_sum(l);
      if (lane == 0) { lds[L_ML + 2 * sw] = mx; lds[L_ML + 2 * sw + 1] = l; }
      lds_wait();
      if (lane == 0) __hip_atomic_fetch_add((LAS unsigned*)lds + L_CNT, 1u, __ATOMIC_RELAXED, __HIP_MEMORY_SCOPE_WORKGROUP);
    }
    STAMP(4);
    f32x2 acc[4] = {{0.f, 0.f}, {0.f, 0.f}, {0.f, 0.f}, {0.f, 0.f}};
#define V_ROW(w, p) _Pragma("unroll") for (int k = 0; k < 4; ++k) acc[k] = __builtin_elementwise_fma(bf_unpack((w)[k]), f32x2{p, p}, acc[k])
#define V_PAIR(tt) do { \
      const f32x4 p0 = *(const LAS f32x4*)(S + (tt) * 32 + r * 8), p1 = *(const LAS f32x4*)(S + (tt) * 32 + r * 8 + 4); \
      _Pragma("unroll") for (int u = 0; u < 8; ++u) { \
        const int n = u + KDIST; \
        buf[n & 7] = ldnt(rv, voff + (n & 3) * 1024, tile_off(2 * (tt) + (n >> 2))); \
        SB(); \
        const float p = u < 4 ? p0[u & 3] : p1[u & 3]; \
        V_ROW(buf[u], p); \
        SB(); \
      } \
    } while (0)
    for (int tt = 0; tt < 31 - NEND; ++tt) V_PAIR(tt);
#pragma unroll
    for (int n = KDIST; n < 8; ++n) { buf[n] = ldnt(rv, voff + (n & 3) * 1024, tile_off(2 * (31 - NEND) + (n >> 2))); SB(); }
#pragma unroll
    for (int n = 0; n < NEND * 8; ++n) { pre[n] = ldnt(rv, voff + (n & 3) * 1024, tile_off(2 * (32 - NEND) + (n >> 2))); SB(); }
#pragma unroll
    for (int pp = 0; pp <= NEND; ++pp) {
      const f32x4 p0 = *(const LAS f32x4*)(S + (31 - NEND + pp) * 32 + r * 8), p1 = *(const LAS f32x4*)(S + (31 - NEND + pp) * 32 + r * 8 + 4);
#pragma unroll
      for (int u = 0; u < 8; ++u) {
        const float p = u < 4 ? p0[u & 3] : p1[u & 3];
        if (pp == 0) { V_ROW(buf[u], p); } else { V_ROW(pre[(pp > 0 ? pp - 1 : 0) * 8 + u], p); }
        asm volatile("" : "+v"(acc[0]), "+v"(acc[1]), "+v"(acc[2]), "+v"(acc[3]));
        SB();
      }
    }
#undef V_PAIR
    STAMP(5);
    *(LAS f32x4*)(lds + L_RED + (sw * 4 + r) * 128 + c * 8) = f32x4{acc[0][0], acc[0][1], acc[1][0], acc[1][1]};
    *(LAS f32x4*)(lds + L_RED + (sw * 4 + r) * 128 + c * 8 + 4) = f32x4{acc[2][0], acc[2][1], acc[3][0], acc[3][1]};
    lds_wait();
    __builtin_amdgcn_s_barrier();
    __builtin_amdgcn_s_barrier();
    __builtin_amdgcn_s_barrier();
    STAMP_FLUSH();
    return;
  }

  const int g8 = b * 4 + sp;
  const u32x4 xv0 = *(const GAS u32x4*)(a.x + lane * 8), xv1 = *(const GAS u32x4*)(a.x + DM + lane * 8);
  const int qrow = g8 * 64 + wave * 8 + 2 * r, mrow = g8 * 64 + wave * 8 + (lane >> 3);
  const unsigned xq0 = *(const gu32*)(a.x + qrow), xq1 = *(const gu32*)(a.x + DM + qrow);
  u32x4 wq0, wq1;
  {
    const rsrc_t rq = __builtin_amdgcn_make_buffer_rsrc((void*)(a.wq + h * DH), 0, (DM * NH * DH - h * DH) * 2, 0x00020000);
    wq0 = ldnt(rq, qrow * (NH * DH * 2) + c * 16, 0);
    wq1 = ldnt(rq, qrow * (NH * DH * 2) + c * 16, NH * DH * 2);
  }
  unsigned epoch = *(const __attribute__((address_space(4))) unsigned*)(a.ws + WS_EPOCH);
  SB();
  if (tid < 4) ((LAS unsigned*)lds)[L_CNT + tid] = 0u;
  lds_wait();
  __builtin_amdgcn_s_barrier();
  SB();
  float mb0, mb1, sd0, sd1;
  {
    float f0[8], f1[8];
#pragma unroll
    for (int k = 0; k < 4; ++k) { f0[2 * k] = bf_lo(xv0[k]); f0[2 * k + 1] = bf_hi(xv0[k]); f1[2 * k] = bf_lo(xv1[k]); f1[2 * k + 1] = bf_hi(xv1[k]); }
    float s0 = 0.f, s1 = 0.f;
#pragma unroll
    for (int k = 0; k < 8; ++k) { s0 += f0[k]; s1 += f1[k]; }
    const float m0 = wave_sum(s0) * (1.f / DM), m1 = wave_sum(s1) * (1.f / DM);
    float v0 = 0.f, v1 = 0.f;
#pragma unroll
    for (int k = 0; k < 8; ++k) { const float d0 = f0[k] - m0, d1 = f1[k] - m1; v0 = fmaf(d0, d0, v0); v1 = fmaf(d1, d1, v1); }
    v0 = wave_sum(v0) * (1.f / DM); v1 = wave_sum(v1) * (1.f / DM);
    mb0 = rbf(m0); sd0 = rbf(__builtin_sqrtf(rbf(rbf(v0) + 1e-5f)));
    mb1 = rbf(m1); sd1 = rbf(__builtin_sqrtf(rbf(rbf(v1) + 1e-5f)));
  }
#define XN(xf, mb, sd) rbf(rbf((xf) - (mb)) / (sd))
  asm volatile("" : "+s"(epoch));
  const unsigned tag = epoch + 0x9E3779B9u;
  STAMP(10);
  {
    const float a0 = XN(bf_lo(xq0), mb0, sd0), a1 = XN(bf_hi(xq0), mb0, sd0), b0 = XN(bf_lo(xq1), mb1, sd1), b1 = XN(bf_hi(xq1), mb1, sd1);
    float t[8];
#pragma unroll
    for (int k = 0; k < 4; ++k) {
      const f32x2 w0 = bf_unpack(wq0[k]), w1 = bf_unpack(wq1[k]);
      const f32x2 qa = __builtin_elementwise_fma(w1, f32x2{a1, a1}, w0 * a0), qb = __builtin_elementwise_fma(w1, f32x2{b1, b1}, w0 * b0);
      t[2 * k] = tr32(qa[0], qb[0]); t[2 * k + 1] = tr32(qa[1], qb[1]);
    }
    f32x4 o;
#pragma unroll
    for (int k = 0; k < 4; ++k) o[k] = tr16(t[k], t[4 + k]);
    *(LAS f32x4*)(lds + L_Y + wave * 256 + (r >> 1) * 128 + c * 8 + (r & 1) * 4) = o;
    lds_wait();
  }
  STAMP(11);
  HSYNC(0);
  STAMP(12);
  gu64* Q = (gu64*)(a.ws + WS_Q) + h * 2048;
  if (tid < 256) {
    const LAS float* P = (const LAS float*)lds + L_Y + tid;
    st_granule(Q + g8 * 256 + tid, tag, ((P[0] + P[256]) + (P[512] + P[768])) + ((P[1024] + P[1280]) + (P[1536] + P[1792])));
  }
  STAMP(9);
  __builtin_amdgcn_s_sleep(QDELAY);
  {
    const unsigned long long t0 = __builtin_amdgcn_s_memrealtime();
    unsigned long long g0, g1;
    while (true) {
      g0 = __hip_atomic_load(Q + wave * 256 + b * 128 + lane, RLX_AGENT);
      g1 = __hip_atomic_load(Q + wave * 256 + b * 128 + 64 + lane, RLX_AGENT);
      if (__all((unsigned)(g0 >> 32) == tag && (unsigned)(g1 >> 32) == tag)) break;
      if (__builtin_amdgcn_s_memrealtime() - t0 > SPIN_TICKS) break;
      __builtin_amdgcn_s_sleep(2);
    }
    lds[L_RED + wave * 128 + lane] = __uint_as_float((unsigned)g0);
    lds[L_RED + wave * 128 + 64 + lane] = __uint_as_float((unsigned)g1);
    lds_wait();
  }
  STAMP(1);
  __builtin_amdgcn_s_barrier();
  asm volatile("" ::: "memory");
  gu64* G = (gu64*)(a.ws + WS_G);
  const int red = blockIdx.x < 32 ? (int)blockIdx.x : -1;
  const unsigned xw = (red >= 0 && tid < 16) ? *(const gu32*)(a.x + (tid >> 3) * DM + red * 16 + (tid & 7) * 2) : 0u;
  if (h < 28) {
    const unsigned short xm0 = a.x[mrow], xm1 = a.x[DM + mrow];
    const rsrc_t r1 = __builtin_amdgcn_make_buffer_rsrc((void*)(a.w1 + h * 64), 0, (DM * FF - h * 64) * 2, 0x00020000);
    const rsrc_t r3 = __builtin_amdgcn_make_buffer_rsrc((void*)(a.w3 + h * 64), 0, (DM * FF - h * 64) * 2, 0x00020000);
    const u32x4 wg = ldnt(r1, mrow * (FF * 2) + (lane & 7) * 16, 0);
    const u32x4 wu = ldnt(r3, mrow * (FF * 2) + (lane & 7) * 16, 0);
    const float a0 = XN(bf_lo(xm0), mb0, sd0), b0 = XN(bf_lo(xm1), mb1, sd1);
    float t[8];
#pragma unroll
    for (int k = 0; k < 4; ++k) {
      const f32x2 g = bf_unpack(wg[k]), u = bf_unpack(wu[k]);
#pragma unroll
      for (int e = 0; e < 2; ++e) t[2 * k + e] = tr16(tr32(g[e] * a0, g[e] * b0), tr32(u[e] * a0, u[e] * b0));
    }
    f32x4 o;
#pragma unroll
    for (int k = 0; k < 4; ++k) { const float lo = t[k] + dpp<ROR8>(t[k]), hi = t[4 + k] + dpp<ROR8>(t[4 + k]); o[k] = (c & 8) ? hi : lo; }
    *(LAS f32x4*)(lds + L_Y + 2048 + wave * 256 + r * 64 + (c & 7) * 8 + (c >> 3) * 4) = o;
    lds_wait();
    HSYNC(1);
    if (tid < 256) {
      const LAS float* P = (const LAS float*)lds + L_Y + 2048 + tid;
      st_granule(G + (h * 8 + g8) * 256 + tid, tag, ((P[0] + P[256]) + (P[512] + P[768])) + ((P[1024] + P[1280]) + (P[1536] + P[1792])));
    }
  }
  f32x2 wf[64];
  {
    const rsrc_t ro = __builtin_amdgcn_make_buffer_rsrc((void*)(a.wo + (size_t)h * DH * DM), 0, DH * DM * 2, 0x00020000);
#pragma unroll
    for (int i0 = 0; i0 < 16; i0 += WOD) {
      u32x4 w[WOD];
#pragma unroll
      for (int i = 0; i < WOD; ++i) w[i] = ldw(ro, voff + ((i0 + i) & 3) * 1024, (wave * 16 + ((i0 + i) & ~3)) * 1024);
      VM_DRAIN();
#pragma unroll
      for (int i = 0; i < WOD; ++i)
#pragma unroll
        for (int k = 0; k < 4; ++k) { wf[(i0 + i) * 4 + k] = bf_unpack(w[i][k]); asm volatile("" : "+v"(wf[(i0 + i) * 4 + k])); }
      TRICKLE();
    }
  }
  f32x2 yacc[4] = {{0.f, 0.f}, {0.f, 0.f}, {0.f, 0.f}, {0.f, 0.f}};
  if (wave < 7) {
    const int f0 = (h * 4 + sp) * 14 + wave * 2;
    const rsrc_t r2 = __builtin_amdgcn_make_buffer_rsrc((void*)(a.w2 + (size_t)f0 * DM), 0, 2 * DM * 2, 0x00020000);
    const u32x4 w20 = ldw(r2, voff, 0), w21 = ldw(r2, voff + 1024, 0);
    const int f = f0 + (r & 1);
    gu64* gp = G + ((f >> 6) * 8 + (c & 7)) * 256 + b * 128 + (c >> 3) * 64 + (f & 63);
    const unsigned long long t0 = __builtin_amdgcn_s_memrealtime();
    unsigned long long g;
    while (true) {
      g = __hip_atomic_load(gp, RLX_AGENT);
      if (__all((unsigned)(g >> 32) == tag)) break;
      if (__builtin_amdgcn_s_memrealtime() - t0 > SPIN_TICKS) break;
      __builtin_amdgcn_s_sleep(16);
    }
    float s = __uint_as_float((unsigned)g);
    s += dpp<XOR1>(s); s += dpp<XOR2>(s); s += dpp<XOR7>(s);
    const float gs = rbf(s), us = rbf(dpp<ROR8>(s));
    const float si = rbf(gs / (1.f + __expf(-gs)));
    const float hv = rbf(si * us);
    const float h0 = __builtin_bit_cast(float, __builtin_amdgcn_readlane(__builtin_bit_cast(int, hv), 0));
    const float h1 = __builtin_bit_cast(float, __builtin_amdgcn_readlane(__builtin_bit_cast(int, hv), 16));
#pragma unroll
    for (int k = 0; k < 4; ++k) yacc[k] = __builtin_elementwise_fma(bf_unpack(w21[k]), f32x2{h1, h1}, bf_unpack(w20[k]) * h0);
  }
  STAMP(2);
  if (wave == 0) {
    while (__hip_atomic_load((LAS unsigned*)lds + L_CNT, __ATOMIC_RELAXED, __HIP_MEMORY_SCOPE_WORKGROUP) < 4u) __builtin_amdgcn_s_sleep(16);
    asm volatile("" ::: "memory");
    const float mw = lds[L_ML + 2 * (lane & 3)], lw = lds[L_ML + 2 * (lane & 3) + 1];
    float m = fmaxf(mw, dpp<XOR1>(mw)); m = fmaxf(m, dpp<XOR2>(m));
    float l = lw * __builtin_amdgcn_exp2f((mw - m) * LOG2E);
    l += dpp<XOR1>(l); l += dpp<XOR2>(l);
    gu64* ml = (gu64*)(a.ws + WS_ML);
    if (lane < 2) st_granule(ml + chunk * 2 + lane, tag, lane == 0 ? m : l);
    const unsigned long long t0 = __builtin_amdgcn_s_memrealtime();
    unsigned long long gm, gl;
    while (true) {
      __builtin_amdgcn_s_sleep(127); __builtin_amdgcn_s_sleep(127);
      gm = __hip_atomic_load(ml + ((chunk & ~3) + (lane & 3)) * 2, RLX_AGENT);
      gl = __hip_atomic_load(ml + ((chunk & ~3) + (lane & 3)) * 2 + 1, RLX_AGENT);
      if (__all((unsigned)(gm >> 32) == tag && (unsigned)(gl >> 32) == tag)) break;
      if (__builtin_amdgcn_s_memrealtime() - t0 > SPIN_TICKS) break;
    }
    const float ms = __uint_as_float((unsigned)gm), ls = __uint_as_float((unsigned)gl);
    float M = fmaxf(ms, dpp<XOR1>(ms)); M = fmaxf(M, dpp<XOR2>(M));
    float L = ls * __builtin_amdgcn_exp2f((ms - M) * LOG2E);
    L += dpp<XOR1>(L); L += dpp<XOR2>(L);
    if (lane < 4) lds[L_FW + lane] = __builtin_amdgcn_exp2f((mw - M) * LOG2E) / L;
    lds_wait();
    STAMP(3);
  }
  __builtin_amdgcn_s_barrier();
  asm volatile("" ::: "memory");
  STAMP(6);
  {
    const LAS float* P = (const LAS float*)lds + L_RED + (4 * r) * 128 + wave * 16 + c;
    float o = ((P[0] + P[128]) + (P[256] + P[384])) * lds[L_FW + r];
    o = xrow_sum(o);
#pragma unroll
    for (int i = 0; i < 16; ++i) {
      const float oi = __builtin_bit_cast(float, __builtin_amdgcn_readlane(__builtin_bit_cast(int, o), i));
#pragma unroll
      for (int k = 0; k < 4; ++k) yacc[k] = __builtin_elementwise_fma(wf[i * 4 + k], f32x2{oi, oi}, yacc[k]);
    }
  }
  *(LAS f32x4*)(lds + L_Y + wave * 512 + lane * 8) = f32x4{yacc[0][0], yacc[0][1], yacc[1][0], yacc[1][1]};
  *(LAS f32x4*)(lds + L_Y + wave * 512 + lane * 8 + 4) = f32x4{yacc[2][0], yacc[2][1], yacc[3][0], yacc[3][1]};
  lds_wait();
  __builtin_amdgcn_s_barrier();
  asm volatile("" ::: "memory");
  gu64* Y = (gu64*)(a.ws + WS_Y);
  {
    const LAS float* P = (const LAS float*)lds + L_Y + tid;
    const float y = ((P[0] + P[512]) + (P[1024] + P[1536])) + ((P[2048] + P[2560]) + (P[3072] + P[3584]));
    st_granule(Y + (size_t)(tid >> 4) * 4096 + chunk * 16 + (tid & 15), tag, y);
  }
  STAMP(7);
  if (red >= 0) {
    const rsrc_t ry = __builtin_amdgcn_make_buffer_rsrc((void*)(a.ws + WS_Y + (size_t)red * 32768), 0, 32768, 0x00020000);
    const unsigned long long t0 = __builtin_amdgcn_s_memrealtime();
    u32x4 g[4];
    bool ok = false;
    while (true) {
      if (!ok) {
#pragma unroll
        for (int k = 0; k < 4; ++k) g[k] = __builtin_amdgcn_raw_buffer_load_b128(ry, tid * 64 + k * 16, 0, 16);
        ok = true;
#pragma unroll
        for (int k = 0; k < 4; ++k) ok = ok && g[k][1] == tag && g[k][3] == tag;
      }
      if (__all(ok)) break;
      if (__builtin_amdgcn_s_memrealtime() - t0 > SPIN_TICKS) break;
      __builtin_amdgcn_s_sleep(FSLEEP);
      asm volatile("" ::: "memory");
    }
    float t[4];
#pragma unroll
    for (int k = 0; k < 2; ++k) {
      t[2 * k] = tr32(__uint_as_float(g[k][0]), __uint_as_float(g[2 + k][0]));
      t[2 * k + 1] = tr32(__uint_as_float(g[k][2]), __uint_as_float(g[2 + k][2]));
    }
    const float u0 = tr16(t[0], t[2]), u1 = tr16(t[1], t[3]);
    const float lo = u0 + dpp<ROR8>(u0), hi = u1 + dpp<ROR8>(u1);
    float s = (c & 8) ? hi : lo;
    s += dpp<XOR2>(s); s += dpp<XOR7>(dpp<XOR1>(s));
    if ((c & 6) == 0) lds[L_FIN + wave * 16 + (c & 1) * 8 + 2 * r + (c >> 3)] = s;
    lds_wait();
  }
  __builtin_amdgcn_s_barrier();
  asm volatile("" ::: "memory");
  if (red >= 0 && tid < 16) {
    const LAS float* P = (const LAS float*)lds + L_FIN + (tid >> 3) * 64 + (tid & 7) * 2;
    const float o0 = bf_lo(xw) + ((P[0] + P[16]) + (P[32] + P[48])), o1 = bf_hi(xw) + ((P[1] + P[17]) + (P[33] + P[49]));
    *(gu32*)(a.out + (tid >> 3) * DM + red * 16 + (tid & 7) * 2) = pack_bf16(o0, o1);
    if (red == 0 && tid == 0) __hip_atomic_store((gu32*)(a.ws + WS_EPOCH), tag, RLX_AGENT);
  }
  STAMP(8);
  STAMP_FLUSH();
}

}

extern "C" void kernel_launch(void* const* d_in, const int* in_sizes, int n_in, void* d_out, int out_size, void* d_ws, size_t ws_size,
                              hipStream_t stream) {
  Args a;
  a.x = (const unsigned short*)d_in[0]; a.k = (const unsigned short*)d_in[1]; a.v = (const unsigned short*)d_in[2];
  a.wq = (const unsigned short*)d_in[3]; a.wo = (const unsigned short*)d_in[4];
  a.w1 = (const unsigned short*)d_in[6]; a.w3 = (const unsigned short*)d_in[7]; a.w2 = (const unsigned short*)d_in[8];
  a.out = (unsigned short*)d_out; a.ws = (unsigned char*)d_ws;
  fused_decode<<<NWG, NTHR, 0, stream>>>(a);
}
```

```cpp
#define STAMPS 1
#define DIAG 1
#define DIAG_I 10
#define DIAG_W0 0
#define DIAG_W1 8
#define DIAG_S 10
#include <hip/hip_runtime.h>
#include <hip/hip_bf16.h>
#include <stdint.h>

#define KDIST 5
#define KSTAG 1
#define MIMIC 1

namespace {

constexpr int NWG = 256, NTHR = 768;
constexpr int DM = 512, NH = 32, SK = 16384, DH = 128, FF = 1792;
constexpr float LOG2E = 1.4426950408889634f;
constexpr float SCALE = 0.08838834764831845f;
constexpr size_t WS_EPOCH = 0, WS_ML = 4096;
constexpr size_t WS_Y = 65536;
constexpr size_t WS_Q = WS_Y + (1u << 20);
constexpr size_t WS_G = WS_Q + (512u << 10);
constexpr size_t WS_STAMP = 8u << 20;
constexpr int L_S = 0;
constexpr int L_RED = L_S + 4096;
constexpr int L_Y = L_RED + 2048;
constexpr int L_Y2 = L_Y + 4096;
constexpr int L_ML = L_Y2 + 4096;
constexpr int L_FW = L_ML + 8;
constexpr int L_FIN = L_FW + 4;
constexpr int L_CNT = L_FIN + 128;
constexpr int L_END = L_CNT + 4;

#define GAS __attribute__((address_space(1)))
#define LAS __attribute__((address_space(3)))
#define RLX_AGENT __ATOMIC_RELAXED, __HIP_MEMORY_SCOPE_AGENT
typedef unsigned u32x4 __attribute__((ext_vector_type(4)));
typedef unsigned u32x2 __attribute__((ext_vector_type(2)));
typedef float f32x2 __attribute__((ext_vector_type(2)));
typedef float f32x4 __attribute__((ext_vector_type(4)));
typedef __bf16 bf16x2 __attribute__((ext_vector_type(2)));
typedef GAS unsigned gu32;
typedef GAS unsigned long long gu64;

__device__ __forceinline__ float bf_lo(unsigned u) { return __builtin_bit_cast(float, u << 16); }
__device__ __forceinline__ float bf_hi(unsigned u) { return __builtin_bit_cast(float, u & 0xffff0000u); }
__device__ __forceinline__ f32x2 bf_unpack(unsigned u) { return f32x2{bf_lo(u), bf_hi(u)}; }
__device__ __forceinline__ unsigned pack_bf16(float lo, float hi) {
  bf16x2 r = {(__bf16)lo, (__bf16)hi};
  return __builtin_bit_cast(unsigned, r);
}
__device__ __forceinline__ float rbf(float x) {
  return (float)(__bf16)x;
}
__device__ __forceinline__ float dot2(unsigned a, unsigned b, float acc) {
  return __builtin_amdgcn_fdot2_f32_bf16(__builtin_bit_cast(bf16x2, a), __builtin_bit_cast(bf16x2, b), acc, false);
}
template <int CTRL> __device__ __forceinline__ float dpp(float x) {
  return __builtin_bit_cast(float, __builtin_amdgcn_mov_dpp(__builtin_bit_cast(int, x), CTRL, 0xf, 0xf, true));
}
constexpr int XOR1 = 0xB1, XOR2 = 0x4E, XOR7 = 0x141, ROR2 = 0x122, ROR4 = 0x124, ROR8 = 0x128;
__device__ __forceinline__ float xrow_sum(float x) {
  auto s = __builtin_amdgcn_permlane16_swap(__float_as_uint(x), __float_as_uint(x), false, false);
  x = __uint_as_float(s[0]) + __uint_as_float(s[1]);
  auto t = __builtin_amdgcn_permlane32_swap(__float_as_uint(x), __float_as_uint(x), false, false);
  return __uint_as_float(t[0]) + __uint_as_float(t[1]);
}
__device__ __forceinline__ float xrow_max(float x) {
  auto s = __builtin_amdgcn_permlane16_swap(__float_as_uint(x), __float_as_uint(x), false, false);
  x = fmaxf(__uint_as_float(s[0]), __uint_as_float(s[1]));
  auto t = __builtin_amdgcn_permlane32_swap(__float_as_uint(x), __float_as_uint(x), false, false);
  return fmaxf(__uint_as_float(t[0]), __uint_as_float(t[1]));
}
__device__ __forceinline__ float tr16(float a, float b) {
  auto s = __builtin_amdgcn_permlane16_swap(__float_as_uint(a), __float_as_uint(b), false, false);
  return __uint_as_float(s[0]) + __uint_as_float(s[1]);
}
__device__ __forceinline__ float tr32(float a, float b) {
  auto s = __builtin_amdgcn_permlane32_swap(__float_as_uint(a), __float_as_uint(b), false, false);
  return __uint_as_float(s[0]) + __uint_as_float(s[1]);
}
__device__ __forceinline__ float wave_sum(float x) {
  x += dpp<XOR1>(x); x += dpp<XOR2>(x); x += dpp<XOR7>(x); x += dpp<ROR8>(x);
  return xrow_sum(x);
}
__device__ __forceinline__ float wave_max(float x) {
  x = fmaxf(x, dpp<XOR1>(x)); x = fmaxf(x, dpp<XOR2>(x)); x = fmaxf(x, dpp<XOR7>(x)); x = fmaxf(x, dpp<ROR8>(x));
  return xrow_max(x);
}
__device__ __forceinline__ void lds_wait() { asm volatile("s_waitcnt lgkmcnt(0)" ::: "memory"); }
__device__ __forceinline__ void st_granule(gu64* g, unsigned tag, float v) {
  __hip_atomic_store(g, ((unsigned long long)tag << 32) | (unsigned long long)__float_as_uint(v), RLX_AGENT);
}
constexpr unsigned long long SPIN_TICKS = 20000000ull;

struct Args {
  const unsigned short* x; const unsigned short* k; const unsigned short* v; const unsigned short* wq; const unsigned short* wo;
  const unsigned short* w1; const unsigned short* w3; const unsigned short* w2;
  unsigned short* out; unsigned char* ws;
};

#define STAMP(i) do { __builtin_amdgcn_sched_barrier(0); unsigned long long t_; asm volatile("s_memrealtime %0\n\ts_waitcnt lgkmcnt(0)" : "=s"(t_) :: "memory"); ts[i] = (unsigned)t_ | 1u; __builtin_amdgcn_sched_barrier(0); } while (0)
#define STAMP_FLUSH() do { const unsigned tg_ = dg_epoch + 0x9E3779B9u; if (lane == 0) { _Pragma("unroll") for (int i_ = 0; i_ < 16; ++i_) st_granule((gu64*)(a.ws + WS_STAMP) + (blockIdx.x * 16 + wave) * 16 + i_, tg_, __uint_as_float(ts[i_])); } \
  if (blockIdx.x == 0 && wave == 0) { \
    int mn0 = 1 << 30, mx = -(1 << 30), mn = 1 << 30; float sum = 0.f; \
    for (int i = 0; i < 4; ++i) for (int w = 0; w < 12; ++w) { \
      gu64* p = (gu64*)(a.ws + WS_STAMP) + ((lane * 4 + i) * 16 + w) * 16; unsigned long long g0, g1; \
      do { g0 = __hip_atomic_load(p, RLX_AGENT); g1 = __hip_atomic_load(p + DIAG_I, RLX_AGENT); } while (((unsigned)(g0 >> 32) != tg_ || (unsigned)(g1 >> 32) != tg_) && (unsigned)__builtin_amdgcn_s_memrealtime() - ts[0] < 1000000u); \
      mn0 = min(mn0, (int)((unsigned)g0 - ts[0])); \
      if (w >= DIAG_W0 && w < DIAG_W1) { const int d = (int)((unsigned)g1 - ts[0]); mx = max(mx, d); mn = min(mn, d); sum += (float)d; } \
    } \
    mn0 = -(int)wave_max((float)-mn0); mx = (int)wave_max((float)mx); mn = -(int)wave_max((float)-mn); sum = wave_sum(sum) / (256.f * (DIAG_W1 - DIAG_W0)); \
    const int st = DIAG == 0 ? 0 : DIAG == 1 ? (int)sum - mn0 : DIAG == 2 ? mx - mn0 : mn - mn0; \
    const int until = __builtin_amdgcn_readfirstlane(mn0 + 30000 + DIAG_S * st); \
    while ((int)((unsigned)__builtin_amdgcn_s_memrealtime() - ts[0]) < until) __builtin_amdgcn_s_sleep(1); \
  } } while (0)
#define STAMP_DECL unsigned ts[16] = {0, 0, 0, 0, 0, 0, 0, 0, 0, 0, 0, 0, 0, 0, 0, 0}

typedef __amdgpu_buffer_rsrc_t rsrc_t;
#define WPOL 0
#define NPRE 3
__device__ __forceinline__ u32x4 ldw(rsrc_t r, int voff, int soff) { return __builtin_amdgcn_raw_buffer_load_b128(r, voff, soff, WPOL); }
__device__ __forceinline__ u32x4 ldnt(rsrc_t r, int voff, int soff) { return __builtin_amdgcn_raw_buffer_load_b128(r, voff, soff, 2); }
#define TRK 0
#define WOD 2
#define QDELAY 32
#define DRY 1
#define FSLEEP 4
#define PDIST KDIST
#define TRICKLE() __builtin_amdgcn_s_sleep(TRK)
#define HSYNC(n) do { \
    if (lane == 0) __hip_atomic_fetch_add((LAS unsigned*)lds + L_CNT + 1 + (n), 1u, __ATOMIC_RELAXED, __HIP_MEMORY_SCOPE_WORKGROUP); \
    while (__hip_atomic_load((LAS unsigned*)lds + L_CNT + 1 + (n), __ATOMIC_RELAXED, __HIP_MEMORY_SCOPE_WORKGROUP) < 8u) __builtin_amdgcn_s_sleep(0); \
    asm volatile("" ::: "memory"); \
  } while (0)
#define FRESH() int lane_ = lane0; asm volatile("" : "+v"(lane_)); \
  const int lane = lane_, r = lane >> 4, c = lane & 15, tid = wave * 64 + lane, voff = lane * 16; (void)r; (void)c; (void)tid; (void)voff
#define SB() __builtin_amdgcn_sched_barrier(0)
#define VM_DRAIN() do { SB(); asm volatile("s_waitcnt vmcnt(0)" ::: "memory"); SB(); } while (0)

__global__ void __launch_bounds__(NTHR, 3) fused_decode(Args a) {
  __shared__ __attribute__((aligned(16))) float lds[L_END];
  const int tid = threadIdx.x, lane = tid & 63, wave = __builtin_amdgcn_readfirstlane(tid >> 6);
  const int r = lane >> 4, c = lane & 15;
  const int xg = blockIdx.x & 7, j = blockIdx.x >> 3;
  const int b = j >> 4, h = xg * 4 + ((j >> 2) & 3), sp = j & 3;
  const int chunk = b * 128 + h * 4 + sp;
  const int voff = lane * 16;
  STAMP_DECL;
  STAMP(0);
  unsigned dg_epoch = *(const __attribute__((address_space(4))) unsigned*)(a.ws + WS_EPOCH);

  if (wave >= 8) {
    const int sw = wave - 8;
    const unsigned short* kp = a.k + (size_t)chunk * 4096 * DH;
    const unsigned short* vp = a.v + (size_t)chunk * 4096 * DH;
    const rsrc_t rk = __builtin_amdgcn_make_buffer_rsrc((void*)kp, 0, 4096 * DH * 2, 0x00020000);
    const rsrc_t rv = __builtin_amdgcn_make_buffer_rsrc((void*)vp, 0, 4096 * DH * 2, 0x00020000);
    const int stag = j * KSTAG;
    auto tile_off = [&](int t) -> int { return (((t + stag) & 63) << 14) + (sw << 12); };
    __builtin_amdgcn_s_setprio(2);
    u32x4 buf[8], pre[NPRE * 8];
    __builtin_amdgcn_s_barrier();
    SB();
#pragma unroll
    for (int u = 0; u < NPRE * 8; ++u) {
      pre[u] = ldnt(rk, voff + (u & 3) * 1024, tile_off(u >> 2)); SB();
      if (u >= PDIST) { asm volatile("s_waitcnt vmcnt(%0)" :: "i"(PDIST)); SB(); }
    }
#pragma unroll
    for (int u = 0; u < 8; ++u) {
      buf[u] = ldnt(rk, voff + (u & 3) * 1024, tile_off(2 * NPRE + (u >> 2))); SB();
      if (u < 7) { asm volatile("s_waitcnt vmcnt(%0)" :: "i"(PDIST)); SB(); }
    }
    STAMP(1);
    __builtin_amdgcn_s_barrier();
    asm volatile("" ::: "memory");
    asm volatile("" : "+s"(dg_epoch));
    unsigned qp[4];
    {
      f32x4 s0 = *(const LAS f32x4*)(lds + L_RED + (2 * r) * 128 + c * 8) + *(const LAS f32x4*)(lds + L_RED + (2 * r + 1) * 128 + c * 8);
      f32x4 s1 = *(const LAS f32x4*)(lds + L_RED + (2 * r) * 128 + c * 8 + 4) + *(const LAS f32x4*)(lds + L_RED + (2 * r + 1) * 128 + c * 8 + 4);
#pragma unroll
      for (int k = 0; k < 4; ++k) { s0[k] = xrow_sum(s0[k]); s1[k] = xrow_sum(s1[k]); }
      qp[0] = pack_bf16(s0[0], s0[1]); qp[1] = pack_bf16(s0[2], s0[3]); qp[2] = pack_bf16(s1[0], s1[1]); qp[3] = pack_bf16(s1[2], s1[3]);
    }
    STAMP(2);
    LAS float* S = (LAS float*)lds + L_S + sw * 1024;
    float mx = -INFINITY;
#define DOT(w) dot2((w)[3], qp[3], dot2((w)[2], qp[2], dot2((w)[1], qp[1], dot2((w)[0], qp[0], 0.f))))
#define K_FIN(tt) do { \
      _Pragma("unroll") for (int u = 0; u < 4; ++u) { const float lo = x[u] + dpp<ROR8>(x[u]), hi = x[u + 4] + dpp<ROR8>(x[u + 4]); x[u] = (c & 8) ? hi : lo; } \
      _Pragma("unroll") for (int u = 0; u < 2; ++u) { const float lo = x[u] + dpp<XOR7>(x[u]), hi = x[u + 2] + dpp<XOR7>(x[u + 2]); x[u] = (c & 4) ? hi : lo; } \
      { const float lo = x[0] + dpp<XOR2>(x[0]), hi = x[1] + dpp<XOR2>(x[1]); x[0] = (c & 2) ? hi : lo; } \
      float sc = x[0] + dpp<XOR1>(x[0]); \
      sc = rbf(rbf(sc) * SCALE); \
      mx = fmaxf(mx, sc); \
      S[(tt) * 32 + r * 8 + (c >> 1)] = sc; \
      SB(); \
    } while (0)
    static_assert(KDIST < 8 && KDIST >= 4 && NPRE >= 1 && PDIST <= NPRE * 8, "ring of 8");
    {
      float x[8];
#pragma unroll
      for (int u = 0; u < 8; ++u) {
        x[u] = DOT(buf[u]);
        SB();
        if (u < KDIST) { buf[u] = ldnt(rk, voff + (u & 3) * 1024, tile_off(2 * NPRE + 2 + (u >> 2))); SB(); }
      }
      K_FIN(NPRE);
    }
#pragma unroll
    for (int pp = 0; pp < NPRE; ++pp) {
      float x[8];
#pragma unroll
      for (int w = 0; w < 8; ++w) x[w] = DOT(pre[pp * 8 + w]);
      K_FIN(pp);
    }
    int last = 31;
    asm volatile("" : "+s"(last));
#pragma nounroll
    for (int tt = NPRE + 1; tt <= last; ++tt) {
      const unsigned short* nb = tt == last ? vp : kp;
      const rsrc_t rn = __builtin_amdgcn_make_buffer_rsrc((void*)nb, 0, 4096 * DH * 2, 0x00020000);
      float x[8];
#pragma unroll
      for (int u = 0; u < 8; ++u) {
        const int n = u + KDIST;
        buf[n & 7] = ldnt(n >= 8 ? rn : rk, voff + (n & 3) * 1024, tile_off(2 * tt + (n >> 2)));
        SB();
        x[u] = DOT(buf[u]);
        SB();
      }
      K_FIN(tt);
    }
    STAMP(3);
    mx = wave_max(mx);
    if (lane == 0) lds[L_ML + sw] = mx;
    lds_wait();
    if (lane == 0) __hip_atomic_fetch_add((LAS unsigned*)lds + L_CNT, 1u, __ATOMIC_RELAXED, __HIP_MEMORY_SCOPE_WORKGROUP);
    const float msub = mx * LOG2E;
    STAMP(4);
    f32x2 acc[4] = {{0.f, 0.f}, {0.f, 0.f}, {0.f, 0.f}, {0.f, 0.f}};
#pragma nounroll
    for (int tt = 0; tt <= last; ++tt) {
      const rsrc_t rn = __builtin_amdgcn_make_buffer_rsrc((void*)vp, 0, tt == last ? 0 : 4096 * DH * 2, 0x00020000);
      const f32x4 s0 = *(const LAS f32x4*)(S + tt * 32 + r * 8), s1 = *(const LAS f32x4*)(S + tt * 32 + r * 8 + 4);
#pragma unroll
      for (int u = 0; u < 8; ++u) {
        const int n = u + KDIST;
        buf[n & 7] = ldnt(n >= 8 ? rn : rv, voff + (n & 3) * 1024, tile_off(2 * tt + (n >> 2)));
        SB();
        const float p = __builtin_amdgcn_exp2f(fmaf(u < 4 ? s0[u & 3] : s1[u & 3], LOG2E, -msub));
#pragma unroll
        for (int k = 0; k < 4; ++k) acc[k] = __builtin_elementwise_fma(bf_unpack(buf[u][k]), f32x2{p, p}, acc[k]);
        SB();
      }
    }
    STAMP(5);
    *(LAS f32x4*)(lds + L_RED + (sw * 4 + r) * 128 + c * 8) = f32x4{acc[0][0], acc[0][1], acc[1][0], acc[1][1]};
    *(LAS f32x4*)(lds + L_RED + (sw * 4 + r) * 128 + c * 8 + 4) = f32x4{acc[2][0], acc[2][1], acc[3][0], acc[3][1]};
    lds_wait();
    __builtin_amdgcn_s_barrier();
    __builtin_amdgcn_s_barrier();
    __builtin_amdgcn_s_barrier();
    STAMP_FLUSH();
    return;
  }

  const int g8 = b * 4 + sp;
  const u32x4 xv0 = *(const GAS u32x4*)(a.x + lane * 8), xv1 = *(const GAS u32x4*)(a.x + DM + lane * 8);
  const int qrow = g8 * 64 + wave * 8 + 2 * r, mrow = g8 * 64 + wave * 8 + (lane >> 3);
  const unsigned xq0 = *(const gu32*)(a.x + qrow), xq1 = *(const gu32*)(a.x + DM + qrow);
  u32x4 wq0, wq1;
  {
    const rsrc_t rq = __builtin_amdgcn_make_buffer_rsrc((void*)(a.wq + h * DH), 0, (DM * NH * DH - h * DH) * 2, 0x00020000);
    wq0 = ldnt(rq, qrow * (NH * DH * 2) + c * 16, 0);
    wq1 = ldnt(rq, qrow * (NH * DH * 2) + c * 16, NH * DH * 2);
  }
  unsigned epoch = *(const __attribute__((address_space(4))) unsigned*)(a.ws + WS_EPOCH);
  SB();
  if (tid < 4) ((LAS unsigned*)lds)[L_CNT + tid] = 0u;
  lds_wait();
  __builtin_amdgcn_s_barrier();
  SB();
  float mb0, mb1, sd0, sd1;
  {
    float f0[8], f1[8];
#pragma unroll
    for (int k = 0; k < 4; ++k) { f0[2 * k] = bf_lo(xv0[k]); f0[2 * k + 1] = bf_hi(xv0[k]); f1[2 * k] = bf_lo(xv1[k]); f1[2 * k + 1] = bf_hi(xv1[k]); }
    float s0 = 0.f, s1 = 0.f;
#pragma unroll
    for (int k = 0; k < 8; ++k) { s0 += f0[k]; s1 += f1[k]; }
    const float m0 = wave_sum(s0) * (1.f / DM), m1 = wave_sum(s1) * (1.f / DM);
    float v0 = 0.f, v1 = 0.f;
#pragma unroll
    for (int k = 0; k < 8; ++k) { const float d0 = f0[k] - m0, d1 = f1[k] - m1; v0 = fmaf(d0, d0, v0); v1 = fmaf(d1, d1, v1); }
    v0 = wave_sum(v0) * (1.f / DM); v1 = wave_sum(v1) * (1.f / DM);
    mb0 = rbf(m0); sd0 = rbf(__builtin_sqrtf(rbf(rbf(v0) + 1e-5f)));
    mb1 = rbf(m1); sd1 = rbf(__builtin_sqrtf(rbf(rbf(v1) + 1e-5f)));
  }
#define XN(xf, mb, sd) rbf(rbf((xf) - (mb)) / (sd))
  asm volatile("" : "+s"(epoch));
  asm volatile("" : "+s"(dg_epoch));
  const unsigned tag = epoch + 0x9E3779B9u;
  STAMP(10);
  {
    const float a0 = XN(bf_lo(xq0), mb0, sd0), a1 = XN(bf_hi(xq0), mb0, sd0), b0 = XN(bf_lo(xq1), mb1, sd1), b1 = XN(bf_hi(xq1), mb1, sd1);
    float t[8];
#pragma unroll
    for (int k = 0; k < 4; ++k) {
      const f32x2 w0 = bf_unpack(wq0[k]), w1 = bf_unpack(wq1[k]);
      const f32x2 qa = __builtin_elementwise_fma(w1, f32x2{a1, a1}, w0 * a0), qb = __builtin_elementwise_fma(w1, f32x2{b1, b1}, w0 * b0);
      t[2 * k] = tr32(qa[0], qb[0]); t[2 * k + 1] = tr32(qa[1], qb[1]);
    }
    f32x4 o;
#pragma unroll
    for (int k = 0; k < 4; ++k) o[k] = tr16(t[k], t[4 + k]);
    *(LAS f32x4*)(lds + L_Y + wave * 256 + (r >> 1) * 128 + c * 8 + (r & 1) * 4) = o;
    lds_wait();
  }
  STAMP(11);
  HSYNC(0);
  STAMP(12);
  gu64* Q = (gu64*)(a.ws + WS_Q) + h * 2048;
  if (tid < 256) {
    const LAS float* P = (const LAS float*)lds + L_Y + tid;
    st_granule(Q + g8 * 256 + tid, tag, ((P[0] + P[256]) + (P[512] + P[768])) + ((P[1024] + P[1280]) + (P[1536] + P[1792])));
  }
  STAMP(9);
  __builtin_amdgcn_s_sleep(QDELAY);
  {
    const unsigned long long t0 = __builtin_amdgcn_s_memrealtime();
    unsigned long long g0, g1;
    while (true) {
      g0 = __hip_atomic_load(Q + wave * 256 + b * 128 + lane, RLX_AGENT);
      g1 = __hip_atomic_load(Q + wave * 256 + b * 128 + 64 + lane, RLX_AGENT);
      asm volatile("" :: "v"(g0), "v"(g1));
      if (ts[14] == 0) STAMP(14); else if (ts[15] == 0) STAMP(15);
      if (__all((unsigned)(g0 >> 32) == tag && (unsigned)(g1 >> 32) == tag)) break;
      if (__builtin_amdgcn_s_memrealtime() - t0 > SPIN_TICKS) break;
      __builtin_amdgcn_s_sleep(2);
    }
    lds[L_RED + wave * 128 + lane] = __uint_as_float((unsigned)g0);
    lds[L_RED + wave * 128 + 64 + lane] = __uint_as_float((unsigned)g1);
    lds_wait();
  }
  STAMP(1);
  __builtin_amdgcn_s_barrier();
  asm volatile("" ::: "memory");
  gu64* G = (gu64*)(a.ws + WS_G);
  const int red = blockIdx.x < 32 ? (int)blockIdx.x : -1;
  const unsigned xw = (red >= 0 && tid < 16) ? *(const gu32*)(a.x + (tid >> 3) * DM + red * 16 + (tid & 7) * 2) : 0u;
  if (h < 28) {
    const unsigned short xm0 = a.x[mrow], xm1 = a.x[DM + mrow];
    const rsrc_t r1 = __builtin_amdgcn_make_buffer_rsrc((void*)(a.w1 + h * 64), 0, (DM * FF - h * 64) * 2, 0x00020000);
    const rsrc_t r3 = __builtin_amdgcn_make_buffer_rsrc((void*)(a.w3 + h * 64), 0, (DM * FF - h * 64) * 2, 0x00020000);
    const u32x4 wg = ldnt(r1, mrow * (FF * 2) + (lane & 7) * 16, 0);
    const u32x4 wu = ldnt(r3, mrow * (FF * 2) + (lane & 7) * 16, 0);
    const float a0 = XN(bf_lo(xm0), mb0, sd0), b0 = XN(bf_lo(xm1), mb1, sd1);
    float t[8];
#pragma unroll
    for (int k = 0; k < 4; ++k) {
      const f32x2 g = bf_unpack(wg[k]), u = bf_unpack(wu[k]);
#pragma unroll
      for (int e = 0; e < 2; ++e) t[2 * k + e] = tr16(tr32(g[e] * a0, g[e] * b0), tr32(u[e] * a0, u[e] * b0));
    }
    f32x4 o;
#pragma unroll
    for (int k = 0; k < 4; ++k) { const float lo = t[k] + dpp<ROR8>(t[k]), hi = t[4 + k] + dpp<ROR8>(t[4 + k]); o[k] = (c & 8) ? hi : lo; }
    *(LAS f32x4*)(lds + L_Y + 2048 + wave * 256 + r * 64 + (c & 7) * 8 + (c >> 3) * 4) = o;
    lds_wait();
    HSYNC(1);
    if (tid < 256) {
      const LAS float* P = (const LAS float*)lds + L_Y + 2048 + tid;
      st_granule(G + (h * 8 + g8) * 256 + tid, tag, ((P[0] + P[256]) + (P[512] + P[768])) + ((P[1024] + P[1280]) + (P[1536] + P[1792])));
    }
  }
  const int lane0 = lane;
  int pass = DRY ? 0 : 1;
  asm volatile("" : "+s"(pass));
#pragma nounroll
  for (; pass < 2; ++pass) {
    f32x2 wf[64];
    f32x2 yacc[4] = {{0.f, 0.f}, {0.f, 0.f}, {0.f, 0.f}, {0.f, 0.f}};
    if (!pass) {
#pragma unroll
      for (int i = 0; i < 64; ++i) asm volatile("" : "=v"(wf[i]));
    } else {
      {
        FRESH();
        const rsrc_t ro = __builtin_amdgcn_make_buffer_rsrc((void*)(a.wo + (size_t)h * DH * DM), 0, DH * DM * 2, 0x00020000);
#pragma unroll
        for (int i0 = 0; i0 < 16; i0 += WOD) {
          u32x4 w[WOD];
#pragma unroll
          for (int i = 0; i < WOD; ++i) w[i] = ldw(ro, voff + ((i0 + i) & 3) * 1024, (wave * 16 + ((i0 + i) & ~3)) * 1024);
          VM_DRAIN();
#pragma unroll
          for (int i = 0; i < WOD; ++i)
#pragma unroll
            for (int k = 0; k < 4; ++k) { wf[(i0 + i) * 4 + k] = bf_unpack(w[i][k]); asm volatile("" : "+v"(wf[(i0 + i) * 4 + k])); }
          TRICKLE();
        }
      }
      if (wave < 7) {
        FRESH();
        const int f0 = (h * 4 + sp) * 14 + wave * 2;
        const rsrc_t r2 = __builtin_amdgcn_make_buffer_rsrc((void*)(a.w2 + (size_t)f0 * DM), 0, 2 * DM * 2, 0x00020000);
        const u32x4 w20 = ldw(r2, voff, 0), w21 = ldw(r2, voff + 1024, 0);
        const int f = f0 + (r & 1);
        gu64* gp = G + ((f >> 6) * 8 + (c & 7)) * 256 + b * 128 + (c >> 3) * 64 + (f & 63);
        const unsigned long long t0 = __builtin_amdgcn_s_memrealtime();
        unsigned long long g;
        while (true) {
          g = __hip_atomic_load(gp, RLX_AGENT);
          if (__all((unsigned)(g >> 32) == tag)) break;
          if (__builtin_amdgcn_s_memrealtime() - t0 > SPIN_TICKS) break;
          __builtin_amdgcn_s_sleep(16);
        }
        float s = __uint_as_float((unsigned)g);
        s += dpp<XOR1>(s); s += dpp<XOR2>(s); s += dpp<XOR7>(s);
        const float gs = rbf(s), us = rbf(dpp<ROR8>(s));
        const float si = rbf(gs / (1.f + __expf(-gs)));
        const float hv = rbf(si * us);
        const float h0 = __builtin_bit_cast(float, __builtin_amdgcn_readlane(__builtin_bit_cast(int, hv), 0));
        const float h1 = __builtin_bit_cast(float, __builtin_amdgcn_readlane(__builtin_bit_cast(int, hv), 16));
#pragma unroll
        for (int k = 0; k < 4; ++k) yacc[k] = __builtin_elementwise_fma(bf_unpack(w21[k]), f32x2{h1, h1}, bf_unpack(w20[k]) * h0);
      }
      STAMP(2);
      if (wave == 0) {
        FRESH();
        while (__hip_atomic_load((LAS unsigned*)lds + L_CNT, __ATOMIC_RELAXED, __HIP_MEMORY_SCOPE_WORKGROUP) < 4u) __builtin_amdgcn_s_sleep(16);
        asm volatile("" ::: "memory");
        const float mw = lds[L_ML + (lane & 3)];
        float m = fmaxf(mw, dpp<XOR1>(mw)); m = fmaxf(m, dpp<XOR2>(m));
        float l = 0.f;
#pragma unroll 4
        for (int i = 0; i < 16; ++i) {
          const f32x4 sv = *(const LAS f32x4*)(lds + L_S + i * 256 + lane * 4);
#pragma unroll
          for (int k = 0; k < 4; ++k) l += __builtin_amdgcn_exp2f((sv[k] - m) * LOG2E);
        }
        l = wave_sum(l);
        gu64* ml = (gu64*)(a.ws + WS_ML);
        if (lane < 2) st_granule(ml + chunk * 2 + lane, tag, lane == 0 ? m : l);
        const unsigned long long t0 = __builtin_amdgcn_s_memrealtime();
        unsigned long long gm, gl;
        while (true) {
          __builtin_amdgcn_s_sleep(127); __builtin_amdgcn_s_sleep(127);
          gm = __hip_atomic_load(ml + ((chunk & ~3) + (lane & 3)) * 2, RLX_AGENT);
          gl = __hip_atomic_load(ml + ((chunk & ~3) + (lane & 3)) * 2 + 1, RLX_AGENT);
          if (__all((unsigned)(gm >> 32) == tag && (unsigned)(gl >> 32) == tag)) break;
          if (__builtin_amdgcn_s_memrealtime() - t0 > SPIN_TICKS) break;
        }
        const float ms = __uint_as_float((unsigned)gm), ls = __uint_as_float((unsigned)gl);
        float M = fmaxf(ms, dpp<XOR1>(ms)); M = fmaxf(M, dpp<XOR2>(M));
        float L = ls * __builtin_amdgcn_exp2f((ms - M) * LOG2E);
        L += dpp<XOR1>(L); L += dpp<XOR2>(L);
        if (lane < 4) lds[L_FW + lane] = __builtin_amdgcn_exp2f((mw - M) * LOG2E) / L;
        lds_wait();
        STAMP(3);
      }
    }
    FRESH();
    if (pass) __builtin_amdgcn_s_barrier();
    asm volatile("" ::: "memory");
    STAMP(6);
    {
      const LAS float* P = (const LAS float*)lds + L_RED + (4 * r) * 128 + wave * 16 + c;
      float o = ((P[0] + P[128]) + (P[256] + P[384])) * lds[L_FW + r];
      o = xrow_sum(o);
#pragma unroll
      for (int i = 0; i < 16; ++i) {
        const float oi = __builtin_bit_cast(float, __builtin_amdgcn_readlane(__builtin_bit_cast(int, o), i));
#pragma unroll
        for (int k = 0; k < 4; ++k) yacc[k] = __builtin_elementwise_fma(wf[i * 4 + k], f32x2{oi, oi}, yacc[k]);
      }
    }
    *(LAS f32x4*)(lds + L_Y2 + wave * 512 + lane * 8) = f32x4{yacc[0][0], yacc[0][1], yacc[1][0], yacc[1][1]};
    *(LAS f32x4*)(lds + L_Y2 + wave * 512 + lane * 8 + 4) = f32x4{yacc[2][0], yacc[2][1], yacc[3][0], yacc[3][1]};
    lds_wait();
    if (pass) __builtin_amdgcn_s_barrier();
    asm volatile("" ::: "memory");
    const rsrc_t ry = __builtin_amdgcn_make_buffer_rsrc((void*)(a.ws + WS_Y), 0, pass ? 1 << 20 : 0, 0x00020000);
    {
      const LAS float* P = (const LAS float*)lds + L_Y2 + tid;
      const float y = ((P[0] + P[512]) + (P[1024] + P[1536])) + ((P[2048] + P[2560]) + (P[3072] + P[3584]));
      __builtin_amdgcn_raw_buffer_store_b64(u32x2{__float_as_uint(y), tag}, ry, (tid >> 4) * 32768 + (tid & 15) * 8, chunk * 128, 16);
    }
    STAMP(7);
    if (red >= 0) {
      const unsigned long long t0 = __builtin_amdgcn_s_memrealtime();
      u32x4 g[4] = {{0u, 0u, 0u, 0u}, {0u, 0u, 0u, 0u}, {0u, 0u, 0u, 0u}, {0u, 0u, 0u, 0u}};
      bool ok = false;
      while (true) {
        if (!ok) {
#pragma unroll
          for (int k = 0; k < 4; ++k) g[k] = __builtin_amdgcn_raw_buffer_load_b128(ry, tid * 64 + k * 16, red * 32768, 16);
          ok = true;
#pragma unroll
          for (int k = 0; k < 4; ++k) ok = ok && g[k][1] == tag && g[k][3] == tag;
        }
        if (__all(ok) || !pass) break;
        if (__builtin_amdgcn_s_memrealtime() - t0 > SPIN_TICKS) break;
        __builtin_amdgcn_s_sleep(FSLEEP);
        asm volatile("" ::: "memory");
      }
      float t[4];
#pragma unroll
      for (int k = 0; k < 2; ++k) {
        t[2 * k] = tr32(__uint_as_float(g[k][0]), __uint_as_float(g[2 + k][0]));
        t[2 * k + 1] = tr32(__uint_as_float(g[k][2]), __uint_as_float(g[2 + k][2]));
      }
      const float u0 = tr16(t[0], t[2]), u1 = tr16(t[1], t[3]);
      const float lo = u0 + dpp<ROR8>(u0), hi = u1 + dpp<ROR8>(u1);
      float s = (c & 8) ? hi : lo;
      s += dpp<XOR2>(s); s += dpp<XOR7>(dpp<XOR1>(s));
      if ((c & 6) == 0) lds[L_FIN + wave * 16 + (c & 1) * 8 + 2 * r + (c >> 3)] = s;
      lds_wait();
    }
    if (pass) __builtin_amdgcn_s_barrier();
    asm volatile("" ::: "memory");
    if (red >= 0 && tid < 16) {
      const LAS float* P = (const LAS float*)lds + L_FIN + (tid >> 3) * 64 + (tid & 7) * 2;
      const float o0 = bf_lo(xw) + ((P[0] + P[16]) + (P[32] + P[48])), o1 = bf_hi(xw) + ((P[1] + P[17]) + (P[33] + P[49]));
      const rsrc_t rout = __builtin_amdgcn_make_buffer_rsrc((void*)a.out, 0, pass ? 2 * DM * 2 : 0, 0x00020000);
      __builtin_amdgcn_raw_buffer_store_b32(pack_bf16(o0, o1), rout, (tid >> 3) * (DM * 2) + (tid & 7) * 4, red * 32, 0);
    }
  }
  if (red == 0 && tid == 0) __hip_atomic_store((gu32*)(a.ws + WS_EPOCH), tag, RLX_AGENT);
  STAMP(8);
  STAMP_FLUSH();
}

}

extern "C" void kernel_launch(void* const* d_in, const int* in_sizes, int n_in, void* d_out, int out_size, void* d_ws, size_t ws_size,
                              hipStream_t stream) {
  Args a;
  a.x = (const unsigned short*)d_in[0]; a.k = (const unsigned short*)d_in[1]; a.v = (const unsigned short*)d_in[2];
  a.wq = (const unsigned short*)d_in[3]; a.wo = (const unsigned short*)d_in[4];
  a.w1 = (const unsigned short*)d_in[6]; a.w3 = (const unsigned short*)d_in[7]; a.w2 = (const unsigned short*)d_in[8];
  a.out = (unsigned short*)d_out; a.ws = (unsigned char*)d_ws;
  fused_decode<<<NWG, NTHR, 0, stream>>>(a);
}
```

```cpp
#define CODEPF 1
#define KDIST 4
#include <hip/hip_runtime.h>
#include <hip/hip_bf16.h>
#include <stdint.h>

#define KSTAG 1
#define MIMIC 1
#define STAMPS 0

namespace {

constexpr int NWG = 256, NTHR = 768;
constexpr int DM = 512, NH = 32, SK = 16384, DH = 128, FF = 1792;
constexpr float LOG2E = 1.4426950408889634f;
constexpr float SCALE = 0.08838834764831845f;
#define MLS 16
constexpr size_t WS_EPOCH = 0, WS_ML = 4u << 20;
constexpr size_t WS_Y = 65536;
constexpr size_t WS_Q = WS_Y + (1u << 20);
constexpr size_t WS_G = WS_Q + (512u << 10);
constexpr size_t WS_STAMP = 8u << 20;
constexpr int L_S = 0;
constexpr int L_RED = L_S + 4096;
constexpr int L_Y = L_RED + 2048;
constexpr int L_Y2 = L_Y + 4096;
constexpr int L_Q = L_Y2 + 4096;
constexpr int L_ML = L_Q + 64;
constexpr int L_FW = L_ML + 8;
constexpr int L_FIN = L_FW + 4;
constexpr int L_CNT = L_FIN + 128;
constexpr int L_END = L_CNT + 4;

#define GAS __attribute__((address_space(1)))
#define LAS __attribute__((address_space(3)))
#define RLX_AGENT __ATOMIC_RELAXED, __HIP_MEMORY_SCOPE_AGENT
typedef unsigned u32x4 __attribute__((ext_vector_type(4)));
typedef unsigned u32x2 __attribute__((ext_vector_type(2)));
typedef float f32x2 __attribute__((ext_vector_type(2)));
typedef float f32x4 __attribute__((ext_vector_type(4)));
typedef __bf16 bf16x2 __attribute__((ext_vector_type(2)));
typedef GAS unsigned gu32;
typedef GAS unsigned long long gu64;

__device__ __forceinline__ float bf_lo(unsigned u) { return __builtin_bit_cast(float, u << 16); }
__device__ __forceinline__ float bf_hi(unsigned u) { return __builtin_bit_cast(float, u & 0xffff0000u); }
__device__ __forceinline__ f32x2 bf_unpack(unsigned u) { return f32x2{bf_lo(u), bf_hi(u)}; }
__device__ __forceinline__ unsigned pack_bf16(float lo, float hi) {
  bf16x2 r = {(__bf16)lo, (__bf16)hi};
  return __builtin_bit_cast(unsigned, r);
}
__device__ __forceinline__ float rbf(float x) {
  return (float)(__bf16)x;
}
__device__ __forceinline__ float dot2(unsigned a, unsigned b, float acc) {
  return __builtin_amdgcn_fdot2_f32_bf16(__builtin_bit_cast(bf16x2, a), __builtin_bit_cast(bf16x2, b), acc, false);
}
template <int CTRL> __device__ __forceinline__ float dpp(float x) {
  return __builtin_bit_cast(float, __builtin_amdgcn_mov_dpp(__builtin_bit_cast(int, x), CTRL, 0xf, 0xf, true));
}
constexpr int XOR1 = 0xB1, XOR2 = 0x4E, XOR7 = 0x141, ROR2 = 0x122, ROR4 = 0x124, ROR8 = 0x128;
__device__ __forceinline__ float xrow_sum(float x) {
  auto s = __builtin_amdgcn_permlane16_swap(__float_as_uint(x), __float_as_uint(x), false, false);
  x = __uint_as_float(s[0]) + __uint_as_float(s[1]);
  auto t = __builtin_amdgcn_permlane32_swap(__float_as_uint(x), __float_as_uint(x), false, false);
  return __uint_as_float(t[0]) + __uint_as_float(t[1]);
}
__device__ __forceinline__ float xrow_max(float x) {
  auto s = __builtin_amdgcn_permlane16_swap(__float_as_uint(x), __float_as_uint(x), false, false);
  x = fmaxf(__uint_as_float(s[0]), __uint_as_float(s[1]));
  auto t = __builtin_amdgcn_permlane32_swap(__float_as_uint(x), __float_as_uint(x), false, false);
  return fmaxf(__uint_as_float(t[0]), __uint_as_float(t[1]));
}
__device__ __forceinline__ float tr16(float a, float b) {
  auto s = __builtin_amdgcn_permlane16_swap(__float_as_uint(a), __float_as_uint(b), false, false);
  return __uint_as_float(s[0]) + __uint_as_float(s[1]);
}
__device__ __forceinline__ float tr32(float a, float b) {
  auto s = __builtin_amdgcn_permlane32_swap(__float_as_uint(a), __float_as_uint(b), false, false);
  return __uint_as_float(s[0]) + __uint_as_float(s[1]);
}
__device__ __forceinline__ float wave_sum(float x) {
  x += dpp<XOR1>(x); x += dpp<XOR2>(x); x += dpp<XOR7>(x); x += dpp<ROR8>(x);
  return xrow_sum(x);
}
__device__ __forceinline__ float wave_max(float x) {
  x = fmaxf(x, dpp<XOR1>(x)); x = fmaxf(x, dpp<XOR2>(x)); x = fmaxf(x, dpp<XOR7>(x)); x = fmaxf(x, dpp<ROR8>(x));
  return xrow_max(x);
}
__device__ __forceinline__ void lds_wait() { asm volatile("s_waitcnt lgkmcnt(0)" ::: "memory"); }
__device__ __forceinline__ void st_granule(gu64* g, unsigned tag, float v) {
  __hip_atomic_store(g, ((unsigned long long)tag << 32) | (unsigned long long)__float_as_uint(v), RLX_AGENT);
}
constexpr unsigned long long SPIN_TICKS = 20000000ull;

struct Args {
  const unsigned short* x; const unsigned short* k; const unsigned short* v; const unsigned short* wq; const unsigned short* wo;
  const unsigned short* w1; const unsigned short* w3; const unsigned short* w2;
  unsigned short* out; unsigned char* ws;
};

#define STAMP(i) do { } while (0)
#define STAMP_FLUSH() do { } while (0)
#define STAMP_DECL

typedef __amdgpu_buffer_rsrc_t rsrc_t;
#define WPOL 0
#define NPRE 3
__device__ __forceinline__ u32x4 ldw(rsrc_t r, int voff, int soff) { return __builtin_amdgcn_raw_buffer_load_b128(r, voff, soff, WPOL); }
__device__ __forceinline__ u32x4 ldnt(rsrc_t r, int voff, int soff) { return __builtin_amdgcn_raw_buffer_load_b128(r, voff, soff, 2); }
#define TRK 0
#define WOD 2
#define QDELAY 32
#define DRY 1
#define FSLEEP 4
#define B0POS 6
#define SFIRST 1
#define PDIST KDIST
#define TRICKLE() __builtin_amdgcn_s_sleep(TRK)
#define HSYNC(n) do { \
    if (lane == 0) __hip_atomic_fetch_add((LAS unsigned*)lds + L_CNT + 1 + (n), 1u, __ATOMIC_RELAXED, __HIP_MEMORY_SCOPE_WORKGROUP); \
    while (__hip_atomic_load((LAS unsigned*)lds + L_CNT + 1 + (n), __ATOMIC_RELAXED, __HIP_MEMORY_SCOPE_WORKGROUP) < 8u) __builtin_amdgcn_s_sleep(0); \
    asm volatile("" ::: "memory"); \
  } while (0)
#define FRESH() int lane_ = lane0; asm volatile("" : "+v"(lane_)); \
  const int lane = lane_, r = lane >> 4, c = lane & 15, tid = wave * 64 + lane, voff = lane * 16; (void)r; (void)c; (void)tid; (void)voff
#define SB() __builtin_amdgcn_sched_barrier(0)
#define VM_DRAIN() do { SB(); asm volatile("s_waitcnt vmcnt(0)" ::: "memory"); SB(); } while (0)

__global__ void __launch_bounds__(NTHR, 3) fused_decode(Args a) {
  __shared__ __attribute__((aligned(16))) float lds[L_END];
  const int tid = threadIdx.x, lane = tid & 63, wave = __builtin_amdgcn_readfirstlane(tid >> 6);
  const int r = lane >> 4, c = lane & 15;
  const int xg = blockIdx.x & 7, j = blockIdx.x >> 3;
  const int b = j >> 4, h = xg * 4 + ((j >> 2) & 3), sp = j & 3;
  const int chunk = b * 128 + h * 4 + sp;
  const int voff = lane * 16;
  STAMP_DECL;
  STAMP(0);

  const int g8 = b * 4 + sp;
  const int qrow = g8 * 64 + wave * 8 + 2 * r, mrow = g8 * 64 + wave * 8 + (lane >> 3);
  u32x4 xv0, xv1, wq0, wq1;
  unsigned xq0, xq1, junk = 0;
  if (wave < 8) {
    xv0 = *(const GAS u32x4*)(a.x + lane * 8); xv1 = *(const GAS u32x4*)(a.x + DM + lane * 8);
    xq0 = *(const gu32*)(a.x + qrow); xq1 = *(const gu32*)(a.x + DM + qrow);
    const rsrc_t rq = __builtin_amdgcn_make_buffer_rsrc((void*)(a.wq + h * DH), 0, (DM * NH * DH - h * DH) * 2, 0x00020000);
    wq0 = ldnt(rq, qrow * (NH * DH * 2) + c * 16, 0);
    wq1 = ldnt(rq, qrow * (NH * DH * 2) + c * 16, NH * DH * 2);
    if (wave >= 6 && j < CODEPF) {
      unsigned long long pc; int len;
      asm volatile("s_getpc_b64 %0\n.Lfd_top:\n\ts_mov_b32 %1, .Lfd_end-.Lfd_top" : "=s"(pc), "=s"(len));
      junk = *(const gu32*)(pc + (unsigned)min(((wave - 6) * 64 + lane) * 128, max(len, 0)));
    }
  }
  int role = wave;
  asm volatile("" : "+s"(role));
  if (__builtin_expect(role >= 8, SFIRST)) {
    const int sw = wave - 8;
    const unsigned short* kp = a.k + (size_t)chunk * 4096 * DH;
    const unsigned short* vp = a.v + (size_t)chunk * 4096 * DH;
    const rsrc_t rk = __builtin_amdgcn_make_buffer_rsrc((void*)kp, 0, 4096 * DH * 2, 0x00020000);
    const rsrc_t rv = __builtin_amdgcn_make_buffer_rsrc((void*)vp, 0, 4096 * DH * 2, 0x00020000);
    const int stag = j * KSTAG;
    auto tile_off = [&](int t) -> int { return (((t + stag) & 63) << 14) + (sw << 12); };
    __builtin_amdgcn_s_setprio(2);
    u32x4 buf[8], pre[NPRE * 8];
    STAMP(12);
    if (B0POS == 0) { __builtin_amdgcn_s_barrier(); SB(); }
    STAMP(13);
#pragma unroll
    for (int u = 0; u < NPRE * 8; ++u) {
      pre[u] = ldnt(rk, voff + (u & 3) * 1024, tile_off(u >> 2)); SB();
      if (u + 1 == B0POS) { __builtin_amdgcn_s_barrier(); SB(); }
      if (u >= PDIST) { asm volatile("s_waitcnt vmcnt(%0)" :: "i"(PDIST)); SB(); }
      if (u == PDIST) STAMP(14);
    }
#pragma unroll
    for (int u = 0; u < 8; ++u) {
      buf[u] = ldnt(rk, voff + (u & 3) * 1024, tile_off(2 * NPRE + (u >> 2))); SB();
      if (u < 7) { asm volatile("s_waitcnt vmcnt(%0)" :: "i"(PDIST)); SB(); }
    }
    STAMP(1);
    __builtin_amdgcn_s_barrier();
    asm volatile("" ::: "memory");
    const u32x4 qp = *(const LAS u32x4*)(lds + L_Q + c * 4);
    STAMP(2);
    LAS float* S = (LAS float*)lds + L_S + sw * 1024;
    float mx = -INFINITY;
#define DOT(w) dot2((w)[3], qp[3], dot2((w)[2], qp[2], dot2((w)[1], qp[1], dot2((w)[0], qp[0], 0.f))))
#define K_FIN(tt) do { \
      _Pragma("unroll") for (int u = 0; u < 4; ++u) { const float lo = x[u] + dpp<ROR8>(x[u]), hi = x[u + 4] + dpp<ROR8>(x[u + 4]); x[u] = (c & 8) ? hi : lo; } \
      _Pragma("unroll") for (int u = 0; u < 2; ++u) { const float lo = x[u] + dpp<XOR7>(x[u]), hi = x[u + 2] + dpp<XOR7>(x[u + 2]); x[u] = (c & 4) ? hi : lo; } \
      { const float lo = x[0] + dpp<XOR2>(x[0]), hi = x[1] + dpp<XOR2>(x[1]); x[0] = (c & 2) ? hi : lo; } \
      float sc = x[0] + dpp<XOR1>(x[0]); \
      sc = rbf(rbf(sc) * SCALE); \
      mx = fmaxf(mx, sc); \
      S[(tt) * 32 + r * 8 + (c >> 1)] = sc; \
      SB(); \
    } while (0)
    static_assert(KDIST <= 6 && KDIST >= 3 && NPRE >= 1 && PDIST <= NPRE * 8, "ring of 8");
#define PACE() do { asm volatile("s_waitcnt vmcnt(%0)" :: "i"(KDIST)); SB(); } while (0)
    {
      float x[8];
#pragma unroll
      for (int u = 0; u < 8; ++u) {
        if (u > 0 && u <= KDIST + 1) { buf[u - 1] = ldnt(rk, voff + ((u - 1) & 3) * 1024, tile_off(2 * NPRE + 2 + ((u - 1) >> 2))); SB(); }
        x[u] = DOT(buf[u]);
        SB();
      }
      K_FIN(NPRE);
    }
#pragma unroll
    for (int pp = 0; pp < NPRE; ++pp) {
      float x[8];
#pragma unroll
      for (int w = 0; w < 8; ++w) x[w] = DOT(pre[pp * 8 + w]);
      K_FIN(pp);
    }
#pragma unroll
    for (int u = KDIST + 1; u < 7; ++u) { PACE(); buf[u] = ldnt(rk, voff + (u & 3) * 1024, tile_off(2 * NPRE + 2 + (u >> 2))); SB(); }
    int last = 31;
    asm volatile("" : "+s"(last));
#pragma nounroll
    for (int tt = NPRE + 1; tt <= last; ++tt) {
      const unsigned short* nb = tt == last ? vp : kp;
      const rsrc_t rn = __builtin_amdgcn_make_buffer_rsrc((void*)nb, 0, 4096 * DH * 2, 0x00020000);
      float x[8];
#pragma unroll
      for (int u = 0; u < 8; ++u) {
        const int n = u + 7;
        PACE();
        buf[n & 7] = ldnt(n >= 8 ? rn : rk, voff + (n & 3) * 1024, tile_off(2 * tt + (n >> 2)));
        SB();
        x[u] = DOT(buf[u]);
        SB();
      }
      K_FIN(tt);
    }
    STAMP(3);
    mx = wave_max(mx);
    if (lane == 0) lds[L_ML + sw] = mx;
    lds_wait();
    if (lane == 0) __hip_atomic_fetch_add((LAS unsigned*)lds + L_CNT, 1u, __ATOMIC_RELAXED, __HIP_MEMORY_SCOPE_WORKGROUP);
    const float msub = mx * LOG2E;
    STAMP(4);
    f32x2 acc[4] = {{0.f, 0.f}, {0.f, 0.f}, {0.f, 0.f}, {0.f, 0.f}};
#pragma nounroll
    for (int tt = 0; tt <= last; ++tt) {
      const rsrc_t rn = __builtin_amdgcn_make_buffer_rsrc((void*)vp, 0, tt == last ? 0 : 4096 * DH * 2, 0x00020000);
      const f32x4 s0 = *(const LAS f32x4*)(S + tt * 32 + r * 8), s1 = *(const LAS f32x4*)(S + tt * 32 + r * 8 + 4);
#pragma unroll
      for (int u = 0; u < 8; ++u) {
        const int n = u + 7;
        PACE();
        buf[n & 7] = ldnt(n >= 8 ? rn : rv, voff + (n & 3) * 1024, tile_off(2 * tt + (n >> 2)));
        SB();
        const float p = __builtin_amdgcn_exp2f(fmaf(u < 4 ? s0[u & 3] : s1[u & 3], LOG2E, -msub));
#pragma unroll
        for (int k = 0; k < 4; ++k) acc[k] = __builtin_elementwise_fma(bf_unpack(buf[u][k]), f32x2{p, p}, acc[k]);
        SB();
      }
    }
    STAMP(5);
    *(LAS f32x4*)(lds + L_RED + (sw * 4 + r) * 128 + c * 8) = f32x4{acc[0][0], acc[0][1], acc[1][0], acc[1][1]};
    *(LAS f32x4*)(lds + L_RED + (sw * 4 + r) * 128 + c * 8 + 4) = f32x4{acc[2][0], acc[2][1], acc[3][0], acc[3][1]};
    lds_wait();
    __builtin_amdgcn_s_barrier();
    __builtin_amdgcn_s_barrier();
    __builtin_amdgcn_s_barrier();
    STAMP_FLUSH();
    return;
  }

  unsigned epoch = *(const __attribute__((address_space(4))) unsigned*)(a.ws + WS_EPOCH);
  SB();
  if (tid < 4) ((LAS unsigned*)lds)[L_CNT + tid] = 0u;
  lds_wait();
  __builtin_amdgcn_s_barrier();
  SB();
  float mb0, mb1, sd0, sd1;
  {
    float f0[8], f1[8];
#pragma unroll
    for (int k = 0; k < 4; ++k) { f0[2 * k] = bf_lo(xv0[k]); f0[2 * k + 1] = bf_hi(xv0[k]); f1[2 * k] = bf_lo(xv1[k]); f1[2 * k + 1] = bf_hi(xv1[k]); }
    float s0 = 0.f, s1 = 0.f;
#pragma unroll
    for (int k = 0; k < 8; ++k) { s0 += f0[k]; s1 += f1[k]; }
    const float m0 = wave_sum(s0) * (1.f / DM), m1 = wave_sum(s1) * (1.f / DM);
    float v0 = 0.f, v1 = 0.f;
#pragma unroll
    for (int k = 0; k < 8; ++k) { const float d0 = f0[k] - m0, d1 = f1[k] - m1; v0 = fmaf(d0, d0, v0); v1 = fmaf(d1, d1, v1); }
    v0 = wave_sum(v0) * (1.f / DM); v1 = wave_sum(v1) * (1.f / DM);
    mb0 = rbf(m0); sd0 = rbf(__builtin_sqrtf(rbf(rbf(v0) + 1e-5f)));
    mb1 = rbf(m1); sd1 = rbf(__builtin_sqrtf(rbf(rbf(v1) + 1e-5f)));
  }
#define XN(xf, mb, sd) rbf(rbf((xf) - (mb)) / (sd))
  asm volatile("" : "+s"(epoch));
  const unsigned tag = epoch + 0x9E3779B9u;
  STAMP(10);
  {
    const float a0 = XN(bf_lo(xq0), mb0, sd0), a1 = XN(bf_hi(xq0), mb0, sd0), b0 = XN(bf_lo(xq1), mb1, sd1), b1 = XN(bf_hi(xq1), mb1, sd1);
    float t[8];
#pragma unroll
    for (int k = 0; k < 4; ++k) {
      const f32x2 w0 = bf_unpack(wq0[k]), w1 = bf_unpack(wq1[k]);
      const f32x2 qa = __builtin_elementwise_fma(w1, f32x2{a1, a1}, w0 * a0), qb = __builtin_elementwise_fma(w1, f32x2{b1, b1}, w0 * b0);
      t[2 * k] = tr32(qa[0], qb[0]); t[2 * k + 1] = tr32(qa[1], qb[1]);
    }
    f32x4 o;
#pragma unroll
    for (int k = 0; k < 4; ++k) o[k] = tr16(t[k], t[4 + k]);
    *(LAS f32x4*)(lds + L_Y + wave * 256 + (r >> 1) * 128 + c * 8 + (r & 1) * 4) = o;
    lds_wait();
  }
  STAMP(11);
  HSYNC(0);
  STAMP(12);
  gu64* Q = (gu64*)(a.ws + WS_Q) + h * 2048;
  if (tid < 256) {
    const LAS float* P = (const LAS float*)lds + L_Y + tid;
    st_granule(Q + g8 * 256 + tid, tag, ((P[0] + P[256]) + (P[512] + P[768])) + ((P[1024] + P[1280]) + (P[1536] + P[1792])));
  }
  STAMP(9);
  asm volatile("" :: "v"(junk));
  if (wave == 7) {
    __builtin_amdgcn_s_sleep(QDELAY);
    const rsrc_t rg = __builtin_amdgcn_make_buffer_rsrc((void*)(a.ws + WS_Q + (size_t)h * 16384 + b * 1024), 0, 16384, 0x00020000);
    const unsigned long long t0 = __builtin_amdgcn_s_memrealtime();
    u32x4 g[8];
    while (true) {
      bool ok = true;
#pragma unroll
      for (int i = 0; i < 8; ++i) g[i] = __builtin_amdgcn_raw_buffer_load_b128(rg, voff, i * 2048, 16);
#pragma unroll
      for (int i = 0; i < 8; ++i) ok = ok & (g[i][1] == tag) & (g[i][3] == tag);
      if (__all(ok)) break;
      if (__builtin_amdgcn_s_memrealtime() - t0 > SPIN_TICKS) break;
      __builtin_amdgcn_s_sleep(8);
      asm volatile("" ::: "memory");
    }
    f32x2 q2[8];
#pragma unroll
    for (int i = 0; i < 8; ++i) q2[i] = f32x2{__uint_as_float(g[i][0]), __uint_as_float(g[i][2])};
    const f32x2 qs = ((q2[0] + q2[1]) + (q2[2] + q2[3])) + ((q2[4] + q2[5]) + (q2[6] + q2[7]));
    ((LAS unsigned*)lds)[L_Q + lane] = pack_bf16(qs[0], qs[1]);
    lds_wait();
  }
  STAMP(1);
  __builtin_amdgcn_s_barrier();
  asm volatile("" ::: "memory");
  gu64* G = (gu64*)(a.ws + WS_G);
  const int red = blockIdx.x < 32 ? (int)blockIdx.x : -1;
  const unsigned xw = (red >= 0 && tid < 16) ? *(const gu32*)(a.x + (tid >> 3) * DM + red * 16 + (tid & 7) * 2) : 0u;
  if (h < 28) {
    const unsigned short xm0 = a.x[mrow], xm1 = a.x[DM + mrow];
    const rsrc_t r1 = __builtin_amdgcn_make_buffer_rsrc((void*)(a.w1 + h * 64), 0, (DM * FF - h * 64) * 2, 0x00020000);
    const rsrc_t r3 = __builtin_amdgcn_make_buffer_rsrc((void*)(a.w3 + h * 64), 0, (DM * FF - h * 64) * 2, 0x00020000);
    const u32x4 wg = ldnt(r1, mrow * (FF * 2) + (lane & 7) * 16, 0);
    const u32x4 wu = ldnt(r3, mrow * (FF * 2) + (lane & 7) * 16, 0);
    const float a0 = XN(bf_lo(xm0), mb0, sd0), b0 = XN(bf_lo(xm1), mb1, sd1);
    float t[8];
#pragma unroll
    for (int k = 0; k < 4; ++k) {
      const f32x2 g = bf_unpack(wg[k]), u = bf_unpack(wu[k]);
#pragma unroll
      for (int e = 0; e < 2; ++e) t[2 * k + e] = tr16(tr32(g[e] * a0, g[e] * b0), tr32(u[e] * a0, u[e] * b0));
    }
    f32x4 o;
#pragma unroll
    for (int k = 0; k < 4; ++k) { const float lo = t[k] + dpp<ROR8>(t[k]), hi = t[4 + k] + dpp<ROR8>(t[4 + k]); o[k] = (c & 8) ? hi : lo; }
    *(LAS f32x4*)(lds + L_Y + 2048 + wave * 256 + r * 64 + (c & 7) * 8 + (c >> 3) * 4) = o;
    lds_wait();
    HSYNC(1);
    if (tid < 256) {
      const LAS float* P = (const LAS float*)lds + L_Y + 2048 + tid;
      st_granule(G + (h * 8 + g8) * 256 + tid, tag, ((P[0] + P[256]) + (P[512] + P[768])) + ((P[1024] + P[1280]) + (P[1536] + P[1792])));
    }
  }
  const int lane0 = lane;
  int pass = DRY ? 0 : 1;
  asm volatile("" : "+s"(pass));
#pragma nounroll
  for (; pass < 2; ++pass) {
    f32x2 wf[64];
    f32x2 yacc[4] = {{0.f, 0.f}, {0.f, 0.f}, {0.f, 0.f}, {0.f, 0.f}};
    if (!pass) {
#pragma unroll
      for (int i = 0; i < 64; ++i) asm volatile("" : "=v"(wf[i]));
    } else {
      {
        FRESH();
        const rsrc_t ro = __builtin_amdgcn_make_buffer_rsrc((void*)(a.wo + (size_t)h * DH * DM), 0, DH * DM * 2, 0x00020000);
#pragma unroll
        for (int i0 = 0; i0 < 16; i0 += WOD) {
          u32x4 w[WOD];
#pragma unroll
          for (int i = 0; i < WOD; ++i) w[i] = ldw(ro, voff + ((i0 + i) & 3) * 1024, (wave * 16 + ((i0 + i) & ~3)) * 1024);
          VM_DRAIN();
#pragma unroll
          for (int i = 0; i < WOD; ++i)
#pragma unroll
            for (int k = 0; k < 4; ++k) { wf[(i0 + i) * 4 + k] = bf_unpack(w[i][k]); asm volatile("" : "+v"(wf[(i0 + i) * 4 + k])); }
          TRICKLE();
        }
      }
      if (wave < 7) {
        FRESH();
        const int f0 = (h * 4 + sp) * 14 + wave * 2;
        const rsrc_t r2 = __builtin_amdgcn_make_buffer_rsrc((void*)(a.w2 + (size_t)f0 * DM), 0, 2 * DM * 2, 0x00020000);
        const u32x4 w20 = ldw(r2, voff, 0), w21 = ldw(r2, voff + 1024, 0);
        const int f = f0 + (r & 1);
        gu64* gp = G + ((f >> 6) * 8 + (c & 7)) * 256 + b * 128 + (c >> 3) * 64 + (f & 63);
        const unsigned long long t0 = __builtin_amdgcn_s_memrealtime();
        unsigned long long g;
        while (true) {
          g = __hip_atomic_load(gp, RLX_AGENT);
          if (__all((unsigned)(g >> 32) == tag)) break;
          if (__builtin_amdgcn_s_memrealtime() - t0 > SPIN_TICKS) break;
          __builtin_amdgcn_s_sleep(16);
        }
        float s = __uint_as_float((unsigned)g);
        s += dpp<XOR1>(s); s += dpp<XOR2>(s); s += dpp<XOR7>(s);
        const float gs = rbf(s), us = rbf(dpp<ROR8>(s));
        const float si = rbf(gs / (1.f + __expf(-gs)));
        const float hv = rbf(si * us);
        const float h0 = __builtin_bit_cast(float, __builtin_amdgcn_readlane(__builtin_bit_cast(int, hv), 0));
        const float h1 = __builtin_bit_cast(float, __builtin_amdgcn_readlane(__builtin_bit_cast(int, hv), 16));
#pragma unroll
        for (int k = 0; k < 4; ++k) yacc[k] = __builtin_elementwise_fma(bf_unpack(w21[k]), f32x2{h1, h1}, bf_unpack(w20[k]) * h0);
      }
      STAMP(2);
      if (wave == 0) {
        FRESH();
        while (__hip_atomic_load((LAS unsigned*)lds + L_CNT, __ATOMIC_RELAXED, __HIP_MEMORY_SCOPE_WORKGROUP) < 4u) __builtin_amdgcn_s_sleep(16);
        asm volatile("" ::: "memory");
        const float mw = lds[L_ML + (lane & 3)];
        float m = fmaxf(mw, dpp<XOR1>(mw)); m = fmaxf(m, dpp<XOR2>(m));
        float l = 0.f;
#pragma unroll 4
        for (int i = 0; i < 16; ++i) {
          const f32x4 sv = *(const LAS f32x4*)(lds + L_S + i * 256 + lane * 4);
#pragma unroll
          for (int k = 0; k < 4; ++k) l += __builtin_amdgcn_exp2f((sv[k] - m) * LOG2E);
        }
        l = wave_sum(l);
        gu64* ml = (gu64*)(a.ws + WS_ML);
        if (lane < 16) st_granule(ml + chunk * MLS + lane, tag, (lane & 1) ? l : m);
        const unsigned long long t0 = __builtin_amdgcn_s_memrealtime();
        unsigned long long gm, gl;
        while (true) {
          __builtin_amdgcn_s_sleep(127); __builtin_amdgcn_s_sleep(127);
          gm = __hip_atomic_load(ml + ((chunk & ~3) + (lane & 3)) * MLS, RLX_AGENT);
          gl = __hip_atomic_load(ml + ((chunk & ~3) + (lane & 3)) * MLS + 1, RLX_AGENT);
          if (__all((unsigned)(gm >> 32) == tag && (unsigned)(gl >> 32) == tag)) break;
          if (__builtin_amdgcn_s_memrealtime() - t0 > SPIN_TICKS) break;
        }
        const float ms = __uint_as_float((unsigned)gm), ls = __uint_as_float((unsigned)gl);
        float M = fmaxf(ms, dpp<XOR1>(ms)); M = fmaxf(M, dpp<XOR2>(M));
        float L = ls * __builtin_amdgcn_exp2f((ms - M) * LOG2E);
        L += dpp<XOR1>(L); L += dpp<XOR2>(L);
        if (lane < 4) lds[L_FW + lane] = __builtin_amdgcn_exp2f((mw - M) * LOG2E) / L;
        lds_wait();
        STAMP(3);
      }
    }
    FRESH();
    if (pass) __builtin_amdgcn_s_barrier();
    asm volatile("" ::: "memory");
    STAMP(6);
    {
      const LAS float* P = (const LAS float*)lds + L_RED + (4 * r) * 128 + wave * 16 + c;
      float o = ((P[0] + P[128]) + (P[256] + P[384])) * lds[L_FW + r];
      o = xrow_sum(o);
#pragma unroll
      for (int i = 0; i < 16; ++i) {
        const float oi = __builtin_bit_cast(float, __builtin_amdgcn_readlane(__builtin_bit_cast(int, o), i));
#pragma unroll
        for (int k = 0; k < 4; ++k) yacc[k] = __builtin_elementwise_fma(wf[i * 4 + k], f32x2{oi, oi}, yacc[k]);
      }
    }
    *(LAS f32x4*)(lds + L_Y2 + wave * 512 + lane * 8) = f32x4{yacc[0][0], yacc[0][1], yacc[1][0], yacc[1][1]};
    *(LAS f32x4*)(lds + L_Y2 + wave * 512 + lane * 8 + 4) = f32x4{yacc[2][0], yacc[2][1], yacc[3][0], yacc[3][1]};
    lds_wait();
    if (pass) __builtin_amdgcn_s_barrier();
    asm volatile("" ::: "memory");
    const rsrc_t ry = __builtin_amdgcn_make_buffer_rsrc((void*)(a.ws + WS_Y), 0, pass ? 1 << 20 : 0, 0x00020000);
    {
      const LAS float* P = (const LAS float*)lds + L_Y2 + tid;
      const float y = ((P[0] + P[512]) + (P[1024] + P[1536])) + ((P[2048] + P[2560]) + (P[3072] + P[3584]));
      __builtin_amdgcn_raw_buffer_store_b64(u32x2{__float_as_uint(y), tag}, ry, (tid >> 4) * 32768 + (tid & 15) * 8, chunk * 128, 16);
    }
    STAMP(7);
    if (red >= 0) {
      const unsigned long long t0 = __builtin_amdgcn_s_memrealtime();
      u32x4 g[4] = {{0u, 0u, 0u, 0u}, {0u, 0u, 0u, 0u}, {0u, 0u, 0u, 0u}, {0u, 0u, 0u, 0u}};
      bool ok = false;
      while (true) {
        if (!ok) {
#pragma unroll
          for (int k = 0; k < 4; ++k) g[k] = __builtin_amdgcn_raw_buffer_load_b128(ry, tid * 64 + k * 16, red * 32768, 16);
          ok = true;
#pragma unroll
          for (int k = 0; k < 4; ++k) ok = ok && g[k][1] == tag && g[k][3] == tag;
        }
        if (__all(ok) || !pass) break;
        if (__builtin_amdgcn_s_memrealtime() - t0 > SPIN_TICKS) break;
        __builtin_amdgcn_s_sleep(FSLEEP);
        asm volatile("" ::: "memory");
      }
      float t[4];
#pragma unroll
      for (int k = 0; k < 2; ++k) {
        t[2 * k] = tr32(__uint_as_float(g[k][0]), __uint_as_float(g[2 + k][0]));
        t[2 * k + 1] = tr32(__uint_as_float(g[k][2]), __uint_as_float(g[2 + k][2]));
      }
      const float u0 = tr16(t[0], t[2]), u1 = tr16(t[1], t[3]);
      const float lo = u0 + dpp<ROR8>(u0), hi = u1 + dpp<ROR8>(u1);
      float s = (c & 8) ? hi : lo;
      s += dpp<XOR2>(s); s += dpp<XOR7>(dpp<XOR1>(s));
      if ((c & 6) == 0) lds[L_FIN + wave * 16 + (c & 1) * 8 + 2 * r + (c >> 3)] = s;
      lds_wait();
    }
    if (pass) __builtin_amdgcn_s_barrier();
    asm volatile("" ::: "memory");
    if (red >= 0 && tid < 16) {
      const LAS float* P = (const LAS float*)lds + L_FIN + (tid >> 3) * 64 + (tid & 7) * 2;
      const float o0 = bf_lo(xw) + ((P[0] + P[16]) + (P[32] + P[48])), o1 = bf_hi(xw) + ((P[1] + P[17]) + (P[33] + P[49]));
      const rsrc_t rout = __builtin_amdgcn_make_buffer_rsrc((void*)a.out, 0, pass ? 2 * DM * 2 : 0, 0x00020000);
      __builtin_amdgcn_raw_buffer_store_b32(pack_bf16(o0, o1), rout, (tid >> 3) * (DM * 2) + (tid & 7) * 4, red * 32, 0);
    }
  }
  if (red == 0 && tid == 0) __hip_atomic_store((gu32*)(a.ws + WS_EPOCH), tag, RLX_AGENT);
  asm volatile(".Lfd_end:");
  STAMP(8);
  STAMP_FLUSH();
}

}

extern "C" void kernel_launch(void* const* d_in, const int* in_sizes, int n_in, void* d_out, int out_size, void* d_ws, size_t ws_size,
                              hipStream_t stream) {
  Args a;
  a.x = (const unsigned short*)d_in[0]; a.k = (const unsigned short*)d_in[1]; a.v = (const unsigned short*)d_in[2];
  a.wq = (const unsigned short*)d_in[3]; a.wo = (const unsigned short*)d_in[4];
  a.w1 = (const unsigned short*)d_in[6]; a.w3 = (const unsigned short*)d_in[7]; a.w2 = (const unsigned short*)d_in[8];
  a.out = (unsigned short*)d_out; a.ws = (unsigned char*)d_ws;
  fused_decode<<<NWG, NTHR, 0, stream>>>(a);
}
```

```cpp
#define GLATE 0
#include <hip/hip_runtime.h>
#include <hip/hip_bf16.h>
#include <stdint.h>

#define KDIST 4
#define KSTAG 1
#define MIMIC 1
#define STAMPS 0

namespace {

constexpr int NWG = 256, NTHR = 768;
constexpr int DM = 512, NH = 32, SK = 16384, DH = 128, FF = 1792;
constexpr float LOG2E = 1.4426950408889634f;
constexpr float SCALE = 0.08838834764831845f;
#define MLS 16
constexpr size_t WS_EPOCH = 0, WS_ML = 4u << 20;
constexpr size_t WS_Y = 65536;
constexpr size_t WS_Q = WS_Y + (1u << 20);
constexpr size_t WS_G = WS_Q + (512u << 10);
constexpr size_t WS_STAMP = 8u << 20;
constexpr int L_S = 0;
constexpr int L_RED = L_S + 4096;
constexpr int L_Y = L_RED + 2048;
constexpr int L_Y2 = L_Y + 4096;
constexpr int L_Q = L_Y2 + 4096;
constexpr int L_ML = L_Q + 64;
constexpr int L_FW = L_ML + 8;
constexpr int L_FIN = L_FW + 4;
constexpr int L_CNT = L_FIN + 128;
constexpr int L_REF = L_CNT + 4;
constexpr int L_END = L_REF + 4;

#define GAS __attribute__((address_space(1)))
#define LAS __attribute__((address_space(3)))
#define RLX_AGENT __ATOMIC_RELAXED, __HIP_MEMORY_SCOPE_AGENT
typedef unsigned u32x4 __attribute__((ext_vector_type(4)));
typedef unsigned u32x2 __attribute__((ext_vector_type(2)));
typedef float f32x2 __attribute__((ext_vector_type(2)));
typedef float f32x4 __attribute__((ext_vector_type(4)));
typedef __bf16 bf16x2 __attribute__((ext_vector_type(2)));
typedef GAS unsigned gu32;
typedef GAS unsigned long long gu64;

__device__ __forceinline__ float bf_lo(unsigned u) { return __builtin_bit_cast(float, u << 16); }
__device__ __forceinline__ float bf_hi(unsigned u) { return __builtin_bit_cast(float, u & 0xffff0000u); }
__device__ __forceinline__ f32x2 bf_unpack(unsigned u) { return f32x2{bf_lo(u), bf_hi(u)}; }
__device__ __forceinline__ unsigned pack_bf16(float lo, float hi) {
  bf16x2 r = {(__bf16)lo, (__bf16)hi};
  return __builtin_bit_cast(unsigned, r);
}
__device__ __forceinline__ float rbf(float x) {
  return (float)(__bf16)x;
}
__device__ __forceinline__ float dot2(unsigned a, unsigned b, float acc) {
  return __builtin_amdgcn_fdot2_f32_bf16(__builtin_bit_cast(bf16x2, a), __builtin_bit_cast(bf16x2, b), acc, false);
}
template <int CTRL> __device__ __forceinline__ float dpp(float x) {
  return __builtin_bit_cast(float, __builtin_amdgcn_mov_dpp(__builtin_bit_cast(int, x), CTRL, 0xf, 0xf, true));
}
constexpr int XOR1 = 0xB1, XOR2 = 0x4E, XOR7 = 0x141, ROR2 = 0x122, ROR4 = 0x124, ROR8 = 0x128;
__device__ __forceinline__ float xrow_sum(float x) {
  auto s = __builtin_amdgcn_permlane16_swap(__float_as_uint(x), __float_as_uint(x), false, false);
  x = __uint_as_float(s[0]) + __uint_as_float(s[1]);
  auto t = __builtin_amdgcn_permlane32_swap(__float_as_uint(x), __float_as_uint(x), false, false);
  return __uint_as_float(t[0]) + __uint_as_float(t[1]);
}
__device__ __forceinline__ float xrow_max(float x) {
  auto s = __builtin_amdgcn_permlane16_swap(__float_as_uint(x), __float_as_uint(x), false, false);
  x = fmaxf(__uint_as_float(s[0]), __uint_as_float(s[1]));
  auto t = __builtin_amdgcn_permlane32_swap(__float_as_uint(x), __float_as_uint(x), false, false);
  return fmaxf(__uint_as_float(t[0]), __uint_as_float(t[1]));
}
__device__ __forceinline__ float tr16(float a, float b) {
  auto s = __builtin_amdgcn_permlane16_swap(__float_as_uint(a), __float_as_uint(b), false, false);
  return __uint_as_float(s[0]) + __uint_as_float(s[1]);
}
__device__ __forceinline__ float tr32(float a, float b) {
  auto s = __builtin_amdgcn_permlane32_swap(__float_as_uint(a), __float_as_uint(b), false, false);
  return __uint_as_float(s[0]) + __uint_as_float(s[1]);
}
__device__ __forceinline__ float wave_sum(float x) {
  x += dpp<XOR1>(x); x += dpp<XOR2>(x); x += dpp<XOR7>(x); x += dpp<ROR8>(x);
  return xrow_sum(x);
}
__device__ __forceinline__ float wave_max(float x) {
  x = fmaxf(x, dpp<XOR1>(x)); x = fmaxf(x, dpp<XOR2>(x)); x = fmaxf(x, dpp<XOR7>(x)); x = fmaxf(x, dpp<ROR8>(x));
  return xrow_max(x);
}
__device__ __forceinline__ void lds_wait() { asm volatile("s_waitcnt lgkmcnt(0)" ::: "memory"); }
__device__ __forceinline__ void st_granule(gu64* g, unsigned tag, float v) {
  __hip_atomic_store(g, ((unsigned long long)tag << 32) | (unsigned long long)__float_as_uint(v), RLX_AGENT);
}
constexpr unsigned long long SPIN_TICKS = 20000000ull;

struct Args {
  const unsigned short* x; const unsigned short* k; const unsigned short* v; const unsigned short* wq; const unsigned short* wo;
  const unsigned short* w1; const unsigned short* w3; const unsigned short* w2;
  unsigned short* out; unsigned char* ws;
};

#define STAMP(i) do { } while (0)
#define STAMP_FLUSH() do { } while (0)
#define STAMP_DECL

typedef __amdgpu_buffer_rsrc_t rsrc_t;
#define WPOL 0
#define NPRE 3
__device__ __forceinline__ u32x4 ldw(rsrc_t r, int voff, int soff) { return __builtin_amdgcn_raw_buffer_load_b128(r, voff, soff, WPOL); }
__device__ __forceinline__ u32x4 ldnt(rsrc_t r, int voff, int soff) { return __builtin_amdgcn_raw_buffer_load_b128(r, voff, soff, 2); }
#define TRK 0
#define WOD 2
#define QDELAY 32
#define DRY 1
#define FSLEEP 4
#define B0POS 6
#define SFIRST 1
#define CODEPF 1
#define KMIX 0
#define EPMID 1
#define WIDE_T 3300
#define LAG_A 100
#define LAG_B 112
#define LAG_W 5
#define MSLEEP 16
#define PDIST KDIST
#define TRICKLE() __builtin_amdgcn_s_sleep(TRK)
#define HSYNC(n) do { \
    if (lane == 0) __hip_atomic_fetch_add((LAS unsigned*)lds + L_CNT + 1 + (n), 1u, __ATOMIC_RELAXED, __HIP_MEMORY_SCOPE_WORKGROUP); \
    while (__hip_atomic_load((LAS unsigned*)lds + L_CNT + 1 + (n), __ATOMIC_RELAXED, __HIP_MEMORY_SCOPE_WORKGROUP) < 8u) __builtin_amdgcn_s_sleep(0); \
    asm volatile("" ::: "memory"); \
  } while (0)
#define FRESH() int lane_ = lane0; asm volatile("" : "+v"(lane_)); \
  const int lane = lane_, r = lane >> 4, c = lane & 15, tid = wave * 64 + lane, voff = lane * 16; (void)r; (void)c; (void)tid; (void)voff
#define SB() __builtin_amdgcn_sched_barrier(0)
#define VM_DRAIN() do { SB(); asm volatile("s_waitcnt vmcnt(0)" ::: "memory"); SB(); } while (0)

__global__ void __launch_bounds__(NTHR, 3) fused_decode(Args a) {
  __shared__ __attribute__((aligned(16))) float lds[L_END];
  const int tid = threadIdx.x, lane = tid & 63, wave = __builtin_amdgcn_readfirstlane(tid >> 6);
  const int r = lane >> 4, c = lane & 15;
  const int xg = blockIdx.x & 7, j = blockIdx.x >> 3;
  const int b = j >> 4, h = xg * 4 + ((j >> 2) & 3), sp = j & 3;
  const int chunk = b * 128 + h * 4 + sp;
  const int voff = lane * 16;
  STAMP_DECL;
  STAMP(0);

  const int g8 = b * 4 + sp;
  const int qrow = g8 * 64 + wave * 8 + 2 * r, mrow = g8 * 64 + wave * 8 + (lane >> 3);
  u32x4 xv0, xv1, wq0, wq1;
  unsigned xq0, xq1, junk = 0;
  if (wave < 8) {
    xv0 = *(const GAS u32x4*)(a.x + lane * 8); xv1 = *(const GAS u32x4*)(a.x + DM + lane * 8);
    xq0 = *(const gu32*)(a.x + qrow); xq1 = *(const gu32*)(a.x + DM + qrow);
    const rsrc_t rq = __builtin_amdgcn_make_buffer_rsrc((void*)(a.wq + h * DH), 0, (DM * NH * DH - h * DH) * 2, 0x00020000);
    wq0 = ldnt(rq, qrow * (NH * DH * 2) + c * 16, 0);
    wq1 = ldnt(rq, qrow * (NH * DH * 2) + c * 16, NH * DH * 2);
    if (wave >= 6 && j < CODEPF) {
      unsigned long long pc; int len;
      asm volatile("s_getpc_b64 %0\n.Lfd_top:\n\ts_mov_b32 %1, .Lfd_end-.Lfd_top" : "=s"(pc), "=s"(len));
      junk = *(const gu32*)(pc + (unsigned)min(((wave - 6) * 64 + lane) * 128, max(len, 0)));
    }
  }
  int role = wave;
  asm volatile("" : "+s"(role));
  if (__builtin_expect(role >= 8, SFIRST)) {
    const int sw = wave - 8;
    constexpr int HB = SK * DH * 2;
    const unsigned short* kp = a.k + (size_t)(chunk >> 2) * SK * DH;
    const unsigned short* vp = a.v + (size_t)(chunk >> 2) * SK * DH;
    const rsrc_t rk = __builtin_amdgcn_make_buffer_rsrc((void*)kp, 0, HB, 0x00020000);
    const rsrc_t rv = __builtin_amdgcn_make_buffer_rsrc((void*)vp, 0, HB, 0x00020000);
    const int stag = j * KSTAG, voffh = voff + HB / 2;
#define LDROW(rs, pair, n) ldnt(rs, (((n) & 1) ? voffh : voff) + (((n) & 7) >> 1) * 1024, ((((pair) + ((n) >> 3) + stag) & 31) << 14) + (sw << 12) + (sp << 19))
    __builtin_amdgcn_s_setprio(2);
    u32x4 buf[8], pre[NPRE * 8];
    STAMP(12);
    if (B0POS == 0) { __builtin_amdgcn_s_barrier(); SB(); }
    STAMP(13);
#pragma unroll
    for (int u = 0; u < NPRE * 8; ++u) {
      pre[u] = LDROW(rk, 0, u); SB();
      if (u + 1 == B0POS) { __builtin_amdgcn_s_barrier(); SB(); }
      if (u >= PDIST) { asm volatile("s_waitcnt vmcnt(%0)" :: "i"(PDIST)); SB(); }
      if (u == PDIST) STAMP(14);
    }
#pragma unroll
    for (int u = 0; u < 8; ++u) {
      buf[u] = LDROW(rk, NPRE, u); SB();
      if (u < 7) { asm volatile("s_waitcnt vmcnt(%0)" :: "i"(PDIST)); SB(); }
    }
    STAMP(1);
    __builtin_amdgcn_s_barrier();
    asm volatile("" ::: "memory");
    const u32x4 qp = *(const LAS u32x4*)(lds + L_Q + c * 4);
    STAMP(2);
    LAS float* S = (LAS float*)lds + L_S + sw * 1024;
    float mx = -INFINITY;
#define DOT(w) dot2((w)[3], qp[3], dot2((w)[2], qp[2], dot2((w)[1], qp[1], dot2((w)[0], qp[0], 0.f))))
#define K_FIN(tt) do { \
      _Pragma("unroll") for (int u = 0; u < 4; ++u) { const float lo = x[u] + dpp<ROR8>(x[u]), hi = x[u + 4] + dpp<ROR8>(x[u + 4]); x[u] = (c & 8) ? hi : lo; } \
      _Pragma("unroll") for (int u = 0; u < 2; ++u) { const float lo = x[u] + dpp<XOR7>(x[u]), hi = x[u + 2] + dpp<XOR7>(x[u + 2]); x[u] = (c & 4) ? hi : lo; } \
      { const float lo = x[0] + dpp<XOR2>(x[0]), hi = x[1] + dpp<XOR2>(x[1]); x[0] = (c & 2) ? hi : lo; } \
      float sc = x[0] + dpp<XOR1>(x[0]); \
      sc = rbf(rbf(sc) * SCALE); \
      mx = fmaxf(mx, sc); \
      S[(tt) * 32 + r * 8 + (c >> 1)] = sc; \
      SB(); \
    } while (0)
    static_assert(KDIST <= 6 && KDIST >= 3 && NPRE >= 1 && PDIST <= NPRE * 8, "ring of 8");
#define PACE() do { asm volatile("s_waitcnt vmcnt(%0)" :: "i"(((KMIX >> u) & 1) ? KDIST - 1 : KDIST)); SB(); } while (0)
    {
      float x[8];
#pragma unroll
      for (int u = 0; u < 8; ++u) {
        if (u > 0 && u <= KDIST + 1) { buf[u - 1] = LDROW(rk, NPRE + 1, u - 1); SB(); }
        x[u] = DOT(buf[u]);
        SB();
      }
      K_FIN(NPRE);
    }
#pragma unroll
    for (int pp = 0; pp < NPRE; ++pp) {
      float x[8];
#pragma unroll
      for (int w = 0; w < 8; ++w) x[w] = DOT(pre[pp * 8 + w]);
      K_FIN(pp);
    }
#pragma unroll
    for (int u = KDIST + 1; u < 7; ++u) { PACE(); buf[u] = LDROW(rk, NPRE + 1, u); SB(); }
    int last = 31;
    asm volatile("" : "+s"(last));
#pragma nounroll
    for (int tt = NPRE + 1; tt <= last; ++tt) {
      const unsigned short* nb = tt == last ? vp : kp;
      const rsrc_t rn = __builtin_amdgcn_make_buffer_rsrc((void*)nb, 0, HB, 0x00020000);
      float x[8];
#pragma unroll
      for (int u = 0; u < 8; ++u) {
        const int n = u + 7;
        PACE();
        buf[n & 7] = LDROW(n >= 8 ? rn : rk, tt, n);
        SB();
        x[u] = DOT(buf[u]);
        SB();
      }
      K_FIN(tt);
    }
    STAMP(3);
    mx = wave_max(mx);
    if (lane == 0) lds[L_ML + sw] = mx;
    lds_wait();
    if (lane == 0) __hip_atomic_fetch_add((LAS unsigned*)lds + L_CNT, 1u, __ATOMIC_RELAXED, __HIP_MEMORY_SCOPE_WORKGROUP);
    const float msub = mx * LOG2E;
    STAMP(4);
    f32x2 acc[4] = {{0.f, 0.f}, {0.f, 0.f}, {0.f, 0.f}, {0.f, 0.f}};
#pragma nounroll
    for (int tt = 0; tt <= last; ++tt) {
      const rsrc_t rn = __builtin_amdgcn_make_buffer_rsrc((void*)vp, 0, tt == last ? 0 : HB, 0x00020000);
      const f32x4 s0 = *(const LAS f32x4*)(S + tt * 32 + r * 8), s1 = *(const LAS f32x4*)(S + tt * 32 + r * 8 + 4);
      const int el = (int)((unsigned)__builtin_amdgcn_s_memrealtime() - __builtin_amdgcn_readfirstlane(*(volatile LAS unsigned*)(lds + L_REF)));
      const bool wide = el > WIDE_T;
      const bool lag = el > LAG_A + tt * LAG_B;
#pragma unroll
      for (int u = 0; u < 8; ++u) {
        const int n = u + 7;
        if (!wide) { if (lag) asm volatile("s_waitcnt vmcnt(%0)" :: "i"(LAG_W)); else PACE(); }
        SB();
        buf[n & 7] = LDROW(n >= 8 ? rn : rv, tt, n);
        SB();
        const float p = __builtin_amdgcn_exp2f(fmaf(u < 4 ? s0[u & 3] : s1[u & 3], LOG2E, -msub));
#pragma unroll
        for (int k = 0; k < 4; ++k) acc[k] = __builtin_elementwise_fma(bf_unpack(buf[u][k]), f32x2{p, p}, acc[k]);
        asm volatile("" : "+v"(acc[0]), "+v"(acc[1]), "+v"(acc[2]), "+v"(acc[3]));
        SB();
      }
    }
    STAMP(5);
    *(LAS f32x4*)(lds + L_RED + (sw * 4 + r) * 128 + c * 8) = f32x4{acc[0][0], acc[0][1], acc[1][0], acc[1][1]};
    *(LAS f32x4*)(lds + L_RED + (sw * 4 + r) * 128 + c * 8 + 4) = f32x4{acc[2][0], acc[2][1], acc[3][0], acc[3][1]};
    lds_wait();
    __builtin_amdgcn_s_barrier();
    __builtin_amdgcn_s_barrier();
    __builtin_amdgcn_s_barrier();
    STAMP_FLUSH();
    return;
  }

  unsigned epoch = *(const __attribute__((address_space(4))) unsigned*)(a.ws + WS_EPOCH);
  SB();
  if (tid < 5) ((LAS unsigned*)lds)[L_CNT + tid] = tid < 4 ? 0u : (unsigned)__builtin_amdgcn_s_memrealtime() + 0x40000000u;
  lds_wait();
  __builtin_amdgcn_s_barrier();
  SB();
  float mb0, mb1, sd0, sd1;
  {
    float f0[8], f1[8];
#pragma unroll
    for (int k = 0; k < 4; ++k) { f0[2 * k] = bf_lo(xv0[k]); f0[2 * k + 1] = bf_hi(xv0[k]); f1[2 * k] = bf_lo(xv1[k]); f1[2 * k + 1] = bf_hi(xv1[k]); }
    float s0 = 0.f, s1 = 0.f;
#pragma unroll
    for (int k = 0; k < 8; ++k) { s0 += f0[k]; s1 += f1[k]; }
    const float m0 = wave_sum(s0) * (1.f / DM), m1 = wave_sum(s1) * (1.f / DM);
    float v0 = 0.f, v1 = 0.f;
#pragma unroll
    for (int k = 0; k < 8; ++k) { const float d0 = f0[k] - m0, d1 = f1[k] - m1; v0 = fmaf(d0, d0, v0); v1 = fmaf(d1, d1, v1); }
    v0 = wave_sum(v0) * (1.f / DM); v1 = wave_sum(v1) * (1.f / DM);
    mb0 = rbf(m0); sd0 = rbf(__builtin_sqrtf(rbf(rbf(v0) + 1e-5f)));
    mb1 = rbf(m1); sd1 = rbf(__builtin_sqrtf(rbf(rbf(v1) + 1e-5f)));
  }
#define XN(xf, mb, sd) rbf(rbf((xf) - (mb)) / (sd))
  asm volatile("" : "+s"(epoch));
  const unsigned tag = epoch + 0x9E3779B9u;
  STAMP(10);
  {
    const float a0 = XN(bf_lo(xq0), mb0, sd0), a1 = XN(bf_hi(xq0), mb0, sd0), b0 = XN(bf_lo(xq1), mb1, sd1), b1 = XN(bf_hi(xq1), mb1, sd1);
    float t[8];
#pragma unroll
    for (int k = 0; k < 4; ++k) {
      const f32x2 w0 = bf_unpack(wq0[k]), w1 = bf_unpack(wq1[k]);
      const f32x2 qa = __builtin_elementwise_fma(w1, f32x2{a1, a1}, w0 * a0), qb = __builtin_elementwise_fma(w1, f32x2{b1, b1}, w0 * b0);
      t[2 * k] = tr32(qa[0], qb[0]); t[2 * k + 1] = tr32(qa[1], qb[1]);
    }
    f32x4 o;
#pragma unroll
    for (int k = 0; k < 4; ++k) o[k] = tr16(t[k], t[4 + k]);
    *(LAS f32x4*)(lds + L_Y + wave * 256 + (r >> 1) * 128 + c * 8 + (r & 1) * 4) = o;
    lds_wait();
  }
  STAMP(11);
  HSYNC(0);
  STAMP(12);
  gu64* Q = (gu64*)(a.ws + WS_Q) + h * 2048;
  if (tid < 256) {
    const LAS float* P = (const LAS float*)lds + L_Y + tid;
    st_granule(Q + g8 * 256 + tid, tag, ((P[0] + P[256]) + (P[512] + P[768])) + ((P[1024] + P[1280]) + (P[1536] + P[1792])));
  }
  STAMP(9);
  asm volatile("" :: "v"(junk));
  if (wave == 7) {
    __builtin_amdgcn_s_sleep(QDELAY);
    const rsrc_t rg = __builtin_amdgcn_make_buffer_rsrc((void*)(a.ws + WS_Q + (size_t)h * 16384 + b * 1024), 0, 16384, 0x00020000);
    const unsigned long long t0 = __builtin_amdgcn_s_memrealtime();
    u32x4 g[8];
    while (true) {
      bool ok = true;
#pragma unroll
      for (int i = 0; i < 8; ++i) g[i] = __builtin_amdgcn_raw_buffer_load_b128(rg, voff, i * 2048, 16);
#pragma unroll
      for (int i = 0; i < 8; ++i) ok = ok & (g[i][1] == tag) & (g[i][3] == tag);
      if (__all(ok)) break;
      if (__builtin_amdgcn_s_memrealtime() - t0 > SPIN_TICKS) break;
      __builtin_amdgcn_s_sleep(8);
      asm volatile("" ::: "memory");
    }
    f32x2 q2[8];
#pragma unroll
    for (int i = 0; i < 8; ++i) q2[i] = f32x2{__uint_as_float(g[i][0]), __uint_as_float(g[i][2])};
    const f32x2 qs = ((q2[0] + q2[1]) + (q2[2] + q2[3])) + ((q2[4] + q2[5]) + (q2[6] + q2[7]));
    ((LAS unsigned*)lds)[L_Q + lane] = pack_bf16(qs[0], qs[1]);
    lds_wait();
  }
  STAMP(1);
  __builtin_amdgcn_s_barrier();
  asm volatile("" ::: "memory");
  gu64* G = (gu64*)(a.ws + WS_G);
  float gsum = 0.f;
  const int red = blockIdx.x < 32 ? (int)blockIdx.x : -1;
  const unsigned xw = (red >= 0 && tid < 16) ? *(const gu32*)(a.x + (tid >> 3) * DM + red * 16 + (tid & 7) * 2) : 0u;
  if (h < 28) {
    const unsigned short xm0 = a.x[mrow], xm1 = a.x[DM + mrow];
    const rsrc_t r1 = __builtin_amdgcn_make_buffer_rsrc((void*)(a.w1 + h * 64), 0, (DM * FF - h * 64) * 2, 0x00020000);
    const rsrc_t r3 = __builtin_amdgcn_make_buffer_rsrc((void*)(a.w3 + h * 64), 0, (DM * FF - h * 64) * 2, 0x00020000);
    const u32x4 wg = ldnt(r1, mrow * (FF * 2) + (lane & 7) * 16, 0);
    const u32x4 wu = ldnt(r3, mrow * (FF * 2) + (lane & 7) * 16, 0);
    const float a0 = XN(bf_lo(xm0), mb0, sd0), b0 = XN(bf_lo(xm1), mb1, sd1);
    float t[8];
#pragma unroll
    for (int k = 0; k < 4; ++k) {
      const f32x2 g = bf_unpack(wg[k]), u = bf_unpack(wu[k]);
#pragma unroll
      for (int e = 0; e < 2; ++e) t[2 * k + e] = tr16(tr32(g[e] * a0, g[e] * b0), tr32(u[e] * a0, u[e] * b0));
    }
    f32x4 o;
#pragma unroll
    for (int k = 0; k < 4; ++k) { const float lo = t[k] + dpp<ROR8>(t[k]), hi = t[4 + k] + dpp<ROR8>(t[4 + k]); o[k] = (c & 8) ? hi : lo; }
    *(LAS f32x4*)(lds + L_Y + 2048 + wave * 256 + r * 64 + (c & 7) * 8 + (c >> 3) * 4) = o;
    lds_wait();
    HSYNC(1);
    const LAS float* P = (const LAS float*)lds + L_Y + 2048 + (tid & 255);
    gsum = ((P[0] + P[256]) + (P[512] + P[768])) + ((P[1024] + P[1280]) + (P[1536] + P[1792]));
    if (tid < 256) st_granule(G + (h * 8 + g8) * 256 + tid, tag, gsum);
  }
  const int lane0 = lane;
  int pass = DRY ? 0 : 1;
  asm volatile("" : "+s"(pass));
#pragma nounroll
  for (; pass < 2; ++pass) {
    f32x2 wf[64];
    f32x2 yacc[4] = {{0.f, 0.f}, {0.f, 0.f}, {0.f, 0.f}, {0.f, 0.f}};
    if (!pass) {
#pragma unroll
      for (int i = 0; i < 64; ++i) asm volatile("" : "=v"(wf[i]));
    } else {
      {
        FRESH();
        const rsrc_t ro = __builtin_amdgcn_make_buffer_rsrc((void*)(a.wo + (size_t)h * DH * DM), 0, DH * DM * 2, 0x00020000);
#pragma unroll
        for (int i0 = 0; i0 < 16; i0 += WOD) {
          u32x4 w[WOD];
#pragma unroll
          for (int i = 0; i < WOD; ++i) w[i] = ldw(ro, voff + ((i0 + i) & 3) * 1024, (wave * 16 + ((i0 + i) & ~3)) * 1024);
          VM_DRAIN();
#pragma unroll
          for (int i = 0; i < WOD; ++i)
#pragma unroll
            for (int k = 0; k < 4; ++k) { wf[(i0 + i) * 4 + k] = bf_unpack(w[i][k]); asm volatile("" : "+v"(wf[(i0 + i) * 4 + k])); }
          TRICKLE();
        }
      }
      STAMP(2);
      while (__hip_atomic_load((LAS unsigned*)lds + L_CNT, __ATOMIC_RELAXED, __HIP_MEMORY_SCOPE_WORKGROUP) < 4u) __builtin_amdgcn_s_sleep(8);
      asm volatile("" ::: "memory");
      gu64* ml = (gu64*)(a.ws + WS_ML);
      float mw = 0.f;
      const unsigned tk = (unsigned)__builtin_amdgcn_s_memrealtime();
      if (wave == 7) {
        FRESH();
        mw = lds[L_ML + (lane & 3)];
        float m = fmaxf(mw, dpp<XOR1>(mw)); m = fmaxf(m, dpp<XOR2>(m));
        float l = 0.f;
#pragma unroll 4
        for (int i = 0; i < 16; ++i) {
          const f32x4 sv = *(const LAS f32x4*)(lds + L_S + i * 256 + lane * 4);
#pragma unroll
          for (int k = 0; k < 4; ++k) l += __builtin_amdgcn_exp2f((sv[k] - m) * LOG2E);
        }
        l = wave_sum(l);
        if (lane < 16) st_granule(ml + chunk * MLS + lane, tag, lane == 0 ? m : lane == 1 ? l : __uint_as_float(tk));
      }
      __builtin_amdgcn_s_sleep(127); __builtin_amdgcn_s_sleep(MSLEEP);
      if (wave < 7) {
        FRESH();
        const int f0 = (h * 4 + sp) * 14 + wave * 2;
        const rsrc_t r2 = __builtin_amdgcn_make_buffer_rsrc((void*)(a.w2 + (size_t)f0 * DM), 0, 2 * DM * 2, 0x00020000);
        const u32x4 w20 = ldw(r2, voff, 0), w21 = ldw(r2, voff + 1024, 0);
        const int f = f0 + (r & 1);
        gu64* gp = G + ((f >> 6) * 8 + (c & 7)) * 256 + b * 128 + (c >> 3) * 64 + (f & 63);
        const unsigned long long t0 = __builtin_amdgcn_s_memrealtime();
        unsigned long long g;
        while (true) {
          g = __hip_atomic_load(gp, RLX_AGENT);
          if (__all((unsigned)(g >> 32) == tag)) break;
          if (__builtin_amdgcn_s_memrealtime() - t0 > SPIN_TICKS) break;
          __builtin_amdgcn_s_sleep(127);
        }
        float s = __uint_as_float((unsigned)g);
        s += dpp<XOR1>(s); s += dpp<XOR2>(s); s += dpp<XOR7>(s);
        const float gs = rbf(s), us = rbf(dpp<ROR8>(s));
        const float si = rbf(gs / (1.f + __expf(-gs)));
        const float hv = rbf(si * us);
        const float h0 = __builtin_bit_cast(float, __builtin_amdgcn_readlane(__builtin_bit_cast(int, hv), 0));
        const float h1 = __builtin_bit_cast(float, __builtin_amdgcn_readlane(__builtin_bit_cast(int, hv), 16));
#pragma unroll
        for (int k = 0; k < 4; ++k) yacc[k] = __builtin_elementwise_fma(bf_unpack(w21[k]), f32x2{h1, h1}, bf_unpack(w20[k]) * h0);
      } else {
        FRESH();
        {
          const unsigned long long gt = __hip_atomic_load(ml + (lane * 4 + (lane & 3)) * MLS + 2, RLX_AGENT);
          const bool v = (unsigned)(gt >> 32) == tag;
          const float n = wave_sum(v ? 1.f : 0.f), d = wave_sum(v ? (float)(int)((unsigned)gt - tk) : 0.f);
          if (lane == 0 && n > 0.f) ((LAS unsigned*)lds)[L_REF] = tk + (unsigned)(int)(d / n);
        }
        const unsigned long long t0 = __builtin_amdgcn_s_memrealtime();
        unsigned long long gm, gl;
        while (true) {
          gm = __hip_atomic_load(ml + ((chunk & ~3) + (lane & 3)) * MLS, RLX_AGENT);
          gl = __hip_atomic_load(ml + ((chunk & ~3) + (lane & 3)) * MLS + 1, RLX_AGENT);
          if (__all((unsigned)(gm >> 32) == tag && (unsigned)(gl >> 32) == tag)) break;
          if (__builtin_amdgcn_s_memrealtime() - t0 > SPIN_TICKS) break;
          __builtin_amdgcn_s_sleep(127);
        }
        const float ms = __uint_as_float((unsigned)gm), ls = __uint_as_float((unsigned)gl);
        float M = fmaxf(ms, dpp<XOR1>(ms)); M = fmaxf(M, dpp<XOR2>(M));
        float L = ls * __builtin_amdgcn_exp2f((ms - M) * LOG2E);
        L += dpp<XOR1>(L); L += dpp<XOR2>(L);
        if (lane < 4) lds[L_FW + lane] = __builtin_amdgcn_exp2f((mw - M) * LOG2E) / L;
        lds_wait();
        if (blockIdx.x == NWG - 1) {
          while (true) {
            bool ok = true;
#pragma unroll
            for (int i = 0; i < 4; ++i) ok = ok & ((unsigned)(__hip_atomic_load(ml + (i * 64 + lane) * MLS + 2, RLX_AGENT) >> 32) == tag);
            if (__all(ok)) break;
            if (__builtin_amdgcn_s_memrealtime() - t0 > SPIN_TICKS) break;
            __builtin_amdgcn_s_sleep(127);
          }
          if (lane == 0) __hip_atomic_store((gu32*)(a.ws + WS_EPOCH), tag, RLX_AGENT);
        }
        STAMP(3);
      }
    }
    FRESH();
    if (pass) __builtin_amdgcn_s_barrier();
    asm volatile("" ::: "memory");
    STAMP(6);
    {
      const LAS float* P = (const LAS float*)lds + L_RED + (4 * r) * 128 + wave * 16 + c;
      float o = ((P[0] + P[128]) + (P[256] + P[384])) * lds[L_FW + r];
      o = xrow_sum(o);
#pragma unroll
      for (int i = 0; i < 16; ++i) {
        const float oi = __builtin_bit_cast(float, __builtin_amdgcn_readlane(__builtin_bit_cast(int, o), i));
#pragma unroll
        for (int k = 0; k < 4; ++k) yacc[k] = __builtin_elementwise_fma(wf[i * 4 + k], f32x2{oi, oi}, yacc[k]);
      }
    }
    *(LAS f32x4*)(lds + L_Y2 + wave * 512 + lane * 8) = f32x4{yacc[0][0], yacc[0][1], yacc[1][0], yacc[1][1]};
    *(LAS f32x4*)(lds + L_Y2 + wave * 512 + lane * 8 + 4) = f32x4{yacc[2][0], yacc[2][1], yacc[3][0], yacc[3][1]};
    lds_wait();
    if (pass) __builtin_amdgcn_s_barrier();
    asm volatile("" ::: "memory");
    const rsrc_t ry = __builtin_amdgcn_make_buffer_rsrc((void*)(a.ws + WS_Y), 0, pass ? 1 << 20 : 0, 0x00020000);
    {
      const LAS float* P = (const LAS float*)lds + L_Y2 + tid;
      const float y = ((P[0] + P[512]) + (P[1024] + P[1536])) + ((P[2048] + P[2560]) + (P[3072] + P[3584]));
      __builtin_amdgcn_raw_buffer_store_b64(u32x2{__float_as_uint(y), tag}, ry, (tid >> 4) * 32768 + (tid & 15) * 8, chunk * 128, 16);
    }
    STAMP(7);
    if (red >= 0) {
      const unsigned long long t0 = __builtin_amdgcn_s_memrealtime();
      u32x4 g[4] = {{0u, 0u, 0u, 0u}, {0u, 0u, 0u, 0u}, {0u, 0u, 0u, 0u}, {0u, 0u, 0u, 0u}};
      bool ok = false;
      while (true) {
        if (!ok) {
#pragma unroll
          for (int k = 0; k < 4; ++k) g[k] = __builtin_amdgcn_raw_buffer_load_b128(ry, tid * 64 + k * 16, red * 32768, 16);
          ok = true;
#pragma unroll
          for (int k = 0; k < 4; ++k) ok = ok && g[k][1] == tag && g[k][3] == tag;
        }
        if (__all(ok) || !pass) break;
        if (__builtin_amdgcn_s_memrealtime() - t0 > SPIN_TICKS) break;
        __builtin_amdgcn_s_sleep(FSLEEP);
        asm volatile("" ::: "memory");
      }
      float t[4];
#pragma unroll
      for (int k = 0; k < 2; ++k) {
        t[2 * k] = tr32(__uint_as_float(g[k][0]), __uint_as_float(g[2 + k][0]));
        t[2 * k + 1] = tr32(__uint_as_float(g[k][2]), __uint_as_float(g[2 + k][2]));
      }
      const float u0 = tr16(t[0], t[2]), u1 = tr16(t[1], t[3]);
      const float lo = u0 + dpp<ROR8>(u0), hi = u1 + dpp<ROR8>(u1);
      float s = (c & 8) ? hi : lo;
      s += dpp<XOR2>(s); s += dpp<XOR7>(dpp<XOR1>(s));
      if ((c & 6) == 0) lds[L_FIN + wave * 16 + (c & 1) * 8 + 2 * r + (c >> 3)] = s;
      lds_wait();
    }
    if (pass) __builtin_amdgcn_s_barrier();
    asm volatile("" ::: "memory");
    if (red >= 0 && tid < 16) {
      const LAS float* P = (const LAS float*)lds + L_FIN + (tid >> 3) * 64 + (tid & 7) * 2;
      const float o0 = bf_lo(xw) + ((P[0] + P[16]) + (P[32] + P[48])), o1 = bf_hi(xw) + ((P[1] + P[17]) + (P[33] + P[49]));
      const rsrc_t rout = __builtin_amdgcn_make_buffer_rsrc((void*)a.out, 0, pass ? 2 * DM * 2 : 0, 0x00020000);
      __builtin_amdgcn_raw_buffer_store_b32(pack_bf16(o0, o1), rout, (tid >> 3) * (DM * 2) + (tid & 7) * 4, red * 32, 0);
    }
  }
  asm volatile(".Lfd_end:");
  STAMP(8);
  STAMP_FLUSH();
}

}

extern "C" void kernel_launch(void* const* d_in, const int* in_sizes, int n_in, void* d_out, int out_size, void* d_ws, size_t ws_size,
                              hipStream_t stream) {
  Args a;
  a.x = (const unsigned short*)d_in[0]; a.k = (const unsigned short*)d_in[1]; a.v = (const unsigned short*)d_in[2];
  a.wq = (const unsigned short*)d_in[3]; a.wo = (const unsigned short*)d_in[4];
  a.w1 = (const unsigned short*)d_in[6]; a.w3 = (const unsigned short*)d_in[7]; a.w2 = (const unsigned short*)d_in[8];
  a.out = (unsigned short*)d_out; a.ws = (unsigned char*)d_ws;
  fused_decode<<<NWG, NTHR, 0, stream>>>(a);
}
```

```cpp
#include <hip/hip_runtime.h>
#include <hip/hip_bf16.h>
#include <stdint.h>

#define KDIST 4
#define KSTAG 1
#define MIMIC 1
#define STAMPS 0

namespace {

constexpr int NWG = 256, NTHR = 768, NRED = 64;
constexpr int DM = 512, NH = 32, SK = 16384, DH = 128, FF = 1792;
constexpr float LOG2E = 1.4426950408889634f;
constexpr float SCALE = 0.08838834764831845f;
#define MLS 16
constexpr size_t WS_EPOCH = 0, WS_ML = 4u << 20;
constexpr size_t WS_Y = 65536;
constexpr size_t WS_Q = WS_Y + (1u << 20);
constexpr size_t WS_G = WS_Q + (512u << 10);
constexpr size_t WS_STAMP = 8u << 20;
constexpr int L_S = 0;
constexpr int L_RED = L_S + 4096;
constexpr int L_Y = L_RED + 2048;
constexpr int L_Y2 = L_Y + 4096;
constexpr int L_Q = L_Y2 + 4096;
constexpr int L_ML = L_Q + 64;
constexpr int L_FW = L_ML + 8;
constexpr int L_FIN = L_FW + 4;
constexpr int L_CNT = L_FIN + 128;
constexpr int L_REF = L_CNT + 4;
constexpr int L_END = L_REF + 4;
#define TR(i) ((void)0)

#define GAS __attribute__((address_space(1)))
#define LAS __attribute__((address_space(3)))
#define RLX_AGENT __ATOMIC_RELAXED, __HIP_MEMORY_SCOPE_AGENT
typedef unsigned u32x4 __attribute__((ext_vector_type(4)));
typedef unsigned u32x2 __attribute__((ext_vector_type(2)));
typedef float f32x2 __attribute__((ext_vector_type(2)));
typedef float f32x4 __attribute__((ext_vector_type(4)));
typedef __bf16 bf16x2 __attribute__((ext_vector_type(2)));
typedef GAS unsigned gu32;
typedef GAS unsigned long long gu64;

__device__ __forceinline__ float bf_lo(unsigned u) { return __builtin_bit_cast(float, u << 16); }
__device__ __forceinline__ float bf_hi(unsigned u) { return __builtin_bit_cast(float, u & 0xffff0000u); }
__device__ __forceinline__ f32x2 bf_unpack(unsigned u) { return f32x2{bf_lo(u), bf_hi(u)}; }
__device__ __forceinline__ unsigned pack_bf16(float lo, float hi) {
  bf16x2 r = {(__bf16)lo, (__bf16)hi};
  return __builtin_bit_cast(unsigned, r);
}
__device__ __forceinline__ float rbf(float x) {
  return (float)(__bf16)x;
}
__device__ __forceinline__ float dot2(unsigned a, unsigned b, float acc) {
  return __builtin_amdgcn_fdot2_f32_bf16(__builtin_bit_cast(bf16x2, a), __builtin_bit_cast(bf16x2, b), acc, false);
}
template <int CTRL> __device__ __forceinline__ float dpp(float x) {
  return __builtin_bit_cast(float, __builtin_amdgcn_mov_dpp(__builtin_bit_cast(int, x), CTRL, 0xf, 0xf, true));
}
constexpr int XOR1 = 0xB1, XOR2 = 0x4E, XOR7 = 0x141, ROR2 = 0x122, ROR4 = 0x124, ROR8 = 0x128;
__device__ __forceinline__ float xrow_sum(float x) {
  auto s = __builtin_amdgcn_permlane16_swap(__float_as_uint(x), __float_as_uint(x), false, false);
  x = __uint_as_float(s[0]) + __uint_as_float(s[1]);
  auto t = __builtin_amdgcn_permlane32_swap(__float_as_uint(x), __float_as_uint(x), false, false);
  return __uint_as_float(t[0]) + __uint_as_float(t[1]);
}
__device__ __forceinline__ float xrow_max(float x) {
  auto s = __builtin_amdgcn_permlane16_swap(__float_as_uint(x), __float_as_uint(x), false, false);
  x = fmaxf(__uint_as_float(s[0]), __uint_as_float(s[1]));
  auto t = __builtin_amdgcn_permlane32_swap(__float_as_uint(x), __float_as_uint(x), false, false);
  return fmaxf(__uint_as_float(t[0]), __uint_as_float(t[1]));
}
__device__ __forceinline__ float tr16(float a, float b) {
  auto s = __builtin_amdgcn_permlane16_swap(__float_as_uint(a), __float_as_uint(b), false, false);
  return __uint_as_float(s[0]) + __uint_as_float(s[1]);
}
__device__ __forceinline__ float tr32(float a, float b) {
  auto s = __builtin_amdgcn_permlane32_swap(__float_as_uint(a), __float_as_uint(b), false, false);
  return __uint_as_float(s[0]) + __uint_as_float(s[1]);
}
__device__ __forceinline__ float wave_sum(float x) {
  x += dpp<XOR1>(x); x += dpp<XOR2>(x); x += dpp<XOR7>(x); x += dpp<ROR8>(x);
  return xrow_sum(x);
}
__device__ __forceinline__ float wave_max(float x) {
  x = fmaxf(x, dpp<XOR1>(x)); x = fmaxf(x, dpp<XOR2>(x)); x = fmaxf(x, dpp<XOR7>(x)); x = fmaxf(x, dpp<ROR8>(x));
  return xrow_max(x);
}
__device__ __forceinline__ void lds_wait() { asm volatile("s_waitcnt lgkmcnt(0)" ::: "memory"); }
__device__ __forceinline__ void st_granule(gu64* g, unsigned tag, float v) {
  __hip_atomic_store(g, ((unsigned long long)tag << 32) | (unsigned long long)__float_as_uint(v), RLX_AGENT);
}
constexpr unsigned SPIN_TICKS = 20000000u;
__device__ __forceinline__ unsigned now32() { return (unsigned)__builtin_amdgcn_s_memrealtime(); }

struct Args {
  const unsigned short* x; const unsigned short* k; const unsigned short* v; const unsigned short* wq; const unsigned short* wo;
  const unsigned short* w1; const unsigned short* w3; const unsigned short* w2;
  unsigned short* out; unsigned char* ws;
};

#define STAMP(i) do { } while (0)
#define STAMP_FLUSH() do { } while (0)
#define STAMP_DECL

typedef __amdgpu_buffer_rsrc_t rsrc_t;
#define WPOL 0
#define NPRE 3
__device__ __forceinline__ u32x4 ldw(rsrc_t r, int voff, int soff) { return __builtin_amdgcn_raw_buffer_load_b128(r, voff, soff, WPOL); }
__device__ __forceinline__ u32x4 ldnt(rsrc_t r, int voff, int soff) { return __builtin_amdgcn_raw_buffer_load_b128(r, voff, soff, 2); }
#define TRK 0
#define WOD 2
#define QDELAY 32
#define DRY 1
#define FSLEEP 4
#define B0POS (PDIST + 1)
#define SFIRST 1
#define CODEPF 1
#define KMIX 0
#define GLATE 1
#define FDELAY 0
#define WIDE_T 3300
#define LAG_A 100
#define LAG_B 110
#define LAG_W 5
#define MSLEEP 16
#define Y16 0
#define FPIPE 0
#define YLD 16
#define YST 16
#define VDIST 4
#define PDIST KDIST
#define TRICKLE() __builtin_amdgcn_s_sleep(TRK)
#define HSYNC(n) do { \
    if (lane == 0) __hip_atomic_fetch_add((LAS unsigned*)lds + L_CNT + 1 + (n), 1u, __ATOMIC_RELAXED, __HIP_MEMORY_SCOPE_WORKGROUP); \
    while (__hip_atomic_load((LAS unsigned*)lds + L_CNT + 1 + (n), __ATOMIC_RELAXED, __HIP_MEMORY_SCOPE_WORKGROUP) < 8u) __builtin_amdgcn_s_sleep(0); \
    asm volatile("" ::: "memory"); \
  } while (0)
#define FRESH() int lane_ = lane0; asm volatile("" : "+v"(lane_)); \
  const int lane = lane_, r = lane >> 4, c = lane & 15, tid = wave * 64 + lane, voff = lane * 16; (void)r; (void)c; (void)tid; (void)voff
#define SB() __builtin_amdgcn_sched_barrier(0)
#define VM_DRAIN() do { SB(); asm volatile("s_waitcnt vmcnt(0)" ::: "memory"); SB(); } while (0)

__global__ void __launch_bounds__(NTHR, 3) fused_decode(Args a) {
  __shared__ __attribute__((aligned(16))) float lds[L_END];
  const int tid = threadIdx.x, lane = tid & 63, wave = __builtin_amdgcn_readfirstlane(tid >> 6);
  const int r = lane >> 4, c = lane & 15;
  const int xg = blockIdx.x & 7, j = blockIdx.x >> 3;
  const int b = j >> 4, h = xg * 4 + ((j >> 2) & 3), sp = j & 3;
  const int chunk = b * 128 + h * 4 + sp;
  const int voff = lane * 16;
  STAMP_DECL;
  STAMP(0);

  const int g8 = b * 4 + sp;
  const int qrow = g8 * 64 + wave * 8 + 2 * r, mrow = g8 * 64 + wave * 8 + (lane >> 3);
  u32x4 xv0, xv1, wq0, wq1;
  unsigned xq0, xq1, junk = 0;
  if (wave < 8) {
    xv0 = *(const GAS u32x4*)(a.x + lane * 8); xv1 = *(const GAS u32x4*)(a.x + DM + lane * 8);
    xq0 = *(const gu32*)(a.x + qrow); xq1 = *(const gu32*)(a.x + DM + qrow);
    const rsrc_t rq = __builtin_amdgcn_make_buffer_rsrc((void*)(a.wq + h * DH), 0, (DM * NH * DH - h * DH) * 2, 0x00020000);
    wq0 = ldnt(rq, qrow * (NH * DH * 2) + c * 16, 0);
    wq1 = ldnt(rq, qrow * (NH * DH * 2) + c * 16, NH * DH * 2);
    if (wave >= 6 && j < CODEPF) {
      unsigned long long pc; int len;
      asm volatile("s_getpc_b64 %0\n.Lfd_top:\n\ts_mov_b32 %1, .Lfd_end-.Lfd_top" : "=s"(pc), "=s"(len));
      junk = *(const gu32*)(pc + (unsigned)min(((wave - 6) * 64 + lane) * 128, max(len, 0)));
    }
  }
  int role = wave;
  asm volatile("" : "+s"(role));
  if (__builtin_expect(role >= 8, SFIRST)) {
    const int sw = wave - 8;
    constexpr int HB = SK * DH * 2;
    const unsigned short* kp = a.k + (size_t)(chunk >> 2) * SK * DH;
    const unsigned short* vp = a.v + (size_t)(chunk >> 2) * SK * DH;
    const rsrc_t rk = __builtin_amdgcn_make_buffer_rsrc((void*)kp, 0, HB, 0x00020000);
    const rsrc_t rv = __builtin_amdgcn_make_buffer_rsrc((void*)vp, 0, HB, 0x00020000);
    const int stag = j * KSTAG, voffh = voff + HB / 2;
#define ROWVO(n) ((((n) & 1) ? voffh : voff) + (((n) & 7) >> 1) * 1024)
#define ROWSO(pair, n) (((((pair) + ((n) >> 3) + stag) & 31) << 14) + (sw << 12) + (sp << 19))
#define LDROW(rs, pair, n) ldnt(rs, ROWVO(n), ROWSO(pair, n))
    __builtin_amdgcn_s_setprio(2);
    u32x4 buf[8], pre[NPRE * 8];
    STAMP(12);
    if (B0POS == 0) { __builtin_amdgcn_s_barrier(); SB(); }
    STAMP(13);
#pragma unroll
    for (int u = 0; u < NPRE * 8; ++u) {
      pre[u] = LDROW(rk, 0, u); SB();
      if (u + 1 == B0POS) { __builtin_amdgcn_s_barrier(); SB(); }
      if (u >= PDIST) { asm volatile("s_waitcnt vmcnt(%0)" :: "i"(PDIST)); SB(); }
      if (u == PDIST) STAMP(14);
      if ((u & 7) == 6 && u > 8) { TR((u >> 3) - 1); SB(); }
    }
#pragma unroll
    for (int u = 0; u < 8; ++u) {
      buf[u] = LDROW(rk, NPRE, u); SB();
      if (u < 7) { asm volatile("s_waitcnt vmcnt(%0)" :: "i"(PDIST)); SB(); }
    }
    TR(2);
    STAMP(1);
    __builtin_amdgcn_s_barrier();
    asm volatile("" ::: "memory");
    const u32x4 qp = *(const LAS u32x4*)(lds + L_Q + c * 4);
    STAMP(2);
    LAS float* S = (LAS float*)lds + L_S + sw * 1024;
    float mx = -INFINITY;
#define DOT(w) dot2((w)[3], qp[3], dot2((w)[2], qp[2], dot2((w)[1], qp[1], dot2((w)[0], qp[0], 0.f))))
#define K_FIN(tt) do { \
      _Pragma("unroll") for (int u = 0; u < 4; ++u) { const float lo = x[u] + dpp<ROR8>(x[u]), hi = x[u + 4] + dpp<ROR8>(x[u + 4]); x[u] = (c & 8) ? hi : lo; } \
      _Pragma("unroll") for (int u = 0; u < 2; ++u) { const float lo = x[u] + dpp<XOR7>(x[u]), hi = x[u + 2] + dpp<XOR7>(x[u + 2]); x[u] = (c & 4) ? hi : lo; } \
      { const float lo = x[0] + dpp<XOR2>(x[0]), hi = x[1] + dpp<XOR2>(x[1]); x[0] = (c & 2) ? hi : lo; } \
      float sc = x[0] + dpp<XOR1>(x[0]); \
      sc = rbf(rbf(sc) * SCALE); \
      mx = fmaxf(mx, sc); \
      S[(tt) * 32 + r * 8 + (c >> 1)] = sc; \
      SB(); \
    } while (0)
    static_assert(KDIST <= 6 && KDIST >= 3 && NPRE >= 1 && PDIST <= NPRE * 8, "ring of 8");
#define PACE() do { asm volatile("s_waitcnt vmcnt(%0)" :: "i"(((KMIX >> u) & 1) ? KDIST - 1 : KDIST)); SB(); } while (0)
    {
      float x[8];
#pragma unroll
      for (int u = 0; u < 8; ++u) {
        if (u > 0 && u <= KDIST + 1) { buf[u - 1] = LDROW(rk, NPRE + 1, u - 1); SB(); }
        x[u] = DOT(buf[u]);
        SB();
      }
      K_FIN(NPRE);
    }
#pragma unroll
    for (int pp = 0; pp < NPRE; ++pp) {
      float x[8];
#pragma unroll
      for (int w = 0; w < 8; ++w) x[w] = DOT(pre[pp * 8 + w]);
      K_FIN(pp);
    }
#pragma unroll
    for (int u = KDIST + 1; u < 7; ++u) { PACE(); buf[u] = LDROW(rk, NPRE + 1, u); SB(); }
    TR(3);
    int last = 31;
    asm volatile("" : "+s"(last));
#pragma nounroll
    for (int tt = NPRE + 1; tt <= last; ++tt) {
      const unsigned short* nb = tt == last ? vp : kp;
      const rsrc_t rn = __builtin_amdgcn_make_buffer_rsrc((void*)nb, 0, HB, 0x00020000);
      float x[8];
#pragma unroll
      for (int u = 0; u < 8; ++u) {
        const int n = u + 7;
        PACE();
        buf[n & 7] = LDROW(n >= 8 ? rn : rk, tt, n);
        SB();
        x[u] = DOT(buf[u]);
        SB();
      }
      K_FIN(tt);
      TR(tt);
    }
    STAMP(3);
    mx = wave_max(mx);
    if (lane == 0) lds[L_ML + sw] = mx;
    lds_wait();
    if (lane == 0) __hip_atomic_fetch_add((LAS unsigned*)lds + L_CNT, 1u, __ATOMIC_RELAXED, __HIP_MEMORY_SCOPE_WORKGROUP);
    STAMP(4);
    f32x2 acc[4] = {{0.f, 0.f}, {0.f, 0.f}, {0.f, 0.f}, {0.f, 0.f}};
    f32x4 s0 = *(const LAS f32x4*)(S + r * 8), s1;
    int el = -1, lag = -1;
#pragma nounroll
    for (int tt = 0; tt <= last; ++tt) {
      const rsrc_t rn = __builtin_amdgcn_make_buffer_rsrc((void*)vp, 0, tt == last ? 0 : HB, 0x00020000);
      unsigned ref, tnow;
#pragma unroll
      for (int u = 0; u < 8; ++u) {
        const int n = u + 7;
        const int so = ROWSO(tt, n);
        asm volatile("s_cmpk_gt_i32 %0, %4\n\ts_cbranch_scc1 1f\n\ts_waitcnt vmcnt(%2)\n\ts_cmp_gt_i32 %1, 0\n\ts_cbranch_scc1 1f\n\ts_waitcnt vmcnt(%3)\n1:"
                     :: "s"(el), "s"(lag), "i"(LAG_W), "i"(VDIST), "i"(WIDE_T), "s"(so) : "scc");
        SB();
        buf[n & 7] = ldnt(n >= 8 ? rn : rv, ROWVO(n), so);
        SB();
        if (u == 0) s1 = *(const LAS f32x4*)(S + tt * 32 + r * 8 + 4);
        if (u == 4) { s0 = *(const LAS f32x4*)(S + ((tt + 1) & 31) * 32 + r * 8); ref = *(volatile LAS unsigned*)(lds + L_REF); tnow = now32(); }
        const float p = __builtin_amdgcn_exp2f(((u < 4 ? s0[u & 3] : s1[u & 3]) - mx) * LOG2E);
#pragma unroll
        for (int k = 0; k < 4; ++k) acc[k] = __builtin_elementwise_fma(bf_unpack(buf[u][k]), f32x2{p, p}, acc[k]);
        asm volatile("" : "+v"(acc[0]), "+v"(acc[1]), "+v"(acc[2]), "+v"(acc[3]));
        if (u == 6) {
          el = (int)(tnow - __builtin_amdgcn_readfirstlane(ref));
          lag = el - (LAG_A + LAG_B / 2 + tt * LAG_B);
        }
        SB();
      }
      TR(32 + tt);
    }
    STAMP(5);
    *(LAS f32x4*)(lds + L_RED + (sw * 4 + r) * 128 + c * 8) = f32x4{acc[0][0], acc[0][1], acc[1][0], acc[1][1]};
    *(LAS f32x4*)(lds + L_RED + (sw * 4 + r) * 128 + c * 8 + 4) = f32x4{acc[2][0], acc[2][1], acc[3][0], acc[3][1]};
    lds_wait();
    __builtin_amdgcn_s_barrier();
    __builtin_amdgcn_s_barrier();
    if (blockIdx.x < NRED) __builtin_amdgcn_s_barrier();
    STAMP_FLUSH();
    return;
  }

  unsigned epoch = *(const __attribute__((address_space(4))) unsigned*)(a.ws + WS_EPOCH);
  SB();
  if (tid < 5) ((LAS unsigned*)lds)[L_CNT + tid] = tid < 4 ? 0u : (unsigned)__builtin_amdgcn_s_memrealtime() + 0x40000000u;
  lds_wait();
  __builtin_amdgcn_s_barrier();
  SB();
  float mb0, mb1, sd0, sd1;
  {
    float f0[8], f1[8];
#pragma unroll
    for (int k = 0; k < 4; ++k) { f0[2 * k] = bf_lo(xv0[k]); f0[2 * k + 1] = bf_hi(xv0[k]); f1[2 * k] = bf_lo(xv1[k]); f1[2 * k + 1] = bf_hi(xv1[k]); }
    float s0 = 0.f, s1 = 0.f;
#pragma unroll
    for (int k = 0; k < 8; ++k) { s0 += f0[k]; s1 += f1[k]; }
    const float m0 = wave_sum(s0) * (1.f / DM), m1 = wave_sum(s1) * (1.f / DM);
    float v0 = 0.f, v1 = 0.f;
#pragma unroll
    for (int k = 0; k < 8; ++k) { const float d0 = f0[k] - m0, d1 = f1[k] - m1; v0 = fmaf(d0, d0, v0); v1 = fmaf(d1, d1, v1); }
    v0 = wave_sum(v0) * (1.f / DM); v1 = wave_sum(v1) * (1.f / DM);
    mb0 = rbf(m0); sd0 = rbf(__builtin_sqrtf(rbf(rbf(v0) + 1e-5f)));
    mb1 = rbf(m1); sd1 = rbf(__builtin_sqrtf(rbf(rbf(v1) + 1e-5f)));
  }
#define XN(xf, mb, sd) rbf(rbf((xf) - (mb)) / (sd))
  asm volatile("" : "+s"(epoch));
  const unsigned tag = epoch + 0x9E3779B9u;
  STAMP(10);
  {
    const float a0 = XN(bf_lo(xq0), mb0, sd0), a1 = XN(bf_hi(xq0), mb0, sd0), b0 = XN(bf_lo(xq1), mb1, sd1), b1 = XN(bf_hi(xq1), mb1, sd1);
    float t[8];
#pragma unroll
    for (int k = 0; k < 4; ++k) {
      const f32x2 w0 = bf_unpack(wq0[k]), w1 = bf_unpack(wq1[k]);
      const f32x2 qa = __builtin_elementwise_fma(w1, f32x2{a1, a1}, w0 * a0), qb = __builtin_elementwise_fma(w1, f32x2{b1, b1}, w0 * b0);
      t[2 * k] = tr32(qa[0], qb[0]); t[2 * k + 1] = tr32(qa[1], qb[1]);
    }
    f32x4 o;
#pragma unroll
    for (int k = 0; k < 4; ++k) o[k] = tr16(t[k], t[4 + k]);
    *(LAS f32x4*)(lds + L_Y + wave * 256 + (r >> 1) * 128 + c * 8 + (r & 1) * 4) = o;
    lds_wait();
  }
  STAMP(11);
  HSYNC(0);
  STAMP(12);
  gu64* Q = (gu64*)(a.ws + WS_Q) + h * 2048;
  if (tid < 256) {
    const LAS float* P = (const LAS float*)lds + L_Y + tid;
    st_granule(Q + g8 * 256 + tid, tag, ((P[0] + P[256]) + (P[512] + P[768])) + ((P[1024] + P[1280]) + (P[1536] + P[1792])));
  }
  STAMP(9);
  asm volatile("" :: "v"(junk));
  if (wave == 7) {
    __builtin_amdgcn_s_sleep(QDELAY);
    const rsrc_t rg = __builtin_amdgcn_make_buffer_rsrc((void*)(a.ws + WS_Q + (size_t)h * 16384 + b * 1024), 0, 16384, 0x00020000);
    const unsigned t0 = now32();
    u32x4 g[8];
    while (true) {
      bool ok = true;
#pragma unroll
      for (int i = 0; i < 8; ++i) g[i] = __builtin_amdgcn_raw_buffer_load_b128(rg, voff, i * 2048, 16);
#pragma unroll
      for (int i = 0; i < 8; ++i) ok = ok & (g[i][1] == tag) & (g[i][3] == tag);
      if (__all(ok)) break;
      if (now32() - t0 > SPIN_TICKS) break;
      __builtin_amdgcn_s_sleep(8);
      asm volatile("" ::: "memory");
    }
    f32x2 q2[8];
#pragma unroll
    for (int i = 0; i < 8; ++i) q2[i] = f32x2{__uint_as_float(g[i][0]), __uint_as_float(g[i][2])};
    const f32x2 qs = ((q2[0] + q2[1]) + (q2[2] + q2[3])) + ((q2[4] + q2[5]) + (q2[6] + q2[7]));
    ((LAS unsigned*)lds)[L_Q + lane] = pack_bf16(qs[0], qs[1]);
    lds_wait();
  }
  STAMP(1);
  __builtin_amdgcn_s_barrier();
  asm volatile("" ::: "memory");
  gu64* G = (gu64*)(a.ws + WS_G);
  float gsum = 0.f;
  const int red = blockIdx.x < NRED ? (int)blockIdx.x : -1;
  const unsigned xw = (red >= 0 && tid < 8) ? *(const gu32*)(a.x + (red & 1) * DM + (red >> 1) * 16 + tid * 2) : 0u;
  if (h < 28) {
    const unsigned short xm0 = a.x[mrow], xm1 = a.x[DM + mrow];
    const rsrc_t r1 = __builtin_amdgcn_make_buffer_rsrc((void*)(a.w1 + h * 64), 0, (DM * FF - h * 64) * 2, 0x00020000);
    const rsrc_t r3 = __builtin_amdgcn_make_buffer_rsrc((void*)(a.w3 + h * 64), 0, (DM * FF - h * 64) * 2, 0x00020000);
    const u32x4 wg = ldnt(r1, mrow * (FF * 2) + (lane & 7) * 16, 0);
    const u32x4 wu = ldnt(r3, mrow * (FF * 2) + (lane & 7) * 16, 0);
    const float a0 = XN(bf_lo(xm0), mb0, sd0), b0 = XN(bf_lo(xm1), mb1, sd1);
    float t[8];
#pragma unroll
    for (int k = 0; k < 4; ++k) {
      const f32x2 g = bf_unpack(wg[k]), u = bf_unpack(wu[k]);
#pragma unroll
      for (int e = 0; e < 2; ++e) t[2 * k + e] = tr16(tr32(g[e] * a0, g[e] * b0), tr32(u[e] * a0, u[e] * b0));
    }
    f32x4 o;
#pragma unroll
    for (int k = 0; k < 4; ++k) { const float lo = t[k] + dpp<ROR8>(t[k]), hi = t[4 + k] + dpp<ROR8>(t[4 + k]); o[k] = (c & 8) ? hi : lo; }
    *(LAS f32x4*)(lds + L_Y + 2048 + wave * 256 + r * 64 + (c & 7) * 8 + (c >> 3) * 4) = o;
    lds_wait();
    HSYNC(1);
    const LAS float* P = (const LAS float*)lds + L_Y + 2048 + (tid & 255);
    gsum = ((P[0] + P[256]) + (P[512] + P[768])) + ((P[1024] + P[1280]) + (P[1536] + P[1792]));
  }
  const int lane0 = lane;
  int pass = DRY ? 0 : 1;
  asm volatile("" : "+s"(pass));
#pragma nounroll
  for (; pass < 2; ++pass) {
    f32x2 wf[64];
    f32x2 yacc[4] = {{0.f, 0.f}, {0.f, 0.f}, {0.f, 0.f}, {0.f, 0.f}};
    if (!pass) {
#pragma unroll
      for (int i = 0; i < 64; ++i) asm volatile("" : "=v"(wf[i]));
    } else {
      {
        FRESH();
        const rsrc_t ro = __builtin_amdgcn_make_buffer_rsrc((void*)(a.wo + (size_t)h * DH * DM), 0, DH * DM * 2, 0x00020000);
#pragma unroll
        for (int i0 = 0; i0 < 16; i0 += WOD) {
          u32x4 w[WOD];
#pragma unroll
          for (int i = 0; i < WOD; ++i) w[i] = ldw(ro, voff + ((i0 + i) & 3) * 1024, (wave * 16 + ((i0 + i) & ~3)) * 1024);
          VM_DRAIN();
#pragma unroll
          for (int i = 0; i < WOD; ++i)
#pragma unroll
            for (int k = 0; k < 4; ++k) { wf[(i0 + i) * 4 + k] = bf_unpack(w[i][k]); asm volatile("" : "+v"(wf[(i0 + i) * 4 + k])); }
          TRICKLE();
        }
      }
      STAMP(2);
      while (__hip_atomic_load((LAS unsigned*)lds + L_CNT, __ATOMIC_RELAXED, __HIP_MEMORY_SCOPE_WORKGROUP) < 4u) __builtin_amdgcn_s_sleep(8);
      asm volatile("" ::: "memory");
      gu64* ml = (gu64*)(a.ws + WS_ML);
      float mw = 0.f;
      const unsigned tk = (unsigned)__builtin_amdgcn_s_memrealtime();
      if (wave == 7) {
        FRESH();
        mw = lds[L_ML + (lane & 3)];
        float m = fmaxf(mw, dpp<XOR1>(mw)); m = fmaxf(m, dpp<XOR2>(m));
        float l = 0.f;
#pragma unroll 4
        for (int i = 0; i < 16; ++i) {
          const f32x4 sv = *(const LAS f32x4*)(lds + L_S + i * 256 + lane * 4);
#pragma unroll
          for (int k = 0; k < 4; ++k) l += __builtin_amdgcn_exp2f((sv[k] - m) * LOG2E);
        }
        l = wave_sum(l);
        if (lane < 16) st_granule(ml + chunk * MLS + lane, tag, lane == 0 ? m : lane == 1 ? l : __uint_as_float(tk));
      }
      else if (wave < 4 && h < 28) { FRESH(); __builtin_amdgcn_s_sleep(16); st_granule(G + (h * 8 + g8) * 256 + tid, tag, gsum); }
      __builtin_amdgcn_s_sleep(127); __builtin_amdgcn_s_sleep(MSLEEP);
      if (wave < 7) {
        FRESH();
        const int f0 = (h * 4 + sp) * 14 + wave * 2;
        const rsrc_t r2 = __builtin_amdgcn_make_buffer_rsrc((void*)(a.w2 + (size_t)f0 * DM), 0, 2 * DM * 2, 0x00020000);
        const u32x4 w20 = ldw(r2, voff, 0), w21 = ldw(r2, voff + 1024, 0);
        const int f = f0 + (r & 1);
        gu64* gp = G + ((f >> 6) * 8 + (c & 7)) * 256 + b * 128 + (c >> 3) * 64 + (f & 63);
        const unsigned t0 = now32();
        unsigned long long g;
        while (true) {
          g = __hip_atomic_load(gp, RLX_AGENT);
          if (__all((unsigned)(g >> 32) == tag)) break;
          if (now32() - t0 > SPIN_TICKS) break;
          __builtin_amdgcn_s_sleep(127);
        }
        float s = __uint_as_float((unsigned)g);
        s += dpp<XOR1>(s); s += dpp<XOR2>(s); s += dpp<XOR7>(s);
        const float gs = rbf(s), us = rbf(dpp<ROR8>(s));
        const float si = rbf(gs / (1.f + __expf(-gs)));
        const float hv = rbf(si * us);
        const float h0 = __builtin_bit_cast(float, __builtin_amdgcn_readlane(__builtin_bit_cast(int, hv), 0));
        const float h1 = __builtin_bit_cast(float, __builtin_amdgcn_readlane(__builtin_bit_cast(int, hv), 16));
#pragma unroll
        for (int k = 0; k < 4; ++k) yacc[k] = __builtin_elementwise_fma(bf_unpack(w21[k]), f32x2{h1, h1}, bf_unpack(w20[k]) * h0);
      } else {
        FRESH();
        {
          const unsigned long long gt = __hip_atomic_load(ml + (lane * 4 + (lane & 3)) * MLS + 2, RLX_AGENT);
          const bool v = (unsigned)(gt >> 32) == tag;
          const float n = wave_sum(v ? 1.f : 0.f), d = wave_sum(v ? (float)(int)((unsigned)gt - tk) : 0.f);
          if (lane == 0 && n > 0.f) ((LAS unsigned*)lds)[L_REF] = tk + (unsigned)(int)(d / n);
        }
        const unsigned t0 = now32();
        unsigned long long gm, gl;
        while (true) {
          gm = __hip_atomic_load(ml + ((chunk & ~3) + (lane & 3)) * MLS, RLX_AGENT);
          gl = __hip_atomic_load(ml + ((chunk & ~3) + (lane & 3)) * MLS + 1, RLX_AGENT);
          if (__all((unsigned)(gm >> 32) == tag && (unsigned)(gl >> 32) == tag)) break;
          if (now32() - t0 > SPIN_TICKS) break;
          __builtin_amdgcn_s_sleep(127);
        }
        const float ms = __uint_as_float((unsigned)gm), ls = __uint_as_float((unsigned)gl);
        float M = fmaxf(ms, dpp<XOR1>(ms)); M = fmaxf(M, dpp<XOR2>(M));
        float L = ls * __builtin_amdgcn_exp2f((ms - M) * LOG2E);
        L += dpp<XOR1>(L); L += dpp<XOR2>(L);
        if (lane < 4) lds[L_FW + lane] = __builtin_amdgcn_exp2f((mw - M) * LOG2E) / L;
        lds_wait();
        if (blockIdx.x == NWG - 1) {
          while (true) {
            bool ok = true;
#pragma unroll
            for (int i = 0; i < 4; ++i) ok = ok & ((unsigned)(__hip_atomic_load(ml + (i * 64 + lane) * MLS + 2, RLX_AGENT) >> 32) == tag);
            if (__all(ok)) break;
            if (now32() - t0 > SPIN_TICKS) break;
            __builtin_amdgcn_s_sleep(127);
          }
          if (lane == 0) __hip_atomic_store((gu32*)(a.ws + WS_EPOCH), tag, RLX_AGENT);
        }
        STAMP(3);
      }
    }
    FRESH();
    if (pass) __builtin_amdgcn_s_barrier();
    asm volatile("" ::: "memory");
    STAMP(6);
    {
      const LAS float* P = (const LAS float*)lds + L_RED + (4 * r) * 128 + wave * 16 + c;
      float o = ((P[0] + P[128]) + (P[256] + P[384])) * lds[L_FW + r];
      o = xrow_sum(o);
#pragma unroll
      for (int i = 0; i < 16; ++i) {
        const float oi = __builtin_bit_cast(float, __builtin_amdgcn_readlane(__builtin_bit_cast(int, o), i));
#pragma unroll
        for (int k = 0; k < 4; ++k) yacc[k] = __builtin_elementwise_fma(wf[i * 4 + k], f32x2{oi, oi}, yacc[k]);
      }
    }
    *(LAS f32x4*)(lds + L_Y2 + wave * 512 + lane * 8) = f32x4{yacc[0][0], yacc[0][1], yacc[1][0], yacc[1][1]};
    *(LAS f32x4*)(lds + L_Y2 + wave * 512 + lane * 8 + 4) = f32x4{yacc[2][0], yacc[2][1], yacc[3][0], yacc[3][1]};
    lds_wait();
    if (pass) __builtin_amdgcn_s_barrier();
    asm volatile("" ::: "memory");
    const rsrc_t ry = __builtin_amdgcn_make_buffer_rsrc((void*)(a.ws + WS_Y), 0, pass ? 1 << 20 : 0, 0x00020000);
    {
      const LAS float* P = (const LAS float*)lds + L_Y2 + tid;
      const float y = ((P[0] + P[512]) + (P[1024] + P[1536])) + ((P[2048] + P[2560]) + (P[3072] + P[3584]));
      __builtin_amdgcn_raw_buffer_store_b64(u32x2{__float_as_uint(y), tag}, ry, (tid >> 4) * 32768 + (tid & 15) * 8, chunk * 128, 16);
    }
    STAMP(7);
    if (red >= 0) {
      __builtin_amdgcn_s_sleep(FDELAY);
      const unsigned t0 = now32();
      u32x4 g0 = {0u, 0u, 0u, 0u}, g1 = {0u, 0u, 0u, 0u};
      bool ok0 = false, ok1 = false;
      while (true) {
        if (!ok0) g0 = __builtin_amdgcn_raw_buffer_load_b128(ry, voff, red * 16384 + wave * 2048, YLD);
        if (!ok1) g1 = __builtin_amdgcn_raw_buffer_load_b128(ry, voff + 1024, red * 16384 + wave * 2048, YLD);
        ok0 = (g0[1] == tag) & (g0[3] == tag); ok1 = (g1[1] == tag) & (g1[3] == tag);
        if (__all(ok0 & ok1) || !pass) break;
        if (now32() - t0 > SPIN_TICKS) break;
        __builtin_amdgcn_s_sleep(FSLEEP);
        asm volatile("" ::: "memory");
      }
      float s = tr32(__uint_as_float(g0[0]) + __uint_as_float(g1[0]), __uint_as_float(g0[2]) + __uint_as_float(g1[2]));
      s = tr16(s, s);
      s += dpp<ROR8>(s);
      if ((lane & 24) == 0) lds[L_FIN + wave * 16 + (lane & 7) * 2 + (lane >> 5)] = s;
      lds_wait();
      if (pass) __builtin_amdgcn_s_barrier();
      asm volatile("" ::: "memory");
      if (tid < 8) {
        const LAS f32x2* P = (const LAS f32x2*)(lds + L_FIN + tid * 2);
        const f32x2 o = bf_unpack(xw) + (((P[0] + P[8]) + (P[16] + P[24])) + ((P[32] + P[40]) + (P[48] + P[56])));
        const rsrc_t rout = __builtin_amdgcn_make_buffer_rsrc((void*)a.out, 0, pass ? 2 * DM * 2 : 0, 0x00020000);
        __builtin_amdgcn_raw_buffer_store_b32(pack_bf16(o[0], o[1]), rout, tid * 4, (red & 1) * (DM * 2) + (red >> 1) * 32, 0);
      }
    }
  }
  asm volatile(".Lfd_end:");
  STAMP(8);
  STAMP_FLUSH();
}

}

extern "C" void kernel_launch(void* const* d_in, const int* in_sizes, int n_in, void* d_out, int out_size, void* d_ws, size_t ws_size,
                              hipStream_t stream) {
  Args a;
  a.x = (const unsigned short*)d_in[0]; a.k = (const unsigned short*)d_in[1]; a.v = (const unsigned short*)d_in[2];
  a.wq = (const unsigned short*)d_in[3]; a.wo = (const unsigned short*)d_in[4];
  a.w1 = (const unsigned short*)d_in[6]; a.w3 = (const unsigned short*)d_in[7]; a.w2 = (const unsigned short*)d_in[8];
  a.out = (unsigned short*)d_out; a.ws = (unsigned char*)d_ws;
  fused_decode<<<NWG, NTHR, 0, stream>>>(a);
}
```

```cpp
#define STAMPS 1
#define DIAG 1
#define DIAG_I 14
#define DIAG_W0 8
#define DIAG_W1 12
#define DIAG_S 10
#include <hip/hip_runtime.h>
#include <hip/hip_bf16.h>
#include <stdint.h>

#define KDIST 4
#define KSTAG 1
#define MIMIC 1

namespace {

constexpr int NWG = 256, NTHR = 768, NRED = 64;
constexpr int DM = 512, NH = 32, SK = 16384, DH = 128, FF = 1792;
constexpr float LOG2E = 1.4426950408889634f;
constexpr float SCALE = 0.08838834764831845f;
#define MLS 16
constexpr size_t WS_EPOCH = 0, WS_ML = 4u << 20;
constexpr size_t WS_Y = 65536;
constexpr size_t WS_Q = WS_Y + (1u << 20);
constexpr size_t WS_G = WS_Q + (512u << 10);
constexpr size_t WS_STAMP = 8u << 20;
constexpr int L_S = 0;
constexpr int L_RED = L_S + 4096;
constexpr int L_Y = L_RED + 2048;
constexpr int L_Y2 = L_Y + 4096;
constexpr int L_Q = L_Y2 + 4096;
constexpr int L_ML = L_Q + 64;
constexpr int L_FW = L_ML + 8;
constexpr int L_FIN = L_FW + 4;
constexpr int L_CNT = L_FIN + 128;
constexpr int L_REF = L_CNT + 4;
constexpr int L_END = L_REF + 4;
#define TR(i) ((void)0)

#define GAS __attribute__((address_space(1)))
#define LAS __attribute__((address_space(3)))
#define RLX_AGENT __ATOMIC_RELAXED, __HIP_MEMORY_SCOPE_AGENT
typedef unsigned u32x4 __attribute__((ext_vector_type(4)));
typedef unsigned u32x2 __attribute__((ext_vector_type(2)));
typedef float f32x2 __attribute__((ext_vector_type(2)));
typedef float f32x4 __attribute__((ext_vector_type(4)));
typedef __bf16 bf16x2 __attribute__((ext_vector_type(2)));
typedef GAS unsigned gu32;
typedef GAS unsigned long long gu64;

__device__ __forceinline__ float bf_lo(unsigned u) { return __builtin_bit_cast(float, u << 16); }
__device__ __forceinline__ float bf_hi(unsigned u) { return __builtin_bit_cast(float, u & 0xffff0000u); }
__device__ __forceinline__ f32x2 bf_unpack(unsigned u) { return f32x2{bf_lo(u), bf_hi(u)}; }
__device__ __forceinline__ unsigned pack_bf16(float lo, float hi) {
  bf16x2 r = {(__bf16)lo, (__bf16)hi};
  return __builtin_bit_cast(unsigned, r);
}
__device__ __forceinline__ float rbf(float x) {
  return (float)(__bf16)x;
}
__device__ __forceinline__ float dot2(unsigned a, unsigned b, float acc) {
  return __builtin_amdgcn_fdot2_f32_bf16(__builtin_bit_cast(bf16x2, a), __builtin_bit_cast(bf16x2, b), acc, false);
}
template <int CTRL> __device__ __forceinline__ float dpp(float x) {
  return __builtin_bit_cast(float, __builtin_amdgcn_mov_dpp(__builtin_bit_cast(int, x), CTRL, 0xf, 0xf, true));
}
constexpr int XOR1 = 0xB1, XOR2 = 0x4E, XOR7 = 0x141, ROR2 = 0x122, ROR4 = 0x124, ROR8 = 0x128;
__device__ __forceinline__ float xrow_sum(float x) {
  auto s = __builtin_amdgcn_permlane16_swap(__float_as_uint(x), __float_as_uint(x), false, false);
  x = __uint_as_float(s[0]) + __uint_as_float(s[1]);
  auto t = __builtin_amdgcn_permlane32_swap(__float_as_uint(x), __float_as_uint(x), false, false);
  return __uint_as_float(t[0]) + __uint_as_float(t[1]);
}
__device__ __forceinline__ float xrow_max(float x) {
  auto s = __builtin_amdgcn_permlane16_swap(__float_as_uint(x), __float_as_uint(x), false, false);
  x = fmaxf(__uint_as_float(s[0]), __uint_as_float(s[1]));
  auto t = __builtin_amdgcn_permlane32_swap(__float_as_uint(x), __float_as_uint(x), false, false);
  return fmaxf(__uint_as_float(t[0]), __uint_as_float(t[1]));
}
__device__ __forceinline__ float tr16(float a, float b) {
  auto s = __builtin_amdgcn_permlane16_swap(__float_as_uint(a), __float_as_uint(b), false, false);
  return __uint_as_float(s[0]) + __uint_as_float(s[1]);
}
__device__ __forceinline__ float tr32(float a, float b) {
  auto s = __builtin_amdgcn_permlane32_swap(__float_as_uint(a), __float_as_uint(b), false, false);
  return __uint_as_float(s[0]) + __uint_as_float(s[1]);
}
__device__ __forceinline__ float wave_sum(float x) {
  x += dpp<XOR1>(x); x += dpp<XOR2>(x); x += dpp<XOR7>(x); x += dpp<ROR8>(x);
  return xrow_sum(x);
}
__device__ __forceinline__ float wave_max(float x) {
  x = fmaxf(x, dpp<XOR1>(x)); x = fmaxf(x, dpp<XOR2>(x)); x = fmaxf(x, dpp<XOR7>(x)); x = fmaxf(x, dpp<ROR8>(x));
  return xrow_max(x);
}
__device__ __forceinline__ void lds_wait() { asm volatile("s_waitcnt lgkmcnt(0)" ::: "memory"); }
__device__ __forceinline__ void st_granule(gu64* g, unsigned tag, float v) {
  __hip_atomic_store(g, ((unsigned long long)tag << 32) | (unsigned long long)__float_as_uint(v), RLX_AGENT);
}
constexpr unsigned SPIN_TICKS = 20000000u;
__device__ __forceinline__ unsigned now32() { return (unsigned)__builtin_amdgcn_s_memrealtime(); }

struct Args {
  const unsigned short* x; const unsigned short* k; const unsigned short* v; const unsigned short* wq; const unsigned short* wo;
  const unsigned short* w1; const unsigned short* w3; const unsigned short* w2;
  unsigned short* out; unsigned char* ws;
};

#define STAMP(i) do { __builtin_amdgcn_sched_barrier(0); unsigned long long t_; asm volatile("s_memrealtime %0\n\ts_waitcnt lgkmcnt(0)" : "=s"(t_) :: "memory"); ts[i] = (unsigned)t_ | 1u; __builtin_amdgcn_sched_barrier(0); } while (0)
#define STAMP_FLUSH() do { const unsigned tg_ = dg_epoch + 0x9E3779B9u; if (lane == 0) { _Pragma("unroll") for (int i_ = 0; i_ < 16; ++i_) st_granule((gu64*)(a.ws + WS_STAMP) + (blockIdx.x * 16 + wave) * 16 + i_, tg_, __uint_as_float(ts[i_])); } \
  if (blockIdx.x == 0 && wave == 0) { \
    int mn0 = 1 << 30, mx = -(1 << 30), mn = 1 << 30; float sum = 0.f; \
    for (int i = 0; i < 4; ++i) for (int w = 0; w < 12; ++w) { \
      gu64* p = (gu64*)(a.ws + WS_STAMP) + ((lane * 4 + i) * 16 + w) * 16; unsigned long long g0, g1; \
      do { g0 = __hip_atomic_load(p, RLX_AGENT); g1 = __hip_atomic_load(p + DIAG_I, RLX_AGENT); } while (((unsigned)(g0 >> 32) != tg_ || (unsigned)(g1 >> 32) != tg_) && (unsigned)__builtin_amdgcn_s_memrealtime() - ts[0] < 1000000u); \
      mn0 = min(mn0, (int)((unsigned)g0 - ts[0])); \
      if (w >= DIAG_W0 && w < DIAG_W1) { const int d = (int)((unsigned)g1 - ts[0]); mx = max(mx, d); mn = min(mn, d); sum += (float)d; } \
    } \
    mn0 = -(int)wave_max((float)-mn0); mx = (int)wave_max((float)mx); mn = -(int)wave_max((float)-mn); sum = wave_sum(sum) / (256.f * (DIAG_W1 - DIAG_W0)); \
    const int st = DIAG == 0 ? 0 : DIAG == 1 ? (int)sum - mn0 : DIAG == 2 ? mx - mn0 : mn - mn0; \
    const int until = __builtin_amdgcn_readfirstlane(mn0 + 30000 + DIAG_S * st); \
    while ((int)((unsigned)__builtin_amdgcn_s_memrealtime() - ts[0]) < until) __builtin_amdgcn_s_sleep(1); \
  } } while (0)
#define STAMP_DECL unsigned ts[16] = {0, 0, 0, 0, 0, 0, 0, 0, 0, 0, 0, 0, 0, 0, 0, 0}

typedef __amdgpu_buffer_rsrc_t rsrc_t;
#define WPOL 0
#define NPRE 3
__device__ __forceinline__ u32x4 ldw(rsrc_t r, int voff, int soff) { return __builtin_amdgcn_raw_buffer_load_b128(r, voff, soff, WPOL); }
__device__ __forceinline__ u32x4 ldnt(rsrc_t r, int voff, int soff) { return __builtin_amdgcn_raw_buffer_load_b128(r, voff, soff, 2); }
#define TRK 0
#define WOD 2
#define QDELAY 32
#define DRY 1
#define FSLEEP 4
#define B0POS (PDIST + 1)
#define SFIRST 1
#define CODEPF 1
#define KMIX 0
#define GLATE 1
#define FDELAY 0
#define WIDE_T 3300
#define LAG_A 100
#define LAG_B 110
#define LAG_W 5
#define MSLEEP 16
#define Y16 0
#define FPIPE 0
#define YLD 16
#define YST 16
#define VDIST 4
#define PDIST KDIST
#define TRICKLE() __builtin_amdgcn_s_sleep(TRK)
#define HSYNC(n) do { \
    if (lane == 0) __hip_atomic_fetch_add((LAS unsigned*)lds + L_CNT + 1 + (n), 1u, __ATOMIC_RELAXED, __HIP_MEMORY_SCOPE_WORKGROUP); \
    while (__hip_atomic_load((LAS unsigned*)lds + L_CNT + 1 + (n), __ATOMIC_RELAXED, __HIP_MEMORY_SCOPE_WORKGROUP) < 8u) __builtin_amdgcn_s_sleep(0); \
    asm volatile("" ::: "memory"); \
  } while (0)
#define FRESH() int lane_ = lane0; asm volatile("" : "+v"(lane_)); \
  const int lane = lane_, r = lane >> 4, c = lane & 15, tid = wave * 64 + lane, voff = lane * 16; (void)r; (void)c; (void)tid; (void)voff
#define SB() __builtin_amdgcn_sched_barrier(0)
#define VM_DRAIN() do { SB(); asm volatile("s_waitcnt vmcnt(0)" ::: "memory"); SB(); } while (0)

__global__ void __launch_bounds__(NTHR, 3) fused_decode(Args a) {
  __shared__ __attribute__((aligned(16))) float lds[L_END];
  const int tid = threadIdx.x, lane = tid & 63, wave = __builtin_amdgcn_readfirstlane(tid >> 6);
  const int r = lane >> 4, c = lane & 15;
  const int xg = blockIdx.x & 7, j = blockIdx.x >> 3;
  const int b = j >> 4, h = xg * 4 + ((j >> 2) & 3), sp = j & 3;
  const int chunk = b * 128 + h * 4 + sp;
  const int voff = lane * 16;
  STAMP_DECL;
  STAMP(0);
  unsigned dg_epoch = *(const __attribute__((address_space(4))) unsigned*)(a.ws + WS_EPOCH);

  const int g8 = b * 4 + sp;
  const int qrow = g8 * 64 + wave * 8 + 2 * r, mrow = g8 * 64 + wave * 8 + (lane >> 3);
  u32x4 xv0, xv1, wq0, wq1;
  unsigned xq0, xq1, junk = 0;
  if (wave < 8) {
    xv0 = *(const GAS u32x4*)(a.x + lane * 8); xv1 = *(const GAS u32x4*)(a.x + DM + lane * 8);
    xq0 = *(const gu32*)(a.x + qrow); xq1 = *(const gu32*)(a.x + DM + qrow);
    const rsrc_t rq = __builtin_amdgcn_make_buffer_rsrc((void*)(a.wq + h * DH), 0, (DM * NH * DH - h * DH) * 2, 0x00020000);
    wq0 = ldnt(rq, qrow * (NH * DH * 2) + c * 16, 0);
    wq1 = ldnt(rq, qrow * (NH * DH * 2) + c * 16, NH * DH * 2);
    if (wave >= 6 && j < CODEPF) {
      unsigned long long pc; int len;
      asm volatile("s_getpc_b64 %0\n.Lfd_top:\n\ts_mov_b32 %1, .Lfd_end-.Lfd_top" : "=s"(pc), "=s"(len));
      junk = *(const gu32*)(pc + (unsigned)min(((wave - 6) * 64 + lane) * 128, max(len, 0)));
    }
  }
  int role = wave;
  asm volatile("" : "+s"(role));
  if (__builtin_expect(role >= 8, SFIRST)) {
    const int sw = wave - 8;
    constexpr int HB = SK * DH * 2;
    const unsigned short* kp = a.k + (size_t)(chunk >> 2) * SK * DH;
    const unsigned short* vp = a.v + (size_t)(chunk >> 2) * SK * DH;
    const rsrc_t rk = __builtin_amdgcn_make_buffer_rsrc((void*)kp, 0, HB, 0x00020000);
    const rsrc_t rv = __builtin_amdgcn_make_buffer_rsrc((void*)vp, 0, HB, 0x00020000);
    const int stag = j * KSTAG, voffh = voff + HB / 2;
#define ROWVO(n) ((((n) & 1) ? voffh : voff) + (((n) & 7) >> 1) * 1024)
#define ROWSO(pair, n) (((((pair) + ((n) >> 3) + stag) & 31) << 14) + (sw << 12) + (sp << 19))
#define LDROW(rs, pair, n) ldnt(rs, ROWVO(n), ROWSO(pair, n))
    __builtin_amdgcn_s_setprio(2);
    u32x4 buf[8], pre[NPRE * 8];
    STAMP(12);
    if (B0POS == 0) { __builtin_amdgcn_s_barrier(); SB(); }
    STAMP(13);
#pragma unroll
    for (int u = 0; u < NPRE * 8; ++u) {
      pre[u] = LDROW(rk, 0, u); SB();
      if (u + 1 == B0POS) { __builtin_amdgcn_s_barrier(); SB(); }
      if (u >= PDIST) { asm volatile("s_waitcnt vmcnt(%0)" :: "i"(PDIST)); SB(); }
      if (u == PDIST) STAMP(14);
      if ((u & 7) == 6 && u > 8) { TR((u >> 3) - 1); SB(); }
    }
#pragma unroll
    for (int u = 0; u < 8; ++u) {
      buf[u] = LDROW(rk, NPRE, u); SB();
      if (u < 7) { asm volatile("s_waitcnt vmcnt(%0)" :: "i"(PDIST)); SB(); }
    }
    TR(2);
    STAMP(1);
    __builtin_amdgcn_s_barrier();
    asm volatile("" ::: "memory");
    asm volatile("" : "+s"(dg_epoch));
    const u32x4 qp = *(const LAS u32x4*)(lds + L_Q + c * 4);
    STAMP(2);
    LAS float* S = (LAS float*)lds + L_S + sw * 1024;
    float mx = -INFINITY;
#define DOT(w) dot2((w)[3], qp[3], dot2((w)[2], qp[2], dot2((w)[1], qp[1], dot2((w)[0], qp[0], 0.f))))
#define K_FIN(tt) do { \
      _Pragma("unroll") for (int u = 0; u < 4; ++u) { const float lo = x[u] + dpp<ROR8>(x[u]), hi = x[u + 4] + dpp<ROR8>(x[u + 4]); x[u] = (c & 8) ? hi : lo; } \
      _Pragma("unroll") for (int u = 0; u < 2; ++u) { const float lo = x[u] + dpp<XOR7>(x[u]), hi = x[u + 2] + dpp<XOR7>(x[u + 2]); x[u] = (c & 4) ? hi : lo; } \
      { const float lo = x[0] + dpp<XOR2>(x[0]), hi = x[1] + dpp<XOR2>(x[1]); x[0] = (c & 2) ? hi : lo; } \
      float sc = x[0] + dpp<XOR1>(x[0]); \
      sc = rbf(rbf(sc) * SCALE); \
      mx = fmaxf(mx, sc); \
      S[(tt) * 32 + r * 8 + (c >> 1)] = sc; \
      SB(); \
    } while (0)
    static_assert(KDIST <= 6 && KDIST >= 3 && NPRE >= 1 && PDIST <= NPRE * 8, "ring of 8");
#define PACE() do { asm volatile("s_waitcnt vmcnt(%0)" :: "i"(((KMIX >> u) & 1) ? KDIST - 1 : KDIST)); SB(); } while (0)
    {
      float x[8];
#pragma unroll
      for (int u = 0; u < 8; ++u) {
        if (u > 0 && u <= KDIST + 1) { buf[u - 1] = LDROW(rk, NPRE + 1, u - 1); SB(); }
        x[u] = DOT(buf[u]);
        SB();
      }
      K_FIN(NPRE);
    }
#pragma unroll
    for (int pp = 0; pp < NPRE; ++pp) {
      float x[8];
#pragma unroll
      for (int w = 0; w < 8; ++w) x[w] = DOT(pre[pp * 8 + w]);
      K_FIN(pp);
    }
#pragma unroll
    for (int u = KDIST + 1; u < 7; ++u) { PACE(); buf[u] = LDROW(rk, NPRE + 1, u); SB(); }
    TR(3);
    int last = 31;
    asm volatile("" : "+s"(last));
#pragma nounroll
    for (int tt = NPRE + 1; tt <= last; ++tt) {
      const unsigned short* nb = tt == last ? vp : kp;
      const rsrc_t rn = __builtin_amdgcn_make_buffer_rsrc((void*)nb, 0, HB, 0x00020000);
      float x[8];
#pragma unroll
      for (int u = 0; u < 8; ++u) {
        const int n = u + 7;
        PACE();
        buf[n & 7] = LDROW(n >= 8 ? rn : rk, tt, n);
        SB();
        x[u] = DOT(buf[u]);
        SB();
      }
      K_FIN(tt);
      TR(tt);
    }
    STAMP(3);
    mx = wave_max(mx);
    if (lane == 0) lds[L_ML + sw] = mx;
    lds_wait();
    if (lane == 0) __hip_atomic_fetch_add((LAS unsigned*)lds + L_CNT, 1u, __ATOMIC_RELAXED, __HIP_MEMORY_SCOPE_WORKGROUP);
    STAMP(4);
    f32x2 acc[4] = {{0.f, 0.f}, {0.f, 0.f}, {0.f, 0.f}, {0.f, 0.f}};
    f32x4 s0 = *(const LAS f32x4*)(S + r * 8), s1;
    int el = -1, lag = -1;
#pragma nounroll
    for (int tt = 0; tt <= last; ++tt) {
      const rsrc_t rn = __builtin_amdgcn_make_buffer_rsrc((void*)vp, 0, tt == last ? 0 : HB, 0x00020000);
      unsigned ref, tnow;
#pragma unroll
      for (int u = 0; u < 8; ++u) {
        const int n = u + 7;
        const int so = ROWSO(tt, n);
        asm volatile("s_cmpk_gt_i32 %0, %4\n\ts_cbranch_scc1 1f\n\ts_waitcnt vmcnt(%2)\n\ts_cmp_gt_i32 %1, 0\n\ts_cbranch_scc1 1f\n\ts_waitcnt vmcnt(%3)\n1:"
                     :: "s"(el), "s"(lag), "i"(LAG_W), "i"(VDIST), "i"(WIDE_T), "s"(so) : "scc");
        SB();
        buf[n & 7] = ldnt(n >= 8 ? rn : rv, ROWVO(n), so);
        SB();
        if (u == 0) s1 = *(const LAS f32x4*)(S + tt * 32 + r * 8 + 4);
        if (u == 4) { s0 = *(const LAS f32x4*)(S + ((tt + 1) & 31) * 32 + r * 8); ref = *(volatile LAS unsigned*)(lds + L_REF); tnow = now32(); }
        const float p = __builtin_amdgcn_exp2f(((u < 4 ? s0[u & 3] : s1[u & 3]) - mx) * LOG2E);
#pragma unroll
        for (int k = 0; k < 4; ++k) acc[k] = __builtin_elementwise_fma(bf_unpack(buf[u][k]), f32x2{p, p}, acc[k]);
        asm volatile("" : "+v"(acc[0]), "+v"(acc[1]), "+v"(acc[2]), "+v"(acc[3]));
        if (u == 6) {
          el = (int)(tnow - __builtin_amdgcn_readfirstlane(ref));
          lag = el - (LAG_A + LAG_B / 2 + tt * LAG_B);
        }
        SB();
      }
      TR(32 + tt);
    }
    STAMP(5);
    *(LAS f32x4*)(lds + L_RED + (sw * 4 + r) * 128 + c * 8) = f32x4{acc[0][0], acc[0][1], acc[1][0], acc[1][1]};
    *(LAS f32x4*)(lds + L_RED + (sw * 4 + r) * 128 + c * 8 + 4) = f32x4{acc[2][0], acc[2][1], acc[3][0], acc[3][1]};
    lds_wait();
    __builtin_amdgcn_s_barrier();
    __builtin_amdgcn_s_barrier();
    if (blockIdx.x < NRED) __builtin_amdgcn_s_barrier();
    STAMP_FLUSH();
    return;
  }

  unsigned epoch = *(const __attribute__((address_space(4))) unsigned*)(a.ws + WS_EPOCH);
  SB();
  if (tid < 5) ((LAS unsigned*)lds)[L_CNT + tid] = tid < 4 ? 0u : (unsigned)__builtin_amdgcn_s_memrealtime() + 0x40000000u;
  lds_wait();
  __builtin_amdgcn_s_barrier();
  SB();
  float mb0, mb1, sd0, sd1;
  {
    float f0[8], f1[8];
#pragma unroll
    for (int k = 0; k < 4; ++k) { f0[2 * k] = bf_lo(xv0[k]); f0[2 * k + 1] = bf_hi(xv0[k]); f1[2 * k] = bf_lo(xv1[k]); f1[2 * k + 1] = bf_hi(xv1[k]); }
    float s0 = 0.f, s1 = 0.f;
#pragma unroll
    for (int k = 0; k < 8; ++k) { s0 += f0[k]; s1 += f1[k]; }
    const float m0 = wave_sum(s0) * (1.f / DM), m1 = wave_sum(s1) * (1.f / DM);
    float v0 = 0.f, v1 = 0.f;
#pragma unroll
    for (int k = 0; k < 8; ++k) { const float d0 = f0[k] - m0, d1 = f1[k] - m1; v0 = fmaf(d0, d0, v0); v1 = fmaf(d1, d1, v1); }
    v0 = wave_sum(v0) * (1.f / DM); v1 = wave_sum(v1) * (1.f / DM);
    mb0 = rbf(m0); sd0 = rbf(__builtin_sqrtf(rbf(rbf(v0) + 1e-5f)));
    mb1 = rbf(m1); sd1 = rbf(__builtin_sqrtf(rbf(rbf(v1) + 1e-5f)));
  }
#define XN(xf, mb, sd) rbf(rbf((xf) - (mb)) / (sd))
  asm volatile("" : "+s"(epoch));
  asm volatile("" : "+s"(dg_epoch));
  const unsigned tag = epoch + 0x9E3779B9u;
  STAMP(10);
  {
    const float a0 = XN(bf_lo(xq0), mb0, sd0), a1 = XN(bf_hi(xq0), mb0, sd0), b0 = XN(bf_lo(xq1), mb1, sd1), b1 = XN(bf_hi(xq1), mb1, sd1);
    float t[8];
#pragma unroll
    for (int k = 0; k < 4; ++k) {
      const f32x2 w0 = bf_unpack(wq0[k]), w1 = bf_unpack(wq1[k]);
      const f32x2 qa = __builtin_elementwise_fma(w1, f32x2{a1, a1}, w0 * a0), qb = __builtin_elementwise_fma(w1, f32x2{b1, b1}, w0 * b0);
      t[2 * k] = tr32(qa[0], qb[0]); t[2 * k + 1] = tr32(qa[1], qb[1]);
    }
    f32x4 o;
#pragma unroll
    for (int k = 0; k < 4; ++k) o[k] = tr16(t[k], t[4 + k]);
    *(LAS f32x4*)(lds + L_Y + wave * 256 + (r >> 1) * 128 + c * 8 + (r & 1) * 4) = o;
    lds_wait();
  }
  STAMP(11);
  HSYNC(0);
  STAMP(12);
  gu64* Q = (gu64*)(a.ws + WS_Q) + h * 2048;
  if (tid < 256) {
    const LAS float* P = (const LAS float*)lds + L_Y + tid;
    st_granule(Q + g8 * 256 + tid, tag, ((P[0] + P[256]) + (P[512] + P[768])) + ((P[1024] + P[1280]) + (P[1536] + P[1792])));
  }
  STAMP(9);
  asm volatile("" :: "v"(junk));
  if (wave == 7) {
    __builtin_amdgcn_s_sleep(QDELAY);
    const rsrc_t rg = __builtin_amdgcn_make_buffer_rsrc((void*)(a.ws + WS_Q + (size_t)h * 16384 + b * 1024), 0, 16384, 0x00020000);
    const unsigned t0 = now32();
    u32x4 g[8];
    while (true) {
      bool ok = true;
#pragma unroll
      for (int i = 0; i < 8; ++i) g[i] = __builtin_amdgcn_raw_buffer_load_b128(rg, voff, i * 2048, 16);
#pragma unroll
      for (int i = 0; i < 8; ++i) ok = ok & (g[i][1] == tag) & (g[i][3] == tag);
      if (__all(ok)) break;
      if (now32() - t0 > SPIN_TICKS) break;
      __builtin_amdgcn_s_sleep(8);
      asm volatile("" ::: "memory");
    }
    f32x2 q2[8];
#pragma unroll
    for (int i = 0; i < 8; ++i) q2[i] = f32x2{__uint_as_float(g[i][0]), __uint_as_float(g[i][2])};
    const f32x2 qs = ((q2[0] + q2[1]) + (q2[2] + q2[3])) + ((q2[4] + q2[5]) + (q2[6] + q2[7]));
    ((LAS unsigned*)lds)[L_Q + lane] = pack_bf16(qs[0], qs[1]);
    lds_wait();
  }
  STAMP(1);
  __builtin_amdgcn_s_barrier();
  asm volatile("" ::: "memory");
  gu64* G = (gu64*)(a.ws + WS_G);
  float gsum = 0.f;
  const int red = blockIdx.x < NRED ? (int)blockIdx.x : -1;
  const unsigned xw = (red >= 0 && tid < 8) ? *(const gu32*)(a.x + (red & 1) * DM + (red >> 1) * 16 + tid * 2) : 0u;
  if (h < 28) {
    const unsigned short xm0 = a.x[mrow], xm1 = a.x[DM + mrow];
    const rsrc_t r1 = __builtin_amdgcn_make_buffer_rsrc((void*)(a.w1 + h * 64), 0, (DM * FF - h * 64) * 2, 0x00020000);
    const rsrc_t r3 = __builtin_amdgcn_make_buffer_rsrc((void*)(a.w3 + h * 64), 0, (DM * FF - h * 64) * 2, 0x00020000);
    const u32x4 wg = ldnt(r1, mrow * (FF * 2) + (lane & 7) * 16, 0);
    const u32x4 wu = ldnt(r3, mrow * (FF * 2) + (lane & 7) * 16, 0);
    const float a0 = XN(bf_lo(xm0), mb0, sd0), b0 = XN(bf_lo(xm1), mb1, sd1);
    float t[8];
#pragma unroll
    for (int k = 0; k < 4; ++k) {
      const f32x2 g = bf_unpack(wg[k]), u = bf_unpack(wu[k]);
#pragma unroll
      for (int e = 0; e < 2; ++e) t[2 * k + e] = tr16(tr32(g[e] * a0, g[e] * b0), tr32(u[e] * a0, u[e] * b0));
    }
    f32x4 o;
#pragma unroll
    for (int k = 0; k < 4; ++k) { const float lo = t[k] + dpp<ROR8>(t[k]), hi = t[4 + k] + dpp<ROR8>(t[4 + k]); o[k] = (c & 8) ? hi : lo; }
    *(LAS f32x4*)(lds + L_Y + 2048 + wave * 256 + r * 64 + (c & 7) * 8 + (c >> 3) * 4) = o;
    lds_wait();
    HSYNC(1);
    const LAS float* P = (const LAS float*)lds + L_Y + 2048 + (tid & 255);
    gsum = ((P[0] + P[256]) + (P[512] + P[768])) + ((P[1024] + P[1280]) + (P[1536] + P[1792]));
  }
  const int lane0 = lane;
  int pass = DRY ? 0 : 1;
  asm volatile("" : "+s"(pass));
#pragma nounroll
  for (; pass < 2; ++pass) {
    f32x2 wf[64];
    f32x2 yacc[4] = {{0.f, 0.f}, {0.f, 0.f}, {0.f, 0.f}, {0.f, 0.f}};
    if (!pass) {
#pragma unroll
      for (int i = 0; i < 64; ++i) asm volatile("" : "=v"(wf[i]));
    } else {
      {
        FRESH();
        const rsrc_t ro = __builtin_amdgcn_make_buffer_rsrc((void*)(a.wo + (size_t)h * DH * DM), 0, DH * DM * 2, 0x00020000);
#pragma unroll
        for (int i0 = 0; i0 < 16; i0 += WOD) {
          u32x4 w[WOD];
#pragma unroll
          for (int i = 0; i < WOD; ++i) w[i] = ldw(ro, voff + ((i0 + i) & 3) * 1024, (wave * 16 + ((i0 + i) & ~3)) * 1024);
          VM_DRAIN();
#pragma unroll
          for (int i = 0; i < WOD; ++i)
#pragma unroll
            for (int k = 0; k < 4; ++k) { wf[(i0 + i) * 4 + k] = bf_unpack(w[i][k]); asm volatile("" : "+v"(wf[(i0 + i) * 4 + k])); }
          TRICKLE();
        }
      }
      STAMP(2);
      while (__hip_atomic_load((LAS unsigned*)lds + L_CNT, __ATOMIC_RELAXED, __HIP_MEMORY_SCOPE_WORKGROUP) < 4u) __builtin_amdgcn_s_sleep(8);
      asm volatile("" ::: "memory");
      gu64* ml = (gu64*)(a.ws + WS_ML);
      float mw = 0.f;
      const unsigned tk = (unsigned)__builtin_amdgcn_s_memrealtime();
      if (wave == 7) {
        FRESH();
        mw = lds[L_ML + (lane & 3)];
        float m = fmaxf(mw, dpp<XOR1>(mw)); m = fmaxf(m, dpp<XOR2>(m));
        float l = 0.f;
#pragma unroll 4
        for (int i = 0; i < 16; ++i) {
          const f32x4 sv = *(const LAS f32x4*)(lds + L_S + i * 256 + lane * 4);
#pragma unroll
          for (int k = 0; k < 4; ++k) l += __builtin_amdgcn_exp2f((sv[k] - m) * LOG2E);
        }
        l = wave_sum(l);
        if (lane < 16) st_granule(ml + chunk * MLS + lane, tag, lane == 0 ? m : lane == 1 ? l : __uint_as_float(tk));
      }
      else if (wave < 4 && h < 28) { FRESH(); __builtin_amdgcn_s_sleep(16); st_granule(G + (h * 8 + g8) * 256 + tid, tag, gsum); }
      __builtin_amdgcn_s_sleep(127); __builtin_amdgcn_s_sleep(MSLEEP);
      if (wave < 7) {
        FRESH();
        const int f0 = (h * 4 + sp) * 14 + wave * 2;
        const rsrc_t r2 = __builtin_amdgcn_make_buffer_rsrc((void*)(a.w2 + (size_t)f0 * DM), 0, 2 * DM * 2, 0x00020000);
        const u32x4 w20 = ldw(r2, voff, 0), w21 = ldw(r2, voff + 1024, 0);
        const int f = f0 + (r & 1);
        gu64* gp = G + ((f >> 6) * 8 + (c & 7)) * 256 + b * 128 + (c >> 3) * 64 + (f & 63);
        const unsigned t0 = now32();
        unsigned long long g;
        while (true) {
          g = __hip_atomic_load(gp, RLX_AGENT);
          if (__all((unsigned)(g >> 32) == tag)) break;
          if (now32() - t0 > SPIN_TICKS) break;
          __builtin_amdgcn_s_sleep(127);
        }
        float s = __uint_as_float((unsigned)g);
        s += dpp<XOR1>(s); s += dpp<XOR2>(s); s += dpp<XOR7>(s);
        const float gs = rbf(s), us = rbf(dpp<ROR8>(s));
        const float si = rbf(gs / (1.f + __expf(-gs)));
        const float hv = rbf(si * us);
        const float h0 = __builtin_bit_cast(float, __builtin_amdgcn_readlane(__builtin_bit_cast(int, hv), 0));
        const float h1 = __builtin_bit_cast(float, __builtin_amdgcn_readlane(__builtin_bit_cast(int, hv), 16));
#pragma unroll
        for (int k = 0; k < 4; ++k) yacc[k] = __builtin_elementwise_fma(bf_unpack(w21[k]), f32x2{h1, h1}, bf_unpack(w20[k]) * h0);
      } else {
        FRESH();
        {
          const unsigned long long gt = __hip_atomic_load(ml + (lane * 4 + (lane & 3)) * MLS + 2, RLX_AGENT);
          const bool v = (unsigned)(gt >> 32) == tag;
          const float n = wave_sum(v ? 1.f : 0.f), d = wave_sum(v ? (float)(int)((unsigned)gt - tk) : 0.f);
          if (lane == 0 && n > 0.f) ((LAS unsigned*)lds)[L_REF] = tk + (unsigned)(int)(d / n);
        }
        const unsigned t0 = now32();
        unsigned long long gm, gl;
        while (true) {
          gm = __hip_atomic_load(ml + ((chunk & ~3) + (lane & 3)) * MLS, RLX_AGENT);
          gl = __hip_atomic_load(ml + ((chunk & ~3) + (lane & 3)) * MLS + 1, RLX_AGENT);
          if (__all((unsigned)(gm >> 32) == tag && (unsigned)(gl >> 32) == tag)) break;
          if (now32() - t0 > SPIN_TICKS) break;
          __builtin_amdgcn_s_sleep(127);
        }
        const float ms = __uint_as_float((unsigned)gm), ls = __uint_as_float((unsigned)gl);
        float M = fmaxf(ms, dpp<XOR1>(ms)); M = fmaxf(M, dpp<XOR2>(M));
        float L = ls * __builtin_amdgcn_exp2f((ms - M) * LOG2E);
        L += dpp<XOR1>(L); L += dpp<XOR2>(L);
        if (lane < 4) lds[L_FW + lane] = __builtin_amdgcn_exp2f((mw - M) * LOG2E) / L;
        lds_wait();
        if (blockIdx.x == NWG - 1) {
          while (true) {
            bool ok = true;
#pragma unroll
            for (int i = 0; i < 4; ++i) ok = ok & ((unsigned)(__hip_atomic_load(ml + (i * 64 + lane) * MLS + 2, RLX_AGENT) >> 32) == tag);
            if (__all(ok)) break;
            if (now32() - t0 > SPIN_TICKS) break;
            __builtin_amdgcn_s_sleep(127);
          }
          if (lane == 0) __hip_atomic_store((gu32*)(a.ws + WS_EPOCH), tag, RLX_AGENT);
        }
        STAMP(3);
      }
    }
    FRESH();
    if (pass) __builtin_amdgcn_s_barrier();
    asm volatile("" ::: "memory");
    STAMP(6);
    {
      const LAS float* P = (const LAS float*)lds + L_RED + (4 * r) * 128 + wave * 16 + c;
      float o = ((P[0] + P[128]) + (P[256] + P[384])) * lds[L_FW + r];
      o = xrow_sum(o);
#pragma unroll
      for (int i = 0; i < 16; ++i) {
        const float oi = __builtin_bit_cast(float, __builtin_amdgcn_readlane(__builtin_bit_cast(int, o), i));
#pragma unroll
        for (int k = 0; k < 4; ++k) yacc[k] = __builtin_elementwise_fma(wf[i * 4 + k], f32x2{oi, oi}, yacc[k]);
      }
    }
    *(LAS f32x4*)(lds + L_Y2 + wave * 512 + lane * 8) = f32x4{yacc[0][0], yacc[0][1], yacc[1][0], yacc[1][1]};
    *(LAS f32x4*)(lds + L_Y2 + wave * 512 + lane * 8 + 4) = f32x4{yacc[2][0], yacc[2][1], yacc[3][0], yacc[3][1]};
    lds_wait();
    if (pass) __builtin_amdgcn_s_barrier();
    asm volatile("" ::: "memory");
    const rsrc_t ry = __builtin_amdgcn_make_buffer_rsrc((void*)(a.ws + WS_Y), 0, pass ? 1 << 20 : 0, 0x00020000);
    {
      const LAS float* P = (const LAS float*)lds + L_Y2 + tid;
      const float y = ((P[0] + P[512]) + (P[1024] + P[1536])) + ((P[2048] + P[2560]) + (P[3072] + P[3584]));
      __builtin_amdgcn_raw_buffer_store_b64(u32x2{__float_as_uint(y), tag}, ry, (tid >> 4) * 32768 + (tid & 15) * 8, chunk * 128, 16);
    }
    STAMP(7);
    if (red >= 0) {
      __builtin_amdgcn_s_sleep(FDELAY);
      const unsigned t0 = now32();
      u32x4 g0 = {0u, 0u, 0u, 0u}, g1 = {0u, 0u, 0u, 0u};
      bool ok0 = false, ok1 = false;
      while (true) {
        if (!ok0) g0 = __builtin_amdgcn_raw_buffer_load_b128(ry, voff, red * 16384 + wave * 2048, YLD);
        if (!ok1) g1 = __builtin_amdgcn_raw_buffer_load_b128(ry, voff + 1024, red * 16384 + wave * 2048, YLD);
        ok0 = (g0[1] == tag) & (g0[3] == tag); ok1 = (g1[1] == tag) & (g1[3] == tag);
        if (__all(ok0 & ok1) || !pass) break;
        if (now32() - t0 > SPIN_TICKS) break;
        __builtin_amdgcn_s_sleep(FSLEEP);
        asm volatile("" ::: "memory");
      }
      float s = tr32(__uint_as_float(g0[0]) + __uint_as_float(g1[0]), __uint_as_float(g0[2]) + __uint_as_float(g1[2]));
      s = tr16(s, s);
      s += dpp<ROR8>(s);
      if ((lane & 24) == 0) lds[L_FIN + wave * 16 + (lane & 7) * 2 + (lane >> 5)] = s;
      lds_wait();
      if (pass) __builtin_amdgcn_s_barrier();
      asm volatile("" ::: "memory");
      if (tid < 8) {
        const LAS f32x2* P = (const LAS f32x2*)(lds + L_FIN + tid * 2);
        const f32x2 o = bf_unpack(xw) + (((P[0] + P[8]) + (P[16] + P[24])) + ((P[32] + P[40]) + (P[48] + P[56])));
        const rsrc_t rout = __builtin_amdgcn_make_buffer_rsrc((void*)a.out, 0, pass ? 2 * DM * 2 : 0, 0x00020000);
        __builtin_amdgcn_raw_buffer_store_b32(pack_bf16(o[0], o[1]), rout, tid * 4, (red & 1) * (DM * 2) + (red >> 1) * 32, 0);
      }
    }
  }
  asm volatile(".Lfd_end:");
  STAMP(8);
  STAMP_FLUSH();
}

}

extern "C" void kernel_launch(void* const* d_in, const int* in_sizes, int n_in, void* d_out, int out_size, void* d_ws, size_t ws_size,
                              hipStream_t stream) {
  Args a;
  a.x = (const unsigned short*)d_in[0]; a.k = (const unsigned short*)d_in[1]; a.v = (const unsigned short*)d_in[2];
  a.wq = (const unsigned short*)d_in[3]; a.wo = (const unsigned short*)d_in[4];
  a.w1 = (const unsigned short*)d_in[6]; a.w3 = (const unsigned short*)d_in[7]; a.w2 = (const unsigned short*)d_in[8];
  a.out = (unsigned short*)d_out; a.ws = (unsigned char*)d_ws;
  fused_decode<<<NWG, NTHR, 0, stream>>>(a);
}
```

```cpp
#define PRETOUCH 1
#include <hip/hip_runtime.h>
#include <hip/hip_bf16.h>
#include <stdint.h>

#define KDIST 4
#define KSTAG 1
#define MIMIC 1
#define STAMPS 0

namespace {

constexpr int NWG = 256, NTHR = 768, NRED = 64;
constexpr int DM = 512, NH = 32, SK = 16384, DH = 128, FF = 1792;
constexpr float LOG2E = 1.4426950408889634f;
constexpr float SCALE = 0.08838834764831845f;
#define MLS 16
constexpr size_t WS_EPOCH = 0, WS_ML = 4u << 20;
constexpr size_t WS_Y = 65536;
constexpr size_t WS_Q = WS_Y + (1u << 20);
constexpr size_t WS_G = WS_Q + (512u << 10);
constexpr size_t WS_STAMP = 8u << 20;
constexpr int L_S = 0;
constexpr int L_RED = L_S + 4096;
constexpr int L_Y = L_RED + 2048;
constexpr int L_Y2 = L_Y + 4096;
constexpr int L_Q = L_Y2 + 4096;
constexpr int L_ML = L_Q + 64;
constexpr int L_FW = L_ML + 8;
constexpr int L_FIN = L_FW + 4;
constexpr int L_CNT = L_FIN + 128;
constexpr int L_REF = L_CNT + 4;
constexpr int L_END = L_REF + 4;
#define TR(i) ((void)0)

#define GAS __attribute__((address_space(1)))
#define LAS __attribute__((address_space(3)))
#define RLX_AGENT __ATOMIC_RELAXED, __HIP_MEMORY_SCOPE_AGENT
typedef unsigned u32x4 __attribute__((ext_vector_type(4)));
typedef unsigned u32x2 __attribute__((ext_vector_type(2)));
typedef float f32x2 __attribute__((ext_vector_type(2)));
typedef float f32x4 __attribute__((ext_vector_type(4)));
typedef __bf16 bf16x2 __attribute__((ext_vector_type(2)));
typedef GAS unsigned gu32;
typedef GAS unsigned long long gu64;

__device__ __forceinline__ float bf_lo(unsigned u) { return __builtin_bit_cast(float, u << 16); }
__device__ __forceinline__ float bf_hi(unsigned u) { return __builtin_bit_cast(float, u & 0xffff0000u); }
__device__ __forceinline__ f32x2 bf_unpack(unsigned u) { return f32x2{bf_lo(u), bf_hi(u)}; }
__device__ __forceinline__ unsigned pack_bf16(float lo, float hi) {
  bf16x2 r = {(__bf16)lo, (__bf16)hi};
  return __builtin_bit_cast(unsigned, r);
}
__device__ __forceinline__ float rbf(float x) {
  return (float)(__bf16)x;
}
__device__ __forceinline__ float dot2(unsigned a, unsigned b, float acc) {
  return __builtin_amdgcn_fdot2_f32_bf16(__builtin_bit_cast(bf16x2, a), __builtin_bit_cast(bf16x2, b), acc, false);
}
template <int CTRL> __device__ __forceinline__ float dpp(float x) {
  return __builtin_bit_cast(float, __builtin_amdgcn_mov_dpp(__builtin_bit_cast(int, x), CTRL, 0xf, 0xf, true));
}
constexpr int XOR1 = 0xB1, XOR2 = 0x4E, XOR7 = 0x141, ROR2 = 0x122, ROR4 = 0x124, ROR8 = 0x128;
__device__ __forceinline__ float xrow_sum(float x) {
  auto s = __builtin_amdgcn_permlane16_swap(__float_as_uint(x), __float_as_uint(x), false, false);
  x = __uint_as_float(s[0]) + __uint_as_float(s[1]);
  auto t = __builtin_amdgcn_permlane32_swap(__float_as_uint(x), __float_as_uint(x), false, false);
  return __uint_as_float(t[0]) + __uint_as_float(t[1]);
}
__device__ __forceinline__ float xrow_max(float x) {
  auto s = __builtin_amdgcn_permlane16_swap(__float_as_uint(x), __float_as_uint(x), false, false);
  x = fmaxf(__uint_as_float(s[0]), __uint_as_float(s[1]));
  auto t = __builtin_amdgcn_permlane32_swap(__float_as_uint(x), __float_as_uint(x), false, false);
  return fmaxf(__uint_as_float(t[0]), __uint_as_float(t[1]));
}
__device__ __forceinline__ float tr16(float a, float b) {
  auto s = __builtin_amdgcn_permlane16_swap(__float_as_uint(a), __float_as_uint(b), false, false);
  return __uint_as_float(s[0]) + __uint_as_float(s[1]);
}
__device__ __forceinline__ float tr32(float a, float b) {
  auto s = __builtin_amdgcn_permlane32_swap(__float_as_uint(a), __float_as_uint(b), false, false);
  return __uint_as_float(s[0]) + __uint_as_float(s[1]);
}
__device__ __forceinline__ float wave_sum(float x) {
  x += dpp<XOR1>(x); x += dpp<XOR2>(x); x += dpp<XOR7>(x); x += dpp<ROR8>(x);
  return xrow_sum(x);
}
__device__ __forceinline__ float wave_max(float x) {
  x = fmaxf(x, dpp<XOR1>(x)); x = fmaxf(x, dpp<XOR2>(x)); x = fmaxf(x, dpp<XOR7>(x)); x = fmaxf(x, dpp<ROR8>(x));
  return xrow_max(x);
}
__device__ __forceinline__ void lds_wait() { asm volatile("s_waitcnt lgkmcnt(0)" ::: "memory"); }
__device__ __forceinline__ void st_granule(gu64* g, unsigned tag, float v) {
  __hip_atomic_store(g, ((unsigned long long)tag << 32) | (unsigned long long)__float_as_uint(v), RLX_AGENT);
}
constexpr unsigned SPIN_TICKS = 20000000u;
__device__ __forceinline__ unsigned now32() { return (unsigned)__builtin_amdgcn_s_memrealtime(); }

struct Args {
  const unsigned short* x; const unsigned short* k; const unsigned short* v; const unsigned short* wq; const unsigned short* wo;
  const unsigned short* w1; const unsigned short* w3; const unsigned short* w2;
  unsigned short* out; unsigned char* ws;
};

#define STAMP(i) do { } while (0)
#define STAMP_FLUSH() do { } while (0)
#define STAMP_DECL

typedef __amdgpu_buffer_rsrc_t rsrc_t;
#define WPOL 0
#define NPRE 3
__device__ __forceinline__ u32x4 ldw(rsrc_t r, int voff, int soff) { return __builtin_amdgcn_raw_buffer_load_b128(r, voff, soff, WPOL); }
__device__ __forceinline__ u32x4 ldnt(rsrc_t r, int voff, int soff) { return __builtin_amdgcn_raw_buffer_load_b128(r, voff, soff, 2); }
#define TRK 0
#define WOD 2
#define QDELAY 32
#define DRY 1
#define FSLEEP 4
#define B0POS (PDIST + 1)
#define SFIRST 1
#define CODEPF 1
#define KMIX 0
#define GLATE 1
#define FDELAY 0
#define WIDE_T 3300
#define LAG_A 100
#define LAG_B 110
#define LAG_W 5
#define MSLEEP 16
#define Y16 0
#define FPIPE 0
#define YLD 16
#define YST 16
#define VDIST 4
#define PDIST KDIST
#define TRICKLE() __builtin_amdgcn_s_sleep(TRK)
#define HSYNC(n) do { \
    if (lane == 0) __hip_atomic_fetch_add((LAS unsigned*)lds + L_CNT + 1 + (n), 1u, __ATOMIC_RELAXED, __HIP_MEMORY_SCOPE_WORKGROUP); \
    while (__hip_atomic_load((LAS unsigned*)lds + L_CNT + 1 + (n), __ATOMIC_RELAXED, __HIP_MEMORY_SCOPE_WORKGROUP) < 8u) __builtin_amdgcn_s_sleep(0); \
    asm volatile("" ::: "memory"); \
  } while (0)
#define FRESH() int lane_ = lane0; asm volatile("" : "+v"(lane_)); \
  const int lane = lane_, r = lane >> 4, c = lane & 15, tid = wave * 64 + lane, voff = lane * 16; (void)r; (void)c; (void)tid; (void)voff
#define SB() __builtin_amdgcn_sched_barrier(0)
#define VM_DRAIN() do { SB(); asm volatile("s_waitcnt vmcnt(0)" ::: "memory"); SB(); } while (0)

__global__ void __launch_bounds__(NTHR, 3) fused_decode(Args a) {
  __shared__ __attribute__((aligned(16))) float lds[L_END];
  const int tid = threadIdx.x, lane = tid & 63, wave = __builtin_amdgcn_readfirstlane(tid >> 6);
  const int r = lane >> 4, c = lane & 15;
  const int xg = blockIdx.x & 7, j = blockIdx.x >> 3;
  const int b = j >> 4, h = xg * 4 + ((j >> 2) & 3), sp = j & 3;
  const int chunk = b * 128 + h * 4 + sp;
  const int voff = lane * 16;
  STAMP_DECL;
  STAMP(0);

  const int g8 = b * 4 + sp;
  const int qrow = g8 * 64 + wave * 8 + 2 * r, mrow = g8 * 64 + wave * 8 + (lane >> 3);
  u32x4 xv0, xv1, wq0, wq1;
  unsigned xq0, xq1, junk = 0;
  if (wave < 8) {
    xv0 = *(const GAS u32x4*)(a.x + lane * 8); xv1 = *(const GAS u32x4*)(a.x + DM + lane * 8);
    xq0 = *(const gu32*)(a.x + qrow); xq1 = *(const gu32*)(a.x + DM + qrow);
    const rsrc_t rq = __builtin_amdgcn_make_buffer_rsrc((void*)(a.wq + h * DH), 0, (DM * NH * DH - h * DH) * 2, 0x00020000);
    wq0 = ldnt(rq, qrow * (NH * DH * 2) + c * 16, 0);
    wq1 = ldnt(rq, qrow * (NH * DH * 2) + c * 16, NH * DH * 2);
    if (wave >= 6 && j < CODEPF) {
      unsigned long long pc; int len;
      asm volatile("s_getpc_b64 %0\n.Lfd_top:\n\ts_mov_b32 %1, .Lfd_end-.Lfd_top" : "=s"(pc), "=s"(len));
      junk = *(const gu32*)(pc + (unsigned)min(((wave - 6) * 64 + lane) * 128, max(len, 0)));
    }
  }
  int role = wave;
  asm volatile("" : "+s"(role));
  if (__builtin_expect(role >= 8, SFIRST)) {
    const int sw = wave - 8;
    constexpr int HB = SK * DH * 2;
    const unsigned short* kp = a.k + (size_t)(chunk >> 2) * SK * DH;
    const unsigned short* vp = a.v + (size_t)(chunk >> 2) * SK * DH;
    const rsrc_t rk = __builtin_amdgcn_make_buffer_rsrc((void*)kp, 0, HB, 0x00020000);
    const rsrc_t rv = __builtin_amdgcn_make_buffer_rsrc((void*)vp, 0, HB, 0x00020000);
    const int stag = j * KSTAG, voffh = voff + HB / 2;
#define ROWVO(n) ((((n) & 1) ? voffh : voff) + (((n) & 7) >> 1) * 1024)
#define ROWSO(pair, n) (((((pair) + ((n) >> 3) + stag) & 31) << 14) + (sw << 12) + (sp << 19))
#define LDROW(rs, pair, n) ldnt(rs, ROWVO(n), ROWSO(pair, n))
    __builtin_amdgcn_s_setprio(2);
    u32x4 buf[8], pre[NPRE * 8];
    STAMP(12);
    if (B0POS == 0) { __builtin_amdgcn_s_barrier(); SB(); }
    STAMP(13);
#pragma unroll
    for (int u = 0; u < NPRE * 8; ++u) {
      pre[u] = LDROW(rk, 0, u); SB();
      if (u + 1 == B0POS) { __builtin_amdgcn_s_barrier(); SB(); }
      if (u >= PDIST) { asm volatile("s_waitcnt vmcnt(%0)" :: "i"(PDIST)); SB(); }
      if (u == PDIST) STAMP(14);
      if ((u & 7) == 6 && u > 8) { TR((u >> 3) - 1); SB(); }
    }
#pragma unroll
    for (int u = 0; u < 8; ++u) {
      buf[u] = LDROW(rk, NPRE, u); SB();
      if (u < 7) { asm volatile("s_waitcnt vmcnt(%0)" :: "i"(PDIST)); SB(); }
    }
    TR(2);
    STAMP(1);
    __builtin_amdgcn_s_barrier();
    asm volatile("" ::: "memory");
    const u32x4 qp = *(const LAS u32x4*)(lds + L_Q + c * 4);
    STAMP(2);
    LAS float* S = (LAS float*)lds + L_S + sw * 1024;
    float mx = -INFINITY;
#define DOT(w) dot2((w)[3], qp[3], dot2((w)[2], qp[2], dot2((w)[1], qp[1], dot2((w)[0], qp[0], 0.f))))
#define K_FIN(tt) do { \
      _Pragma("unroll") for (int u = 0; u < 4; ++u) { const float lo = x[u] + dpp<ROR8>(x[u]), hi = x[u + 4] + dpp<ROR8>(x[u + 4]); x[u] = (c & 8) ? hi : lo; } \
      _Pragma("unroll") for (int u = 0; u < 2; ++u) { const float lo = x[u] + dpp<XOR7>(x[u]), hi = x[u + 2] + dpp<XOR7>(x[u + 2]); x[u] = (c & 4) ? hi : lo; } \
      { const float lo = x[0] + dpp<XOR2>(x[0]), hi = x[1] + dpp<XOR2>(x[1]); x[0] = (c & 2) ? hi : lo; } \
      float sc = x[0] + dpp<XOR1>(x[0]); \
      sc = rbf(rbf(sc) * SCALE); \
      mx = fmaxf(mx, sc); \
      S[(tt) * 32 + r * 8 + (c >> 1)] = sc; \
      SB(); \
    } while (0)
    static_assert(KDIST <= 6 && KDIST >= 3 && NPRE >= 1 && PDIST <= NPRE * 8, "ring of 8");
#define PACE() do { asm volatile("s_waitcnt vmcnt(%0)" :: "i"(((KMIX >> u) & 1) ? KDIST - 1 : KDIST)); SB(); } while (0)
    {
      float x[8];
#pragma unroll
      for (int u = 0; u < 8; ++u) {
        if (u > 0 && u <= KDIST + 1) { buf[u - 1] = LDROW(rk, NPRE + 1, u - 1); SB(); }
        x[u] = DOT(buf[u]);
        SB();
      }
      K_FIN(NPRE);
    }
#pragma unroll
    for (int pp = 0; pp < NPRE; ++pp) {
      float x[8];
#pragma unroll
      for (int w = 0; w < 8; ++w) x[w] = DOT(pre[pp * 8 + w]);
      K_FIN(pp);
    }
#pragma unroll
    for (int u = KDIST + 1; u < 7; ++u) { PACE(); buf[u] = LDROW(rk, NPRE + 1, u); SB(); }
    TR(3);
    int last = 31;
    asm volatile("" : "+s"(last));
#pragma nounroll
    for (int tt = NPRE + 1; tt <= last; ++tt) {
      const unsigned short* nb = tt == last ? vp : kp;
      const rsrc_t rn = __builtin_amdgcn_make_buffer_rsrc((void*)nb, 0, HB, 0x00020000);
      float x[8];
#pragma unroll
      for (int u = 0; u < 8; ++u) {
        const int n = u + 7;
        PACE();
        buf[n & 7] = LDROW(n >= 8 ? rn : rk, tt, n);
        SB();
        x[u] = DOT(buf[u]);
        SB();
      }
      K_FIN(tt);
      TR(tt);
    }
    STAMP(3);
    mx = wave_max(mx);
    if (lane == 0) lds[L_ML + sw] = mx;
    lds_wait();
    if (lane == 0) __hip_atomic_fetch_add((LAS unsigned*)lds + L_CNT, 1u, __ATOMIC_RELAXED, __HIP_MEMORY_SCOPE_WORKGROUP);
    STAMP(4);
    f32x2 acc[4] = {{0.f, 0.f}, {0.f, 0.f}, {0.f, 0.f}, {0.f, 0.f}};
    f32x4 s0 = *(const LAS f32x4*)(S + r * 8), s1;
    int el = -1, lag = -1;
#pragma nounroll
    for (int tt = 0; tt <= last; ++tt) {
      const rsrc_t rn = __builtin_amdgcn_make_buffer_rsrc((void*)vp, 0, tt == last ? 0 : HB, 0x00020000);
      unsigned ref, tnow;
#pragma unroll
      for (int u = 0; u < 8; ++u) {
        const int n = u + 7;
        const int so = ROWSO(tt, n);
        asm volatile("s_cmpk_gt_i32 %0, %4\n\ts_cbranch_scc1 1f\n\ts_waitcnt vmcnt(%2)\n\ts_cmp_gt_i32 %1, 0\n\ts_cbranch_scc1 1f\n\ts_waitcnt vmcnt(%3)\n1:"
                     :: "s"(el), "s"(lag), "i"(LAG_W), "i"(VDIST), "i"(WIDE_T), "s"(so) : "scc");
        SB();
        buf[n & 7] = ldnt(n >= 8 ? rn : rv, ROWVO(n), so);
        SB();
        if (u == 0) s1 = *(const LAS f32x4*)(S + tt * 32 + r * 8 + 4);
        if (u == 4) { s0 = *(const LAS f32x4*)(S + ((tt + 1) & 31) * 32 + r * 8); ref = *(volatile LAS unsigned*)(lds + L_REF); tnow = now32(); }
        const float p = __builtin_amdgcn_exp2f(((u < 4 ? s0[u & 3] : s1[u & 3]) - mx) * LOG2E);
#pragma unroll
        for (int k = 0; k < 4; ++k) acc[k] = __builtin_elementwise_fma(bf_unpack(buf[u][k]), f32x2{p, p}, acc[k]);
        asm volatile("" : "+v"(acc[0]), "+v"(acc[1]), "+v"(acc[2]), "+v"(acc[3]));
        if (u == 6) {
          el = (int)(tnow - __builtin_amdgcn_readfirstlane(ref));
          lag = el - (LAG_A + LAG_B / 2 + tt * LAG_B);
        }
        SB();
      }
      TR(32 + tt);
    }
    STAMP(5);
    *(LAS f32x4*)(lds + L_RED + (sw * 4 + r) * 128 + c * 8) = f32x4{acc[0][0], acc[0][1], acc[1][0], acc[1][1]};
    *(LAS f32x4*)(lds + L_RED + (sw * 4 + r) * 128 + c * 8 + 4) = f32x4{acc[2][0], acc[2][1], acc[3][0], acc[3][1]};
    lds_wait();
    __builtin_amdgcn_s_barrier();
    __builtin_amdgcn_s_barrier();
    if (blockIdx.x < NRED) __builtin_amdgcn_s_barrier();
    STAMP_FLUSH();
    return;
  }

  unsigned epoch = *(const __attribute__((address_space(4))) unsigned*)(a.ws + WS_EPOCH);
  SB();
  if (tid < 5) ((LAS unsigned*)lds)[L_CNT + tid] = tid < 4 ? 0u : (unsigned)__builtin_amdgcn_s_memrealtime() + 0x40000000u;
  lds_wait();
  __builtin_amdgcn_s_barrier();
  SB();
  float mb0, mb1, sd0, sd1;
  {
    float f0[8], f1[8];
#pragma unroll
    for (int k = 0; k < 4; ++k) { f0[2 * k] = bf_lo(xv0[k]); f0[2 * k + 1] = bf_hi(xv0[k]); f1[2 * k] = bf_lo(xv1[k]); f1[2 * k + 1] = bf_hi(xv1[k]); }
    float s0 = 0.f, s1 = 0.f;
#pragma unroll
    for (int k = 0; k < 8; ++k) { s0 += f0[k]; s1 += f1[k]; }
    const float m0 = wave_sum(s0) * (1.f / DM), m1 = wave_sum(s1) * (1.f / DM);
    float v0 = 0.f, v1 = 0.f;
#pragma unroll
    for (int k = 0; k < 8; ++k) { const float d0 = f0[k] - m0, d1 = f1[k] - m1; v0 = fmaf(d0, d0, v0); v1 = fmaf(d1, d1, v1); }
    v0 = wave_sum(v0) * (1.f / DM); v1 = wave_sum(v1) * (1.f / DM);
    mb0 = rbf(m0); sd0 = rbf(__builtin_sqrtf(rbf(rbf(v0) + 1e-5f)));
    mb1 = rbf(m1); sd1 = rbf(__builtin_sqrtf(rbf(rbf(v1) + 1e-5f)));
  }
#define XN(xf, mb, sd) rbf(rbf((xf) - (mb)) / (sd))
  asm volatile("" : "+s"(epoch));
  const unsigned tag = epoch + 0x9E3779B9u;
  STAMP(10);
  {
    const float a0 = XN(bf_lo(xq0), mb0, sd0), a1 = XN(bf_hi(xq0), mb0, sd0), b0 = XN(bf_lo(xq1), mb1, sd1), b1 = XN(bf_hi(xq1), mb1, sd1);
    float t[8];
#pragma unroll
    for (int k = 0; k < 4; ++k) {
      const f32x2 w0 = bf_unpack(wq0[k]), w1 = bf_unpack(wq1[k]);
      const f32x2 qa = __builtin_elementwise_fma(w1, f32x2{a1, a1}, w0 * a0), qb = __builtin_elementwise_fma(w1, f32x2{b1, b1}, w0 * b0);
      t[2 * k] = tr32(qa[0], qb[0]); t[2 * k + 1] = tr32(qa[1], qb[1]);
    }
    f32x4 o;
#pragma unroll
    for (int k = 0; k < 4; ++k) o[k] = tr16(t[k], t[4 + k]);
    *(LAS f32x4*)(lds + L_Y + wave * 256 + (r >> 1) * 128 + c * 8 + (r & 1) * 4) = o;
    lds_wait();
  }
  STAMP(11);
  HSYNC(0);
  STAMP(12);
  gu64* Q = (gu64*)(a.ws + WS_Q) + h * 2048;
  if (tid < 256) {
    const LAS float* P = (const LAS float*)lds + L_Y + tid;
    st_granule(Q + g8 * 256 + tid, tag, ((P[0] + P[256]) + (P[512] + P[768])) + ((P[1024] + P[1280]) + (P[1536] + P[1792])));
  }
  STAMP(9);
  asm volatile("" :: "v"(junk));
  if (wave == 7) {
    __builtin_amdgcn_s_sleep(QDELAY);
    const rsrc_t rg = __builtin_amdgcn_make_buffer_rsrc((void*)(a.ws + WS_Q + (size_t)h * 16384 + b * 1024), 0, 16384, 0x00020000);
    const unsigned t0 = now32();
    u32x4 g[8];
    while (true) {
      bool ok = true;
#pragma unroll
      for (int i = 0; i < 8; ++i) g[i] = __builtin_amdgcn_raw_buffer_load_b128(rg, voff, i * 2048, 16);
#pragma unroll
      for (int i = 0; i < 8; ++i) ok = ok & (g[i][1] == tag) & (g[i][3] == tag);
      if (__all(ok)) break;
      if (now32() - t0 > SPIN_TICKS) break;
      __builtin_amdgcn_s_sleep(8);
      asm volatile("" ::: "memory");
    }
    f32x2 q2[8];
#pragma unroll
    for (int i = 0; i < 8; ++i) q2[i] = f32x2{__uint_as_float(g[i][0]), __uint_as_float(g[i][2])};
    const f32x2 qs = ((q2[0] + q2[1]) + (q2[2] + q2[3])) + ((q2[4] + q2[5]) + (q2[6] + q2[7]));
    ((LAS unsigned*)lds)[L_Q + lane] = pack_bf16(qs[0], qs[1]);
    lds_wait();
  }
  STAMP(1);
  __builtin_amdgcn_s_barrier();
  asm volatile("" ::: "memory");
  gu64* G = (gu64*)(a.ws + WS_G);
  float gsum = 0.f;
  const int red = blockIdx.x < NRED ? (int)blockIdx.x : -1;
  const unsigned xw = (red >= 0 && tid < 8) ? *(const gu32*)(a.x + (red & 1) * DM + (red >> 1) * 16 + tid * 2) : 0u;
  if (h < 28) {
    const unsigned short xm0 = a.x[mrow], xm1 = a.x[DM + mrow];
    const rsrc_t r1 = __builtin_amdgcn_make_buffer_rsrc((void*)(a.w1 + h * 64), 0, (DM * FF - h * 64) * 2, 0x00020000);
    const rsrc_t r3 = __builtin_amdgcn_make_buffer_rsrc((void*)(a.w3 + h * 64), 0, (DM * FF - h * 64) * 2, 0x00020000);
    const u32x4 wg = ldnt(r1, mrow * (FF * 2) + (lane & 7) * 16, 0);
    const u32x4 wu = ldnt(r3, mrow * (FF * 2) + (lane & 7) * 16, 0);
    const float a0 = XN(bf_lo(xm0), mb0, sd0), b0 = XN(bf_lo(xm1), mb1, sd1);
    float t[8];
#pragma unroll
    for (int k = 0; k < 4; ++k) {
      const f32x2 g = bf_unpack(wg[k]), u = bf_unpack(wu[k]);
#pragma unroll
      for (int e = 0; e < 2; ++e) t[2 * k + e] = tr16(tr32(g[e] * a0, g[e] * b0), tr32(u[e] * a0, u[e] * b0));
    }
    f32x4 o;
#pragma unroll
    for (int k = 0; k < 4; ++k) { const float lo = t[k] + dpp<ROR8>(t[k]), hi = t[4 + k] + dpp<ROR8>(t[4 + k]); o[k] = (c & 8) ? hi : lo; }
    *(LAS f32x4*)(lds + L_Y + 2048 + wave * 256 + r * 64 + (c & 7) * 8 + (c >> 3) * 4) = o;
    lds_wait();
    HSYNC(1);
    const LAS float* P = (const LAS float*)lds + L_Y + 2048 + (tid & 255);
    gsum = ((P[0] + P[256]) + (P[512] + P[768])) + ((P[1024] + P[1280]) + (P[1536] + P[1792]));
  }
  const int lane0 = lane;
  int pass = DRY ? 0 : 1;
  asm volatile("" : "+s"(pass));
#pragma nounroll
  for (; pass < 2; ++pass) {
    f32x2 wf[64];
    f32x2 yacc[4] = {{0.f, 0.f}, {0.f, 0.f}, {0.f, 0.f}, {0.f, 0.f}};
    if (!pass) {
#pragma unroll
      for (int i = 0; i < 64; ++i) asm volatile("" : "=v"(wf[i]));
    } else {
      {
        FRESH();
        const rsrc_t ro = __builtin_amdgcn_make_buffer_rsrc((void*)(a.wo + (size_t)h * DH * DM), 0, DH * DM * 2, 0x00020000);
#pragma unroll
        for (int i0 = 0; i0 < 16; i0 += WOD) {
          u32x4 w[WOD];
#pragma unroll
          for (int i = 0; i < WOD; ++i) w[i] = ldw(ro, voff + ((i0 + i) & 3) * 1024, (wave * 16 + ((i0 + i) & ~3)) * 1024);
          VM_DRAIN();
#pragma unroll
          for (int i = 0; i < WOD; ++i)
#pragma unroll
            for (int k = 0; k < 4; ++k) { wf[(i0 + i) * 4 + k] = bf_unpack(w[i][k]); asm volatile("" : "+v"(wf[(i0 + i) * 4 + k])); }
          TRICKLE();
        }
      }
      STAMP(2);
      while (__hip_atomic_load((LAS unsigned*)lds + L_CNT, __ATOMIC_RELAXED, __HIP_MEMORY_SCOPE_WORKGROUP) < 4u) __builtin_amdgcn_s_sleep(8);
      asm volatile("" ::: "memory");
      gu64* ml = (gu64*)(a.ws + WS_ML);
      float mw = 0.f;
      const unsigned tk = (unsigned)__builtin_amdgcn_s_memrealtime();
      if (wave == 7) {
        FRESH();
        mw = lds[L_ML + (lane & 3)];
        float m = fmaxf(mw, dpp<XOR1>(mw)); m = fmaxf(m, dpp<XOR2>(m));
        float l = 0.f;
#pragma unroll 4
        for (int i = 0; i < 16; ++i) {
          const f32x4 sv = *(const LAS f32x4*)(lds + L_S + i * 256 + lane * 4);
#pragma unroll
          for (int k = 0; k < 4; ++k) l += __builtin_amdgcn_exp2f((sv[k] - m) * LOG2E);
        }
        l = wave_sum(l);
        if (lane < 16) st_granule(ml + chunk * MLS + lane, tag, lane == 0 ? m : lane == 1 ? l : __uint_as_float(tk));
      }
      else if (wave < 4 && h < 28) { FRESH(); __builtin_amdgcn_s_sleep(16); st_granule(G + (h * 8 + g8) * 256 + tid, tag, gsum); }
      __builtin_amdgcn_s_sleep(127); __builtin_amdgcn_s_sleep(MSLEEP);
      if (wave < 7) {
        FRESH();
        const int f0 = (h * 4 + sp) * 14 + wave * 2;
        const rsrc_t r2 = __builtin_amdgcn_make_buffer_rsrc((void*)(a.w2 + (size_t)f0 * DM), 0, 2 * DM * 2, 0x00020000);
        const u32x4 w20 = ldw(r2, voff, 0), w21 = ldw(r2, voff + 1024, 0);
        const int f = f0 + (r & 1);
        gu64* gp = G + ((f >> 6) * 8 + (c & 7)) * 256 + b * 128 + (c >> 3) * 64 + (f & 63);
        const unsigned t0 = now32();
        unsigned long long g;
        while (true) {
          g = __hip_atomic_load(gp, RLX_AGENT);
          if (__all((unsigned)(g >> 32) == tag)) break;
          if (now32() - t0 > SPIN_TICKS) break;
          __builtin_amdgcn_s_sleep(127);
        }
        float s = __uint_as_float((unsigned)g);
        s += dpp<XOR1>(s); s += dpp<XOR2>(s); s += dpp<XOR7>(s);
        const float gs = rbf(s), us = rbf(dpp<ROR8>(s));
        const float si = rbf(gs / (1.f + __expf(-gs)));
        const float hv = rbf(si * us);
        const float h0 = __builtin_bit_cast(float, __builtin_amdgcn_readlane(__builtin_bit_cast(int, hv), 0));
        const float h1 = __builtin_bit_cast(float, __builtin_amdgcn_readlane(__builtin_bit_cast(int, hv), 16));
#pragma unroll
        for (int k = 0; k < 4; ++k) yacc[k] = __builtin_elementwise_fma(bf_unpack(w21[k]), f32x2{h1, h1}, bf_unpack(w20[k]) * h0);
      } else {
        FRESH();
        {
          const unsigned long long gt = __hip_atomic_load(ml + (lane * 4 + (lane & 3)) * MLS + 2, RLX_AGENT);
          const bool v = (unsigned)(gt >> 32) == tag;
          const float n = wave_sum(v ? 1.f : 0.f), d = wave_sum(v ? (float)(int)((unsigned)gt - tk) : 0.f);
          if (lane == 0 && n > 0.f) ((LAS unsigned*)lds)[L_REF] = tk + (unsigned)(int)(d / n);
        }
        const unsigned t0 = now32();
        unsigned long long gm, gl;
        while (true) {
          gm = __hip_atomic_load(ml + ((chunk & ~3) + (lane & 3)) * MLS, RLX_AGENT);
          gl = __hip_atomic_load(ml + ((chunk & ~3) + (lane & 3)) * MLS + 1, RLX_AGENT);
          if (__all((unsigned)(gm >> 32) == tag && (unsigned)(gl >> 32) == tag)) break;
          if (now32() - t0 > SPIN_TICKS) break;
          __builtin_amdgcn_s_sleep(127);
        }
        const float ms = __uint_as_float((unsigned)gm), ls = __uint_as_float((unsigned)gl);
        float M = fmaxf(ms, dpp<XOR1>(ms)); M = fmaxf(M, dpp<XOR2>(M));
        float L = ls * __builtin_amdgcn_exp2f((ms - M) * LOG2E);
        L += dpp<XOR1>(L); L += dpp<XOR2>(L);
        if (lane < 4) lds[L_FW + lane] = __builtin_amdgcn_exp2f((mw - M) * LOG2E) / L;
        lds_wait();
        {
          const unsigned char* pa = lane < 32 ? a.ws + WS_Y + lane * 32768 + chunk * 128 : (const unsigned char*)a.out;
          const unsigned jk = __hip_atomic_load((gu32*)pa, RLX_AGENT);
          asm volatile("" :: "v"(jk));
        }
        if (blockIdx.x == NWG - 1) {
          while (true) {
            bool ok = true;
#pragma unroll
            for (int i = 0; i < 4; ++i) ok = ok & ((unsigned)(__hip_atomic_load(ml + (i * 64 + lane) * MLS + 2, RLX_AGENT) >> 32) == tag);
            if (__all(ok)) break;
            if (now32() - t0 > SPIN_TICKS) break;
            __builtin_amdgcn_s_sleep(127);
          }
          if (lane == 0) __hip_atomic_store((gu32*)(a.ws + WS_EPOCH), tag, RLX_AGENT);
        }
        STAMP(3);
      }
    }
    FRESH();
    if (pass) __builtin_amdgcn_s_barrier();
    asm volatile("" ::: "memory");
    STAMP(6);
    {
      const LAS float* P = (const LAS float*)lds + L_RED + (4 * r) * 128 + wave * 16 + c;
      float o = ((P[0] + P[128]) + (P[256] + P[384])) * lds[L_FW + r];
      o = xrow_sum(o);
#pragma unroll
      for (int i = 0; i < 16; ++i) {
        const float oi = __builtin_bit_cast(float, __builtin_amdgcn_readlane(__builtin_bit_cast(int, o), i));
#pragma unroll
        for (int k = 0; k < 4; ++k) yacc[k] = __builtin_elementwise_fma(wf[i * 4 + k], f32x2{oi, oi}, yacc[k]);
      }
    }
    *(LAS f32x4*)(lds + L_Y2 + wave * 512 + lane * 8) = f32x4{yacc[0][0], yacc[0][1], yacc[1][0], yacc[1][1]};
    *(LAS f32x4*)(lds + L_Y2 + wave * 512 + lane * 8 + 4) = f32x4{yacc[2][0], yacc[2][1], yacc[3][0], yacc[3][1]};
    lds_wait();
    if (pass) __builtin_amdgcn_s_barrier();
    asm volatile("" ::: "memory");
    const rsrc_t ry = __builtin_amdgcn_make_buffer_rsrc((void*)(a.ws + WS_Y), 0, pass ? 1 << 20 : 0, 0x00020000);
    {
      const LAS float* P = (const LAS float*)lds + L_Y2 + tid;
      const float y = ((P[0] + P[512]) + (P[1024] + P[1536])) + ((P[2048] + P[2560]) + (P[3072] + P[3584]));
      __builtin_amdgcn_raw_buffer_store_b64(u32x2{__float_as_uint(y), tag}, ry, (tid >> 4) * 32768 + (tid & 15) * 8, chunk * 128, 16);
    }
    STAMP(7);
    if (red >= 0) {
      __builtin_amdgcn_s_sleep(FDELAY);
      const unsigned t0 = now32();
      u32x4 g0 = {0u, 0u, 0u, 0u}, g1 = {0u, 0u, 0u, 0u};
      bool ok0 = false, ok1 = false;
      while (true) {
        if (!ok0) g0 = __builtin_amdgcn_raw_buffer_load_b128(ry, voff, red * 16384 + wave * 2048, YLD);
        if (!ok1) g1 = __builtin_amdgcn_raw_buffer_load_b128(ry, voff + 1024, red * 16384 + wave * 2048, YLD);
        ok0 = (g0[1] == tag) & (g0[3] == tag); ok1 = (g1[1] == tag) & (g1[3] == tag);
        if (__all(ok0 & ok1) || !pass) break;
        if (now32() - t0 > SPIN_TICKS) break;
        __builtin_amdgcn_s_sleep(FSLEEP);
        asm volatile("" ::: "memory");
      }
      float s = tr32(__uint_as_float(g0[0]) + __uint_as_float(g1[0]), __uint_as_float(g0[2]) + __uint_as_float(g1[2]));
      s = tr16(s, s);
      s += dpp<ROR8>(s);
      if ((lane & 24) == 0) lds[L_FIN + wave * 16 + (lane & 7) * 2 + (lane >> 5)] = s;
      lds_wait();
      if (pass) __builtin_amdgcn_s_barrier();
      asm volatile("" ::: "memory");
      if (tid < 8) {
        const LAS f32x2* P = (const LAS f32x2*)(lds + L_FIN + tid * 2);
        const f32x2 o = bf_unpack(xw) + (((P[0] + P[8]) + (P[16] + P[24])) + ((P[32] + P[40]) + (P[48] + P[56])));
        const rsrc_t rout = __builtin_amdgcn_make_buffer_rsrc((void*)a.out, 0, pass ? 2 * DM * 2 : 0, 0x00020000);
        __builtin_amdgcn_raw_buffer_store_b32(pack_bf16(o[0], o[1]), rout, tid * 4, (red & 1) * (DM * 2) + (red >> 1) * 32, 0);
      }
    }
  }
  asm volatile(".Lfd_end:");
  STAMP(8);
  STAMP_FLUSH();
}

}

extern "C" void kernel_launch(void* const* d_in, const int* in_sizes, int n_in, void* d_out, int out_size, void* d_ws, size_t ws_size,
                              hipStream_t stream) {
  Args a;
  a.x = (const unsigned short*)d_in[0]; a.k = (const unsigned short*)d_in[1]; a.v = (const unsigned short*)d_in[2];
  a.wq = (const unsigned short*)d_in[3]; a.wo = (const unsigned short*)d_in[4];
  a.w1 = (const unsigned short*)d_in[6]; a.w3 = (const unsigned short*)d_in[7]; a.w2 = (const unsigned short*)d_in[8];
  a.out = (unsigned short*)d_out; a.ws = (unsigned char*)d_ws;
  fused_decode<<<NWG, NTHR, 0, stream>>>(a);
}
```

```cpp
#include <hip/hip_runtime.h>
#include <hip/hip_bf16.h>
#include <stdint.h>

#define KDIST 4
#define KSTAG 1
#define MIMIC 1
#define LATEFLUSH 0
#define STAMPS 0

namespace {

constexpr int NWG = 256, NTHR = 768, NRED = 64;
constexpr int DM = 512, NH = 32, SK = 16384, DH = 128, FF = 1792;
constexpr float LOG2E = 1.4426950408889634f;
constexpr float SCALE = 0.08838834764831845f;
#define MLS 16
constexpr size_t WS_EPOCH = 0, WS_ML = 4u << 20;
constexpr size_t WS_Y = 65536;
constexpr size_t WS_Q = WS_Y + (1u << 20);
constexpr size_t WS_G = WS_Q + (512u << 10);
constexpr size_t WS_STAMP = 8u << 20;
constexpr int L_S = 0;
constexpr int L_RED = L_S + 4096;
constexpr int L_Y = L_RED + 2048;
constexpr int L_Y2 = L_Y + 4096;
constexpr int L_Q = L_Y2 + 4096;
constexpr int L_ML = L_Q + 64;
constexpr int L_FW = L_ML + 8;
constexpr int L_FIN = L_FW + 4;
constexpr int L_CNT = L_FIN + 128;
constexpr int L_REF = L_CNT + 4;
constexpr int L_END = L_REF + 4;
#define TR(i) ((void)0)

#define GAS __attribute__((address_space(1)))
#define LAS __attribute__((address_space(3)))
#define RLX_AGENT __ATOMIC_RELAXED, __HIP_MEMORY_SCOPE_AGENT
typedef unsigned u32x4 __attribute__((ext_vector_type(4)));
typedef unsigned u32x2 __attribute__((ext_vector_type(2)));
typedef float f32x2 __attribute__((ext_vector_type(2)));
typedef float f32x4 __attribute__((ext_vector_type(4)));
typedef __bf16 bf16x2 __attribute__((ext_vector_type(2)));
typedef GAS unsigned gu32;
typedef GAS unsigned long long gu64;

__device__ __forceinline__ float bf_lo(unsigned u) { return __builtin_bit_cast(float, u << 16); }
__device__ __forceinline__ float bf_hi(unsigned u) { return __builtin_bit_cast(float, u & 0xffff0000u); }
__device__ __forceinline__ f32x2 bf_unpack(unsigned u) { return f32x2{bf_lo(u), bf_hi(u)}; }
__device__ __forceinline__ unsigned pack_bf16(float lo, float hi) {
  bf16x2 r = {(__bf16)lo, (__bf16)hi};
  return __builtin_bit_cast(unsigned, r);
}
__device__ __forceinline__ float rbf(float x) {
  return (float)(__bf16)x;
}
__device__ __forceinline__ float dot2(unsigned a, unsigned b, float acc) {
  return __builtin_amdgcn_fdot2_f32_bf16(__builtin_bit_cast(bf16x2, a), __builtin_bit_cast(bf16x2, b), acc, false);
}
template <int CTRL> __device__ __forceinline__ float dpp(float x) {
  return __builtin_bit_cast(float, __builtin_amdgcn_mov_dpp(__builtin_bit_cast(int, x), CTRL, 0xf, 0xf, true));
}
constexpr int XOR1 = 0xB1, XOR2 = 0x4E, XOR7 = 0x141, ROR2 = 0x122, ROR4 = 0x124, ROR8 = 0x128;
__device__ __forceinline__ float xrow_sum(float x) {
  auto s = __builtin_amdgcn_permlane16_swap(__float_as_uint(x), __float_as_uint(x), false, false);
  x = __uint_as_float(s[0]) + __uint_as_float(s[1]);
  auto t = __builtin_amdgcn_permlane32_swap(__float_as_uint(x), __float_as_uint(x), false, false);
  return __uint_as_float(t[0]) + __uint_as_float(t[1]);
}
__device__ __forceinline__ float xrow_max(float x) {
  auto s = __builtin_amdgcn_permlane16_swap(__float_as_uint(x), __float_as_uint(x), false, false);
  x = fmaxf(__uint_as_float(s[0]), __uint_as_float(s[1]));
  auto t = __builtin_amdgcn_permlane32_swap(__float_as_uint(x), __float_as_uint(x), false, false);
  return fmaxf(__uint_as_float(t[0]), __uint_as_float(t[1]));
}
__device__ __forceinline__ float tr16(float a, float b) {
  auto s = __builtin_amdgcn_permlane16_swap(__float_as_uint(a), __float_as_uint(b), false, false);
  return __uint_as_float(s[0]) + __uint_as_float(s[1]);
}
__device__ __forceinline__ float tr32(float a, float b) {
  auto s = __builtin_amdgcn_permlane32_swap(__float_as_uint(a), __float_as_uint(b), false, false);
  return __uint_as_float(s[0]) + __uint_as_float(s[1]);
}
__device__ __forceinline__ float wave_sum(float x) {
  x += dpp<XOR1>(x); x += dpp<XOR2>(x); x += dpp<XOR7>(x); x += dpp<ROR8>(x);
  return xrow_sum(x);
}
__device__ __forceinline__ float wave_max(float x) {
  x = fmaxf(x, dpp<XOR1>(x)); x = fmaxf(x, dpp<XOR2>(x)); x = fmaxf(x, dpp<XOR7>(x)); x = fmaxf(x, dpp<ROR8>(x));
  return xrow_max(x);
}
__device__ __forceinline__ void lds_wait() { asm volatile("s_waitcnt lgkmcnt(0)" ::: "memory"); }
__device__ __forceinline__ void st_granule(gu64* g, unsigned tag, float v) {
  __hip_atomic_store(g, ((unsigned long long)tag << 32) | (unsigned long long)__float_as_uint(v), RLX_AGENT);
}
constexpr unsigned SPIN_TICKS = 20000000u;
__device__ __forceinline__ unsigned now32() { return (unsigned)__builtin_amdgcn_s_memrealtime(); }

struct Args {
  const unsigned short* x; const unsigned short* k; const unsigned short* v; const unsigned short* wq; const unsigned short* wo;
  const unsigned short* w1; const unsigned short* w3; const unsigned short* w2;
  unsigned short* out; unsigned char* ws;
};

#define STAMP(i) do { } while (0)
#define STAMP_FLUSH() do { } while (0)
#define STAMP_DECL

typedef __amdgpu_buffer_rsrc_t rsrc_t;
#define WPOL 0
#define NPRE 3
__device__ __forceinline__ u32x4 ldw(rsrc_t r, int voff, int soff) { return __builtin_amdgcn_raw_buffer_load_b128(r, voff, soff, WPOL); }
__device__ __forceinline__ u32x4 ldnt(rsrc_t r, int voff, int soff) { return __builtin_amdgcn_raw_buffer_load_b128(r, voff, soff, 2); }
#define TRK 0
#define WOD 2
#define QDELAY 32
#define DRY 1
#define FSLEEP 4
#define B0POS (PDIST + 1)
#define SFIRST 1
#define CODEPF 1
#define KMIX 0
#define GLATE 1
#define FDELAY 0
#define WIDE_T 3300
#define LAG_A 100
#define LAG_B 110
#define LAG_W 5
#define MSLEEP 16
#define Y16 0
#define FPIPE 0
#define YLD 16
#define YST 16
#define VDIST 4
#define PRETOUCH 1
#define REDMAP 1
#define OUTT 15
#define PDIST KDIST
#define TRICKLE() __builtin_amdgcn_s_sleep(TRK)
#define HSYNC(n) do { \
    if (lane == 0) __hip_atomic_fetch_add((LAS unsigned*)lds + L_CNT + 1 + (n), 1u, __ATOMIC_RELAXED, __HIP_MEMORY_SCOPE_WORKGROUP); \
    while (__hip_atomic_load((LAS unsigned*)lds + L_CNT + 1 + (n), __ATOMIC_RELAXED, __HIP_MEMORY_SCOPE_WORKGROUP) < 8u) __builtin_amdgcn_s_sleep(0); \
    asm volatile("" ::: "memory"); \
  } while (0)
#define FRESH() int lane_ = lane0; asm volatile("" : "+v"(lane_)); \
  const int lane = lane_, r = lane >> 4, c = lane & 15, tid = wave * 64 + lane, voff = lane * 16; (void)r; (void)c; (void)tid; (void)voff
#define SB() __builtin_amdgcn_sched_barrier(0)
#define VM_DRAIN() do { SB(); asm volatile("s_waitcnt vmcnt(0)" ::: "memory"); SB(); } while (0)

__global__ void __launch_bounds__(NTHR, 3) fused_decode(Args a) {
  __shared__ __attribute__((aligned(16))) float lds[L_END];
  const int tid = threadIdx.x, lane = tid & 63, wave = __builtin_amdgcn_readfirstlane(tid >> 6);
  const int r = lane >> 4, c = lane & 15;
  const int xg = blockIdx.x & 7, j = blockIdx.x >> 3;
  const int b = j >> 4, h = xg * 4 + ((j >> 2) & 3), sp = j & 3;
  const int chunk = b * 128 + h * 4 + sp;
  const int voff = lane * 16;
  STAMP_DECL;
  STAMP(0);

  const int g8 = b * 4 + sp;
  const int qrow = g8 * 64 + wave * 8 + 2 * r, mrow = g8 * 64 + wave * 8 + (lane >> 3);
  u32x4 xv0, xv1, wq0, wq1;
  unsigned xq0, xq1, junk = 0;
  if (wave < 8) {
    xv0 = *(const GAS u32x4*)(a.x + lane * 8); xv1 = *(const GAS u32x4*)(a.x + DM + lane * 8);
    xq0 = *(const gu32*)(a.x + qrow); xq1 = *(const gu32*)(a.x + DM + qrow);
    const rsrc_t rq = __builtin_amdgcn_make_buffer_rsrc((void*)(a.wq + h * DH), 0, (DM * NH * DH - h * DH) * 2, 0x00020000);
    wq0 = ldnt(rq, qrow * (NH * DH * 2) + c * 16, 0);
    wq1 = ldnt(rq, qrow * (NH * DH * 2) + c * 16, NH * DH * 2);
    if (wave >= 6 && j < CODEPF) {
      unsigned long long pc; int len;
      asm volatile("s_getpc_b64 %0\n.Lfd_top:\n\ts_mov_b32 %1, .Lfd_end-.Lfd_top" : "=s"(pc), "=s"(len));
      junk = *(const gu32*)(pc + (unsigned)min(((wave - 6) * 64 + lane) * 128, max(len, 0)));
    }
  }
  int role = wave;
  asm volatile("" : "+s"(role));
  if (__builtin_expect(role >= 8, SFIRST)) {
    const int sw = wave - 8;
    constexpr int HB = SK * DH * 2;
    const unsigned short* kp = a.k + (size_t)(chunk >> 2) * SK * DH;
    const unsigned short* vp = a.v + (size_t)(chunk >> 2) * SK * DH;
    const rsrc_t rk = __builtin_amdgcn_make_buffer_rsrc((void*)kp, 0, HB, 0x00020000);
    const rsrc_t rv = __builtin_amdgcn_make_buffer_rsrc((void*)vp, 0, HB, 0x00020000);
    const int stag = j * KSTAG, voffh = voff + HB / 2;
#define ROWVO(n) ((((n) & 1) ? voffh : voff) + (((n) & 7) >> 1) * 1024)
#define ROWSO(pair, n) (((((pair) + ((n) >> 3) + stag) & 31) << 14) + (sw << 12) + (sp << 19))
#define LDROW(rs, pair, n) ldnt(rs, ROWVO(n), ROWSO(pair, n))
    __builtin_amdgcn_s_setprio(2);
    u32x4 buf[8], pre[NPRE * 8];
    STAMP(12);
    if (B0POS == 0) { __builtin_amdgcn_s_barrier(); SB(); }
    STAMP(13);
#pragma unroll
    for (int u = 0; u < NPRE * 8; ++u) {
      pre[u] = LDROW(rk, 0, u); SB();
      if (u + 1 == B0POS) { __builtin_amdgcn_s_barrier(); SB(); }
      if (u >= PDIST) { asm volatile("s_waitcnt vmcnt(%0)" :: "i"(PDIST)); SB(); }
      if (u == PDIST) STAMP(14);
      if ((u & 7) == 6 && u > 8) { TR((u >> 3) - 1); SB(); }
    }
#pragma unroll
    for (int u = 0; u < 8; ++u) {
      buf[u] = LDROW(rk, NPRE, u); SB();
      if (u < 7) { asm volatile("s_waitcnt vmcnt(%0)" :: "i"(PDIST)); SB(); }
    }
    TR(2);
    STAMP(1);
    __builtin_amdgcn_s_barrier();
    asm volatile("" ::: "memory");
    const u32x4 qp = *(const LAS u32x4*)(lds + L_Q + c * 4);
    STAMP(2);
    LAS float* S = (LAS float*)lds + L_S + sw * 1024;
    float mx = -INFINITY;
#define DOT(w) dot2((w)[3], qp[3], dot2((w)[2], qp[2], dot2((w)[1], qp[1], dot2((w)[0], qp[0], 0.f))))
#define K_FIN(tt) do { \
      _Pragma("unroll") for (int u = 0; u < 4; ++u) { const float lo = x[u] + dpp<ROR8>(x[u]), hi = x[u + 4] + dpp<ROR8>(x[u + 4]); x[u] = (c & 8) ? hi : lo; } \
      _Pragma("unroll") for (int u = 0; u < 2; ++u) { const float lo = x[u] + dpp<XOR7>(x[u]), hi = x[u + 2] + dpp<XOR7>(x[u + 2]); x[u] = (c & 4) ? hi : lo; } \
      { const float lo = x[0] + dpp<XOR2>(x[0]), hi = x[1] + dpp<XOR2>(x[1]); x[0] = (c & 2) ? hi : lo; } \
      float sc = x[0] + dpp<XOR1>(x[0]); \
      sc = rbf(rbf(sc) * SCALE); \
      mx = fmaxf(mx, sc); \
      S[(tt) * 32 + r * 8 + (c >> 1)] = sc; \
      SB(); \
    } while (0)
    static_assert(KDIST <= 6 && KDIST >= 3 && NPRE >= 1 && PDIST <= NPRE * 8, "ring of 8");
#define PACE() do { asm volatile("s_waitcnt vmcnt(%0)" :: "i"(((KMIX >> u) & 1) ? KDIST - 1 : KDIST)); SB(); } while (0)
    {
      float x[8];
#pragma unroll
      for (int u = 0; u < 8; ++u) {
        if (u > 0 && u <= KDIST + 1) { buf[u - 1] = LDROW(rk, NPRE + 1, u - 1); SB(); }
        x[u] = DOT(buf[u]);
        SB();
      }
      K_FIN(NPRE);
    }
#pragma unroll
    for (int pp = 0; pp < NPRE; ++pp) {
      float x[8];
#pragma unroll
      for (int w = 0; w < 8; ++w) x[w] = DOT(pre[pp * 8 + w]);
      K_FIN(pp);
    }
#pragma unroll
    for (int u = KDIST + 1; u < 7; ++u) { PACE(); buf[u] = LDROW(rk, NPRE + 1, u); SB(); }
    TR(3);
    int last = 31;
    asm volatile("" : "+s"(last));
#pragma nounroll
    for (int tt = NPRE + 1; tt <= last; ++tt) {
      const unsigned short* nb = tt == last ? vp : kp;
      const rsrc_t rn = __builtin_amdgcn_make_buffer_rsrc((void*)nb, 0, HB, 0x00020000);
      float x[8];
#pragma unroll
      for (int u = 0; u < 8; ++u) {
        const int n = u + 7;
        PACE();
        buf[n & 7] = LDROW(n >= 8 ? rn : rk, tt, n);
        SB();
        x[u] = DOT(buf[u]);
        SB();
      }
      K_FIN(tt);
      TR(tt);
    }
    STAMP(3);
    mx = wave_max(mx);
    if (lane == 0) lds[L_ML + sw] = mx;
    lds_wait();
    if (lane == 0) __hip_atomic_fetch_add((LAS unsigned*)lds + L_CNT, 1u, __ATOMIC_RELAXED, __HIP_MEMORY_SCOPE_WORKGROUP);
    STAMP(4);
    f32x2 acc[4] = {{0.f, 0.f}, {0.f, 0.f}, {0.f, 0.f}, {0.f, 0.f}};
    f32x4 s0 = *(const LAS f32x4*)(S + r * 8), s1;
    int el = -1, lag = -1;
#pragma nounroll
    for (int tt = 0; tt <= last; ++tt) {
      const rsrc_t rn = __builtin_amdgcn_make_buffer_rsrc((void*)vp, 0, tt == last ? 0 : HB, 0x00020000);
      unsigned ref, tnow;
#pragma unroll
      for (int u = 0; u < 8; ++u) {
        const int n = u + 7;
        const int so = ROWSO(tt, n);
        asm volatile("s_cmpk_gt_i32 %0, %4\n\ts_cbranch_scc1 1f\n\ts_waitcnt vmcnt(%2)\n\ts_cmp_gt_i32 %1, 0\n\ts_cbranch_scc1 1f\n\ts_waitcnt vmcnt(%3)\n1:"
                     :: "s"(el), "s"(lag), "i"(LAG_W), "i"(VDIST), "i"(WIDE_T), "s"(so) : "scc");
        SB();
        buf[n & 7] = ldnt(n >= 8 ? rn : rv, ROWVO(n), so);
        SB();
        if (u == 0) s1 = *(const LAS f32x4*)(S + tt * 32 + r * 8 + 4);
        if (u == 4) { s0 = *(const LAS f32x4*)(S + ((tt + 1) & 31) * 32 + r * 8); ref = *(volatile LAS unsigned*)(lds + L_REF); tnow = now32(); }
        const float p = __builtin_amdgcn_exp2f(((u < 4 ? s0[u & 3] : s1[u & 3]) - mx) * LOG2E);
#pragma unroll
        for (int k = 0; k < 4; ++k) acc[k] = __builtin_elementwise_fma(bf_unpack(buf[u][k]), f32x2{p, p}, acc[k]);
        asm volatile("" : "+v"(acc[0]), "+v"(acc[1]), "+v"(acc[2]), "+v"(acc[3]));
        if (u == 6) {
          el = (int)(tnow - __builtin_amdgcn_readfirstlane(ref));
          lag = el - (LAG_A + LAG_B / 2 + tt * LAG_B);
        }
        SB();
      }
      TR(32 + tt);
    }
    STAMP(5);
    *(LAS f32x4*)(lds + L_RED + (sw * 4 + r) * 128 + c * 8) = f32x4{acc[0][0], acc[0][1], acc[1][0], acc[1][1]};
    *(LAS f32x4*)(lds + L_RED + (sw * 4 + r) * 128 + c * 8 + 4) = f32x4{acc[2][0], acc[2][1], acc[3][0], acc[3][1]};
    lds_wait();
    __builtin_amdgcn_s_barrier();
    __builtin_amdgcn_s_barrier();
    if (blockIdx.x < NRED) __builtin_amdgcn_s_barrier();
    STAMP_FLUSH();
    return;
  }

  unsigned epoch = *(const __attribute__((address_space(4))) unsigned*)(a.ws + WS_EPOCH);
  SB();
  if (tid < 5) ((LAS unsigned*)lds)[L_CNT + tid] = tid < 4 ? 0u : (unsigned)__builtin_amdgcn_s_memrealtime() + 0x40000000u;
  lds_wait();
  __builtin_amdgcn_s_barrier();
  SB();
  float mb0, mb1, sd0, sd1;
  {
    float f0[8], f1[8];
#pragma unroll
    for (int k = 0; k < 4; ++k) { f0[2 * k] = bf_lo(xv0[k]); f0[2 * k + 1] = bf_hi(xv0[k]); f1[2 * k] = bf_lo(xv1[k]); f1[2 * k + 1] = bf_hi(xv1[k]); }
    float s0 = 0.f, s1 = 0.f;
#pragma unroll
    for (int k = 0; k < 8; ++k) { s0 += f0[k]; s1 += f1[k]; }
    const float m0 = wave_sum(s0) * (1.f / DM), m1 = wave_sum(s1) * (1.f / DM);
    float v0 = 0.f, v1 = 0.f;
#pragma unroll
    for (int k = 0; k < 8; ++k) { const float d0 = f0[k] - m0, d1 = f1[k] - m1; v0 = fmaf(d0, d0, v0); v1 = fmaf(d1, d1, v1); }
    v0 = wave_sum(v0) * (1.f / DM); v1 = wave_sum(v1) * (1.f / DM);
    mb0 = rbf(m0); sd0 = rbf(__builtin_sqrtf(rbf(rbf(v0) + 1e-5f)));
    mb1 = rbf(m1); sd1 = rbf(__builtin_sqrtf(rbf(rbf(v1) + 1e-5f)));
  }
#define XN(xf, mb, sd) rbf(rbf((xf) - (mb)) / (sd))
  asm volatile("" : "+s"(epoch));
  const unsigned tag = epoch + 0x9E3779B9u;
  STAMP(10);
  {
    const float a0 = XN(bf_lo(xq0), mb0, sd0), a1 = XN(bf_hi(xq0), mb0, sd0), b0 = XN(bf_lo(xq1), mb1, sd1), b1 = XN(bf_hi(xq1), mb1, sd1);
    float t[8];
#pragma unroll
    for (int k = 0; k < 4; ++k) {
      const f32x2 w0 = bf_unpack(wq0[k]), w1 = bf_unpack(wq1[k]);
      const f32x2 qa = __builtin_elementwise_fma(w1, f32x2{a1, a1}, w0 * a0), qb = __builtin_elementwise_fma(w1, f32x2{b1, b1}, w0 * b0);
      t[2 * k] = tr32(qa[0], qb[0]); t[2 * k + 1] = tr32(qa[1], qb[1]);
    }
    f32x4 o;
#pragma unroll
    for (int k = 0; k < 4; ++k) o[k] = tr16(t[k], t[4 + k]);
    *(LAS f32x4*)(lds + L_Y + wave * 256 + (r >> 1) * 128 + c * 8 + (r & 1) * 4) = o;
    lds_wait();
  }
  STAMP(11);
  HSYNC(0);
  STAMP(12);
  gu64* Q = (gu64*)(a.ws + WS_Q) + h * 2048;
  if (tid < 256) {
    const LAS float* P = (const LAS float*)lds + L_Y + tid;
    st_granule(Q + g8 * 256 + tid, tag, ((P[0] + P[256]) + (P[512] + P[768])) + ((P[1024] + P[1280]) + (P[1536] + P[1792])));
  }
  STAMP(9);
  asm volatile("" :: "v"(junk));
  if (wave == 7) {
    __builtin_amdgcn_s_sleep(QDELAY);
    const rsrc_t rg = __builtin_amdgcn_make_buffer_rsrc((void*)(a.ws + WS_Q + (size_t)h * 16384 + b * 1024), 0, 16384, 0x00020000);
    const unsigned t0 = now32();
    u32x4 g[8];
    while (true) {
      bool ok = true;
#pragma unroll
      for (int i = 0; i < 8; ++i) g[i] = __builtin_amdgcn_raw_buffer_load_b128(rg, voff, i * 2048, 16);
#pragma unroll
      for (int i = 0; i < 8; ++i) ok = ok & (g[i][1] == tag) & (g[i][3] == tag);
      if (__all(ok)) break;
      if (now32() - t0 > SPIN_TICKS) break;
      __builtin_amdgcn_s_sleep(8);
      asm volatile("" ::: "memory");
    }
    f32x2 q2[8];
#pragma unroll
    for (int i = 0; i < 8; ++i) q2[i] = f32x2{__uint_as_float(g[i][0]), __uint_as_float(g[i][2])};
    const f32x2 qs = ((q2[0] + q2[1]) + (q2[2] + q2[3])) + ((q2[4] + q2[5]) + (q2[6] + q2[7]));
    ((LAS unsigned*)lds)[L_Q + lane] = pack_bf16(qs[0], qs[1]);
    lds_wait();
  }
  STAMP(1);
  __builtin_amdgcn_s_barrier();
  asm volatile("" ::: "memory");
  gu64* G = (gu64*)(a.ws + WS_G);
  float gsum = 0.f;
  const int red = blockIdx.x < NRED ? (REDMAP ? (xg * 4 + (j & 3)) * 2 + (j >> 2) : (int)blockIdx.x) : -1;
  const unsigned xw = (red >= 0 && tid < 8) ? *(const gu32*)(a.x + (red & 1) * DM + (red >> 1) * 16 + tid * 2) : 0u;
  if (h < 28) {
    const unsigned short xm0 = a.x[mrow], xm1 = a.x[DM + mrow];
    const rsrc_t r1 = __builtin_amdgcn_make_buffer_rsrc((void*)(a.w1 + h * 64), 0, (DM * FF - h * 64) * 2, 0x00020000);
    const rsrc_t r3 = __builtin_amdgcn_make_buffer_rsrc((void*)(a.w3 + h * 64), 0, (DM * FF - h * 64) * 2, 0x00020000);
    const u32x4 wg = ldnt(r1, mrow * (FF * 2) + (lane & 7) * 16, 0);
    const u32x4 wu = ldnt(r3, mrow * (FF * 2) + (lane & 7) * 16, 0);
    const float a0 = XN(bf_lo(xm0), mb0, sd0), b0 = XN(bf_lo(xm1), mb1, sd1);
    float t[8];
#pragma unroll
    for (int k = 0; k < 4; ++k) {
      const f32x2 g = bf_unpack(wg[k]), u = bf_unpack(wu[k]);
#pragma unroll
      for (int e = 0; e < 2; ++e) t[2 * k + e] = tr16(tr32(g[e] * a0, g[e] * b0), tr32(u[e] * a0, u[e] * b0));
    }
    f32x4 o;
#pragma unroll
    for (int k = 0; k < 4; ++k) { const float lo = t[k] + dpp<ROR8>(t[k]), hi = t[4 + k] + dpp<ROR8>(t[4 + k]); o[k] = (c & 8) ? hi : lo; }
    *(LAS f32x4*)(lds + L_Y + 2048 + wave * 256 + r * 64 + (c & 7) * 8 + (c >> 3) * 4) = o;
    lds_wait();
    HSYNC(1);
    const LAS float* P = (const LAS float*)lds + L_Y + 2048 + (tid & 255);
    gsum = ((P[0] + P[256]) + (P[512] + P[768])) + ((P[1024] + P[1280]) + (P[1536] + P[1792]));
  }
  const int lane0 = lane;
  int pass = DRY ? 0 : 1;
  asm volatile("" : "+s"(pass));
#pragma nounroll
  for (; pass < 2; ++pass) {
    f32x2 wf[64];
    f32x2 yacc[4] = {{0.f, 0.f}, {0.f, 0.f}, {0.f, 0.f}, {0.f, 0.f}};
    if (!pass) {
#pragma unroll
      for (int i = 0; i < 64; ++i) asm volatile("" : "=v"(wf[i]));
    } else {
      {
        FRESH();
        const rsrc_t ro = __builtin_amdgcn_make_buffer_rsrc((void*)(a.wo + (size_t)h * DH * DM), 0, DH * DM * 2, 0x00020000);
#pragma unroll
        for (int i0 = 0; i0 < 16; i0 += WOD) {
          u32x4 w[WOD];
#pragma unroll
          for (int i = 0; i < WOD; ++i) w[i] = ldw(ro, voff + ((i0 + i) & 3) * 1024, (wave * 16 + ((i0 + i) & ~3)) * 1024);
          VM_DRAIN();
#pragma unroll
          for (int i = 0; i < WOD; ++i)
#pragma unroll
            for (int k = 0; k < 4; ++k) { wf[(i0 + i) * 4 + k] = bf_unpack(w[i][k]); asm volatile("" : "+v"(wf[(i0 + i) * 4 + k])); }
          TRICKLE();
        }
      }
      STAMP(2);
      while (__hip_atomic_load((LAS unsigned*)lds + L_CNT, __ATOMIC_RELAXED, __HIP_MEMORY_SCOPE_WORKGROUP) < 4u) __builtin_amdgcn_s_sleep(8);
      asm volatile("" ::: "memory");
      gu64* ml = (gu64*)(a.ws + WS_ML);
      float mw = 0.f;
      const unsigned tk = (unsigned)__builtin_amdgcn_s_memrealtime();
      if (wave == 7) {
        FRESH();
        mw = lds[L_ML + (lane & 3)];
        float m = fmaxf(mw, dpp<XOR1>(mw)); m = fmaxf(m, dpp<XOR2>(m));
        float l = 0.f;
#pragma unroll 4
        for (int i = 0; i < 16; ++i) {
          const f32x4 sv = *(const LAS f32x4*)(lds + L_S + i * 256 + lane * 4);
#pragma unroll
          for (int k = 0; k < 4; ++k) l += __builtin_amdgcn_exp2f((sv[k] - m) * LOG2E);
        }
        l = wave_sum(l);
        if (lane < 16) st_granule(ml + chunk * MLS + lane, tag, lane == 0 ? m : lane == 1 ? l : __uint_as_float(tk));
      }
      else if (wave < 4 && h < 28) { FRESH(); __builtin_amdgcn_s_sleep(16); st_granule(G + (h * 8 + g8) * 256 + tid, tag, gsum); }
      __builtin_amdgcn_s_sleep(127); __builtin_amdgcn_s_sleep(MSLEEP);
      if (wave < 7) {
        FRESH();
        const int f0 = (h * 4 + sp) * 14 + wave * 2;
        const rsrc_t r2 = __builtin_amdgcn_make_buffer_rsrc((void*)(a.w2 + (size_t)f0 * DM), 0, 2 * DM * 2, 0x00020000);
        const u32x4 w20 = ldw(r2, voff, 0), w21 = ldw(r2, voff + 1024, 0);
        const int f = f0 + (r & 1);
        gu64* gp = G + ((f >> 6) * 8 + (c & 7)) * 256 + b * 128 + (c >> 3) * 64 + (f & 63);
        const unsigned t0 = now32();
        unsigned long long g;
        while (true) {
          g = __hip_atomic_load(gp, RLX_AGENT);
          if (__all((unsigned)(g >> 32) == tag)) break;
          if (now32() - t0 > SPIN_TICKS) break;
          __builtin_amdgcn_s_sleep(127);
        }
        float s = __uint_as_float((unsigned)g);
        s += dpp<XOR1>(s); s += dpp<XOR2>(s); s += dpp<XOR7>(s);
        const float gs = rbf(s), us = rbf(dpp<ROR8>(s));
        const float si = rbf(gs / (1.f + __expf(-gs)));
        const float hv = rbf(si * us);
        const float h0 = __builtin_bit_cast(float, __builtin_amdgcn_readlane(__builtin_bit_cast(int, hv), 0));
        const float h1 = __builtin_bit_cast(float, __builtin_amdgcn_readlane(__builtin_bit_cast(int, hv), 16));
#pragma unroll
        for (int k = 0; k < 4; ++k) yacc[k] = __builtin_elementwise_fma(bf_unpack(w21[k]), f32x2{h1, h1}, bf_unpack(w20[k]) * h0);
      } else {
        FRESH();
        {
          const unsigned long long gt = __hip_atomic_load(ml + (lane * 4 + (lane & 3)) * MLS + 2, RLX_AGENT);
          const bool v = (unsigned)(gt >> 32) == tag;
          const float n = wave_sum(v ? 1.f : 0.f), d = wave_sum(v ? (float)(int)((unsigned)gt - tk) : 0.f);
          if (lane == 0 && n > 0.f) ((LAS unsigned*)lds)[L_REF] = tk + (unsigned)(int)(d / n);
        }
        const unsigned t0 = now32();
        unsigned long long gm, gl;
        while (true) {
          gm = __hip_atomic_load(ml + ((chunk & ~3) + (lane & 3)) * MLS, RLX_AGENT);
          gl = __hip_atomic_load(ml + ((chunk & ~3) + (lane & 3)) * MLS + 1, RLX_AGENT);
          if (__all((unsigned)(gm >> 32) == tag && (unsigned)(gl >> 32) == tag)) break;
          if (now32() - t0 > SPIN_TICKS) break;
          __builtin_amdgcn_s_sleep(127);
        }
        const float ms = __uint_as_float((unsigned)gm), ls = __uint_as_float((unsigned)gl);
        float M = fmaxf(ms, dpp<XOR1>(ms)); M = fmaxf(M, dpp<XOR2>(M));
        float L = ls * __builtin_amdgcn_exp2f((ms - M) * LOG2E);
        L += dpp<XOR1>(L); L += dpp<XOR2>(L);
        if (lane < 4) lds[L_FW + lane] = __builtin_amdgcn_exp2f((mw - M) * LOG2E) / L;
        lds_wait();
        {
          const unsigned char* pa = lane < 32 ? a.ws + WS_Y + lane * 32768 + chunk * 128 : (const unsigned char*)a.out + (lane & OUTT) * 128;
          const unsigned jk = __hip_atomic_load((gu32*)pa, RLX_AGENT);
          asm volatile("" :: "v"(jk));
        }
        if (blockIdx.x == NWG - 1) {
          while (true) {
            bool ok = true;
#pragma unroll
            for (int i = 0; i < 4; ++i) ok = ok & ((unsigned)(__hip_atomic_load(ml + (i * 64 + lane) * MLS + 2, RLX_AGENT) >> 32) == tag);
            if (__all(ok)) break;
            if (now32() - t0 > SPIN_TICKS) break;
            __builtin_amdgcn_s_sleep(127);
          }
          if (lane == 0) __hip_atomic_store((gu32*)(a.ws + WS_EPOCH), tag, RLX_AGENT);
        }
        STAMP(3);
      }
    }
    FRESH();
    if (pass) __builtin_amdgcn_s_barrier();
    asm volatile("" ::: "memory");
    STAMP(6);
    {
      const LAS float* P = (const LAS float*)lds + L_RED + (4 * r) * 128 + wave * 16 + c;
      float o = ((P[0] + P[128]) + (P[256] + P[384])) * lds[L_FW + r];
      o = xrow_sum(o);
#pragma unroll
      for (int i = 0; i < 16; ++i) {
        const float oi = __builtin_bit_cast(float, __builtin_amdgcn_readlane(__builtin_bit_cast(int, o), i));
#pragma unroll
        for (int k = 0; k < 4; ++k) yacc[k] = __builtin_elementwise_fma(wf[i * 4 + k], f32x2{oi, oi}, yacc[k]);
      }
    }
    *(LAS f32x4*)(lds + L_Y2 + wave * 512 + lane * 8) = f32x4{yacc[0][0], yacc[0][1], yacc[1][0], yacc[1][1]};
    *(LAS f32x4*)(lds + L_Y2 + wave * 512 + lane * 8 + 4) = f32x4{yacc[2][0], yacc[2][1], yacc[3][0], yacc[3][1]};
    lds_wait();
    if (pass) __builtin_amdgcn_s_barrier();
    asm volatile("" ::: "memory");
    const rsrc_t ry = __builtin_amdgcn_make_buffer_rsrc((void*)(a.ws + WS_Y), 0, pass ? 1 << 20 : 0, 0x00020000);
    {
      const LAS float* P = (const LAS float*)lds + L_Y2 + tid;
      const float y = ((P[0] + P[512]) + (P[1024] + P[1536])) + ((P[2048] + P[2560]) + (P[3072] + P[3584]));
      __builtin_amdgcn_raw_buffer_store_b64(u32x2{__float_as_uint(y), tag}, ry, (tid >> 4) * 32768 + (tid & 15) * 8, chunk * 128, 16);
    }
    STAMP(7);
    if (red >= 0) {
      __builtin_amdgcn_s_sleep(FDELAY);
      const unsigned t0 = now32();
      u32x4 g0 = {0u, 0u, 0u, 0u}, g1 = {0u, 0u, 0u, 0u};
      bool ok0 = false, ok1 = false;
      while (true) {
        if (!ok0) g0 = __builtin_amdgcn_raw_buffer_load_b128(ry, voff, red * 16384 + wave * 2048, YLD);
        if (!ok1) g1 = __builtin_amdgcn_raw_buffer_load_b128(ry, voff + 1024, red * 16384 + wave * 2048, YLD);
        ok0 = (g0[1] == tag) & (g0[3] == tag); ok1 = (g1[1] == tag) & (g1[3] == tag);
        if (__all(ok0 & ok1) || !pass) break;
        if (now32() - t0 > SPIN_TICKS) break;
        __builtin_amdgcn_s_sleep(FSLEEP);
        asm volatile("" ::: "memory");
      }
      float s = tr32(__uint_as_float(g0[0]) + __uint_as_float(g1[0]), __uint_as_float(g0[2]) + __uint_as_float(g1[2]));
      s = tr16(s, s);
      s += dpp<ROR8>(s);
      if ((lane & 24) == 0) lds[L_FIN + wave * 16 + (lane & 7) * 2 + (lane >> 5)] = s;
      lds_wait();
      if (pass) __builtin_amdgcn_s_barrier();
      asm volatile("" ::: "memory");
      if (tid < 8) {
        const LAS f32x2* P = (const LAS f32x2*)(lds + L_FIN + tid * 2);
        const f32x2 o = bf_unpack(xw) + (((P[0] + P[8]) + (P[16] + P[24])) + ((P[32] + P[40]) + (P[48] + P[56])));
        const rsrc_t rout = __builtin_amdgcn_make_buffer_rsrc((void*)a.out, 0, pass ? 2 * DM * 2 : 0, 0x00020000);
        __builtin_amdgcn_raw_buffer_store_b32(pack_bf16(o[0], o[1]), rout, tid * 4, (red & 1) * (DM * 2) + (red >> 1) * 32, 0);
      }
    }
  }
  asm volatile(".Lfd_end:");
  STAMP(8);
  STAMP_FLUSH();
}

}

extern "C" void kernel_launch(void* const* d_in, const int* in_sizes, int n_in, void* d_out, int out_size, void* d_ws, size_t ws_size,
                              hipStream_t stream) {
  Args a;
  a.x = (const unsigned short*)d_in[0]; a.k = (const unsigned short*)d_in[1]; a.v = (const unsigned short*)d_in[2];
  a.wq = (const unsigned short*)d_in[3]; a.wo = (const unsigned short*)d_in[4];
  a.w1 = (const unsigned short*)d_in[6]; a.w3 = (const unsigned short*)d_in[7]; a.w2 = (const unsigned short*)d_in[8];
  a.out = (unsigned short*)d_out; a.ws = (unsigned char*)d_ws;
  fused_decode<<<NWG, NTHR, 0, stream>>>(a);
}
```

```cpp
#define WPOL 2
#define W2POL 2
#include <hip/hip_runtime.h>
#include <hip/hip_bf16.h>
#include <stdint.h>

#define KDIST 4
#define KSTAG 1
#define MIMIC 1
#define LATEFLUSH 0
#define STAMPS 0

namespace {

constexpr int NWG = 256, NTHR = 768, NRED = 64;
constexpr int DM = 512, NH = 32, SK = 16384, DH = 128, FF = 1792;
constexpr float LOG2E = 1.4426950408889634f;
constexpr float SCALE = 0.08838834764831845f;
#define MLS 16
constexpr size_t WS_EPOCH = 0, WS_ML = 4u << 20;
constexpr size_t WS_Y = 65536;
constexpr size_t WS_Q = WS_Y + (1u << 20);
constexpr size_t WS_G = WS_Q + (512u << 10);
constexpr size_t WS_STAMP = 8u << 20;
constexpr int L_S = 0;
constexpr int L_RED = L_S + 4096;
constexpr int L_Y = L_RED + 2048;
constexpr int L_Y2 = L_Y + 4096;
constexpr int L_Q = L_Y2 + 4096;
constexpr int L_ML = L_Q + 64;
constexpr int L_FW = L_ML + 8;
constexpr int L_FIN = L_FW + 4;
constexpr int L_CNT = L_FIN + 128;
constexpr int L_REF = L_CNT + 4;
constexpr int L_END = L_REF + 4;
#define TR(i) ((void)0)

#define GAS __attribute__((address_space(1)))
#define LAS __attribute__((address_space(3)))
#define RLX_AGENT __ATOMIC_RELAXED, __HIP_MEMORY_SCOPE_AGENT
typedef unsigned u32x4 __attribute__((ext_vector_type(4)));
typedef unsigned u32x2 __attribute__((ext_vector_type(2)));
typedef float f32x2 __attribute__((ext_vector_type(2)));
typedef float f32x4 __attribute__((ext_vector_type(4)));
typedef __bf16 bf16x2 __attribute__((ext_vector_type(2)));
typedef GAS unsigned gu32;
typedef GAS unsigned long long gu64;

__device__ __forceinline__ float bf_lo(unsigned u) { return __builtin_bit_cast(float, u << 16); }
__device__ __forceinline__ float bf_hi(unsigned u) { return __builtin_bit_cast(float, u & 0xffff0000u); }
__device__ __forceinline__ f32x2 bf_unpack(unsigned u) { return f32x2{bf_lo(u), bf_hi(u)}; }
__device__ __forceinline__ unsigned pack_bf16(float lo, float hi) {
  bf16x2 r = {(__bf16)lo, (__bf16)hi};
  return __builtin_bit_cast(unsigned, r);
}
__device__ __forceinline__ float rbf(float x) {
  return (float)(__bf16)x;
}
__device__ __forceinline__ float dot2(unsigned a, unsigned b, float acc) {
  return __builtin_amdgcn_fdot2_f32_bf16(__builtin_bit_cast(bf16x2, a), __builtin_bit_cast(bf16x2, b), acc, false);
}
template <int CTRL> __device__ __forceinline__ float dpp(float x) {
  return __builtin_bit_cast(float, __builtin_amdgcn_mov_dpp(__builtin_bit_cast(int, x), CTRL, 0xf, 0xf, true));
}
constexpr int XOR1 = 0xB1, XOR2 = 0x4E, XOR7 = 0x141, ROR2 = 0x122, ROR4 = 0x124, ROR8 = 0x128;
__device__ __forceinline__ float xrow_sum(float x) {
  auto s = __builtin_amdgcn_permlane16_swap(__float_as_uint(x), __float_as_uint(x), false, false);
  x = __uint_as_float(s[0]) + __uint_as_float(s[1]);
  auto t = __builtin_amdgcn_permlane32_swap(__float_as_uint(x), __float_as_uint(x), false, false);
  return __uint_as_float(t[0]) + __uint_as_float(t[1]);
}
__device__ __forceinline__ float xrow_max(float x) {
  auto s = __builtin_amdgcn_permlane16_swap(__float_as_uint(x), __float_as_uint(x), false, false);
  x = fmaxf(__uint_as_float(s[0]), __uint_as_float(s[1]));
  auto t = __builtin_amdgcn_permlane32_swap(__float_as_uint(x), __float_as_uint(x), false, false);
  return fmaxf(__uint_as_float(t[0]), __uint_as_float(t[1]));
}
__device__ __forceinline__ float tr16(float a, float b) {
  auto s = __builtin_amdgcn_permlane16_swap(__float_as_uint(a), __float_as_uint(b), false, false);
  return __uint_as_float(s[0]) + __uint_as_float(s[1]);
}
__device__ __forceinline__ float tr32(float a, float b) {
  auto s = __builtin_amdgcn_permlane32_swap(__float_as_uint(a), __float_as_uint(b), false, false);
  return __uint_as_float(s[0]) + __uint_as_float(s[1]);
}
__device__ __forceinline__ float wave_sum(float x) {
  x += dpp<XOR1>(x); x += dpp<XOR2>(x); x += dpp<XOR7>(x); x += dpp<ROR8>(x);
  return xrow_sum(x);
}
__device__ __forceinline__ float wave_max(float x) {
  x = fmaxf(x, dpp<XOR1>(x)); x = fmaxf(x, dpp<XOR2>(x)); x = fmaxf(x, dpp<XOR7>(x)); x = fmaxf(x, dpp<ROR8>(x));
  return xrow_max(x);
}
__device__ __forceinline__ void lds_wait() { asm volatile("s_waitcnt lgkmcnt(0)" ::: "memory"); }
__device__ __forceinline__ void st_granule(gu64* g, unsigned tag, float v) {
  __hip_atomic_store(g, ((unsigned long long)tag << 32) | (unsigned long long)__float_as_uint(v), RLX_AGENT);
}
constexpr unsigned SPIN_TICKS = 20000000u;
__device__ __forceinline__ unsigned now32() { return (unsigned)__builtin_amdgcn_s_memrealtime(); }

struct Args {
  const unsigned short* x; const unsigned short* k; const unsigned short* v; const unsigned short* wq; const unsigned short* wo;
  const unsigned short* w1; const unsigned short* w3; const unsigned short* w2;
  unsigned short* out; unsigned char* ws;
};

#define STAMP(i) do { } while (0)
#define STAMP_FLUSH() do { } while (0)
#define STAMP_DECL

typedef __amdgpu_buffer_rsrc_t rsrc_t;
#define NPRE 3
__device__ __forceinline__ u32x4 ldw(rsrc_t r, int voff, int soff) { return __builtin_amdgcn_raw_buffer_load_b128(r, voff, soff, WPOL); }
__device__ __forceinline__ u32x4 ldnt(rsrc_t r, int voff, int soff) { return __builtin_amdgcn_raw_buffer_load_b128(r, voff, soff, 2); }
#define TRK 0
#define WOD 2
#define QDELAY 32
#define DRY 1
#define FSLEEP 4
#define B0POS (PDIST + 1)
#define SFIRST 1
#define CODEPF 1
#define KMIX 0
#define GLATE 1
#define FDELAY 0
#define WIDE_T 3300
#define LAG_A 100
#define LAG_B 110
#define LAG_W 5
#define MSLEEP 16
#define Y16 0
#define FPIPE 0
#define YLD 16
#define YST 16
#define VDIST 4
#define PRETOUCH 1
#define REDMAP 1
#define PDIST KDIST
#define TRICKLE() __builtin_amdgcn_s_sleep(TRK)
#define HSYNC(n) do { \
    if (lane == 0) __hip_atomic_fetch_add((LAS unsigned*)lds + L_CNT + 1 + (n), 1u, __ATOMIC_RELAXED, __HIP_MEMORY_SCOPE_WORKGROUP); \
    while (__hip_atomic_load((LAS unsigned*)lds + L_CNT + 1 + (n), __ATOMIC_RELAXED, __HIP_MEMORY_SCOPE_WORKGROUP) < 8u) __builtin_amdgcn_s_sleep(0); \
    asm volatile("" ::: "memory"); \
  } while (0)
#define FRESH() int lane_ = lane0; asm volatile("" : "+v"(lane_)); \
  const int lane = lane_, r = lane >> 4, c = lane & 15, tid = wave * 64 + lane, voff = lane * 16; (void)r; (void)c; (void)tid; (void)voff
#define SB() __builtin_amdgcn_sched_barrier(0)
#define VM_DRAIN() do { SB(); asm volatile("s_waitcnt vmcnt(0)" ::: "memory"); SB(); } while (0)

__global__ void __launch_bounds__(NTHR, 3) fused_decode(Args a) {
  __shared__ __attribute__((aligned(16))) float lds[L_END];
  const int tid = threadIdx.x, lane = tid & 63, wave = __builtin_amdgcn_readfirstlane(tid >> 6);
  const int r = lane >> 4, c = lane & 15;
  const int xg = blockIdx.x & 7, j = blockIdx.x >> 3;
  const int b = j >> 4, h = xg * 4 + ((j >> 2) & 3), sp = j & 3;
  const int chunk = b * 128 + h * 4 + sp;
  const int voff = lane * 16;
  STAMP_DECL;
  STAMP(0);

  const int g8 = b * 4 + sp;
  const int qrow = g8 * 64 + wave * 8 + 2 * r, mrow = g8 * 64 + wave * 8 + (lane >> 3);
  u32x4 xv0, xv1, wq0, wq1;
  unsigned xq0, xq1, junk = 0;
  if (wave < 8) {
    xv0 = *(const GAS u32x4*)(a.x + lane * 8); xv1 = *(const GAS u32x4*)(a.x + DM + lane * 8);
    xq0 = *(const gu32*)(a.x + qrow); xq1 = *(const gu32*)(a.x + DM + qrow);
    const rsrc_t rq = __builtin_amdgcn_make_buffer_rsrc((void*)(a.wq + h * DH), 0, (DM * NH * DH - h * DH) * 2, 0x00020000);
    wq0 = ldnt(rq, qrow * (NH * DH * 2) + c * 16, 0);
    wq1 = ldnt(rq, qrow * (NH * DH * 2) + c * 16, NH * DH * 2);
    if (wave >= 6 && j < CODEPF) {
      unsigned long long pc; int len;
      asm volatile("s_getpc_b64 %0\n.Lfd_top:\n\ts_mov_b32 %1, .Lfd_end-.Lfd_top" : "=s"(pc), "=s"(len));
      junk = *(const gu32*)(pc + (unsigned)min(((wave - 6) * 64 + lane) * 128, max(len, 0)));
    }
  }
  int role = wave;
  asm volatile("" : "+s"(role));
  if (__builtin_expect(role >= 8, SFIRST)) {
    const int sw = wave - 8;
    constexpr int HB = SK * DH * 2;
    const unsigned short* kp = a.k + (size_t)(chunk >> 2) * SK * DH;
    const unsigned short* vp = a.v + (size_t)(chunk >> 2) * SK * DH;
    const rsrc_t rk = __builtin_amdgcn_make_buffer_rsrc((void*)kp, 0, HB, 0x00020000);
    const rsrc_t rv = __builtin_amdgcn_make_buffer_rsrc((void*)vp, 0, HB, 0x00020000);
    const int stag = j * KSTAG, voffh = voff + HB / 2;
#define ROWVO(n) ((((n) & 1) ? voffh : voff) + (((n) & 7) >> 1) * 1024)
#define ROWSO(pair, n) (((((pair) + ((n) >> 3) + stag) & 31) << 14) + (sw << 12) + (sp << 19))
#define LDROW(rs, pair, n) ldnt(rs, ROWVO(n), ROWSO(pair, n))
    __builtin_amdgcn_s_setprio(2);
    u32x4 buf[8], pre[NPRE * 8];
    STAMP(12);
    if (B0POS == 0) { __builtin_amdgcn_s_barrier(); SB(); }
    STAMP(13);
#pragma unroll
    for (int u = 0; u < NPRE * 8; ++u) {
      pre[u] = LDROW(rk, 0, u); SB();
      if (u + 1 == B0POS) { __builtin_amdgcn_s_barrier(); SB(); }
      if (u >= PDIST) { asm volatile("s_waitcnt vmcnt(%0)" :: "i"(PDIST)); SB(); }
      if (u == PDIST) STAMP(14);
      if ((u & 7) == 6 && u > 8) { TR((u >> 3) - 1); SB(); }
    }
#pragma unroll
    for (int u = 0; u < 8; ++u) {
      buf[u] = LDROW(rk, NPRE, u); SB();
      if (u < 7) { asm volatile("s_waitcnt vmcnt(%0)" :: "i"(PDIST)); SB(); }
    }
    TR(2);
    STAMP(1);
    __builtin_amdgcn_s_barrier();
    asm volatile("" ::: "memory");
    const u32x4 qp = *(const LAS u32x4*)(lds + L_Q + c * 4);
    STAMP(2);
    LAS float* S = (LAS float*)lds + L_S + sw * 1024;
    float mx = -INFINITY;
#define DOT(w) dot2((w)[3], qp[3], dot2((w)[2], qp[2], dot2((w)[1], qp[1], dot2((w)[0], qp[0], 0.f))))
#define K_FIN(tt) do { \
      _Pragma("unroll") for (int u = 0; u < 4; ++u) { const float lo = x[u] + dpp<ROR8>(x[u]), hi = x[u + 4] + dpp<ROR8>(x[u + 4]); x[u] = (c & 8) ? hi : lo; } \
      _Pragma("unroll") for (int u = 0; u < 2; ++u) { const float lo = x[u] + dpp<XOR7>(x[u]), hi = x[u + 2] + dpp<XOR7>(x[u + 2]); x[u] = (c & 4) ? hi : lo; } \
      { const float lo = x[0] + dpp<XOR2>(x[0]), hi = x[1] + dpp<XOR2>(x[1]); x[0] = (c & 2) ? hi : lo; } \
      float sc = x[0] + dpp<XOR1>(x[0]); \
      sc = rbf(rbf(sc) * SCALE); \
      mx = fmaxf(mx, sc); \
      S[(tt) * 32 + r * 8 + (c >> 1)] = sc; \
      SB(); \
    } while (0)
    static_assert(KDIST <= 6 && KDIST >= 3 && NPRE >= 1 && PDIST <= NPRE * 8, "ring of 8");
#define PACE() do { asm volatile("s_waitcnt vmcnt(%0)" :: "i"(((KMIX >> u) & 1) ? KDIST - 1 : KDIST)); SB(); } while (0)
    {
      float x[8];
#pragma unroll
      for (int u = 0; u < 8; ++u) {
        if (u > 0 && u <= KDIST + 1) { buf[u - 1] = LDROW(rk, NPRE + 1, u - 1); SB(); }
        x[u] = DOT(buf[u]);
        SB();
      }
      K_FIN(NPRE);
    }
#pragma unroll
    for (int pp = 0; pp < NPRE; ++pp) {
      float x[8];
#pragma unroll
      for (int w = 0; w < 8; ++w) x[w] = DOT(pre[pp * 8 + w]);
      K_FIN(pp);
    }
#pragma unroll
    for (int u = KDIST + 1; u < 7; ++u) { PACE(); buf[u] = LDROW(rk, NPRE + 1, u); SB(); }
    TR(3);
    int last = 31;
    asm volatile("" : "+s"(last));
#pragma nounroll
    for (int tt = NPRE + 1; tt <= last; ++tt) {
      const unsigned short* nb = tt == last ? vp : kp;
      const rsrc_t rn = __builtin_amdgcn_make_buffer_rsrc((void*)nb, 0, HB, 0x00020000);
      float x[8];
#pragma unroll
      for (int u = 0; u < 8; ++u) {
        const int n = u + 7;
        PACE();
        buf[n & 7] = LDROW(n >= 8 ? rn : rk, tt, n);
        SB();
        x[u] = DOT(buf[u]);
        SB();
      }
      K_FIN(tt);
      TR(tt);
    }
    STAMP(3);
    mx = wave_max(mx);
    if (lane == 0) lds[L_ML + sw] = mx;
    lds_wait();
    if (lane == 0) __hip_atomic_fetch_add((LAS unsigned*)lds + L_CNT, 1u, __ATOMIC_RELAXED, __HIP_MEMORY_SCOPE_WORKGROUP);
    STAMP(4);
    f32x2 acc[4] = {{0.f, 0.f}, {0.f, 0.f}, {0.f, 0.f}, {0.f, 0.f}};
    f32x4 s0 = *(const LAS f32x4*)(S + r * 8), s1;
    int el = -1, lag = -1;
#pragma nounroll
    for (int tt = 0; tt <= last; ++tt) {
      const rsrc_t rn = __builtin_amdgcn_make_buffer_rsrc((void*)vp, 0, tt == last ? 0 : HB, 0x00020000);
      unsigned ref, tnow;
#pragma unroll
      for (int u = 0; u < 8; ++u) {
        const int n = u + 7;
        const int so = ROWSO(tt, n);
        asm volatile("s_cmpk_gt_i32 %0, %4\n\ts_cbranch_scc1 1f\n\ts_waitcnt vmcnt(%2)\n\ts_cmp_gt_i32 %1, 0\n\ts_cbranch_scc1 1f\n\ts_waitcnt vmcnt(%3)\n1:"
                     :: "s"(el), "s"(lag), "i"(LAG_W), "i"(VDIST), "i"(WIDE_T), "s"(so) : "scc");
        SB();
        buf[n & 7] = ldnt(n >= 8 ? rn : rv, ROWVO(n), so);
        SB();
        if (u == 0) s1 = *(const LAS f32x4*)(S + tt * 32 + r * 8 + 4);
        if (u == 4) { s0 = *(const LAS f32x4*)(S + ((tt + 1) & 31) * 32 + r * 8); ref = *(volatile LAS unsigned*)(lds + L_REF); tnow = now32(); }
        const float p = __builtin_amdgcn_exp2f(((u < 4 ? s0[u & 3] : s1[u & 3]) - mx) * LOG2E);
#pragma unroll
        for (int k = 0; k < 4; ++k) acc[k] = __builtin_elementwise_fma(bf_unpack(buf[u][k]), f32x2{p, p}, acc[k]);
        asm volatile("" : "+v"(acc[0]), "+v"(acc[1]), "+v"(acc[2]), "+v"(acc[3]));
        if (u == 6) {
          el = (int)(tnow - __builtin_amdgcn_readfirstlane(ref));
          lag = el - (LAG_A + LAG_B / 2 + tt * LAG_B);
        }
        SB();
      }
      TR(32 + tt);
    }
    STAMP(5);
    *(LAS f32x4*)(lds + L_RED + (sw * 4 + r) * 128 + c * 8) = f32x4{acc[0][0], acc[0][1], acc[1][0], acc[1][1]};
    *(LAS f32x4*)(lds + L_RED + (sw * 4 + r) * 128 + c * 8 + 4) = f32x4{acc[2][0], acc[2][1], acc[3][0], acc[3][1]};
    lds_wait();
    __builtin_amdgcn_s_barrier();
    __builtin_amdgcn_s_barrier();
    if (blockIdx.x < NRED) __builtin_amdgcn_s_barrier();
    STAMP_FLUSH();
    return;
  }

  unsigned epoch = *(const __attribute__((address_space(4))) unsigned*)(a.ws + WS_EPOCH);
  SB();
  if (tid < 5) ((LAS unsigned*)lds)[L_CNT + tid] = tid < 4 ? 0u : (unsigned)__builtin_amdgcn_s_memrealtime() + 0x40000000u;
  lds_wait();
  __builtin_amdgcn_s_barrier();
  SB();
  float mb0, mb1, sd0, sd1;
  {
    float f0[8], f1[8];
#pragma unroll
    for (int k = 0; k < 4; ++k) { f0[2 * k] = bf_lo(xv0[k]); f0[2 * k + 1] = bf_hi(xv0[k]); f1[2 * k] = bf_lo(xv1[k]); f1[2 * k + 1] = bf_hi(xv1[k]); }
    float s0 = 0.f, s1 = 0.f;
#pragma unroll
    for (int k = 0; k < 8; ++k) { s0 += f0[k]; s1 += f1[k]; }
    const float m0 = wave_sum(s0) * (1.f / DM), m1 = wave_sum(s1) * (1.f / DM);
    float v0 = 0.f, v1 = 0.f;
#pragma unroll
    for (int k = 0; k < 8; ++k) { const float d0 = f0[k] - m0, d1 = f1[k] - m1; v0 = fmaf(d0, d0, v0); v1 = fmaf(d1, d1, v1); }
    v0 = wave_sum(v0) * (1.f / DM); v1 = wave_sum(v1) * (1.f / DM);
    mb0 = rbf(m0); sd0 = rbf(__builtin_sqrtf(rbf(rbf(v0) + 1e-5f)));
    mb1 = rbf(m1); sd1 = rbf(__builtin_sqrtf(rbf(rbf(v1) + 1e-5f)));
  }
#define XN(xf, mb, sd) rbf(rbf((xf) - (mb)) / (sd))
  asm volatile("" : "+s"(epoch));
  const unsigned tag = epoch + 0x9E3779B9u;
  STAMP(10);
  {
    const float a0 = XN(bf_lo(xq0), mb0, sd0), a1 = XN(bf_hi(xq0), mb0, sd0), b0 = XN(bf_lo(xq1), mb1, sd1), b1 = XN(bf_hi(xq1), mb1, sd1);
    float t[8];
#pragma unroll
    for (int k = 0; k < 4; ++k) {
      const f32x2 w0 = bf_unpack(wq0[k]), w1 = bf_unpack(wq1[k]);
      const f32x2 qa = __builtin_elementwise_fma(w1, f32x2{a1, a1}, w0 * a0), qb = __builtin_elementwise_fma(w1, f32x2{b1, b1}, w0 * b0);
      t[2 * k] = tr32(qa[0], qb[0]); t[2 * k + 1] = tr32(qa[1], qb[1]);
    }
    f32x4 o;
#pragma unroll
    for (int k = 0; k < 4; ++k) o[k] = tr16(t[k], t[4 + k]);
    *(LAS f32x4*)(lds + L_Y + wave * 256 + (r >> 1) * 128 + c * 8 + (r & 1) * 4) = o;
    lds_wait();
  }
  STAMP(11);
  HSYNC(0);
  STAMP(12);
  gu64* Q = (gu64*)(a.ws + WS_Q) + h * 2048;
  if (tid < 256) {
    const LAS float* P = (const LAS float*)lds + L_Y + tid;
    st_granule(Q + g8 * 256 + tid, tag, ((P[0] + P[256]) + (P[512] + P[768])) + ((P[1024] + P[1280]) + (P[1536] + P[1792])));
  }
  STAMP(9);
  asm volatile("" :: "v"(junk));
  if (wave == 7) {
    __builtin_amdgcn_s_sleep(QDELAY);
    const rsrc_t rg = __builtin_amdgcn_make_buffer_rsrc((void*)(a.ws + WS_Q + (size_t)h * 16384 + b * 1024), 0, 16384, 0x00020000);
    const unsigned t0 = now32();
    u32x4 g[8];
    while (true) {
      bool ok = true;
#pragma unroll
      for (int i = 0; i < 8; ++i) g[i] = __builtin_amdgcn_raw_buffer_load_b128(rg, voff, i * 2048, 16);
#pragma unroll
      for (int i = 0; i < 8; ++i) ok = ok & (g[i][1] == tag) & (g[i][3] == tag);
      if (__all(ok)) break;
      if (now32() - t0 > SPIN_TICKS) break;
      __builtin_amdgcn_s_sleep(8);
      asm volatile("" ::: "memory");
    }
    f32x2 q2[8];
#pragma unroll
    for (int i = 0; i < 8; ++i) q2[i] = f32x2{__uint_as_float(g[i][0]), __uint_as_float(g[i][2])};
    const f32x2 qs = ((q2[0] + q2[1]) + (q2[2] + q2[3])) + ((q2[4] + q2[5]) + (q2[6] + q2[7]));
    ((LAS unsigned*)lds)[L_Q + lane] = pack_bf16(qs[0], qs[1]);
    lds_wait();
  }
  STAMP(1);
  __builtin_amdgcn_s_barrier();
  asm volatile("" ::: "memory");
  gu64* G = (gu64*)(a.ws + WS_G);
  float gsum = 0.f;
  const int red = blockIdx.x < NRED ? (REDMAP ? (xg * 4 + (j & 3)) * 2 + (j >> 2) : (int)blockIdx.x) : -1;
  const unsigned xw = (red >= 0 && tid < 8) ? *(const gu32*)(a.x + (red & 1) * DM + (red >> 1) * 16 + tid * 2) : 0u;
  if (h < 28) {
    const unsigned short xm0 = a.x[mrow], xm1 = a.x[DM + mrow];
    const rsrc_t r1 = __builtin_amdgcn_make_buffer_rsrc((void*)(a.w1 + h * 64), 0, (DM * FF - h * 64) * 2, 0x00020000);
    const rsrc_t r3 = __builtin_amdgcn_make_buffer_rsrc((void*)(a.w3 + h * 64), 0, (DM * FF - h * 64) * 2, 0x00020000);
    const u32x4 wg = ldnt(r1, mrow * (FF * 2) + (lane & 7) * 16, 0);
    const u32x4 wu = ldnt(r3, mrow * (FF * 2) + (lane & 7) * 16, 0);
    const float a0 = XN(bf_lo(xm0), mb0, sd0), b0 = XN(bf_lo(xm1), mb1, sd1);
    float t[8];
#pragma unroll
    for (int k = 0; k < 4; ++k) {
      const f32x2 g = bf_unpack(wg[k]), u = bf_unpack(wu[k]);
#pragma unroll
      for (int e = 0; e < 2; ++e) t[2 * k + e] = tr16(tr32(g[e] * a0, g[e] * b0), tr32(u[e] * a0, u[e] * b0));
    }
    f32x4 o;
#pragma unroll
    for (int k = 0; k < 4; ++k) { const float lo = t[k] + dpp<ROR8>(t[k]), hi = t[4 + k] + dpp<ROR8>(t[4 + k]); o[k] = (c & 8) ? hi : lo; }
    *(LAS f32x4*)(lds + L_Y + 2048 + wave * 256 + r * 64 + (c & 7) * 8 + (c >> 3) * 4) = o;
    lds_wait();
    HSYNC(1);
    const LAS float* P = (const LAS float*)lds + L_Y + 2048 + (tid & 255);
    gsum = ((P[0] + P[256]) + (P[512] + P[768])) + ((P[1024] + P[1280]) + (P[1536] + P[1792]));
  }
  const int lane0 = lane;
  int pass = DRY ? 0 : 1;
  asm volatile("" : "+s"(pass));
#pragma nounroll
  for (; pass < 2; ++pass) {
    f32x2 wf[64];
    f32x2 yacc[4] = {{0.f, 0.f}, {0.f, 0.f}, {0.f, 0.f}, {0.f, 0.f}};
    if (!pass) {
#pragma unroll
      for (int i = 0; i < 64; ++i) asm volatile("" : "=v"(wf[i]));
    } else {
      {
        FRESH();
        const rsrc_t ro = __builtin_amdgcn_make_buffer_rsrc((void*)(a.wo + (size_t)h * DH * DM), 0, DH * DM * 2, 0x00020000);
#pragma unroll
        for (int i0 = 0; i0 < 16; i0 += WOD) {
          u32x4 w[WOD];
#pragma unroll
          for (int i = 0; i < WOD; ++i) w[i] = ldw(ro, voff + ((i0 + i) & 3) * 1024, (wave * 16 + ((i0 + i) & ~3)) * 1024);
          VM_DRAIN();
#pragma unroll
          for (int i = 0; i < WOD; ++i)
#pragma unroll
            for (int k = 0; k < 4; ++k) { wf[(i0 + i) * 4 + k] = bf_unpack(w[i][k]); asm volatile("" : "+v"(wf[(i0 + i) * 4 + k])); }
          TRICKLE();
        }
      }
      STAMP(2);
      while (__hip_atomic_load((LAS unsigned*)lds + L_CNT, __ATOMIC_RELAXED, __HIP_MEMORY_SCOPE_WORKGROUP) < 4u) __builtin_amdgcn_s_sleep(8);
      asm volatile("" ::: "memory");
      gu64* ml = (gu64*)(a.ws + WS_ML);
      float mw = 0.f;
      const unsigned tk = (unsigned)__builtin_amdgcn_s_memrealtime();
      if (wave == 7) {
        FRESH();
        mw = lds[L_ML + (lane & 3)];
        float m = fmaxf(mw, dpp<XOR1>(mw)); m = fmaxf(m, dpp<XOR2>(m));
        float l = 0.f;
#pragma unroll 4
        for (int i = 0; i < 16; ++i) {
          const f32x4 sv = *(const LAS f32x4*)(lds + L_S + i * 256 + lane * 4);
#pragma unroll
          for (int k = 0; k < 4; ++k) l += __builtin_amdgcn_exp2f((sv[k] - m) * LOG2E);
        }
        l = wave_sum(l);
        if (lane < 16) st_granule(ml + chunk * MLS + lane, tag, lane == 0 ? m : lane == 1 ? l : __uint_as_float(tk));
      }
      else if (wave < 4 && h < 28) { FRESH(); __builtin_amdgcn_s_sleep(16); st_granule(G + (h * 8 + g8) * 256 + tid, tag, gsum); }
      __builtin_amdgcn_s_sleep(127); __builtin_amdgcn_s_sleep(MSLEEP);
      if (wave < 7) {
        FRESH();
        const int f0 = (h * 4 + sp) * 14 + wave * 2;
        const rsrc_t r2 = __builtin_amdgcn_make_buffer_rsrc((void*)(a.w2 + (size_t)f0 * DM), 0, 2 * DM * 2, 0x00020000);
        const u32x4 w20 = __builtin_amdgcn_raw_buffer_load_b128(r2, voff, 0, W2POL), w21 = __builtin_amdgcn_raw_buffer_load_b128(r2, voff + 1024, 0, W2POL);
        const int f = f0 + (r & 1);
        gu64* gp = G + ((f >> 6) * 8 + (c & 7)) * 256 + b * 128 + (c >> 3) * 64 + (f & 63);
        const unsigned t0 = now32();
        unsigned long long g;
        while (true) {
          g = __hip_atomic_load(gp, RLX_AGENT);
          if (__all((unsigned)(g >> 32) == tag)) break;
          if (now32() - t0 > SPIN_TICKS) break;
          __builtin_amdgcn_s_sleep(127);
        }
        float s = __uint_as_float((unsigned)g);
        s += dpp<XOR1>(s); s += dpp<XOR2>(s); s += dpp<XOR7>(s);
        const float gs = rbf(s), us = rbf(dpp<ROR8>(s));
        const float si = rbf(gs / (1.f + __expf(-gs)));
        const float hv = rbf(si * us);
        const float h0 = __builtin_bit_cast(float, __builtin_amdgcn_readlane(__builtin_bit_cast(int, hv), 0));
        const float h1 = __builtin_bit_cast(float, __builtin_amdgcn_readlane(__builtin_bit_cast(int, hv), 16));
#pragma unroll
        for (int k = 0; k < 4; ++k) yacc[k] = __builtin_elementwise_fma(bf_unpack(w21[k]), f32x2{h1, h1}, bf_unpack(w20[k]) * h0);
      } else {
        FRESH();
        {
          const unsigned long long gt = __hip_atomic_load(ml + (lane * 4 + (lane & 3)) * MLS + 2, RLX_AGENT);
          const bool v = (unsigned)(gt >> 32) == tag;
          const float n = wave_sum(v ? 1.f : 0.f), d = wave_sum(v ? (float)(int)((unsigned)gt - tk) : 0.f);
          if (lane == 0 && n > 0.f) ((LAS unsigned*)lds)[L_REF] = tk + (unsigned)(int)(d / n);
        }
        const unsigned t0 = now32();
        unsigned long long gm, gl;
        while (true) {
          gm = __hip_atomic_load(ml + ((chunk & ~3) + (lane & 3)) * MLS, RLX_AGENT);
          gl = __hip_atomic_load(ml + ((chunk & ~3) + (lane & 3)) * MLS + 1, RLX_AGENT);
          if (__all((unsigned)(gm >> 32) == tag && (unsigned)(gl >> 32) == tag)) break;
          if (now32() - t0 > SPIN_TICKS) break;
          __builtin_amdgcn_s_sleep(127);
        }
        const float ms = __uint_as_float((unsigned)gm), ls = __uint_as_float((unsigned)gl);
        float M = fmaxf(ms, dpp<XOR1>(ms)); M = fmaxf(M, dpp<XOR2>(M));
        float L = ls * __builtin_amdgcn_exp2f((ms - M) * LOG2E);
        L += dpp<XOR1>(L); L += dpp<XOR2>(L);
        if (lane < 4) lds[L_FW + lane] = __builtin_amdgcn_exp2f((mw - M) * LOG2E) / L;
        lds_wait();
        {
          const unsigned char* pa = (lane < 32 || red < 0) ? a.ws + WS_Y + (lane & 31) * 32768 + chunk * 128 : (const unsigned char*)a.out + (red & 1) * (DM * 2) + (red >> 1) * 32;
          const unsigned jk = __hip_atomic_load((gu32*)pa, RLX_AGENT);
          asm volatile("" :: "v"(jk));
        }
        if (blockIdx.x == NWG - 1) {
          while (true) {
            bool ok = true;
#pragma unroll
            for (int i = 0; i < 4; ++i) ok = ok & ((unsigned)(__hip_atomic_load(ml + (i * 64 + lane) * MLS + 2, RLX_AGENT) >> 32) == tag);
            if (__all(ok)) break;
            if (now32() - t0 > SPIN_TICKS) break;
            __builtin_amdgcn_s_sleep(127);
          }
          if (lane == 0) __hip_atomic_store((gu32*)(a.ws + WS_EPOCH), tag, RLX_AGENT);
        }
        STAMP(3);
      }
    }
    FRESH();
    if (pass) __builtin_amdgcn_s_barrier();
    asm volatile("" ::: "memory");
    STAMP(6);
    {
      const LAS float* P = (const LAS float*)lds + L_RED + (4 * r) * 128 + wave * 16 + c;
      float o = ((P[0] + P[128]) + (P[256] + P[384])) * lds[L_FW + r];
      o = xrow_sum(o);
#pragma unroll
      for (int i = 0; i < 16; ++i) {
        const float oi = __builtin_bit_cast(float, __builtin_amdgcn_readlane(__builtin_bit_cast(int, o), i));
#pragma unroll
        for (int k = 0; k < 4; ++k) yacc[k] = __builtin_elementwise_fma(wf[i * 4 + k], f32x2{oi, oi}, yacc[k]);
      }
    }
    *(LAS f32x4*)(lds + L_Y2 + wave * 512 + lane * 8) = f32x4{yacc[0][0], yacc[0][1], yacc[1][0], yacc[1][1]};
    *(LAS f32x4*)(lds + L_Y2 + wave * 512 + lane * 8 + 4) = f32x4{yacc[2][0], yacc[2][1], yacc[3][0], yacc[3][1]};
    lds_wait();
    if (pass) __builtin_amdgcn_s_barrier();
    asm volatile("" ::: "memory");
    const rsrc_t ry = __builtin_amdgcn_make_buffer_rsrc((void*)(a.ws + WS_Y), 0, pass ? 1 << 20 : 0, 0x00020000);
    {
      const LAS float* P = (const LAS float*)lds + L_Y2 + tid;
      const float y = ((P[0] + P[512]) + (P[1024] + P[1536])) + ((P[2048] + P[2560]) + (P[3072] + P[3584]));
      __builtin_amdgcn_raw_buffer_store_b64(u32x2{__float_as_uint(y), tag}, ry, (tid >> 4) * 32768 + (tid & 15) * 8, chunk * 128, 16);
    }
    STAMP(7);
    if (red >= 0) {
      __builtin_amdgcn_s_sleep(FDELAY);
      const unsigned t0 = now32();
      u32x4 g0 = {0u, 0u, 0u, 0u}, g1 = {0u, 0u, 0u, 0u};
      bool ok0 = false, ok1 = false;
      while (true) {
        if (!ok0) g0 = __builtin_amdgcn_raw_buffer_load_b128(ry, voff, red * 16384 + wave * 2048, YLD);
        if (!ok1) g1 = __builtin_amdgcn_raw_buffer_load_b128(ry, voff + 1024, red * 16384 + wave * 2048, YLD);
        ok0 = (g0[1] == tag) & (g0[3] == tag); ok1 = (g1[1] == tag) & (g1[3] == tag);
        if (__all(ok0 & ok1) || !pass) break;
        if (now32() - t0 > SPIN_TICKS) break;
        __builtin_amdgcn_s_sleep(FSLEEP);
        asm volatile("" ::: "memory");
      }
      float s = tr32(__uint_as_float(g0[0]) + __uint_as_float(g1[0]), __uint_as_float(g0[2]) + __uint_as_float(g1[2]));
      s = tr16(s, s);
      s += dpp<ROR8>(s);
      if ((lane & 24) == 0) lds[L_FIN + wave * 16 + (lane & 7) * 2 + (lane >> 5)] = s;
      lds_wait();
      if (pass) __builtin_amdgcn_s_barrier();
      asm volatile("" ::: "memory");
      if (tid < 8) {
        const LAS f32x2* P = (const LAS f32x2*)(lds + L_FIN + tid * 2);
        const f32x2 o = bf_unpack(xw) + (((P[0] + P[8]) + (P[16] + P[24])) + ((P[32] + P[40]) + (P[48] + P[56])));
        const rsrc_t rout = __builtin_amdgcn_make_buffer_rsrc((void*)a.out, 0, pass ? 2 * DM * 2 : 0, 0x00020000);
        __builtin_amdgcn_raw_buffer_store_b32(pack_bf16(o[0], o[1]), rout, tid * 4, (red & 1) * (DM * 2) + (red >> 1) * 32, 0);
      }
    }
  }
  asm volatile(".Lfd_end:");
  STAMP(8);
  STAMP_FLUSH();
}

}

extern "C" void kernel_launch(void* const* d_in, const int* in_sizes, int n_in, void* d_out, int out_size, void* d_ws, size_t ws_size,
                              hipStream_t stream) {
  Args a;
  a.x = (const unsigned short*)d_in[0]; a.k = (const unsigned short*)d_in[1]; a.v = (const unsigned short*)d_in[2];
  a.wq = (const unsigned short*)d_in[3]; a.wo = (const unsigned short*)d_in[4];
  a.w1 = (const unsigned short*)d_in[6]; a.w3 = (const unsigned short*)d_in[7]; a.w2 = (const unsigned short*)d_in[8];
  a.out = (unsigned short*)d_out; a.ws = (unsigned char*)d_ws;
  fused_decode<<<NWG, NTHR, 0, stream>>>(a);
}
```

```cpp
#define STOUCH 0
#include <hip/hip_runtime.h>
#include <hip/hip_bf16.h>
#include <stdint.h>

#define KDIST 4
#define KSTAG 1
#define GNT 0
#define QLD 16
#define MIMIC 1
#define LATEFLUSH 0
#define STAMPS 0

namespace {

constexpr int NWG = 256, NTHR = 768, NRED = 64;
constexpr int DM = 512, NH = 32, SK = 16384, DH = 128, FF = 1792;
constexpr float LOG2E = 1.4426950408889634f;
constexpr float SCALE = 0.08838834764831845f;
#define MLS 16
constexpr size_t WS_EPOCH = 0, WS_ML = 8192;
constexpr size_t WS_Y = 65536;
constexpr int YB = 32768 + 128;
constexpr size_t WS_Q = WS_Y + (1u << 20) + 8192;
constexpr size_t WS_G = WS_Q + (512u << 10);
constexpr size_t WS_STAMP = 8u << 20;
constexpr int L_S = 0;
constexpr int L_RED = L_S + 4096;
constexpr int L_Y = L_RED + 2048;
constexpr int L_Y2 = L_Y + 4096;
constexpr int L_Q = L_Y2 + 4096;
constexpr int L_ML = L_Q + 64;
constexpr int L_FW = L_ML + 8;
constexpr int L_FIN = L_FW + 4;
constexpr int L_CNT = L_FIN + 128;
constexpr int L_REF = L_CNT + 4;
constexpr int L_END = L_REF + 4;
#define TR(i) ((void)0)

#define GAS __attribute__((address_space(1)))
#define LAS __attribute__((address_space(3)))
#define RLX_AGENT __ATOMIC_RELAXED, __HIP_MEMORY_SCOPE_AGENT
typedef unsigned u32x4 __attribute__((ext_vector_type(4)));
typedef unsigned u32x2 __attribute__((ext_vector_type(2)));
typedef float f32x2 __attribute__((ext_vector_type(2)));
typedef float f32x4 __attribute__((ext_vector_type(4)));
typedef __bf16 bf16x2 __attribute__((ext_vector_type(2)));
typedef GAS unsigned gu32;
typedef GAS unsigned long long gu64;

__device__ __forceinline__ float bf_lo(unsigned u) { return __builtin_bit_cast(float, u << 16); }
__device__ __forceinline__ float bf_hi(unsigned u) { return __builtin_bit_cast(float, u & 0xffff0000u); }
__device__ __forceinline__ f32x2 bf_unpack(unsigned u) { return f32x2{bf_lo(u), bf_hi(u)}; }
__device__ __forceinline__ unsigned pack_bf16(float lo, float hi) {
  bf16x2 r = {(__bf16)lo, (__bf16)hi};
  return __builtin_bit_cast(unsigned, r);
}
__device__ __forceinline__ float rbf(float x) {
  return (float)(__bf16)x;
}
__device__ __forceinline__ float dot2(unsigned a, unsigned b, float acc) {
  return __builtin_amdgcn_fdot2_f32_bf16(__builtin_bit_cast(bf16x2, a), __builtin_bit_cast(bf16x2, b), acc, false);
}
template <int CTRL> __device__ __forceinline__ float dpp(float x) {
  return __builtin_bit_cast(float, __builtin_amdgcn_mov_dpp(__builtin_bit_cast(int, x), CTRL, 0xf, 0xf, true));
}
constexpr int XOR1 = 0xB1, XOR2 = 0x4E, XOR7 = 0x141, ROR2 = 0x122, ROR4 = 0x124, ROR8 = 0x128;
__device__ __forceinline__ float xrow_sum(float x) {
  auto s = __builtin_amdgcn_permlane16_swap(__float_as_uint(x), __float_as_uint(x), false, false);
  x = __uint_as_float(s[0]) + __uint_as_float(s[1]);
  auto t = __builtin_amdgcn_permlane32_swap(__float_as_uint(x), __float_as_uint(x), false, false);
  return __uint_as_float(t[0]) + __uint_as_float(t[1]);
}
__device__ __forceinline__ float xrow_max(float x) {
  auto s = __builtin_amdgcn_permlane16_swap(__float_as_uint(x), __float_as_uint(x), false, false);
  x = fmaxf(__uint_as_float(s[0]), __uint_as_float(s[1]));
  auto t = __builtin_amdgcn_permlane32_swap(__float_as_uint(x), __float_as_uint(x), false, false);
  return fmaxf(__uint_as_float(t[0]), __uint_as_float(t[1]));
}
__device__ __forceinline__ float tr16(float a, float b) {
  auto s = __builtin_amdgcn_permlane16_swap(__float_as_uint(a), __float_as_uint(b), false, false);
  return __uint_as_float(s[0]) + __uint_as_float(s[1]);
}
__device__ __forceinline__ float tr32(float a, float b) {
  auto s = __builtin_amdgcn_permlane32_swap(__float_as_uint(a), __float_as_uint(b), false, false);
  return __uint_as_float(s[0]) + __uint_as_float(s[1]);
}
__device__ __forceinline__ float wave_sum(float x) {
  x += dpp<XOR1>(x); x += dpp<XOR2>(x); x += dpp<XOR7>(x); x += dpp<ROR8>(x);
  return xrow_sum(x);
}
__device__ __forceinline__ float wave_max(float x) {
  x = fmaxf(x, dpp<XOR1>(x)); x = fmaxf(x, dpp<XOR2>(x)); x = fmaxf(x, dpp<XOR7>(x)); x = fmaxf(x, dpp<ROR8>(x));
  return xrow_max(x);
}
__device__ __forceinline__ void lds_wait() { asm volatile("s_waitcnt lgkmcnt(0)" ::: "memory"); }
__device__ __forceinline__ void st_granule(gu64* g, unsigned tag, float v) {
  __hip_atomic_store(g, ((unsigned long long)tag << 32) | (unsigned long long)__float_as_uint(v), RLX_AGENT);
}
constexpr unsigned SPIN_TICKS = 20000000u;
__device__ __forceinline__ unsigned now32() { return (unsigned)__builtin_amdgcn_s_memrealtime(); }

struct Args {
  const unsigned short* x; const unsigned short* k; const unsigned short* v; const unsigned short* wq; const unsigned short* wo;
  const unsigned short* w1; const unsigned short* w3; const unsigned short* w2;
  unsigned short* out; unsigned char* ws;
};

#define STAMP(i) do { } while (0)
#define STAMP_FLUSH() do { } while (0)
#define STAMP_DECL

typedef __amdgpu_buffer_rsrc_t rsrc_t;
#define WPOL 2
#define NPRE 3
__device__ __forceinline__ u32x4 ldw(rsrc_t r, int voff, int soff) { return __builtin_amdgcn_raw_buffer_load_b128(r, voff, soff, WPOL); }
__device__ __forceinline__ u32x4 ldnt(rsrc_t r, int voff, int soff) { return __builtin_amdgcn_raw_buffer_load_b128(r, voff, soff, 2); }
#define TRK 0
#define WOD 2
#define QDELAY 32
#define DRY 1
#define FSLEEP 4
#define B0POS (PDIST + 1)
#define SFIRST 1
#define CODEPF 1
#define KMIX 0
#define GLATE 1
#define FDELAY 0
#define WIDE_T 3300
#define LAG_A 100
#define LAG_B 110
#define LAG_W 5
#define MSLEEP 16
#define Y16 0
#define FPIPE 0
#define YLD 16
#define YST 16
#define VDIST 4
#define PRETOUCH 1
#define REDMAP 1
#define W2POL 2
#define PTM 31
#define PDIST KDIST
#define TRICKLE() __builtin_amdgcn_s_sleep(TRK)
#define HSYNC(n) do { \
    if (lane == 0) __hip_atomic_fetch_add((LAS unsigned*)lds + L_CNT + 1 + (n), 1u, __ATOMIC_RELAXED, __HIP_MEMORY_SCOPE_WORKGROUP); \
    while (__hip_atomic_load((LAS unsigned*)lds + L_CNT + 1 + (n), __ATOMIC_RELAXED, __HIP_MEMORY_SCOPE_WORKGROUP) < 8u) __builtin_amdgcn_s_sleep(0); \
    asm volatile("" ::: "memory"); \
  } while (0)
#define FRESH() int lane_ = lane0; asm volatile("" : "+v"(lane_)); \
  const int lane = lane_, r = lane >> 4, c = lane & 15, tid = wave * 64 + lane, voff = lane * 16; (void)r; (void)c; (void)tid; (void)voff
#define SB() __builtin_amdgcn_sched_barrier(0)
#define VM_DRAIN() do { SB(); asm volatile("s_waitcnt vmcnt(0)" ::: "memory"); SB(); } while (0)

__global__ void __launch_bounds__(NTHR, 3) fused_decode(Args a) {
  __shared__ __attribute__((aligned(16))) float lds[L_END];
  const int tid = threadIdx.x, lane = tid & 63, wave = __builtin_amdgcn_readfirstlane(tid >> 6);
  const int r = lane >> 4, c = lane & 15;
  const int xg = blockIdx.x & 7, j = blockIdx.x >> 3;
  const int b = j >> 4, h = xg * 4 + ((j >> 2) & 3), sp = j & 3;
  const int chunk = b * 128 + h * 4 + sp;
  const int voff = lane * 16;
  STAMP_DECL;
  STAMP(0);

  const int g8 = b * 4 + sp;
  const int qrow = g8 * 64 + wave * 8 + 2 * r, mrow = g8 * 64 + wave * 8 + (lane >> 3);
  u32x4 xv0, xv1, wq0, wq1;
  unsigned xq0, xq1, junk = 0;
  if (wave < 8) {
    xv0 = *(const GAS u32x4*)(a.x + lane * 8); xv1 = *(const GAS u32x4*)(a.x + DM + lane * 8);
    xq0 = *(const gu32*)(a.x + qrow); xq1 = *(const gu32*)(a.x + DM + qrow);
    const rsrc_t rq = __builtin_amdgcn_make_buffer_rsrc((void*)(a.wq + h * DH), 0, (DM * NH * DH - h * DH) * 2, 0x00020000);
    wq0 = ldnt(rq, qrow * (NH * DH * 2) + c * 16, 0);
    wq1 = ldnt(rq, qrow * (NH * DH * 2) + c * 16, NH * DH * 2);
    if (wave >= 6 && j < CODEPF) {
      unsigned long long pc; int len;
      asm volatile("s_getpc_b64 %0\n.Lfd_top:\n\ts_mov_b32 %1, .Lfd_end-.Lfd_top" : "=s"(pc), "=s"(len));
      junk = *(const gu32*)(pc + (unsigned)min(((wave - 6) * 64 + lane) * 128, max(len, 0)));
    }
  }
  int role = wave;
  asm volatile("" : "+s"(role));
  if (__builtin_expect(role >= 8, SFIRST)) {
    const int sw = wave - 8;
    constexpr int HB = SK * DH * 2;
    const unsigned short* kp = a.k + (size_t)(chunk >> 2) * SK * DH;
    const unsigned short* vp = a.v + (size_t)(chunk >> 2) * SK * DH;
    const rsrc_t rk = __builtin_amdgcn_make_buffer_rsrc((void*)kp, 0, HB, 0x00020000);
    const rsrc_t rv = __builtin_amdgcn_make_buffer_rsrc((void*)vp, 0, HB, 0x00020000);
    const int stag = j * KSTAG, voffh = voff + HB / 2;
#define ROWVO(n) ((((n) & 1) ? voffh : voff) + (((n) & 7) >> 1) * 1024)
#define ROWSO(pair, n) (((((pair) + ((n) >> 3) + stag) & 31) << 14) + (sw << 12) + (sp << 19))
#define LDROW(rs, pair, n) ldnt(rs, ROWVO(n), ROWSO(pair, n))
    __builtin_amdgcn_s_setprio(2);
    u32x4 buf[8], pre[NPRE * 8];
    unsigned sj0, sj1, sj2, sj3;
    STAMP(12);
    if (B0POS == 0) { __builtin_amdgcn_s_barrier(); SB(); }
    STAMP(13);
#pragma unroll
    for (int u = 0; u < NPRE * 8; ++u) {
      pre[u] = LDROW(rk, 0, u); SB();
      if (u + 1 == B0POS) { __builtin_amdgcn_s_barrier(); SB(); }
      if (u >= PDIST) { asm volatile("s_waitcnt vmcnt(%0)" :: "i"(PDIST)); SB(); }
      if (u == PDIST) STAMP(14);
      if ((u & 7) == 6 && u > 8) { TR((u >> 3) - 1); SB(); }
    }
#pragma unroll
    for (int u = 0; u < 8; ++u) {
      buf[u] = LDROW(rk, NPRE, u); SB();
      if (u < 7) { asm volatile("s_waitcnt vmcnt(%0)" :: "i"(PDIST)); SB(); }
    }
    TR(2);
    STAMP(1);
    __builtin_amdgcn_s_barrier();
    asm volatile("" ::: "memory");
    const u32x4 qp = *(const LAS u32x4*)(lds + L_Q + c * 4);
    STAMP(2);
    LAS float* S = (LAS float*)lds + L_S + sw * 1024;
    float mx = -INFINITY;
#define DOT(w) dot2((w)[3], qp[3], dot2((w)[2], qp[2], dot2((w)[1], qp[1], dot2((w)[0], qp[0], 0.f))))
#define K_FIN(tt) do { \
      _Pragma("unroll") for (int u = 0; u < 4; ++u) { const float lo = x[u] + dpp<ROR8>(x[u]), hi = x[u + 4] + dpp<ROR8>(x[u + 4]); x[u] = (c & 8) ? hi : lo; } \
      _Pragma("unroll") for (int u = 0; u < 2; ++u) { const float lo = x[u] + dpp<XOR7>(x[u]), hi = x[u + 2] + dpp<XOR7>(x[u + 2]); x[u] = (c & 4) ? hi : lo; } \
      { const float lo = x[0] + dpp<XOR2>(x[0]), hi = x[1] + dpp<XOR2>(x[1]); x[0] = (c & 2) ? hi : lo; } \
      float sc = x[0] + dpp<XOR1>(x[0]); \
      sc = rbf(rbf(sc) * SCALE); \
      mx = fmaxf(mx, sc); \
      S[(tt) * 32 + r * 8 + (c >> 1)] = sc; \
      SB(); \
    } while (0)
    static_assert(KDIST <= 6 && KDIST >= 3 && NPRE >= 1 && PDIST <= NPRE * 8, "ring of 8");
#define PACE() do { asm volatile("s_waitcnt vmcnt(%0)" :: "i"(((KMIX >> u) & 1) ? KDIST - 1 : KDIST)); SB(); } while (0)
    {
      float x[8];
#pragma unroll
      for (int u = 0; u < 8; ++u) {
        if (u > 0 && u <= KDIST + 1) { buf[u - 1] = LDROW(rk, NPRE + 1, u - 1); SB(); }
        x[u] = DOT(buf[u]);
        SB();
      }
      K_FIN(NPRE);
    }
#pragma unroll
    for (int pp = 0; pp < NPRE; ++pp) {
      float x[8];
#pragma unroll
      for (int w = 0; w < 8; ++w) x[w] = DOT(pre[pp * 8 + w]);
      K_FIN(pp);
    }
#pragma unroll
    for (int u = KDIST + 1; u < 7; ++u) { PACE(); buf[u] = LDROW(rk, NPRE + 1, u); SB(); }
    TR(3);
    int last = 31;
    asm volatile("" : "+s"(last));
#pragma nounroll
    for (int tt = NPRE + 1; tt <= last; ++tt) {
      const unsigned short* nb = tt == last ? vp : kp;
      const rsrc_t rn = __builtin_amdgcn_make_buffer_rsrc((void*)nb, 0, HB, 0x00020000);
      float x[8];
#pragma unroll
      for (int u = 0; u < 8; ++u) {
        const int n = u + 7;
        PACE();
        buf[n & 7] = LDROW(n >= 8 ? rn : rk, tt, n);
        SB();
        x[u] = DOT(buf[u]);
        SB();
      }
      K_FIN(tt);
      TR(tt);
    }
    STAMP(3);
    mx = wave_max(mx);
    if (lane == 0) lds[L_ML + sw] = mx;
    lds_wait();
    if (lane == 0) __hip_atomic_fetch_add((LAS unsigned*)lds + L_CNT, 1u, __ATOMIC_RELAXED, __HIP_MEMORY_SCOPE_WORKGROUP);
    STAMP(4);
    f32x2 acc[4] = {{0.f, 0.f}, {0.f, 0.f}, {0.f, 0.f}, {0.f, 0.f}};
    f32x4 s0 = *(const LAS f32x4*)(S + r * 8), s1;
    int el = -1, lag = -1;
#pragma nounroll
    for (int tt = 0; tt <= last; ++tt) {
      const rsrc_t rn = __builtin_amdgcn_make_buffer_rsrc((void*)vp, 0, tt == last ? 0 : HB, 0x00020000);
      unsigned ref, tnow;
#pragma unroll
      for (int u = 0; u < 8; ++u) {
        const int n = u + 7;
        const int so = ROWSO(tt, n);
        asm volatile("s_cmpk_gt_i32 %0, %4\n\ts_cbranch_scc1 1f\n\ts_waitcnt vmcnt(%2)\n\ts_cmp_gt_i32 %1, 0\n\ts_cbranch_scc1 1f\n\ts_waitcnt vmcnt(%3)\n1:"
                     :: "s"(el), "s"(lag), "i"(LAG_W), "i"(VDIST), "i"(WIDE_T), "s"(so) : "scc");
        SB();
        buf[n & 7] = ldnt(n >= 8 ? rn : rv, ROWVO(n), so);
        SB();
        if (u == 0) s1 = *(const LAS f32x4*)(S + tt * 32 + r * 8 + 4);
        if (u == 4) { s0 = *(const LAS f32x4*)(S + ((tt + 1) & 31) * 32 + r * 8); ref = *(volatile LAS unsigned*)(lds + L_REF); tnow = now32(); }
        const float p = __builtin_amdgcn_exp2f(((u < 4 ? s0[u & 3] : s1[u & 3]) - mx) * LOG2E);
#pragma unroll
        for (int k = 0; k < 4; ++k) acc[k] = __builtin_elementwise_fma(bf_unpack(buf[u][k]), f32x2{p, p}, acc[k]);
        asm volatile("" : "+v"(acc[0]), "+v"(acc[1]), "+v"(acc[2]), "+v"(acc[3]));
        if (u == 6) {
          el = (int)(tnow - __builtin_amdgcn_readfirstlane(ref));
          lag = el - (LAG_A + LAG_B / 2 + tt * LAG_B);
        }
        SB();
      }
      TR(32 + tt);
    }
    STAMP(5);
    *(LAS f32x4*)(lds + L_RED + (sw * 4 + r) * 128 + c * 8) = f32x4{acc[0][0], acc[0][1], acc[1][0], acc[1][1]};
    *(LAS f32x4*)(lds + L_RED + (sw * 4 + r) * 128 + c * 8 + 4) = f32x4{acc[2][0], acc[2][1], acc[3][0], acc[3][1]};
    lds_wait();
    __builtin_amdgcn_s_barrier();
    __builtin_amdgcn_s_barrier();
    if (blockIdx.x < NRED) __builtin_amdgcn_s_barrier();
    STAMP_FLUSH();
    return;
  }

  unsigned epoch = *(const __attribute__((address_space(4))) unsigned*)(a.ws + WS_EPOCH);
  SB();
  if (tid < 5) ((LAS unsigned*)lds)[L_CNT + tid] = tid < 4 ? 0u : (unsigned)__builtin_amdgcn_s_memrealtime() + 0x40000000u;
  lds_wait();
  __builtin_amdgcn_s_barrier();
  SB();
  float mb0, mb1, sd0, sd1;
  {
    float f0[8], f1[8];
#pragma unroll
    for (int k = 0; k < 4; ++k) { f0[2 * k] = bf_lo(xv0[k]); f0[2 * k + 1] = bf_hi(xv0[k]); f1[2 * k] = bf_lo(xv1[k]); f1[2 * k + 1] = bf_hi(xv1[k]); }
    float s0 = 0.f, s1 = 0.f;
#pragma unroll
    for (int k = 0; k < 8; ++k) { s0 += f0[k]; s1 += f1[k]; }
    const float m0 = wave_sum(s0) * (1.f / DM), m1 = wave_sum(s1) * (1.f / DM);
    float v0 = 0.f, v1 = 0.f;
#pragma unroll
    for (int k = 0; k < 8; ++k) { const float d0 = f0[k] - m0, d1 = f1[k] - m1; v0 = fmaf(d0, d0, v0); v1 = fmaf(d1, d1, v1); }
    v0 = wave_sum(v0) * (1.f / DM); v1 = wave_sum(v1) * (1.f / DM);
    mb0 = rbf(m0); sd0 = rbf(__builtin_sqrtf(rbf(rbf(v0) + 1e-5f)));
    mb1 = rbf(m1); sd1 = rbf(__builtin_sqrtf(rbf(rbf(v1) + 1e-5f)));
  }
#define XN(xf, mb, sd) rbf(rbf((xf) - (mb)) / (sd))
  asm volatile("" : "+s"(epoch));
  const unsigned tag = epoch + 0x9E3779B9u;
  STAMP(10);
  {
    const float a0 = XN(bf_lo(xq0), mb0, sd0), a1 = XN(bf_hi(xq0), mb0, sd0), b0 = XN(bf_lo(xq1), mb1, sd1), b1 = XN(bf_hi(xq1), mb1, sd1);
    float t[8];
#pragma unroll
    for (int k = 0; k < 4; ++k) {
      const f32x2 w0 = bf_unpack(wq0[k]), w1 = bf_unpack(wq1[k]);
      const f32x2 qa = __builtin_elementwise_fma(w1, f32x2{a1, a1}, w0 * a0), qb = __builtin_elementwise_fma(w1, f32x2{b1, b1}, w0 * b0);
      t[2 * k] = tr32(qa[0], qb[0]); t[2 * k + 1] = tr32(qa[1], qb[1]);
    }
    f32x4 o;
#pragma unroll
    for (int k = 0; k < 4; ++k) o[k] = tr16(t[k], t[4 + k]);
    *(LAS f32x4*)(lds + L_Y + wave * 256 + (r >> 1) * 128 + c * 8 + (r & 1) * 4) = o;
    lds_wait();
  }
  STAMP(11);
  HSYNC(0);
  STAMP(12);
  gu64* Q = (gu64*)(a.ws + WS_Q) + h * 2048;
  if (tid < 256) {
    const LAS float* P = (const LAS float*)lds + L_Y + tid;
    st_granule(Q + g8 * 256 + tid, tag, ((P[0] + P[256]) + (P[512] + P[768])) + ((P[1024] + P[1280]) + (P[1536] + P[1792])));
  }
  STAMP(9);
  asm volatile("" :: "v"(junk));
  if (wave == 7) {
    __builtin_amdgcn_s_sleep(QDELAY);
    const rsrc_t rg = __builtin_amdgcn_make_buffer_rsrc((void*)(a.ws + WS_Q + (size_t)h * 16384 + b * 1024), 0, 16384, 0x00020000);
    const unsigned t0 = now32();
    u32x4 g[8];
    while (true) {
      bool ok = true;
#pragma unroll
      for (int i = 0; i < 8; ++i) g[i] = __builtin_amdgcn_raw_buffer_load_b128(rg, voff, i * 2048, QLD);
#pragma unroll
      for (int i = 0; i < 8; ++i) ok = ok & (g[i][1] == tag) & (g[i][3] == tag);
      if (__all(ok)) break;
      if (now32() - t0 > SPIN_TICKS) break;
      __builtin_amdgcn_s_sleep(8);
      asm volatile("" ::: "memory");
    }
    f32x2 q2[8];
#pragma unroll
    for (int i = 0; i < 8; ++i) q2[i] = f32x2{__uint_as_float(g[i][0]), __uint_as_float(g[i][2])};
    const f32x2 qs = ((q2[0] + q2[1]) + (q2[2] + q2[3])) + ((q2[4] + q2[5]) + (q2[6] + q2[7]));
    ((LAS unsigned*)lds)[L_Q + lane] = pack_bf16(qs[0], qs[1]);
    lds_wait();
  }
  STAMP(1);
  __builtin_amdgcn_s_barrier();
  asm volatile("" ::: "memory");
  gu64* G = (gu64*)(a.ws + WS_G);
  float gsum = 0.f;
  const int red = blockIdx.x < NRED ? (REDMAP ? (xg * 4 + (j & 3)) * 2 + (j >> 2) : (int)blockIdx.x) : -1;
  const unsigned xw = (red >= 0 && tid < 8) ? *(const gu32*)(a.x + (red & 1) * DM + (red >> 1) * 16 + tid * 2) : 0u;
  if (h < 28) {
    const unsigned short xm0 = a.x[mrow], xm1 = a.x[DM + mrow];
    const rsrc_t r1 = __builtin_amdgcn_make_buffer_rsrc((void*)(a.w1 + h * 64), 0, (DM * FF - h * 64) * 2, 0x00020000);
    const rsrc_t r3 = __builtin_amdgcn_make_buffer_rsrc((void*)(a.w3 + h * 64), 0, (DM * FF - h * 64) * 2, 0x00020000);
    const u32x4 wg = ldnt(r1, mrow * (FF * 2) + (lane & 7) * 16, 0);
    const u32x4 wu = ldnt(r3, mrow * (FF * 2) + (lane & 7) * 16, 0);
    const float a0 = XN(bf_lo(xm0), mb0, sd0), b0 = XN(bf_lo(xm1), mb1, sd1);
    float t[8];
#pragma unroll
    for (int k = 0; k < 4; ++k) {
      const f32x2 g = bf_unpack(wg[k]), u = bf_unpack(wu[k]);
#pragma unroll
      for (int e = 0; e < 2; ++e) t[2 * k + e] = tr16(tr32(g[e] * a0, g[e] * b0), tr32(u[e] * a0, u[e] * b0));
    }
    f32x4 o;
#pragma unroll
    for (int k = 0; k < 4; ++k) { const float lo = t[k] + dpp<ROR8>(t[k]), hi = t[4 + k] + dpp<ROR8>(t[4 + k]); o[k] = (c & 8) ? hi : lo; }
    *(LAS f32x4*)(lds + L_Y + 2048 + wave * 256 + r * 64 + (c & 7) * 8 + (c >> 3) * 4) = o;
    lds_wait();
    HSYNC(1);
    const LAS float* P = (const LAS float*)lds + L_Y + 2048 + (tid & 255);
    gsum = ((P[0] + P[256]) + (P[512] + P[768])) + ((P[1024] + P[1280]) + (P[1536] + P[1792]));
  }
  const int lane0 = lane;
  int pass = DRY ? 0 : 1;
  asm volatile("" : "+s"(pass));
#pragma nounroll
  for (; pass < 2; ++pass) {
    f32x2 wf[64];
    f32x2 yacc[4] = {{0.f, 0.f}, {0.f, 0.f}, {0.f, 0.f}, {0.f, 0.f}};
    if (!pass) {
#pragma unroll
      for (int i = 0; i < 64; ++i) asm volatile("" : "=v"(wf[i]));
    } else {
      {
        FRESH();
        const rsrc_t ro = __builtin_amdgcn_make_buffer_rsrc((void*)(a.wo + (size_t)h * DH * DM), 0, DH * DM * 2, 0x00020000);
#pragma unroll
        for (int i0 = 0; i0 < 16; i0 += WOD) {
          u32x4 w[WOD];
#pragma unroll
          for (int i = 0; i < WOD; ++i) w[i] = ldw(ro, voff + ((i0 + i) & 3) * 1024, (wave * 16 + ((i0 + i) & ~3)) * 1024);
          VM_DRAIN();
#pragma unroll
          for (int i = 0; i < WOD; ++i)
#pragma unroll
            for (int k = 0; k < 4; ++k) { wf[(i0 + i) * 4 + k] = bf_unpack(w[i][k]); asm volatile("" : "+v"(wf[(i0 + i) * 4 + k])); }
          TRICKLE();
        }
      }
      STAMP(2);
      while (__hip_atomic_load((LAS unsigned*)lds + L_CNT, __ATOMIC_RELAXED, __HIP_MEMORY_SCOPE_WORKGROUP) < 4u) __builtin_amdgcn_s_sleep(8);
      asm volatile("" ::: "memory");
      gu64* ml = (gu64*)(a.ws + WS_ML);
      float mw = 0.f;
      const unsigned tk = (unsigned)__builtin_amdgcn_s_memrealtime();
      if (wave == 7) {
        FRESH();
        mw = lds[L_ML + (lane & 3)];
        float m = fmaxf(mw, dpp<XOR1>(mw)); m = fmaxf(m, dpp<XOR2>(m));
        float l = 0.f;
#pragma unroll 4
        for (int i = 0; i < 16; ++i) {
          const f32x4 sv = *(const LAS f32x4*)(lds + L_S + i * 256 + lane * 4);
#pragma unroll
          for (int k = 0; k < 4; ++k) l += __builtin_amdgcn_exp2f((sv[k] - m) * LOG2E);
        }
        l = wave_sum(l);
        if (lane < 16) st_granule(ml + chunk * MLS + lane, tag, lane == 0 ? m : lane == 1 ? l : __uint_as_float(tk));
      }
      else if (wave < 4 && h < 28) { FRESH(); __builtin_amdgcn_s_sleep(16); st_granule(G + (h * 8 + g8) * 256 + tid, tag, gsum); }
      __builtin_amdgcn_s_sleep(127); __builtin_amdgcn_s_sleep(MSLEEP);
      if (wave < 7) {
        FRESH();
        const int f0 = (h * 4 + sp) * 14 + wave * 2;
        const rsrc_t r2 = __builtin_amdgcn_make_buffer_rsrc((void*)(a.w2 + (size_t)f0 * DM), 0, 2 * DM * 2, 0x00020000);
        const u32x4 w20 = __builtin_amdgcn_raw_buffer_load_b128(r2, voff, 0, W2POL), w21 = __builtin_amdgcn_raw_buffer_load_b128(r2, voff + 1024, 0, W2POL);
        const int f = f0 + (r & 1);
        gu64* gp = G + ((f >> 6) * 8 + (c & 7)) * 256 + b * 128 + (c >> 3) * 64 + (f & 63);
        const unsigned t0 = now32();
        unsigned long long g;
        while (true) {
          g = __hip_atomic_load(gp, RLX_AGENT);
          if (__all((unsigned)(g >> 32) == tag)) break;
          if (now32() - t0 > SPIN_TICKS) break;
          __builtin_amdgcn_s_sleep(127);
        }
        float s = __uint_as_float((unsigned)g);
        s += dpp<XOR1>(s); s += dpp<XOR2>(s); s += dpp<XOR7>(s);
        const float gs = rbf(s), us = rbf(dpp<ROR8>(s));
        const float si = rbf(gs / (1.f + __expf(-gs)));
        const float hv = rbf(si * us);
        const float h0 = __builtin_bit_cast(float, __builtin_amdgcn_readlane(__builtin_bit_cast(int, hv), 0));
        const float h1 = __builtin_bit_cast(float, __builtin_amdgcn_readlane(__builtin_bit_cast(int, hv), 16));
#pragma unroll
        for (int k = 0; k < 4; ++k) yacc[k] = __builtin_elementwise_fma(bf_unpack(w21[k]), f32x2{h1, h1}, bf_unpack(w20[k]) * h0);
      } else {
        FRESH();
        {
          const unsigned long long gt = __hip_atomic_load(ml + (lane * 4 + (lane & 3)) * MLS + 2, RLX_AGENT);
          const bool v = (unsigned)(gt >> 32) == tag;
          const float n = wave_sum(v ? 1.f : 0.f), d = wave_sum(v ? (float)(int)((unsigned)gt - tk) : 0.f);
          if (lane == 0 && n > 0.f) ((LAS unsigned*)lds)[L_REF] = tk + (unsigned)(int)(d / n);
        }
        const unsigned t0 = now32();
        unsigned long long gm, gl;
        while (true) {
          gm = __hip_atomic_load(ml + ((chunk & ~3) + (lane & 3)) * MLS, RLX_AGENT);
          gl = __hip_atomic_load(ml + ((chunk & ~3) + (lane & 3)) * MLS + 1, RLX_AGENT);
          if (__all((unsigned)(gm >> 32) == tag && (unsigned)(gl >> 32) == tag)) break;
          if (now32() - t0 > SPIN_TICKS) break;
          __builtin_amdgcn_s_sleep(127);
        }
        const float ms = __uint_as_float((unsigned)gm), ls = __uint_as_float((unsigned)gl);
        float M = fmaxf(ms, dpp<XOR1>(ms)); M = fmaxf(M, dpp<XOR2>(M));
        float L = ls * __builtin_amdgcn_exp2f((ms - M) * LOG2E);
        L += dpp<XOR1>(L); L += dpp<XOR2>(L);
        if (lane < 4) lds[L_FW + lane] = __builtin_amdgcn_exp2f((mw - M) * LOG2E) / L;
        lds_wait();
        {
          const unsigned char* pa = (lane < 32 || red < 0) ? a.ws + WS_Y + (lane & PTM) * YB + 32768 : (const unsigned char*)a.out + (red & 1) * (DM * 2) + (red >> 1) * 32;
          const unsigned jk = *(const volatile gu32*)pa;
          asm volatile("" :: "v"(jk));
        }
        if (blockIdx.x == NWG - 1) {
          while (true) {
            bool ok = true;
#pragma unroll
            for (int i = 0; i < 4; ++i) ok = ok & ((unsigned)(__hip_atomic_load(ml + (i * 64 + lane) * MLS + 2, RLX_AGENT) >> 32) == tag);
            if (__all(ok)) break;
            if (now32() - t0 > SPIN_TICKS) break;
            __builtin_amdgcn_s_sleep(127);
          }
          if (lane == 0) __hip_atomic_store((gu32*)(a.ws + WS_EPOCH), tag, RLX_AGENT);
        }
        STAMP(3);
      }
    }
    FRESH();
    if (pass) __builtin_amdgcn_s_barrier();
    asm volatile("" ::: "memory");
    STAMP(6);
    {
      const LAS float* P = (const LAS float*)lds + L_RED + (4 * r) * 128 + wave * 16 + c;
      float o = ((P[0] + P[128]) + (P[256] + P[384])) * lds[L_FW + r];
      o = xrow_sum(o);
#pragma unroll
      for (int i = 0; i < 16; ++i) {
        const float oi = __builtin_bit_cast(float, __builtin_amdgcn_readlane(__builtin_bit_cast(int, o), i));
#pragma unroll
        for (int k = 0; k < 4; ++k) yacc[k] = __builtin_elementwise_fma(wf[i * 4 + k], f32x2{oi, oi}, yacc[k]);
      }
    }
    *(LAS f32x4*)(lds + L_Y2 + wave * 512 + lane * 8) = f32x4{yacc[0][0], yacc[0][1], yacc[1][0], yacc[1][1]};
    *(LAS f32x4*)(lds + L_Y2 + wave * 512 + lane * 8 + 4) = f32x4{yacc[2][0], yacc[2][1], yacc[3][0], yacc[3][1]};
    lds_wait();
    if (pass) __builtin_amdgcn_s_barrier();
    asm volatile("" ::: "memory");
    const rsrc_t ry = __builtin_amdgcn_make_buffer_rsrc((void*)(a.ws + WS_Y), 0, pass ? 32 * YB : 0, 0x00020000);
    {
      const LAS float* P = (const LAS float*)lds + L_Y2 + tid;
      const float y = ((P[0] + P[512]) + (P[1024] + P[1536])) + ((P[2048] + P[2560]) + (P[3072] + P[3584]));
      __builtin_amdgcn_raw_buffer_store_b64(u32x2{__float_as_uint(y), tag}, ry, (tid >> 4) * YB + (tid & 15) * 8, chunk * 128, YST);
    }
    STAMP(7);
    if (red >= 0) {
      __builtin_amdgcn_s_sleep(FDELAY);
      const unsigned t0 = now32();
      u32x4 g0 = {0u, 0u, 0u, 0u}, g1 = {0u, 0u, 0u, 0u};
      bool ok0 = false, ok1 = false;
      while (true) {
        if (!ok0) g0 = __builtin_amdgcn_raw_buffer_load_b128(ry, voff, (red >> 1) * YB + (red & 1) * 16384 + wave * 2048, YLD);
        if (!ok1) g1 = __builtin_amdgcn_raw_buffer_load_b128(ry, voff + 1024, (red >> 1) * YB + (red & 1) * 16384 + wave * 2048, YLD);
        ok0 = (g0[1] == tag) & (g0[3] == tag); ok1 = (g1[1] == tag) & (g1[3] == tag);
        if (__all(ok0 & ok1) || !pass) break;
        if (now32() - t0 > SPIN_TICKS) break;
        __builtin_amdgcn_s_sleep(FSLEEP);
        asm volatile("" ::: "memory");
      }
      float s = tr32(__uint_as_float(g0[0]) + __uint_as_float(g1[0]), __uint_as_float(g0[2]) + __uint_as_float(g1[2]));
      s = tr16(s, s);
      s += dpp<ROR8>(s);
      if ((lane & 24) == 0) lds[L_FIN + wave * 16 + (lane & 7) * 2 + (lane >> 5)] = s;
      lds_wait();
      if (pass) __builtin_amdgcn_s_barrier();
      asm volatile("" ::: "memory");
      if (tid < 8) {
        const LAS f32x2* P = (const LAS f32x2*)(lds + L_FIN + tid * 2);
        const f32x2 o = bf_unpack(xw) + (((P[0] + P[8]) + (P[16] + P[24])) + ((P[32] + P[40]) + (P[48] + P[56])));
        const rsrc_t rout = __builtin_amdgcn_make_buffer_rsrc((void*)a.out, 0, pass ? 2 * DM * 2 : 0, 0x00020000);
        __builtin_amdgcn_raw_buffer_store_b32(pack_bf16(o[0], o[1]), rout, tid * 4, (red & 1) * (DM * 2) + (red >> 1) * 32, 0);
      }
    }
  }
  asm volatile(".Lfd_end:");
  STAMP(8);
  STAMP_FLUSH();
}

}

extern "C" void kernel_launch(void* const* d_in, const int* in_sizes, int n_in, void* d_out, int out_size, void* d_ws, size_t ws_size,
                              hipStream_t stream) {
  Args a;
  a.x = (const unsigned short*)d_in[0]; a.k = (const unsigned short*)d_in[1]; a.v = (const unsigned short*)d_in[2];
  a.wq = (const unsigned short*)d_in[3]; a.wo = (const unsigned short*)d_in[4];
  a.w1 = (const unsigned short*)d_in[6]; a.w3 = (const unsigned short*)d_in[7]; a.w2 = (const unsigned short*)d_in[8];
  a.out = (unsigned short*)d_out; a.ws = (unsigned char*)d_ws;
  fused_decode<<<NWG, NTHR, 0, stream>>>(a);
}
```

```cpp
#define STAMPS 1
#define DIAG 1
#define DIAG_I 9
#define DIAG_W0 0
#define DIAG_W1 8
#define DIAG_S 10
#include <hip/hip_runtime.h>
#include <hip/hip_bf16.h>
#include <stdint.h>

#define KDIST 4
#define KSTAG 1
#define GNT 0
#define QLD 16
#define MIMIC 1
#define LATEFLUSH 0

namespace {

constexpr int NWG = 256, NTHR = 768, NRED = 64;
constexpr int DM = 512, NH = 32, SK = 16384, DH = 128, FF = 1792;
constexpr float LOG2E = 1.4426950408889634f;
constexpr float SCALE = 0.08838834764831845f;
#define MLS 16
constexpr size_t WS_EPOCH = 0, WS_ML = 8192;
constexpr size_t WS_Y = 65536;
constexpr int YB = 32768 + 128;
constexpr size_t WS_Q = WS_Y + (1u << 20) + 8192;
constexpr size_t WS_G = WS_Q + (512u << 10);
constexpr size_t WS_STAMP = 8u << 20;
constexpr int L_S = 0;
constexpr int L_RED = L_S + 4096;
constexpr int L_Y = L_RED + 2048;
constexpr int L_Y2 = L_Y + 4096;
constexpr int L_Q = L_Y2 + 4096;
constexpr int L_ML = L_Q + 64;
constexpr int L_FW = L_ML + 8;
constexpr int L_FIN = L_FW + 4;
constexpr int L_CNT = L_FIN + 128;
constexpr int L_REF = L_CNT + 4;
constexpr int L_END = L_REF + 4;
#define TR(i) ((void)0)

#define GAS __attribute__((address_space(1)))
#define LAS __attribute__((address_space(3)))
#define RLX_AGENT __ATOMIC_RELAXED, __HIP_MEMORY_SCOPE_AGENT
typedef unsigned u32x4 __attribute__((ext_vector_type(4)));
typedef unsigned u32x2 __attribute__((ext_vector_type(2)));
typedef float f32x2 __attribute__((ext_vector_type(2)));
typedef float f32x4 __attribute__((ext_vector_type(4)));
typedef __bf16 bf16x2 __attribute__((ext_vector_type(2)));
typedef GAS unsigned gu32;
typedef GAS unsigned long long gu64;

__device__ __forceinline__ float bf_lo(unsigned u) { return __builtin_bit_cast(float, u << 16); }
__device__ __forceinline__ float bf_hi(unsigned u) { return __builtin_bit_cast(float, u & 0xffff0000u); }
__device__ __forceinline__ f32x2 bf_unpack(unsigned u) { return f32x2{bf_lo(u), bf_hi(u)}; }
__device__ __forceinline__ unsigned pack_bf16(float lo, float hi) {
  bf16x2 r = {(__bf16)lo, (__bf16)hi};
  return __builtin_bit_cast(unsigned, r);
}
__device__ __forceinline__ float rbf(float x) {
  return (float)(__bf16)x;
}
__device__ __forceinline__ float dot2(unsigned a, unsigned b, float acc) {
  return __builtin_amdgcn_fdot2_f32_bf16(__builtin_bit_cast(bf16x2, a), __builtin_bit_cast(bf16x2, b), acc, false);
}
template <int CTRL> __device__ __forceinline__ float dpp(float x) {
  return __builtin_bit_cast(float, __builtin_amdgcn_mov_dpp(__builtin_bit_cast(int, x), CTRL, 0xf, 0xf, true));
}
constexpr int XOR1 = 0xB1, XOR2 = 0x4E, XOR7 = 0x141, ROR2 = 0x122, ROR4 = 0x124, ROR8 = 0x128;
__device__ __forceinline__ float xrow_sum(float x) {
  auto s = __builtin_amdgcn_permlane16_swap(__float_as_uint(x), __float_as_uint(x), false, false);
  x = __uint_as_float(s[0]) + __uint_as_float(s[1]);
  auto t = __builtin_amdgcn_permlane32_swap(__float_as_uint(x), __float_as_uint(x), false, false);
  return __uint_as_float(t[0]) + __uint_as_float(t[1]);
}
__device__ __forceinline__ float xrow_max(float x) {
  auto s = __builtin_amdgcn_permlane16_swap(__float_as_uint(x), __float_as_uint(x), false, false);
  x = fmaxf(__uint_as_float(s[0]), __uint_as_float(s[1]));
  auto t = __builtin_amdgcn_permlane32_swap(__float_as_uint(x), __float_as_uint(x), false, false);
  return fmaxf(__uint_as_float(t[0]), __uint_as_float(t[1]));
}
__device__ __forceinline__ float tr16(float a, float b) {
  auto s = __builtin_amdgcn_permlane16_swap(__float_as_uint(a), __float_as_uint(b), false, false);
  return __uint_as_float(s[0]) + __uint_as_float(s[1]);
}
__device__ __forceinline__ float tr32(float a, float b) {
  auto s = __builtin_amdgcn_permlane32_swap(__float_as_uint(a), __float_as_uint(b), false, false);
  return __uint_as_float(s[0]) + __uint_as_float(s[1]);
}
__device__ __forceinline__ float wave_sum(float x) {
  x += dpp<XOR1>(x); x += dpp<XOR2>(x); x += dpp<XOR7>(x); x += dpp<ROR8>(x);
  return xrow_sum(x);
}
__device__ __forceinline__ float wave_max(float x) {
  x = fmaxf(x, dpp<XOR1>(x)); x = fmaxf(x, dpp<XOR2>(x)); x = fmaxf(x, dpp<XOR7>(x)); x = fmaxf(x, dpp<ROR8>(x));
  return xrow_max(x);
}
__device__ __forceinline__ void lds_wait() { asm volatile("s_waitcnt lgkmcnt(0)" ::: "memory"); }
__device__ __forceinline__ void st_granule(gu64* g, unsigned tag, float v) {
  __hip_atomic_store(g, ((unsigned long long)tag << 32) | (unsigned long long)__float_as_uint(v), RLX_AGENT);
}
constexpr unsigned SPIN_TICKS = 20000000u;
__device__ __forceinline__ unsigned now32() { return (unsigned)__builtin_amdgcn_s_memrealtime(); }

struct Args {
  const unsigned short* x; const unsigned short* k; const unsigned short* v; const unsigned short* wq; const unsigned short* wo;
  const unsigned short* w1; const unsigned short* w3; const unsigned short* w2;
  unsigned short* out; unsigned char* ws;
};

#define STAMP(i) do { __builtin_amdgcn_sched_barrier(0); unsigned long long t_; asm volatile("s_memrealtime %0\n\ts_waitcnt lgkmcnt(0)" : "=s"(t_) :: "memory"); ts[i] = (unsigned)t_ | 1u; __builtin_amdgcn_sched_barrier(0); } while (0)
#define STAMP_FLUSH() do { const unsigned tg_ = dg_epoch + 0x9E3779B9u; while (now32() - ts[0] < 12000u) __builtin_amdgcn_s_sleep(64); if (lane == 0) { _Pragma("unroll") for (int i_ = 0; i_ < 16; ++i_) st_granule((gu64*)(a.ws + WS_STAMP) + (blockIdx.x * 16 + wave) * 16 + i_, tg_, __uint_as_float(ts[i_])); } \
  if (blockIdx.x == 0 && wave == 0) { \
    int mn0 = 1 << 30, mx = -(1 << 30), mn = 1 << 30; float sum = 0.f; \
    for (int i = 0; i < 4; ++i) for (int w = 0; w < 12; ++w) { \
      gu64* p = (gu64*)(a.ws + WS_STAMP) + ((lane * 4 + i) * 16 + w) * 16; unsigned long long g0, g1; \
      do { g0 = __hip_atomic_load(p, RLX_AGENT); g1 = __hip_atomic_load(p + DIAG_I, RLX_AGENT); } while (((unsigned)(g0 >> 32) != tg_ || (unsigned)(g1 >> 32) != tg_) && (unsigned)__builtin_amdgcn_s_memrealtime() - ts[0] < 1000000u); \
      mn0 = min(mn0, (int)((unsigned)g0 - ts[0])); \
      if (w >= DIAG_W0 && w < DIAG_W1) { const int d = (int)((unsigned)g1 - ts[0]); mx = max(mx, d); mn = min(mn, d); sum += (float)d; } \
    } \
    mn0 = -(int)wave_max((float)-mn0); mx = (int)wave_max((float)mx); mn = -(int)wave_max((float)-mn); sum = wave_sum(sum) / (256.f * (DIAG_W1 - DIAG_W0)); \
    const int st = DIAG == 0 ? 0 : DIAG == 1 ? (int)sum - mn0 : DIAG == 2 ? mx - mn0 : mn - mn0; \
    const int until = __builtin_amdgcn_readfirstlane(mn0 + 30000 + DIAG_S * st); \
    while ((int)((unsigned)__builtin_amdgcn_s_memrealtime() - ts[0]) < until) __builtin_amdgcn_s_sleep(1); \
  } } while (0)
#define STAMP_DECL unsigned ts[16] = {0, 0, 0, 0, 0, 0, 0, 0, 0, 0, 0, 0, 0, 0, 0, 0}

typedef __amdgpu_buffer_rsrc_t rsrc_t;
#define WPOL 2
#define NPRE 3
__device__ __forceinline__ u32x4 ldw(rsrc_t r, int voff, int soff) { return __builtin_amdgcn_raw_buffer_load_b128(r, voff, soff, WPOL); }
__device__ __forceinline__ u32x4 ldnt(rsrc_t r, int voff, int soff) { return __builtin_amdgcn_raw_buffer_load_b128(r, voff, soff, 2); }
#define TRK 0
#define WOD 2
#define QDELAY 32
#define DRY 1
#define FSLEEP 4
#define B0POS (PDIST + 1)
#define SFIRST 1
#define CODEPF 1
#define KMIX 0
#define GLATE 1
#define FDELAY 0
#define WIDE_T 3300
#define LAG_A 100
#define LAG_B 110
#define LAG_W 5
#define MSLEEP 16
#define Y16 0
#define FPIPE 0
#define YLD 16
#define YST 16
#define VDIST 4
#define PRETOUCH 1
#define REDMAP 1
#define W2POL 2
#define PTM 31
#define PDIST KDIST
#define TRICKLE() __builtin_amdgcn_s_sleep(TRK)
#define HSYNC(n) do { \
    if (lane == 0) __hip_atomic_fetch_add((LAS unsigned*)lds + L_CNT + 1 + (n), 1u, __ATOMIC_RELAXED, __HIP_MEMORY_SCOPE_WORKGROUP); \
    while (__hip_atomic_load((LAS unsigned*)lds + L_CNT + 1 + (n), __ATOMIC_RELAXED, __HIP_MEMORY_SCOPE_WORKGROUP) < 8u) __builtin_amdgcn_s_sleep(0); \
    asm volatile("" ::: "memory"); \
  } while (0)
#define FRESH() int lane_ = lane0; asm volatile("" : "+v"(lane_)); \
  const int lane = lane_, r = lane >> 4, c = lane & 15, tid = wave * 64 + lane, voff = lane * 16; (void)r; (void)c; (void)tid; (void)voff
#define SB() __builtin_amdgcn_sched_barrier(0)
#define VM_DRAIN() do { SB(); asm volatile("s_waitcnt vmcnt(0)" ::: "memory"); SB(); } while (0)

__global__ void __launch_bounds__(NTHR, 3) fused_decode(Args a) {
  __shared__ __attribute__((aligned(16))) float lds[L_END];
  const int tid = threadIdx.x, lane = tid & 63, wave = __builtin_amdgcn_readfirstlane(tid >> 6);
  const int r = lane >> 4, c = lane & 15;
  const int xg = blockIdx.x & 7, j = blockIdx.x >> 3;
  const int b = j >> 4, h = xg * 4 + ((j >> 2) & 3), sp = j & 3;
  const int chunk = b * 128 + h * 4 + sp;
  const int voff = lane * 16;
  STAMP_DECL;
  STAMP(0);
  unsigned dg_epoch = *(const __attribute__((address_space(4))) unsigned*)(a.ws + WS_EPOCH);

  const int g8 = b * 4 + sp;
  const int qrow = g8 * 64 + wave * 8 + 2 * r, mrow = g8 * 64 + wave * 8 + (lane >> 3);
  u32x4 xv0, xv1, wq0, wq1;
  unsigned xq0, xq1, junk = 0;
  if (wave < 8) {
    xv0 = *(const GAS u32x4*)(a.x + lane * 8); xv1 = *(const GAS u32x4*)(a.x + DM + lane * 8);
    xq0 = *(const gu32*)(a.x + qrow); xq1 = *(const gu32*)(a.x + DM + qrow);
    const rsrc_t rq = __builtin_amdgcn_make_buffer_rsrc((void*)(a.wq + h * DH), 0, (DM * NH * DH - h * DH) * 2, 0x00020000);
    wq0 = ldnt(rq, qrow * (NH * DH * 2) + c * 16, 0);
    wq1 = ldnt(rq, qrow * (NH * DH * 2) + c * 16, NH * DH * 2);
    if (wave >= 6 && j < CODEPF) {
      unsigned long long pc; int len;
      asm volatile("s_getpc_b64 %0\n.Lfd_top:\n\ts_mov_b32 %1, .Lfd_end-.Lfd_top" : "=s"(pc), "=s"(len));
      junk = *(const gu32*)(pc + (unsigned)min(((wave - 6) * 64 + lane) * 128, max(len, 0)));
    }
  }
  int role = wave;
  asm volatile("" : "+s"(role));
  if (__builtin_expect(role >= 8, SFIRST)) {
    const int sw = wave - 8;
    constexpr int HB = SK * DH * 2;
    const unsigned short* kp = a.k + (size_t)(chunk >> 2) * SK * DH;
    const unsigned short* vp = a.v + (size_t)(chunk >> 2) * SK * DH;
    const rsrc_t rk = __builtin_amdgcn_make_buffer_rsrc((void*)kp, 0, HB, 0x00020000);
    const rsrc_t rv = __builtin_amdgcn_make_buffer_rsrc((void*)vp, 0, HB, 0x00020000);
    const int stag = j * KSTAG, voffh = voff + HB / 2;
#define ROWVO(n) ((((n) & 1) ? voffh : voff) + (((n) & 7) >> 1) * 1024)
#define ROWSO(pair, n) (((((pair) + ((n) >> 3) + stag) & 31) << 14) + (sw << 12) + (sp << 19))
#define LDROW(rs, pair, n) ldnt(rs, ROWVO(n), ROWSO(pair, n))
    __builtin_amdgcn_s_setprio(2);
    u32x4 buf[8], pre[NPRE * 8];
    STAMP(12);
    if (B0POS == 0) { __builtin_amdgcn_s_barrier(); SB(); }
    STAMP(13);
#pragma unroll
    for (int u = 0; u < NPRE * 8; ++u) {
      pre[u] = LDROW(rk, 0, u); SB();
      if (u + 1 == B0POS) { __builtin_amdgcn_s_barrier(); SB(); }
      if (u >= PDIST) { asm volatile("s_waitcnt vmcnt(%0)" :: "i"(PDIST)); SB(); }
      if (u == PDIST) STAMP(14);
      if ((u & 7) == 6 && u > 8) { TR((u >> 3) - 1); SB(); }
    }
#pragma unroll
    for (int u = 0; u < 8; ++u) {
      buf[u] = LDROW(rk, NPRE, u); SB();
      if (u < 7) { asm volatile("s_waitcnt vmcnt(%0)" :: "i"(PDIST)); SB(); }
    }
    TR(2);
    STAMP(1);
    __builtin_amdgcn_s_barrier();
    asm volatile("" ::: "memory");
    asm volatile("" : "+s"(dg_epoch));
    const u32x4 qp = *(const LAS u32x4*)(lds + L_Q + c * 4);
    STAMP(2);
    LAS float* S = (LAS float*)lds + L_S + sw * 1024;
    float mx = -INFINITY;
#define DOT(w) dot2((w)[3], qp[3], dot2((w)[2], qp[2], dot2((w)[1], qp[1], dot2((w)[0], qp[0], 0.f))))
#define K_FIN(tt) do { \
      _Pragma("unroll") for (int u = 0; u < 4; ++u) { const float lo = x[u] + dpp<ROR8>(x[u]), hi = x[u + 4] + dpp<ROR8>(x[u + 4]); x[u] = (c & 8) ? hi : lo; } \
      _Pragma("unroll") for (int u = 0; u < 2; ++u) { const float lo = x[u] + dpp<XOR7>(x[u]), hi = x[u + 2] + dpp<XOR7>(x[u + 2]); x[u] = (c & 4) ? hi : lo; } \
      { const float lo = x[0] + dpp<XOR2>(x[0]), hi = x[1] + dpp<XOR2>(x[1]); x[0] = (c & 2) ? hi : lo; } \
      float sc = x[0] + dpp<XOR1>(x[0]); \
      sc = rbf(rbf(sc) * SCALE); \
      mx = fmaxf(mx, sc); \
      S[(tt) * 32 + r * 8 + (c >> 1)] = sc; \
      SB(); \
    } while (0)
    static_assert(KDIST <= 6 && KDIST >= 3 && NPRE >= 1 && PDIST <= NPRE * 8, "ring of 8");
#define PACE() do { asm volatile("s_waitcnt vmcnt(%0)" :: "i"(((KMIX >> u) & 1) ? KDIST - 1 : KDIST)); SB(); } while (0)
    {
      float x[8];
#pragma unroll
      for (int u = 0; u < 8; ++u) {
        if (u > 0 && u <= KDIST + 1) { buf[u - 1] = LDROW(rk, NPRE + 1, u - 1); SB(); }
        x[u] = DOT(buf[u]);
        SB();
      }
      K_FIN(NPRE);
    }
#pragma unroll
    for (int pp = 0; pp < NPRE; ++pp) {
      float x[8];
#pragma unroll
      for (int w = 0; w < 8; ++w) x[w] = DOT(pre[pp * 8 + w]);
      K_FIN(pp);
    }
#pragma unroll
    for (int u = KDIST + 1; u < 7; ++u) { PACE(); buf[u] = LDROW(rk, NPRE + 1, u); SB(); }
    TR(3);
    int last = 31;
    asm volatile("" : "+s"(last));
#pragma nounroll
    for (int tt = NPRE + 1; tt <= last; ++tt) {
      const unsigned short* nb = tt == last ? vp : kp;
      const rsrc_t rn = __builtin_amdgcn_make_buffer_rsrc((void*)nb, 0, HB, 0x00020000);
      float x[8];
#pragma unroll
      for (int u = 0; u < 8; ++u) {
        const int n = u + 7;
        PACE();
        buf[n & 7] = LDROW(n >= 8 ? rn : rk, tt, n);
        SB();
        x[u] = DOT(buf[u]);
        SB();
      }
      K_FIN(tt);
      TR(tt);
    }
    STAMP(3);
    mx = wave_max(mx);
    if (lane == 0) lds[L_ML + sw] = mx;
    lds_wait();
    if (lane == 0) __hip_atomic_fetch_add((LAS unsigned*)lds + L_CNT, 1u, __ATOMIC_RELAXED, __HIP_MEMORY_SCOPE_WORKGROUP);
    STAMP(4);
    f32x2 acc[4] = {{0.f, 0.f}, {0.f, 0.f}, {0.f, 0.f}, {0.f, 0.f}};
    f32x4 s0 = *(const LAS f32x4*)(S + r * 8), s1;
    int el = -1, lag = -1;
#pragma nounroll
    for (int tt = 0; tt <= last; ++tt) {
      const rsrc_t rn = __builtin_amdgcn_make_buffer_rsrc((void*)vp, 0, tt == last ? 0 : HB, 0x00020000);
      unsigned ref, tnow;
#pragma unroll
      for (int u = 0; u < 8; ++u) {
        const int n = u + 7;
        const int so = ROWSO(tt, n);
        asm volatile("s_cmpk_gt_i32 %0, %4\n\ts_cbranch_scc1 1f\n\ts_waitcnt vmcnt(%2)\n\ts_cmp_gt_i32 %1, 0\n\ts_cbranch_scc1 1f\n\ts_waitcnt vmcnt(%3)\n1:"
                     :: "s"(el), "s"(lag), "i"(LAG_W), "i"(VDIST), "i"(WIDE_T), "s"(so) : "scc");
        SB();
        buf[n & 7] = ldnt(n >= 8 ? rn : rv, ROWVO(n), so);
        SB();
        if (u == 0) s1 = *(const LAS f32x4*)(S + tt * 32 + r * 8 + 4);
        if (u == 4) { s0 = *(const LAS f32x4*)(S + ((tt + 1) & 31) * 32 + r * 8); ref = *(volatile LAS unsigned*)(lds + L_REF); tnow = now32(); }
        const float p = __builtin_amdgcn_exp2f(((u < 4 ? s0[u & 3] : s1[u & 3]) - mx) * LOG2E);
#pragma unroll
        for (int k = 0; k < 4; ++k) acc[k] = __builtin_elementwise_fma(bf_unpack(buf[u][k]), f32x2{p, p}, acc[k]);
        asm volatile("" : "+v"(acc[0]), "+v"(acc[1]), "+v"(acc[2]), "+v"(acc[3]));
        if (u == 6) {
          el = (int)(tnow - __builtin_amdgcn_readfirstlane(ref));
          lag = el - (LAG_A + LAG_B / 2 + tt * LAG_B);
        }
        SB();
      }
      TR(32 + tt);
    }
    STAMP(5);
    *(LAS f32x4*)(lds + L_RED + (sw * 4 + r) * 128 + c * 8) = f32x4{acc[0][0], acc[0][1], acc[1][0], acc[1][1]};
    *(LAS f32x4*)(lds + L_RED + (sw * 4 + r) * 128 + c * 8 + 4) = f32x4{acc[2][0], acc[2][1], acc[3][0], acc[3][1]};
    lds_wait();
    __builtin_amdgcn_s_barrier();
    __builtin_amdgcn_s_barrier();
    if (blockIdx.x < NRED) __builtin_amdgcn_s_barrier();
    STAMP_FLUSH();
    return;
  }

  unsigned epoch = *(const __attribute__((address_space(4))) unsigned*)(a.ws + WS_EPOCH);
  SB();
  if (tid < 5) ((LAS unsigned*)lds)[L_CNT + tid] = tid < 4 ? 0u : (unsigned)__builtin_amdgcn_s_memrealtime() + 0x40000000u;
  lds_wait();
  __builtin_amdgcn_s_barrier();
  SB();
  float mb0, mb1, sd0, sd1;
  {
    float f0[8], f1[8];
#pragma unroll
    for (int k = 0; k < 4; ++k) { f0[2 * k] = bf_lo(xv0[k]); f0[2 * k + 1] = bf_hi(xv0[k]); f1[2 * k] = bf_lo(xv1[k]); f1[2 * k + 1] = bf_hi(xv1[k]); }
    float s0 = 0.f, s1 = 0.f;
#pragma unroll
    for (int k = 0; k < 8; ++k) { s0 += f0[k]; s1 += f1[k]; }
    const float m0 = wave_sum(s0) * (1.f / DM), m1 = wave_sum(s1) * (1.f / DM);
    float v0 = 0.f, v1 = 0.f;
#pragma unroll
    for (int k = 0; k < 8; ++k) { const float d0 = f0[k] - m0, d1 = f1[k] - m1; v0 = fmaf(d0, d0, v0); v1 = fmaf(d1, d1, v1); }
    v0 = wave_sum(v0) * (1.f / DM); v1 = wave_sum(v1) * (1.f / DM);
    mb0 = rbf(m0); sd0 = rbf(__builtin_sqrtf(rbf(rbf(v0) + 1e-5f)));
    mb1 = rbf(m1); sd1 = rbf(__builtin_sqrtf(rbf(rbf(v1) + 1e-5f)));
  }
#define XN(xf, mb, sd) rbf(rbf((xf) - (mb)) / (sd))
  asm volatile("" : "+s"(epoch));
  asm volatile("" : "+s"(dg_epoch));
  const unsigned tag = epoch + 0x9E3779B9u;
  STAMP(10);
  {
    const float a0 = XN(bf_lo(xq0), mb0, sd0), a1 = XN(bf_hi(xq0), mb0, sd0), b0 = XN(bf_lo(xq1), mb1, sd1), b1 = XN(bf_hi(xq1), mb1, sd1);
    float t[8];
#pragma unroll
    for (int k = 0; k < 4; ++k) {
      const f32x2 w0 = bf_unpack(wq0[k]), w1 = bf_unpack(wq1[k]);
      const f32x2 qa = __builtin_elementwise_fma(w1, f32x2{a1, a1}, w0 * a0), qb = __builtin_elementwise_fma(w1, f32x2{b1, b1}, w0 * b0);
      t[2 * k] = tr32(qa[0], qb[0]); t[2 * k + 1] = tr32(qa[1], qb[1]);
    }
    f32x4 o;
#pragma unroll
    for (int k = 0; k < 4; ++k) o[k] = tr16(t[k], t[4 + k]);
    *(LAS f32x4*)(lds + L_Y + wave * 256 + (r >> 1) * 128 + c * 8 + (r & 1) * 4) = o;
    lds_wait();
  }
  STAMP(11);
  HSYNC(0);
  STAMP(12);
  gu64* Q = (gu64*)(a.ws + WS_Q) + h * 2048;
  if (tid < 256) {
    const LAS float* P = (const LAS float*)lds + L_Y + tid;
    st_granule(Q + g8 * 256 + tid, tag, ((P[0] + P[256]) + (P[512] + P[768])) + ((P[1024] + P[1280]) + (P[1536] + P[1792])));
  }
  STAMP(9);
  asm volatile("" :: "v"(junk));
  if (wave == 7) {
    __builtin_amdgcn_s_sleep(QDELAY);
    const rsrc_t rg = __builtin_amdgcn_make_buffer_rsrc((void*)(a.ws + WS_Q + (size_t)h * 16384 + b * 1024), 0, 16384, 0x00020000);
    const unsigned t0 = now32();
    u32x4 g[8];
    while (true) {
      bool ok = true;
#pragma unroll
      for (int i = 0; i < 8; ++i) g[i] = __builtin_amdgcn_raw_buffer_load_b128(rg, voff, i * 2048, QLD);
#pragma unroll
      for (int i = 0; i < 8; ++i) ok = ok & (g[i][1] == tag) & (g[i][3] == tag);
      if (__all(ok)) break;
      if (now32() - t0 > SPIN_TICKS) break;
      __builtin_amdgcn_s_sleep(8);
      asm volatile("" ::: "memory");
    }
    f32x2 q2[8];
#pragma unroll
    for (int i = 0; i < 8; ++i) q2[i] = f32x2{__uint_as_float(g[i][0]), __uint_as_float(g[i][2])};
    const f32x2 qs = ((q2[0] + q2[1]) + (q2[2] + q2[3])) + ((q2[4] + q2[5]) + (q2[6] + q2[7]));
    ((LAS unsigned*)lds)[L_Q + lane] = pack_bf16(qs[0], qs[1]);
    lds_wait();
  }
  STAMP(1);
  __builtin_amdgcn_s_barrier();
  asm volatile("" ::: "memory");
  gu64* G = (gu64*)(a.ws + WS_G);
  float gsum = 0.f;
  const int red = blockIdx.x < NRED ? (REDMAP ? (xg * 4 + (j & 3)) * 2 + (j >> 2) : (int)blockIdx.x) : -1;
  const unsigned xw = (red >= 0 && tid < 8) ? *(const gu32*)(a.x + (red & 1) * DM + (red >> 1) * 16 + tid * 2) : 0u;
  if (h < 28) {
    const unsigned short xm0 = a.x[mrow], xm1 = a.x[DM + mrow];
    const rsrc_t r1 = __builtin_amdgcn_make_buffer_rsrc((void*)(a.w1 + h * 64), 0, (DM * FF - h * 64) * 2, 0x00020000);
    const rsrc_t r3 = __builtin_amdgcn_make_buffer_rsrc((void*)(a.w3 + h * 64), 0, (DM * FF - h * 64) * 2, 0x00020000);
    const u32x4 wg = ldnt(r1, mrow * (FF * 2) + (lane & 7) * 16, 0);
    const u32x4 wu = ldnt(r3, mrow * (FF * 2) + (lane & 7) * 16, 0);
    const float a0 = XN(bf_lo(xm0), mb0, sd0), b0 = XN(bf_lo(xm1), mb1, sd1);
    float t[8];
#pragma unroll
    for (int k = 0; k < 4; ++k) {
      const f32x2 g = bf_unpack(wg[k]), u = bf_unpack(wu[k]);
#pragma unroll
      for (int e = 0; e < 2; ++e) t[2 * k + e] = tr16(tr32(g[e] * a0, g[e] * b0), tr32(u[e] * a0, u[e] * b0));
    }
    f32x4 o;
#pragma unroll
    for (int k = 0; k < 4; ++k) { const float lo = t[k] + dpp<ROR8>(t[k]), hi = t[4 + k] + dpp<ROR8>(t[4 + k]); o[k] = (c & 8) ? hi : lo; }
    *(LAS f32x4*)(lds + L_Y + 2048 + wave * 256 + r * 64 + (c & 7) * 8 + (c >> 3) * 4) = o;
    lds_wait();
    HSYNC(1);
    const LAS float* P = (const LAS float*)lds + L_Y + 2048 + (tid & 255);
    gsum = ((P[0] + P[256]) + (P[512] + P[768])) + ((P[1024] + P[1280]) + (P[1536] + P[1792]));
  }
  const int lane0 = lane;
  int pass = DRY ? 0 : 1;
  asm volatile("" : "+s"(pass));
#pragma nounroll
  for (; pass < 2; ++pass) {
    f32x2 wf[64];
    f32x2 yacc[4] = {{0.f, 0.f}, {0.f, 0.f}, {0.f, 0.f}, {0.f, 0.f}};
    if (!pass) {
#pragma unroll
      for (int i = 0; i < 64; ++i) asm volatile("" : "=v"(wf[i]));
    } else {
      {
        FRESH();
        const rsrc_t ro = __builtin_amdgcn_make_buffer_rsrc((void*)(a.wo + (size_t)h * DH * DM), 0, DH * DM * 2, 0x00020000);
#pragma unroll
        for (int i0 = 0; i0 < 16; i0 += WOD) {
          u32x4 w[WOD];
#pragma unroll
          for (int i = 0; i < WOD; ++i) w[i] = ldw(ro, voff + ((i0 + i) & 3) * 1024, (wave * 16 + ((i0 + i) & ~3)) * 1024);
          VM_DRAIN();
#pragma unroll
          for (int i = 0; i < WOD; ++i)
#pragma unroll
            for (int k = 0; k < 4; ++k) { wf[(i0 + i) * 4 + k] = bf_unpack(w[i][k]); asm volatile("" : "+v"(wf[(i0 + i) * 4 + k])); }
          TRICKLE();
        }
      }
      STAMP(2);
      while (__hip_atomic_load((LAS unsigned*)lds + L_CNT, __ATOMIC_RELAXED, __HIP_MEMORY_SCOPE_WORKGROUP) < 4u) __builtin_amdgcn_s_sleep(8);
      asm volatile("" ::: "memory");
      gu64* ml = (gu64*)(a.ws + WS_ML);
      float mw = 0.f;
      const unsigned tk = (unsigned)__builtin_amdgcn_s_memrealtime();
      if (wave == 7) {
        FRESH();
        mw = lds[L_ML + (lane & 3)];
        float m = fmaxf(mw, dpp<XOR1>(mw)); m = fmaxf(m, dpp<XOR2>(m));
        float l = 0.f;
#pragma unroll 4
        for (int i = 0; i < 16; ++i) {
          const f32x4 sv = *(const LAS f32x4*)(lds + L_S + i * 256 + lane * 4);
#pragma unroll
          for (int k = 0; k < 4; ++k) l += __builtin_amdgcn_exp2f((sv[k] - m) * LOG2E);
        }
        l = wave_sum(l);
        if (lane < 16) st_granule(ml + chunk * MLS + lane, tag, lane == 0 ? m : lane == 1 ? l : __uint_as_float(tk));
      }
      else if (wave < 4 && h < 28) { FRESH(); __builtin_amdgcn_s_sleep(16); st_granule(G + (h * 8 + g8) * 256 + tid, tag, gsum); }
      __builtin_amdgcn_s_sleep(127); __builtin_amdgcn_s_sleep(MSLEEP);
      if (wave < 7) {
        FRESH();
        const int f0 = (h * 4 + sp) * 14 + wave * 2;
        const rsrc_t r2 = __builtin_amdgcn_make_buffer_rsrc((void*)(a.w2 + (size_t)f0 * DM), 0, 2 * DM * 2, 0x00020000);
        const u32x4 w20 = __builtin_amdgcn_raw_buffer_load_b128(r2, voff, 0, W2POL), w21 = __builtin_amdgcn_raw_buffer_load_b128(r2, voff + 1024, 0, W2POL);
        const int f = f0 + (r & 1);
        gu64* gp = G + ((f >> 6) * 8 + (c & 7)) * 256 + b * 128 + (c >> 3) * 64 + (f & 63);
        const unsigned t0 = now32();
        unsigned long long g;
        while (true) {
          g = __hip_atomic_load(gp, RLX_AGENT);
          if (__all((unsigned)(g >> 32) == tag)) break;
          if (now32() - t0 > SPIN_TICKS) break;
          __builtin_amdgcn_s_sleep(127);
        }
        float s = __uint_as_float((unsigned)g);
        s += dpp<XOR1>(s); s += dpp<XOR2>(s); s += dpp<XOR7>(s);
        const float gs = rbf(s), us = rbf(dpp<ROR8>(s));
        const float si = rbf(gs / (1.f + __expf(-gs)));
        const float hv = rbf(si * us);
        const float h0 = __builtin_bit_cast(float, __builtin_amdgcn_readlane(__builtin_bit_cast(int, hv), 0));
        const float h1 = __builtin_bit_cast(float, __builtin_amdgcn_readlane(__builtin_bit_cast(int, hv), 16));
#pragma unroll
        for (int k = 0; k < 4; ++k) yacc[k] = __builtin_elementwise_fma(bf_unpack(w21[k]), f32x2{h1, h1}, bf_unpack(w20[k]) * h0);
      } else {
        FRESH();
        {
          const unsigned long long gt = __hip_atomic_load(ml + (lane * 4 + (lane & 3)) * MLS + 2, RLX_AGENT);
          const bool v = (unsigned)(gt >> 32) == tag;
          const float n = wave_sum(v ? 1.f : 0.f), d = wave_sum(v ? (float)(int)((unsigned)gt - tk) : 0.f);
          if (lane == 0 && n > 0.f) ((LAS unsigned*)lds)[L_REF] = tk + (unsigned)(int)(d / n);
        }
        const unsigned t0 = now32();
        unsigned long long gm, gl;
        while (true) {
          gm = __hip_atomic_load(ml + ((chunk & ~3) + (lane & 3)) * MLS, RLX_AGENT);
          gl = __hip_atomic_load(ml + ((chunk & ~3) + (lane & 3)) * MLS + 1, RLX_AGENT);
          if (__all((unsigned)(gm >> 32) == tag && (unsigned)(gl >> 32) == tag)) break;
          if (now32() - t0 > SPIN_TICKS) break;
          __builtin_amdgcn_s_sleep(127);
        }
        const float ms = __uint_as_float((unsigned)gm), ls = __uint_as_float((unsigned)gl);
        float M = fmaxf(ms, dpp<XOR1>(ms)); M = fmaxf(M, dpp<XOR2>(M));
        float L = ls * __builtin_amdgcn_exp2f((ms - M) * LOG2E);
        L += dpp<XOR1>(L); L += dpp<XOR2>(L);
        if (lane < 4) lds[L_FW + lane] = __builtin_amdgcn_exp2f((mw - M) * LOG2E) / L;
        lds_wait();
        {
          const unsigned char* pa = (lane < 32 || red < 0) ? a.ws + WS_Y + (lane & PTM) * YB + 32768 : (const unsigned char*)a.out + (red & 1) * (DM * 2) + (red >> 1) * 32;
          const unsigned jk = *(const volatile gu32*)pa;
          asm volatile("" :: "v"(jk));
        }
        if (blockIdx.x == NWG - 1) {
          while (true) {
            bool ok = true;
#pragma unroll
            for (int i = 0; i < 4; ++i) ok = ok & ((unsigned)(__hip_atomic_load(ml + (i * 64 + lane) * MLS + 2, RLX_AGENT) >> 32) == tag);
            if (__all(ok)) break;
            if (now32() - t0 > SPIN_TICKS) break;
            __builtin_amdgcn_s_sleep(127);
          }
          if (lane == 0) __hip_atomic_store((gu32*)(a.ws + WS_EPOCH), tag, RLX_AGENT);
        }
        STAMP(3);
      }
    }
    FRESH();
    if (pass) __builtin_amdgcn_s_barrier();
    asm volatile("" ::: "memory");
    STAMP(6);
    {
      const LAS float* P = (const LAS float*)lds + L_RED + (4 * r) * 128 + wave * 16 + c;
      float o = ((P[0] + P[128]) + (P[256] + P[384])) * lds[L_FW + r];
      o = xrow_sum(o);
#pragma unroll
      for (int i = 0; i < 16; ++i) {
        const float oi = __builtin_bit_cast(float, __builtin_amdgcn_readlane(__builtin_bit_cast(int, o), i));
#pragma unroll
        for (int k = 0; k < 4; ++k) yacc[k] = __builtin_elementwise_fma(wf[i * 4 + k], f32x2{oi, oi}, yacc[k]);
      }
    }
    *(LAS f32x4*)(lds + L_Y2 + wave * 512 + lane * 8) = f32x4{yacc[0][0], yacc[0][1], yacc[1][0], yacc[1][1]};
    *(LAS f32x4*)(lds + L_Y2 + wave * 512 + lane * 8 + 4) = f32x4{yacc[2][0], yacc[2][1], yacc[3][0], yacc[3][1]};
    lds_wait();
    if (pass) __builtin_amdgcn_s_barrier();
    asm volatile("" ::: "memory");
    const rsrc_t ry = __builtin_amdgcn_make_buffer_rsrc((void*)(a.ws + WS_Y), 0, pass ? 32 * YB : 0, 0x00020000);
    {
      const LAS float* P = (const LAS float*)lds + L_Y2 + tid;
      const float y = ((P[0] + P[512]) + (P[1024] + P[1536])) + ((P[2048] + P[2560]) + (P[3072] + P[3584]));
      __builtin_amdgcn_raw_buffer_store_b64(u32x2{__float_as_uint(y), tag}, ry, (tid >> 4) * YB + (tid & 15) * 8, chunk * 128, YST);
    }
    STAMP(7);
    if (red >= 0) {
      __builtin_amdgcn_s_sleep(FDELAY);
      const unsigned t0 = now32();
      u32x4 g0 = {0u, 0u, 0u, 0u}, g1 = {0u, 0u, 0u, 0u};
      bool ok0 = false, ok1 = false;
      while (true) {
        if (!ok0) g0 = __builtin_amdgcn_raw_buffer_load_b128(ry, voff, (red >> 1) * YB + (red & 1) * 16384 + wave * 2048, YLD);
        if (!ok1) g1 = __builtin_amdgcn_raw_buffer_load_b128(ry, voff + 1024, (red >> 1) * YB + (red & 1) * 16384 + wave * 2048, YLD);
        ok0 = (g0[1] == tag) & (g0[3] == tag); ok1 = (g1[1] == tag) & (g1[3] == tag);
        if (__all(ok0 & ok1) || !pass) break;
        if (now32() - t0 > SPIN_TICKS) break;
        __builtin_amdgcn_s_sleep(FSLEEP);
        asm volatile("" ::: "memory");
      }
      float s = tr32(__uint_as_float(g0[0]) + __uint_as_float(g1[0]), __uint_as_float(g0[2]) + __uint_as_float(g1[2]));
      s = tr16(s, s);
      s += dpp<ROR8>(s);
      if ((lane & 24) == 0) lds[L_FIN + wave * 16 + (lane & 7) * 2 + (lane >> 5)] = s;
      lds_wait();
      if (pass) __builtin_amdgcn_s_barrier();
      asm volatile("" ::: "memory");
      if (tid < 8) {
        const LAS f32x2* P = (const LAS f32x2*)(lds + L_FIN + tid * 2);
        const f32x2 o = bf_unpack(xw) + (((P[0] + P[8]) + (P[16] + P[24])) + ((P[32] + P[40]) + (P[48] + P[56])));
        const rsrc_t rout = __builtin_amdgcn_make_buffer_rsrc((void*)a.out, 0, pass ? 2 * DM * 2 : 0, 0x00020000);
        __builtin_amdgcn_raw_buffer_store_b32(pack_bf16(o[0], o[1]), rout, tid * 4, (red & 1) * (DM * 2) + (red >> 1) * 32, 0);
      }
    }
  }
  asm volatile(".Lfd_end:");
  STAMP(8);
  STAMP_FLUSH();
}

}

extern "C" void kernel_launch(void* const* d_in, const int* in_sizes, int n_in, void* d_out, int out_size, void* d_ws, size_t ws_size,
                              hipStream_t stream) {
  Args a;
  a.x = (const unsigned short*)d_in[0]; a.k = (const unsigned short*)d_in[1]; a.v = (const unsigned short*)d_in[2];
  a.wq = (const unsigned short*)d_in[3]; a.wo = (const unsigned short*)d_in[4];
  a.w1 = (const unsigned short*)d_in[6]; a.w3 = (const unsigned short*)d_in[7]; a.w2 = (const unsigned short*)d_in[8];
  a.out = (unsigned short*)d_out; a.ws = (unsigned char*)d_ws;
  fused_decode<<<NWG, NTHR, 0, stream>>>(a);
}
```

```cpp
#define PRETOUCH 2
#include <hip/hip_runtime.h>
#include <hip/hip_bf16.h>
#include <stdint.h>

#define KDIST 4
#define KSTAG 1
#define GNT 0
#define QLD 16
#define MIMIC 1
#define LATEFLUSH 0
#define STAMPS 0

namespace {

constexpr int NWG = 256, NTHR = 768, NRED = 32;
constexpr int DM = 512, NH = 32, SK = 16384, DH = 128, FF = 1792;
constexpr float LOG2E = 1.4426950408889634f;
constexpr float SCALE = 0.08838834764831845f;
#define MLS 16
constexpr size_t WS_EPOCH = 0, WS_ML = 8192;
constexpr size_t WS_Y = 65536;
constexpr int YB = 32768 + 128;
constexpr size_t WS_Q = WS_Y + (1u << 20) + 8192;
constexpr size_t WS_G = WS_Q + (256u << 10);
constexpr size_t WS_STAMP = 8u << 20;
constexpr int L_S = 0;
constexpr int L_RED = L_S + 4096;
constexpr int L_Y = L_RED + 2048;
constexpr int L_Y2 = L_Y + 4096;
constexpr int L_Q = L_Y2 + 4096;
constexpr int L_ML = L_Q + 64;
constexpr int L_FW = L_ML + 8;
constexpr int L_FIN = L_FW + 4;
constexpr int L_CNT = L_FIN + 256;
constexpr int L_REF = L_CNT + 4;
constexpr int L_END = L_REF + 4;
#define TR(i) ((void)0)

#define GAS __attribute__((address_space(1)))
#define LAS __attribute__((address_space(3)))
#define RLX_AGENT __ATOMIC_RELAXED, __HIP_MEMORY_SCOPE_AGENT
typedef unsigned u32x4 __attribute__((ext_vector_type(4)));
typedef unsigned u32x2 __attribute__((ext_vector_type(2)));
typedef float f32x2 __attribute__((ext_vector_type(2)));
typedef float f32x4 __attribute__((ext_vector_type(4)));
typedef __bf16 bf16x2 __attribute__((ext_vector_type(2)));
typedef GAS unsigned gu32;
typedef GAS unsigned long long gu64;

__device__ __forceinline__ float bf_lo(unsigned u) { return __builtin_bit_cast(float, u << 16); }
__device__ __forceinline__ float bf_hi(unsigned u) { return __builtin_bit_cast(float, u & 0xffff0000u); }
__device__ __forceinline__ f32x2 bf_unpack(unsigned u) { return f32x2{bf_lo(u), bf_hi(u)}; }
__device__ __forceinline__ unsigned pack_bf16(float lo, float hi) {
  bf16x2 r = {(__bf16)lo, (__bf16)hi};
  return __builtin_bit_cast(unsigned, r);
}
__device__ __forceinline__ float rbf(float x) {
  return (float)(__bf16)x;
}
__device__ __forceinline__ float dot2(unsigned a, unsigned b, float acc) {
  return __builtin_amdgcn_fdot2_f32_bf16(__builtin_bit_cast(bf16x2, a), __builtin_bit_cast(bf16x2, b), acc, false);
}
template <int CTRL> __device__ __forceinline__ float dpp(float x) {
  return __builtin_bit_cast(float, __builtin_amdgcn_mov_dpp(__builtin_bit_cast(int, x), CTRL, 0xf, 0xf, true));
}
constexpr int XOR1 = 0xB1, XOR2 = 0x4E, XOR3 = 0x1B, XOR7 = 0x141, ROR2 = 0x122, ROR4 = 0x124, ROR8 = 0x128;
__device__ __forceinline__ float xrow_sum(float x) {
  auto s = __builtin_amdgcn_permlane16_swap(__float_as_uint(x), __float_as_uint(x), false, false);
  x = __uint_as_float(s[0]) + __uint_as_float(s[1]);
  auto t = __builtin_amdgcn_permlane32_swap(__float_as_uint(x), __float_as_uint(x), false, false);
  return __uint_as_float(t[0]) + __uint_as_float(t[1]);
}
__device__ __forceinline__ float xrow_max(float x) {
  auto s = __builtin_amdgcn_permlane16_swap(__float_as_uint(x), __float_as_uint(x), false, false);
  x = fmaxf(__uint_as_float(s[0]), __uint_as_float(s[1]));
  auto t = __builtin_amdgcn_permlane32_swap(__float_as_uint(x), __float_as_uint(x), false, false);
  return fmaxf(__uint_as_float(t[0]), __uint_as_float(t[1]));
}
__device__ __forceinline__ float tr16(float a, float b) {
  auto s = __builtin_amdgcn_permlane16_swap(__float_as_uint(a), __float_as_uint(b), false, false);
  return __uint_as_float(s[0]) + __uint_as_float(s[1]);
}
__device__ __forceinline__ float tr32(float a, float b) {
  auto s = __builtin_amdgcn_permlane32_swap(__float_as_uint(a), __float_as_uint(b), false, false);
  return __uint_as_float(s[0]) + __uint_as_float(s[1]);
}
__device__ __forceinline__ float wave_sum(float x) {
  x += dpp<XOR1>(x); x += dpp<XOR2>(x); x += dpp<XOR7>(x); x += dpp<ROR8>(x);
  return xrow_sum(x);
}
__device__ __forceinline__ float wave_max(float x) {
  x = fmaxf(x, dpp<XOR1>(x)); x = fmaxf(x, dpp<XOR2>(x)); x = fmaxf(x, dpp<XOR7>(x)); x = fmaxf(x, dpp<ROR8>(x));
  return xrow_max(x);
}
__device__ __forceinline__ void lds_wait() { asm volatile("s_waitcnt lgkmcnt(0)" ::: "memory"); }
__device__ __forceinline__ void st_granule(gu64* g, unsigned tag, float v) {
  __hip_atomic_store(g, ((unsigned long long)tag << 32) | (unsigned long long)__float_as_uint(v), RLX_AGENT);
}
constexpr unsigned SPIN_TICKS = 20000000u;
__device__ __forceinline__ unsigned now32() { return (unsigned)__builtin_amdgcn_s_memrealtime(); }

struct Args {
  const unsigned short* x; const unsigned short* k; const unsigned short* v; const unsigned short* wq; const unsigned short* wo;
  const unsigned short* w1; const unsigned short* w3; const unsigned short* w2;
  unsigned short* out; unsigned char* ws;
};

#define STAMP(i) do { } while (0)
#define STAMP_FLUSH() do { } while (0)
#define STAMP_DECL

typedef __amdgpu_buffer_rsrc_t rsrc_t;
#define WPOL 2
#define NPRE 3
__device__ __forceinline__ u32x4 ldw(rsrc_t r, int voff, int soff) { return __builtin_amdgcn_raw_buffer_load_b128(r, voff, soff, WPOL); }
__device__ __forceinline__ u32x4 ldnt(rsrc_t r, int voff, int soff) { return __builtin_amdgcn_raw_buffer_load_b128(r, voff, soff, 2); }
#define TRK 0
#define WOD 2
#define QDELAY 32
#define DRY 1
#define FSLEEP 4
#define B0POS (PDIST + 1)
#define SFIRST 1
#define CODEPF 1
#define KMIX 0
#define GLATE 1
#define FDELAY 0
#define WIDE_T 3300
#define LAG_A 100
#define LAG_B 110
#define LAG_W 5
#define MSLEEP 16
#define Y16 0
#define FPIPE 0
#define YLD 16
#define YST 16
#define VDIST 4
#define REDMAP 1
#define W2POL 2
#define PTM 15
#define PDIST KDIST
#define TRICKLE() __builtin_amdgcn_s_sleep(TRK)
#define HSYNC(n) do { \
    if (lane == 0) __hip_atomic_fetch_add((LAS unsigned*)lds + L_CNT + 1 + (n), 1u, __ATOMIC_RELAXED, __HIP_MEMORY_SCOPE_WORKGROUP); \
    while (__hip_atomic_load((LAS unsigned*)lds + L_CNT + 1 + (n), __ATOMIC_RELAXED, __HIP_MEMORY_SCOPE_WORKGROUP) < 8u) __builtin_amdgcn_s_sleep(0); \
    asm volatile("" ::: "memory"); \
  } while (0)
#define FRESH() int lane_ = lane0; asm volatile("" : "+v"(lane_)); \
  const int lane = lane_, r = lane >> 4, c = lane & 15, tid = wave * 64 + lane, voff = lane * 16; (void)r; (void)c; (void)tid; (void)voff
#define SB() __builtin_amdgcn_sched_barrier(0)
#define VM_DRAIN() do { SB(); asm volatile("s_waitcnt vmcnt(0)" ::: "memory"); SB(); } while (0)

__global__ void __launch_bounds__(NTHR, 3) fused_decode(Args a) {
  __shared__ __attribute__((aligned(16))) float lds[L_END];
  const int tid = threadIdx.x, lane = tid & 63, wave = __builtin_amdgcn_readfirstlane(tid >> 6);
  const int r = lane >> 4, c = lane & 15;
  const int xg = blockIdx.x & 7, j = blockIdx.x >> 3;
  const int b = j >> 4, h = xg * 4 + ((j >> 2) & 3), sp = j & 3;
  const int chunk = b * 128 + h * 4 + sp;
  const int voff = lane * 16;
  STAMP_DECL;
  STAMP(0);

  const int g8 = b * 4 + sp;
  const int prow = (g8 >> 2) * 256 + wave * 32 + 2 * (lane >> 2);
  u32x4 xv0, xv1, wq0, wq1;
  unsigned xq0, xq1, junk = 0;
  if (wave < 8) {
    xv0 = *(const GAS u32x4*)(a.x + lane * 8); xv1 = *(const GAS u32x4*)(a.x + DM + lane * 8);
    xq0 = *(const gu32*)(a.x + prow); xq1 = *(const gu32*)(a.x + DM + prow);
    const rsrc_t rq = __builtin_amdgcn_make_buffer_rsrc((void*)a.wq, 0, DM * NH * DH * 2, 0x00020000);
    wq0 = ldnt(rq, prow * (NH * DH * 2) + (lane & 3) * 16, h * (DH * 2) + (g8 & 3) * 64);
    wq1 = ldnt(rq, prow * (NH * DH * 2) + (lane & 3) * 16, h * (DH * 2) + (g8 & 3) * 64 + NH * DH * 2);
    if (wave >= 6 && j < CODEPF) {
      unsigned long long pc; int len;
      asm volatile("s_getpc_b64 %0\n.Lfd_top:\n\ts_mov_b32 %1, .Lfd_end-.Lfd_top" : "=s"(pc), "=s"(len));
      junk = *(const gu32*)(pc + (unsigned)min(((wave - 6) * 64 + lane) * 128, max(len, 0)));
    }
  }
  int role = wave;
  asm volatile("" : "+s"(role));
  if (__builtin_expect(role >= 8, SFIRST)) {
    const int sw = wave - 8;
    constexpr int HB = SK * DH * 2;
    const unsigned short* kp = a.k + (size_t)(chunk >> 2) * SK * DH;
    const unsigned short* vp = a.v + (size_t)(chunk >> 2) * SK * DH;
    const rsrc_t rk = __builtin_amdgcn_make_buffer_rsrc((void*)kp, 0, HB, 0x00020000);
    const rsrc_t rv = __builtin_amdgcn_make_buffer_rsrc((void*)vp, 0, HB, 0x00020000);
    const int stag = j * KSTAG, voffh = voff + HB / 2;
#define ROWVO(n) ((((n) & 1) ? voffh : voff) + (((n) & 7) >> 1) * 1024)
#define ROWSO(pair, n) (((((pair) + ((n) >> 3) + stag) & 31) << 14) + (sw << 12) + (sp << 19))
#define LDROW(rs, pair, n) ldnt(rs, ROWVO(n), ROWSO(pair, n))
    __builtin_amdgcn_s_setprio(2);
    u32x4 buf[8], pre[NPRE * 8];
    STAMP(12);
    if (B0POS == 0) { __builtin_amdgcn_s_barrier(); SB(); }
    STAMP(13);
#pragma unroll
    for (int u = 0; u < NPRE * 8; ++u) {
      pre[u] = LDROW(rk, 0, u); SB();
      if (u + 1 == B0POS) { __builtin_amdgcn_s_barrier(); SB(); }
      if (u >= PDIST) { asm volatile("s_waitcnt vmcnt(%0)" :: "i"(PDIST)); SB(); }
      if (u == PDIST) STAMP(14);
      if ((u & 7) == 6 && u > 8) { TR((u >> 3) - 1); SB(); }
    }
#pragma unroll
    for (int u = 0; u < 8; ++u) {
      buf[u] = LDROW(rk, NPRE, u); SB();
      if (u < 7) { asm volatile("s_waitcnt vmcnt(%0)" :: "i"(PDIST)); SB(); }
    }
    TR(2);
    STAMP(1);
    __builtin_amdgcn_s_barrier();
    asm volatile("" ::: "memory");
    const u32x4 qp = *(const LAS u32x4*)(lds + L_Q + c * 4);
    STAMP(2);
    LAS float* S = (LAS float*)lds + L_S + sw * 1024;
    float mx = -INFINITY;
#define DOT(w) dot2((w)[3], qp[3], dot2((w)[2], qp[2], dot2((w)[1], qp[1], dot2((w)[0], qp[0], 0.f))))
#define K_FIN(tt) do { \
      _Pragma("unroll") for (int u = 0; u < 4; ++u) { const float lo = x[u] + dpp<ROR8>(x[u]), hi = x[u + 4] + dpp<ROR8>(x[u + 4]); x[u] = (c & 8) ? hi : lo; } \
      _Pragma("unroll") for (int u = 0; u < 2; ++u) { const float lo = x[u] + dpp<XOR7>(x[u]), hi = x[u + 2] + dpp<XOR7>(x[u + 2]); x[u] = (c & 4) ? hi : lo; } \
      { const float lo = x[0] + dpp<XOR2>(x[0]), hi = x[1] + dpp<XOR2>(x[1]); x[0] = (c & 2) ? hi : lo; } \
      float sc = x[0] + dpp<XOR1>(x[0]); \
      sc = rbf(rbf(sc) * SCALE); \
      mx = fmaxf(mx, sc); \
      S[(tt) * 32 + r * 8 + (c >> 1)] = sc; \
      SB(); \
    } while (0)
    static_assert(KDIST <= 6 && KDIST >= 3 && NPRE >= 1 && PDIST <= NPRE * 8, "ring of 8");
#define PACE() do { asm volatile("s_waitcnt vmcnt(%0)" :: "i"(((KMIX >> u) & 1) ? KDIST - 1 : KDIST)); SB(); } while (0)
    {
      float x[8];
#pragma unroll
      for (int u = 0; u < 8; ++u) {
        if (u > 0 && u <= KDIST + 1) { buf[u - 1] = LDROW(rk, NPRE + 1, u - 1); SB(); }
        x[u] = DOT(buf[u]);
        SB();
      }
      K_FIN(NPRE);
    }
#pragma unroll
    for (int pp = 0; pp < NPRE; ++pp) {
      float x[8];
#pragma unroll
      for (int w = 0; w < 8; ++w) x[w] = DOT(pre[pp * 8 + w]);
      K_FIN(pp);
    }
#pragma unroll
    for (int u = KDIST + 1; u < 7; ++u) { PACE(); buf[u] = LDROW(rk, NPRE + 1, u); SB(); }
    TR(3);
    STAMP(11);
    int last = 31;
    asm volatile("" : "+s"(last));
#pragma nounroll
    for (int tt = NPRE + 1; tt <= last; ++tt) {
      const unsigned short* nb = tt == last ? vp : kp;
      const rsrc_t rn = __builtin_amdgcn_make_buffer_rsrc((void*)nb, 0, HB, 0x00020000);
      float x[8];
#pragma unroll
      for (int u = 0; u < 8; ++u) {
        const int n = u + 7;
        PACE();
        buf[n & 7] = LDROW(n >= 8 ? rn : rk, tt, n);
        SB();
        x[u] = DOT(buf[u]);
        SB();
      }
      K_FIN(tt);
      TR(tt);
    }
    STAMP(3);
    mx = wave_max(mx);
    if (lane == 0) lds[L_ML + sw] = mx;
    lds_wait();
    if (lane == 0) __hip_atomic_fetch_add((LAS unsigned*)lds + L_CNT, 1u, __ATOMIC_RELAXED, __HIP_MEMORY_SCOPE_WORKGROUP);
    STAMP(4);
    f32x2 acc[4] = {{0.f, 0.f}, {0.f, 0.f}, {0.f, 0.f}, {0.f, 0.f}};
    f32x4 s0 = *(const LAS f32x4*)(S + r * 8), s1;
    int el = -1, lag = -1;
#pragma nounroll
    for (int tt = 0; tt <= last; ++tt) {
      const rsrc_t rn = __builtin_amdgcn_make_buffer_rsrc((void*)vp, 0, tt == last ? 0 : HB, 0x00020000);
      unsigned ref, tnow;
#pragma unroll
      for (int u = 0; u < 8; ++u) {
        const int n = u + 7;
        const int so = ROWSO(tt, n);
        asm volatile("s_cmpk_gt_i32 %0, %4\n\ts_cbranch_scc1 1f\n\ts_waitcnt vmcnt(%2)\n\ts_cmp_gt_i32 %1, 0\n\ts_cbranch_scc1 1f\n\ts_waitcnt vmcnt(%3)\n1:"
                     :: "s"(el), "s"(lag), "i"(LAG_W), "i"(VDIST), "i"(WIDE_T), "s"(so) : "scc");
        SB();
        buf[n & 7] = ldnt(n >= 8 ? rn : rv, ROWVO(n), so);
        SB();
        if (u == 0) s1 = *(const LAS f32x4*)(S + tt * 32 + r * 8 + 4);
        if (u == 4) { s0 = *(const LAS f32x4*)(S + ((tt + 1) & 31) * 32 + r * 8); ref = *(volatile LAS unsigned*)(lds + L_REF); tnow = now32(); }
        const float p = __builtin_amdgcn_exp2f(((u < 4 ? s0[u & 3] : s1[u & 3]) - mx) * LOG2E);
#pragma unroll
        for (int k = 0; k < 4; ++k) acc[k] = __builtin_elementwise_fma(bf_unpack(buf[u][k]), f32x2{p, p}, acc[k]);
        asm volatile("" : "+v"(acc[0]), "+v"(acc[1]), "+v"(acc[2]), "+v"(acc[3]));
        if (u == 6) {
          el = (int)(tnow - __builtin_amdgcn_readfirstlane(ref));
          lag = el - (LAG_A + LAG_B / 2 + tt * LAG_B);
        }
        SB();
      }
      TR(32 + tt);
    }
    STAMP(5);
    *(LAS f32x4*)(lds + L_RED + (sw * 4 + r) * 128 + c * 8) = f32x4{acc[0][0], acc[0][1], acc[1][0], acc[1][1]};
    *(LAS f32x4*)(lds + L_RED + (sw * 4 + r) * 128 + c * 8 + 4) = f32x4{acc[2][0], acc[2][1], acc[3][0], acc[3][1]};
    lds_wait();
    __builtin_amdgcn_s_barrier();
    __builtin_amdgcn_s_barrier();
    if (blockIdx.x < NRED) __builtin_amdgcn_s_barrier();
    STAMP_FLUSH();
    return;
  }

  unsigned epoch = *(const __attribute__((address_space(4))) unsigned*)(a.ws + WS_EPOCH);
  SB();
  if (tid < 5) ((LAS unsigned*)lds)[L_CNT + tid] = tid < 4 ? 0u : (unsigned)__builtin_amdgcn_s_memrealtime() + 0x40000000u;
  lds_wait();
  __builtin_amdgcn_s_barrier();
  SB();
  float mb0, mb1, sd0, sd1;
  {
    float f0[8], f1[8];
#pragma unroll
    for (int k = 0; k < 4; ++k) { f0[2 * k] = bf_lo(xv0[k]); f0[2 * k + 1] = bf_hi(xv0[k]); f1[2 * k] = bf_lo(xv1[k]); f1[2 * k + 1] = bf_hi(xv1[k]); }
    float s0 = 0.f, s1 = 0.f;
#pragma unroll
    for (int k = 0; k < 8; ++k) { s0 += f0[k]; s1 += f1[k]; }
    const float m0 = wave_sum(s0) * (1.f / DM), m1 = wave_sum(s1) * (1.f / DM);
    float v0 = 0.f, v1 = 0.f;
#pragma unroll
    for (int k = 0; k < 8; ++k) { const float d0 = f0[k] - m0, d1 = f1[k] - m1; v0 = fmaf(d0, d0, v0); v1 = fmaf(d1, d1, v1); }
    v0 = wave_sum(v0) * (1.f / DM); v1 = wave_sum(v1) * (1.f / DM);
    mb0 = rbf(m0); sd0 = rbf(__builtin_sqrtf(rbf(rbf(v0) + 1e-5f)));
    mb1 = rbf(m1); sd1 = rbf(__builtin_sqrtf(rbf(rbf(v1) + 1e-5f)));
  }
#define XN(xf, mb, sd) rbf(rbf((xf) - (mb)) / (sd))
  asm volatile("" : "+s"(epoch));
  const unsigned tag = epoch + 0x9E3779B9u;
  STAMP(10);
  const float xa0 = XN(bf_lo(xq0), mb0, sd0), xa1 = XN(bf_hi(xq0), mb0, sd0), xb0 = XN(bf_lo(xq1), mb1, sd1), xb1 = XN(bf_hi(xq1), mb1, sd1);
#define PARTIAL(w0_, w1_, base, n, res) do { \
    float t[8]; \
    _Pragma("unroll") for (int k = 0; k < 4; ++k) { \
      const f32x2 w0 = bf_unpack((w0_)[k]), w1 = bf_unpack((w1_)[k]); \
      const f32x2 qa = __builtin_elementwise_fma(w1, f32x2{xa1, xa1}, w0 * xa0), qb = __builtin_elementwise_fma(w1, f32x2{xb1, xb1}, w0 * xb0); \
      t[2 * k] = tr32(qa[0], qb[0]); t[2 * k + 1] = tr32(qa[1], qb[1]);             \
    } \
    _Pragma("unroll") for (int k = 0; k < 4; ++k) t[k] = tr16(t[k], t[4 + k]);        \
    f32x2 o; \
    _Pragma("unroll") for (int k = 0; k < 2; ++k) { const float lo = t[k] + dpp<ROR8>(t[k]), hi = t[2 + k] + dpp<ROR8>(t[2 + k]); o[k] = (c & 8) ? hi : lo; } \
    const float lo = o[0] + dpp<XOR7>(dpp<XOR3>(o[0])), hi = o[1] + dpp<XOR7>(dpp<XOR3>(o[1]));     \
    lds[(base) + wave * 64 + (r >> 1) * 32 + (c & 3) * 8 + (r & 1) * 4 + (c >> 3) * 2 + ((c >> 2) & 1)] = (c & 4) ? hi : lo; \
    lds_wait(); \
    HSYNC(n); \
    const LAS float* P = (const LAS float*)lds + (base) + lane; \
    res = ((P[0] + P[64]) + (P[128] + P[192])) + ((P[256] + P[320]) + (P[384] + P[448])); \
  } while (0)
  {
    float qs;
    PARTIAL(wq0, wq1, L_Y, 0, qs);
    if (wave == 0) st_granule((gu64*)(a.ws + WS_Q) + (h * 8 + g8) * 64 + lane, tag, qs);
  }
  STAMP(9);
  asm volatile("" :: "v"(junk));
  if (wave == 7) {
    __builtin_amdgcn_s_sleep(QDELAY);
    const rsrc_t rg = __builtin_amdgcn_make_buffer_rsrc((void*)(a.ws + WS_Q + (size_t)h * 4096), 0, 4096, 0x00020000);
    const unsigned t0 = now32();
    u32x4 g[2];
    while (true) {
      bool ok = true;
#pragma unroll
      for (int i = 0; i < 2; ++i)
        g[i] = __builtin_amdgcn_raw_buffer_load_b128(rg, r * 512 + c * 16, i * 2048 + b * 256, QLD);
#pragma unroll
      for (int i = 0; i < 2; ++i) ok = ok & (g[i][1] == tag) & (g[i][3] == tag);
      if (__all(ok)) break;
      if (now32() - t0 > SPIN_TICKS) break;
      __builtin_amdgcn_s_sleep(8);
      asm volatile("" ::: "memory");
    }
    const f32x2 qs = f32x2{__uint_as_float(g[0][0]), __uint_as_float(g[0][2])} + f32x2{__uint_as_float(g[1][0]), __uint_as_float(g[1][2])};
    ((LAS unsigned*)lds)[L_Q + lane] = pack_bf16(qs[0], qs[1]);
    lds_wait();
  }
  STAMP(1);
  __builtin_amdgcn_s_barrier();
  asm volatile("" ::: "memory");
  gu64* G = (gu64*)(a.ws + WS_G);
  float gsum = 0.f;
  const int red = blockIdx.x < NRED ? (xg * 2 + (j & 1)) * 2 + (j >> 1) : -1;
  const unsigned xw = (red >= 0 && tid < 16) ? *(const gu32*)(a.x + (red & 1) * DM + (red >> 1) * 32 + tid * 2) : 0u;
  if (h < 28) {
    const rsrc_t rm = __builtin_amdgcn_make_buffer_rsrc((void*)((g8 & 2) ? a.w3 : a.w1), 0, DM * FF * 2, 0x00020000);
    const int so = h * 128 + (g8 & 1) * 64;
    const u32x4 wm0 = ldnt(rm, prow * (FF * 2) + (lane & 3) * 16, so), wm1 = ldnt(rm, prow * (FF * 2) + (lane & 3) * 16, so + FF * 2);
    PARTIAL(wm0, wm1, L_Y + 1024, 1, gsum);
  }
  const int lane0 = lane;
  int pass = DRY ? 0 : 1;
  asm volatile("" : "+s"(pass));
#pragma nounroll
  for (; pass < 2; ++pass) {
    f32x2 wf[64];
    f32x2 yacc[4] = {{0.f, 0.f}, {0.f, 0.f}, {0.f, 0.f}, {0.f, 0.f}};
    if (!pass) {
#pragma unroll
      for (int i = 0; i < 64; ++i) asm volatile("" : "=v"(wf[i]));
    } else {
      {
        FRESH();
        const rsrc_t ro = __builtin_amdgcn_make_buffer_rsrc((void*)(a.wo + (size_t)h * DH * DM), 0, DH * DM * 2, 0x00020000);
#pragma unroll
        for (int i0 = 0; i0 < 16; i0 += WOD) {
          u32x4 w[WOD];
#pragma unroll
          for (int i = 0; i < WOD; ++i) w[i] = ldw(ro, voff + ((i0 + i) & 3) * 1024, (wave * 16 + ((i0 + i) & ~3)) * 1024);
          VM_DRAIN();
#pragma unroll
          for (int i = 0; i < WOD; ++i)
#pragma unroll
            for (int k = 0; k < 4; ++k) { wf[(i0 + i) * 4 + k] = bf_unpack(w[i][k]); asm volatile("" : "+v"(wf[(i0 + i) * 4 + k])); }
          TRICKLE();
        }
      }
      STAMP(2);
      while (__hip_atomic_load((LAS unsigned*)lds + L_CNT, __ATOMIC_RELAXED, __HIP_MEMORY_SCOPE_WORKGROUP) < 4u) __builtin_amdgcn_s_sleep(8);
      asm volatile("" ::: "memory");
      gu64* ml = (gu64*)(a.ws + WS_ML);
      float mw = 0.f;
      const unsigned tk = (unsigned)__builtin_amdgcn_s_memrealtime();
      if (wave == 7) {
        FRESH();
        mw = lds[L_ML + (lane & 3)];
        float m = fmaxf(mw, dpp<XOR1>(mw)); m = fmaxf(m, dpp<XOR2>(m));
        float l = 0.f;
#pragma unroll 4
        for (int i = 0; i < 16; ++i) {
          const f32x4 sv = *(const LAS f32x4*)(lds + L_S + i * 256 + lane * 4);
#pragma unroll
          for (int k = 0; k < 4; ++k) l += __builtin_amdgcn_exp2f((sv[k] - m) * LOG2E);
        }
        l = wave_sum(l);
        if (lane < 16) st_granule(ml + chunk * MLS + lane, tag, lane == 0 ? m : lane == 1 ? l : __uint_as_float(tk));
      }
      else if (wave == 0 && h < 28) { FRESH(); __builtin_amdgcn_s_sleep(16); st_granule(G + (h * 8 + g8) * 64 + lane, tag, gsum); }
      __builtin_amdgcn_s_sleep(127); __builtin_amdgcn_s_sleep(MSLEEP);
      if (wave < 7) {
        FRESH();
        const int f0 = (h * 4 + sp) * 14 + wave * 2;
        const rsrc_t r2 = __builtin_amdgcn_make_buffer_rsrc((void*)(a.w2 + (size_t)f0 * DM), 0, 2 * DM * 2, 0x00020000);
        const u32x4 w20 = __builtin_amdgcn_raw_buffer_load_b128(r2, voff, 0, W2POL), w21 = __builtin_amdgcn_raw_buffer_load_b128(r2, voff + 1024, 0, W2POL);
        const int f = f0 + (r & 1);
        gu64* gp = G + ((f >> 6) * 8 + (c & 1) * 4 + (c >> 3) * 2 + ((f >> 5) & 1)) * 64 + b * 32 + (f & 31);
        const unsigned t0 = now32();
        unsigned long long g;
        while (true) {
          g = __hip_atomic_load(gp, RLX_AGENT);
          if (__all((unsigned)(g >> 32) == tag)) break;
          if (now32() - t0 > SPIN_TICKS) break;
          __builtin_amdgcn_s_sleep(127);
        }
        float s = __uint_as_float((unsigned)g);
        s += dpp<XOR1>(s);
        const float gs = rbf(s), us = rbf(dpp<ROR8>(s));
        const float si = rbf(gs / (1.f + __expf(-gs)));
        const float hv = rbf(si * us);
        const float h0 = __builtin_bit_cast(float, __builtin_amdgcn_readlane(__builtin_bit_cast(int, hv), 0));
        const float h1 = __builtin_bit_cast(float, __builtin_amdgcn_readlane(__builtin_bit_cast(int, hv), 16));
#pragma unroll
        for (int k = 0; k < 4; ++k) yacc[k] = __builtin_elementwise_fma(bf_unpack(w21[k]), f32x2{h1, h1}, bf_unpack(w20[k]) * h0);
      } else {
        FRESH();
        {
          const unsigned long long gt = __hip_atomic_load(ml + (lane * 4 + (lane & 3)) * MLS + 2, RLX_AGENT);
          const bool v = (unsigned)(gt >> 32) == tag;
          const float n = wave_sum(v ? 1.f : 0.f), d = wave_sum(v ? (float)(int)((unsigned)gt - tk) : 0.f);
          if (lane == 0 && n > 0.f) ((LAS unsigned*)lds)[L_REF] = tk + (unsigned)(int)(d / n);
        }
        const unsigned t0 = now32();
        unsigned long long gm, gl;
        while (true) {
          gm = __hip_atomic_load(ml + ((chunk & ~3) + (lane & 3)) * MLS, RLX_AGENT);
          gl = __hip_atomic_load(ml + ((chunk & ~3) + (lane & 3)) * MLS + 1, RLX_AGENT);
          if (__all((unsigned)(gm >> 32) == tag && (unsigned)(gl >> 32) == tag)) break;
          if (now32() - t0 > SPIN_TICKS) break;
          __builtin_amdgcn_s_sleep(127);
        }
        const float ms = __uint_as_float((unsigned)gm), ls = __uint_as_float((unsigned)gl);
        float M = fmaxf(ms, dpp<XOR1>(ms)); M = fmaxf(M, dpp<XOR2>(M));
        float L = ls * __builtin_amdgcn_exp2f((ms - M) * LOG2E);
        L += dpp<XOR1>(L); L += dpp<XOR2>(L);
        if (lane < 4) lds[L_FW + lane] = __builtin_amdgcn_exp2f((mw - M) * LOG2E) / L;
        lds_wait();
        {
          const unsigned char* pa = (lane < 32 || red < 0) ? a.ws + WS_Y + (lane & PTM) * YB + (PRETOUCH == 2 ? chunk * 128 : 32768) : (const unsigned char*)a.out + (red & 1) * (DM * 2) + (red >> 1) * 64;
          const unsigned jk = *(const volatile gu32*)pa;
          asm volatile("" :: "v"(jk));
        }
        if (blockIdx.x == NWG - 1) {
          while (true) {
            bool ok = true;
#pragma unroll
            for (int i = 0; i < 4; ++i) ok = ok & ((unsigned)(__hip_atomic_load(ml + (i * 64 + lane) * MLS + 2, RLX_AGENT) >> 32) == tag);
            if (__all(ok)) break;
            if (now32() - t0 > SPIN_TICKS) break;
            __builtin_amdgcn_s_sleep(127);
          }
          if (lane == 0) __hip_atomic_store((gu32*)(a.ws + WS_EPOCH), tag, RLX_AGENT);
        }
        STAMP(3);
      }
    }
    FRESH();
    if (pass) __builtin_amdgcn_s_barrier();
    asm volatile("" ::: "memory");
    STAMP(6);
    {
      const LAS float* P = (const LAS float*)lds + L_RED + (4 * r) * 128 + wave * 16 + c;
      float o = ((P[0] + P[128]) + (P[256] + P[384])) * lds[L_FW + r];
      o = xrow_sum(o);
#pragma unroll
      for (int i = 0; i < 16; ++i) {
        const float oi = __builtin_bit_cast(float, __builtin_amdgcn_readlane(__builtin_bit_cast(int, o), i));
#pragma unroll
        for (int k = 0; k < 4; ++k) yacc[k] = __builtin_elementwise_fma(wf[i * 4 + k], f32x2{oi, oi}, yacc[k]);
      }
    }
    *(LAS f32x4*)(lds + L_Y2 + wave * 512 + lane * 8) = f32x4{yacc[0][0], yacc[0][1], yacc[1][0], yacc[1][1]};
    *(LAS f32x4*)(lds + L_Y2 + wave * 512 + lane * 8 + 4) = f32x4{yacc[2][0], yacc[2][1], yacc[3][0], yacc[3][1]};
    lds_wait();
    if (pass) __builtin_amdgcn_s_barrier();
    asm volatile("" ::: "memory");
    const rsrc_t ry = __builtin_amdgcn_make_buffer_rsrc((void*)(a.ws + WS_Y), 0, pass ? 16 * YB : 0, 0x00020000);
    {
      const LAS float* P = (const LAS float*)lds + L_Y2 + tid;
      const float y = ((P[0] + P[512]) + (P[1024] + P[1536])) + ((P[2048] + P[2560]) + (P[3072] + P[3584]));
      const unsigned u = __float_as_uint(y);
      const unsigned w = (((u & 0x7f800000u) == 0x7f800000u ? u | ((u & 0x7fffffu) ? 0x400000u : 0u) : u + 0x400u) & 0xfffff800u) | ((tag >> ((lane & 1) * 11)) & 0x7ffu);
      const unsigned w1 = __builtin_amdgcn_mov_dpp(w, XOR1, 0xf, 0xf, true);
      if (!(lane & 1)) __builtin_amdgcn_raw_buffer_store_b64(u32x2{w, w1}, ry, (tid >> 5) * YB + (tid & 31) * 4, chunk * 128, YST);
    }
    STAMP(7);
    if (red >= 0) {
      const unsigned t0 = now32();
      u32x4 g0 = {0u, 0u, 0u, 0u}, g1 = {0u, 0u, 0u, 0u};
      bool ok0 = false, ok1 = false;
      const unsigned ta = tag & 0x7ffu, tb = (tag >> 11) & 0x7ffu;
      while (true) {
        if (!ok0) g0 = __builtin_amdgcn_raw_buffer_load_b128(ry, voff, (red >> 1) * YB + (red & 1) * 16384 + wave * 2048, YLD);
        if (!ok1) g1 = __builtin_amdgcn_raw_buffer_load_b128(ry, voff + 1024, (red >> 1) * YB + (red & 1) * 16384 + wave * 2048, YLD);
        ok0 = ((((g0[0] ^ ta) | (g0[1] ^ tb) | (g0[2] ^ ta) | (g0[3] ^ tb)) & 0x7ffu) == 0u);
        ok1 = ((((g1[0] ^ ta) | (g1[1] ^ tb) | (g1[2] ^ ta) | (g1[3] ^ tb)) & 0x7ffu) == 0u);
        if (__all(ok0 & ok1) || !pass) break;
        if (now32() - t0 > SPIN_TICKS) break;
        __builtin_amdgcn_s_sleep(FSLEEP);
        asm volatile("" ::: "memory");
      }
      float t[4];
#pragma unroll
      for (int k = 0; k < 4; ++k) t[k] = __uint_as_float(g0[k] & 0xfffff800u) + __uint_as_float(g1[k] & 0xfffff800u);
      float s = tr16(tr32(t[0], t[2]), tr32(t[1], t[3]));
      s += dpp<ROR8>(s);
      if (!(lane & 8)) lds[L_FIN + wave * 32 + (lane & 7) * 4 + r] = s;
      lds_wait();
      if (pass) __builtin_amdgcn_s_barrier();
      asm volatile("" ::: "memory");
      if (tid < 16) {
        const LAS f32x2* P = (const LAS f32x2*)(lds + L_FIN + tid * 2);
        const f32x2 o = bf_unpack(xw) + (((P[0] + P[16]) + (P[32] + P[48])) + ((P[64] + P[80]) + (P[96] + P[112])));
        const rsrc_t rout = __builtin_amdgcn_make_buffer_rsrc((void*)a.out, 0, pass ? 2 * DM * 2 : 0, 0x00020000);
        __builtin_amdgcn_raw_buffer_store_b32(pack_bf16(o[0], o[1]), rout, tid * 4, (red & 1) * (DM * 2) + (red >> 1) * 64, 0);
      }
    }
  }
  asm volatile(".Lfd_end:");
  STAMP(8);
  STAMP_FLUSH();
}

}

extern "C" void kernel_launch(void* const* d_in, const int* in_sizes, int n_in, void* d_out, int out_size, void* d_ws, size_t ws_size,
                              hipStream_t stream) {
  Args a;
  a.x = (const unsigned short*)d_in[0]; a.k = (const unsigned short*)d_in[1]; a.v = (const unsigned short*)d_in[2];
  a.wq = (const unsigned short*)d_in[3]; a.wo = (const unsigned short*)d_in[4];
  a.w1 = (const unsigned short*)d_in[6]; a.w3 = (const unsigned short*)d_in[7]; a.w2 = (const unsigned short*)d_in[8];
  a.out = (unsigned short*)d_out; a.ws = (unsigned char*)d_ws;
  fused_decode<<<NWG, NTHR, 0, stream>>>(a);
}
```

```cpp
#include <hip/hip_runtime.h>
#include <hip/hip_bf16.h>
#include <stdint.h>

#define KDIST 4
#define KSTAG 1
#define GNT 0
#define QLD 16
#define MIMIC 1
#define LATEFLUSH 0
#define STAMPS 0

namespace {

constexpr int NWG = 256, NTHR = 768, NRED = 32;
constexpr int DM = 512, NH = 32, SK = 16384, DH = 128, FF = 1792;
constexpr float LOG2E = 1.4426950408889634f;
constexpr float SCALE = 0.08838834764831845f;
#define MLS 16
constexpr size_t WS_EPOCH = 0, WS_ML = 8192;
constexpr size_t WS_Y = 65536;
constexpr int YB = 32768 + 128;
constexpr size_t WS_Q = WS_Y + (1u << 20) + 8192;
constexpr size_t WS_G = WS_Q + (256u << 10);
constexpr size_t WS_STAMP = 8u << 20;
constexpr int L_S = 0;
constexpr int L_RED = L_S + 4096;
constexpr int L_Y = L_RED + 2048;
constexpr int L_Y2 = L_Y + 4096;
constexpr int L_Q = L_Y2 + 4096;
constexpr int L_ML = L_Q + 64;
constexpr int L_FW = L_ML + 8;
constexpr int L_FIN = L_FW + 4;
constexpr int L_CNT = L_FIN + 256;
constexpr int L_REF = L_CNT + 4;
constexpr int L_END = L_REF + 4;
#define TR(i) ((void)0)

#define GAS __attribute__((address_space(1)))
#define LAS __attribute__((address_space(3)))
#define RLX_AGENT __ATOMIC_RELAXED, __HIP_MEMORY_SCOPE_AGENT
typedef unsigned u32x4 __attribute__((ext_vector_type(4)));
typedef unsigned u32x2 __attribute__((ext_vector_type(2)));
typedef float f32x2 __attribute__((ext_vector_type(2)));
typedef float f32x4 __attribute__((ext_vector_type(4)));
typedef __bf16 bf16x2 __attribute__((ext_vector_type(2)));
typedef GAS unsigned gu32;
typedef GAS unsigned long long gu64;

__device__ __forceinline__ float bf_lo(unsigned u) { return __builtin_bit_cast(float, u << 16); }
__device__ __forceinline__ float bf_hi(unsigned u) { return __builtin_bit_cast(float, u & 0xffff0000u); }
__device__ __forceinline__ f32x2 bf_unpack(unsigned u) { return f32x2{bf_lo(u), bf_hi(u)}; }
__device__ __forceinline__ unsigned pack_bf16(float lo, float hi) {
  bf16x2 r = {(__bf16)lo, (__bf16)hi};
  return __builtin_bit_cast(unsigned, r);
}
__device__ __forceinline__ float rbf(float x) {
  return (float)(__bf16)x;
}
__device__ __forceinline__ float dot2(unsigned a, unsigned b, float acc) {
  return __builtin_amdgcn_fdot2_f32_bf16(__builtin_bit_cast(bf16x2, a), __builtin_bit_cast(bf16x2, b), acc, false);
}
template <int CTRL> __device__ __forceinline__ float dpp(float x) {
  return __builtin_bit_cast(float, __builtin_amdgcn_mov_dpp(__builtin_bit_cast(int, x), CTRL, 0xf, 0xf, true));
}
constexpr int XOR1 = 0xB1, XOR2 = 0x4E, XOR3 = 0x1B, XOR7 = 0x141, ROR2 = 0x122, ROR4 = 0x124, ROR8 = 0x128;
__device__ __forceinline__ float xrow_sum(float x) {
  auto s = __builtin_amdgcn_permlane16_swap(__float_as_uint(x), __float_as_uint(x), false, false);
  x = __uint_as_float(s[0]) + __uint_as_float(s[1]);
  auto t = __builtin_amdgcn_permlane32_swap(__float_as_uint(x), __float_as_uint(x), false, false);
  return __uint_as_float(t[0]) + __uint_as_float(t[1]);
}
__device__ __forceinline__ float xrow_max(float x) {
  auto s = __builtin_amdgcn_permlane16_swap(__float_as_uint(x), __float_as_uint(x), false, false);
  x = fmaxf(__uint_as_float(s[0]), __uint_as_float(s[1]));
  auto t = __builtin_amdgcn_permlane32_swap(__float_as_uint(x), __float_as_uint(x), false, false);
  return fmaxf(__uint_as_float(t[0]), __uint_as_float(t[1]));
}
__device__ __forceinline__ float tr16(float a, float b) {
  auto s = __builtin_amdgcn_permlane16_swap(__float_as_uint(a), __float_as_uint(b), false, false);
  return __uint_as_float(s[0]) + __uint_as_float(s[1]);
}
__device__ __forceinline__ float tr32(float a, float b) {
  auto s = __builtin_amdgcn_permlane32_swap(__float_as_uint(a), __float_as_uint(b), false, false);
  return __uint_as_float(s[0]) + __uint_as_float(s[1]);
}
__device__ __forceinline__ float wave_sum(float x) {
  x += dpp<XOR1>(x); x += dpp<XOR2>(x); x += dpp<XOR7>(x); x += dpp<ROR8>(x);
  return xrow_sum(x);
}
__device__ __forceinline__ float wave_max(float x) {
  x = fmaxf(x, dpp<XOR1>(x)); x = fmaxf(x, dpp<XOR2>(x)); x = fmaxf(x, dpp<XOR7>(x)); x = fmaxf(x, dpp<ROR8>(x));
  return xrow_max(x);
}
__device__ __forceinline__ void lds_wait() { asm volatile("s_waitcnt lgkmcnt(0)" ::: "memory"); }
__device__ __forceinline__ void st_granule(gu64* g, unsigned tag, float v) {
  __hip_atomic_store(g, ((unsigned long long)tag << 32) | (unsigned long long)__float_as_uint(v), RLX_AGENT);
}
constexpr unsigned SPIN_TICKS = 20000000u;
__device__ __forceinline__ unsigned now32() { return (unsigned)__builtin_amdgcn_s_memrealtime(); }

struct Args {
  const unsigned short* x; const unsigned short* k; const unsigned short* v; const unsigned short* wq; const unsigned short* wo;
  const unsigned short* w1; const unsigned short* w3; const unsigned short* w2;
  unsigned short* out; unsigned char* ws;
};

#define STAMP(i) do { } while (0)
#define STAMP_FLUSH() do { } while (0)
#define STAMP_DECL

typedef __amdgpu_buffer_rsrc_t rsrc_t;
#define WPOL 2
#define NPRE 3
__device__ __forceinline__ u32x4 ldw(rsrc_t r, int voff, int soff) { return __builtin_amdgcn_raw_buffer_load_b128(r, voff, soff, WPOL); }
__device__ __forceinline__ u32x4 ldnt(rsrc_t r, int voff, int soff) { return __builtin_amdgcn_raw_buffer_load_b128(r, voff, soff, 2); }
#define TRK 0
#define WOD 2
#define QDELAY 32
#define DRY 1
#define FSLEEP 4
#define B0POS (PDIST + 1)
#define SFIRST 1
#define CODEPF 1
#define KMIX 0
#define GLATE 1
#define FDELAY 0
#define WIDE_T 3300
#define LAG_A 100
#define LAG_B 110
#define LAG_W 5
#define MSLEEP 16
#define Y16 0
#define FPIPE 0
#define YLD 16
#define YST 16
#define VDIST 4
#define PRETOUCH 1
#define REDMAP 1
#define W2POL 2
#define RTOUCH 0
#define QTOUCH 0
#define PDIST KDIST
#define TRICKLE() __builtin_amdgcn_s_sleep(TRK)
#define HSYNC(n) do { \
    if (lane == 0) __hip_atomic_fetch_add((LAS unsigned*)lds + L_CNT + 1 + (n), 1u, __ATOMIC_RELAXED, __HIP_MEMORY_SCOPE_WORKGROUP); \
    while (__hip_atomic_load((LAS unsigned*)lds + L_CNT + 1 + (n), __ATOMIC_RELAXED, __HIP_MEMORY_SCOPE_WORKGROUP) < 8u) __builtin_amdgcn_s_sleep(0); \
    asm volatile("" ::: "memory"); \
  } while (0)
#define FRESH() int lane_ = lane0; asm volatile("" : "+v"(lane_)); \
  const int lane = lane_, r = lane >> 4, c = lane & 15, tid = wave * 64 + lane, voff = lane * 16; (void)r; (void)c; (void)tid; (void)voff
#define SB() __builtin_amdgcn_sched_barrier(0)
#define VM_DRAIN() do { SB(); asm volatile("s_waitcnt vmcnt(0)" ::: "memory"); SB(); } while (0)

__global__ void __launch_bounds__(NTHR, 3) fused_decode(Args a) {
  __shared__ __attribute__((aligned(16))) float lds[L_END];
  const int tid = threadIdx.x, lane = tid & 63, wave = __builtin_amdgcn_readfirstlane(tid >> 6);
  const int r = lane >> 4, c = lane & 15;
  const int xg = blockIdx.x & 7, j = blockIdx.x >> 3;
  const int b = j >> 4, h = xg * 4 + ((j >> 2) & 3), sp = j & 3;
  const int chunk = b * 128 + h * 4 + sp;
  const int voff = lane * 16;
  STAMP_DECL;
  STAMP(0);

  const int g8 = b * 4 + sp;
  const int prow = (g8 >> 2) * 256 + wave * 32 + 2 * (lane >> 2);
  u32x4 xv0, xv1, wq0, wq1;
  unsigned xq0, xq1, junk = 0;
  if (wave < 8) {
    xv0 = *(const GAS u32x4*)(a.x + lane * 8); xv1 = *(const GAS u32x4*)(a.x + DM + lane * 8);
    xq0 = *(const gu32*)(a.x + prow); xq1 = *(const gu32*)(a.x + DM + prow);
    const rsrc_t rq = __builtin_amdgcn_make_buffer_rsrc((void*)a.wq, 0, DM * NH * DH * 2, 0x00020000);
    wq0 = ldnt(rq, prow * (NH * DH * 2) + (lane & 3) * 16, h * (DH * 2) + (g8 & 3) * 64);
    wq1 = ldnt(rq, prow * (NH * DH * 2) + (lane & 3) * 16, h * (DH * 2) + (g8 & 3) * 64 + NH * DH * 2);
    if (wave >= 6 && j < CODEPF) {
      unsigned long long pc; int len;
      asm volatile("s_getpc_b64 %0\n.Lfd_top:\n\ts_mov_b32 %1, .Lfd_end-.Lfd_top" : "=s"(pc), "=s"(len));
      junk = *(const gu32*)(pc + (unsigned)min(((wave - 6) * 64 + lane) * 128, max(len, 0)));
    }
  }
  int role = wave;
  asm volatile("" : "+s"(role));
  if (__builtin_expect(role >= 8, SFIRST)) {
    const int sw = wave - 8;
    constexpr int HB = SK * DH * 2;
    const unsigned short* kp = a.k + (size_t)(chunk >> 2) * SK * DH;
    const unsigned short* vp = a.v + (size_t)(chunk >> 2) * SK * DH;
    const rsrc_t rk = __builtin_amdgcn_make_buffer_rsrc((void*)kp, 0, HB, 0x00020000);
    const rsrc_t rv = __builtin_amdgcn_make_buffer_rsrc((void*)vp, 0, HB, 0x00020000);
    const int stag = j * KSTAG, voffh = voff + HB / 2;
#define ROWVO(n) ((((n) & 1) ? voffh : voff) + (((n) & 7) >> 1) * 1024)
#define ROWSO(pair, n) (((((pair) + ((n) >> 3) + stag) & 31) << 14) + (sw << 12) + (sp << 19))
#define LDROW(rs, pair, n) ldnt(rs, ROWVO(n), ROWSO(pair, n))
    __builtin_amdgcn_s_setprio(2);
    u32x4 buf[8], pre[NPRE * 8];
    STAMP(12);
    if (B0POS == 0) { __builtin_amdgcn_s_barrier(); SB(); }
    STAMP(13);
#pragma unroll
    for (int u = 0; u < NPRE * 8; ++u) {
      pre[u] = LDROW(rk, 0, u); SB();
      if (u + 1 == B0POS) { __builtin_amdgcn_s_barrier(); SB(); }
      if (u >= PDIST) { asm volatile("s_waitcnt vmcnt(%0)" :: "i"(PDIST)); SB(); }
      if (u == PDIST) STAMP(14);
      if ((u & 7) == 6 && u > 8) { TR((u >> 3) - 1); SB(); }
    }
#pragma unroll
    for (int u = 0; u < 8; ++u) {
      buf[u] = LDROW(rk, NPRE, u); SB();
      if (u < 7) { asm volatile("s_waitcnt vmcnt(%0)" :: "i"(PDIST)); SB(); }
    }
    TR(2);
    STAMP(1);
    __builtin_amdgcn_s_barrier();
    asm volatile("" ::: "memory");
    const u32x4 qp = *(const LAS u32x4*)(lds + L_Q + c * 4);
    STAMP(2);
    LAS float* S = (LAS float*)lds + L_S + sw * 1024;
    float mx = -INFINITY;
#define DOT(w) dot2((w)[3], qp[3], dot2((w)[2], qp[2], dot2((w)[1], qp[1], dot2((w)[0], qp[0], 0.f))))
#define K_FIN(tt) do { \
      _Pragma("unroll") for (int u = 0; u < 4; ++u) { const float lo = x[u] + dpp<ROR8>(x[u]), hi = x[u + 4] + dpp<ROR8>(x[u + 4]); x[u] = (c & 8) ? hi : lo; } \
      _Pragma("unroll") for (int u = 0; u < 2; ++u) { const float lo = x[u] + dpp<XOR7>(x[u]), hi = x[u + 2] + dpp<XOR7>(x[u + 2]); x[u] = (c & 4) ? hi : lo; } \
      { const float lo = x[0] + dpp<XOR2>(x[0]), hi = x[1] + dpp<XOR2>(x[1]); x[0] = (c & 2) ? hi : lo; } \
      float sc = x[0] + dpp<XOR1>(x[0]); \
      sc = rbf(rbf(sc) * SCALE); \
      mx = fmaxf(mx, sc); \
      S[(tt) * 32 + r * 8 + (c >> 1)] = sc; \
      SB(); \
    } while (0)
    static_assert(KDIST <= 6 && KDIST >= 3 && NPRE >= 1 && PDIST <= NPRE * 8, "ring of 8");
#define PACE() do { asm volatile("s_waitcnt vmcnt(%0)" :: "i"(((KMIX >> u) & 1) ? KDIST - 1 : KDIST)); SB(); } while (0)
    {
      float x[8];
#pragma unroll
      for (int u = 0; u < 8; ++u) {
        if (u > 0 && u <= KDIST + 1) { buf[u - 1] = LDROW(rk, NPRE + 1, u - 1); SB(); }
        x[u] = DOT(buf[u]);
        SB();
      }
      K_FIN(NPRE);
    }
#pragma unroll
    for (int pp = 0; pp < NPRE; ++pp) {
      float x[8];
#pragma unroll
      for (int w = 0; w < 8; ++w) x[w] = DOT(pre[pp * 8 + w]);
      K_FIN(pp);
    }
#pragma unroll
    for (int u = KDIST + 1; u < 7; ++u) { PACE(); buf[u] = LDROW(rk, NPRE + 1, u); SB(); }
    TR(3);
    STAMP(11);
    int last = 31;
    asm volatile("" : "+s"(last));
#pragma nounroll
    for (int tt = NPRE + 1; tt <= last; ++tt) {
      const unsigned short* nb = tt == last ? vp : kp;
      const rsrc_t rn = __builtin_amdgcn_make_buffer_rsrc((void*)nb, 0, HB, 0x00020000);
      float x[8];
#pragma unroll
      for (int u = 0; u < 8; ++u) {
        const int n = u + 7;
        PACE();
        buf[n & 7] = LDROW(n >= 8 ? rn : rk, tt, n);
        SB();
        x[u] = DOT(buf[u]);
        SB();
      }
      K_FIN(tt);
      TR(tt);
    }
    STAMP(3);
    mx = wave_max(mx);
    if (lane == 0) lds[L_ML + sw] = mx;
    lds_wait();
    if (lane == 0) __hip_atomic_fetch_add((LAS unsigned*)lds + L_CNT, 1u, __ATOMIC_RELAXED, __HIP_MEMORY_SCOPE_WORKGROUP);
    STAMP(4);
    f32x2 acc[4] = {{0.f, 0.f}, {0.f, 0.f}, {0.f, 0.f}, {0.f, 0.f}};
    f32x4 s0 = *(const LAS f32x4*)(S + r * 8), s1;
    int el = -1, lag = -1;
#pragma nounroll
    for (int tt = 0; tt <= last; ++tt) {
      const rsrc_t rn = __builtin_amdgcn_make_buffer_rsrc((void*)vp, 0, tt == last ? 0 : HB, 0x00020000);
      unsigned ref, tnow;
#pragma unroll
      for (int u = 0; u < 8; ++u) {
        const int n = u + 7;
        const int so = ROWSO(tt, n);
        asm volatile("s_cmpk_gt_i32 %0, %4\n\ts_cbranch_scc1 1f\n\ts_waitcnt vmcnt(%2)\n\ts_cmp_gt_i32 %1, 0\n\ts_cbranch_scc1 1f\n\ts_waitcnt vmcnt(%3)\n1:"
                     :: "s"(el), "s"(lag), "i"(LAG_W), "i"(VDIST), "i"(WIDE_T), "s"(so) : "scc");
        SB();
        buf[n & 7] = ldnt(n >= 8 ? rn : rv, ROWVO(n), so);
        SB();
        if (u == 0) s1 = *(const LAS f32x4*)(S + tt * 32 + r * 8 + 4);
        if (u == 4) { s0 = *(const LAS f32x4*)(S + ((tt + 1) & 31) * 32 + r * 8); ref = *(volatile LAS unsigned*)(lds + L_REF); tnow = now32(); }
        const float p = __builtin_amdgcn_exp2f(((u < 4 ? s0[u & 3] : s1[u & 3]) - mx) * LOG2E);
#pragma unroll
        for (int k = 0; k < 4; ++k) acc[k] = __builtin_elementwise_fma(bf_unpack(buf[u][k]), f32x2{p, p}, acc[k]);
        asm volatile("" : "+v"(acc[0]), "+v"(acc[1]), "+v"(acc[2]), "+v"(acc[3]));
        if (u == 6) {
          el = (int)(tnow - __builtin_amdgcn_readfirstlane(ref));
          lag = el - (LAG_A + LAG_B / 2 + tt * LAG_B);
        }
        SB();
      }
      TR(32 + tt);
    }
    STAMP(5);
    *(LAS f32x4*)(lds + L_RED + (sw * 4 + r) * 128 + c * 8) = f32x4{acc[0][0], acc[0][1], acc[1][0], acc[1][1]};
    *(LAS f32x4*)(lds + L_RED + (sw * 4 + r) * 128 + c * 8 + 4) = f32x4{acc[2][0], acc[2][1], acc[3][0], acc[3][1]};
    lds_wait();
    __builtin_amdgcn_s_barrier();
    __builtin_amdgcn_s_barrier();
    if (blockIdx.x < NRED) __builtin_amdgcn_s_barrier();
    STAMP_FLUSH();
    return;
  }

  unsigned epoch = *(const __attribute__((address_space(4))) unsigned*)(a.ws + WS_EPOCH);
  SB();
  if (tid < 5) ((LAS unsigned*)lds)[L_CNT + tid] = tid < 4 ? 0u : (unsigned)__builtin_amdgcn_s_memrealtime() + 0x40000000u;
  lds_wait();
  __builtin_amdgcn_s_barrier();
  SB();
  float mb0, mb1, sd0, sd1;
  {
    float f0[8], f1[8];
#pragma unroll
    for (int k = 0; k < 4; ++k) { f0[2 * k] = bf_lo(xv0[k]); f0[2 * k + 1] = bf_hi(xv0[k]); f1[2 * k] = bf_lo(xv1[k]); f1[2 * k + 1] = bf_hi(xv1[k]); }
    float s0 = 0.f, s1 = 0.f;
#pragma unroll
    for (int k = 0; k < 8; ++k) { s0 += f0[k]; s1 += f1[k]; }
    const float m0 = wave_sum(s0) * (1.f / DM), m1 = wave_sum(s1) * (1.f / DM);
    float v0 = 0.f, v1 = 0.f;
#pragma unroll
    for (int k = 0; k < 8; ++k) { const float d0 = f0[k] - m0, d1 = f1[k] - m1; v0 = fmaf(d0, d0, v0); v1 = fmaf(d1, d1, v1); }
    v0 = wave_sum(v0) * (1.f / DM); v1 = wave_sum(v1) * (1.f / DM);
    mb0 = rbf(m0); sd0 = rbf(__builtin_sqrtf(rbf(rbf(v0) + 1e-5f)));
    mb1 = rbf(m1); sd1 = rbf(__builtin_sqrtf(rbf(rbf(v1) + 1e-5f)));
  }
#define XN(xf, mb, sd) rbf(rbf((xf) - (mb)) / (sd))
  asm volatile("" : "+s"(epoch));
  const unsigned tag = epoch + 0x9E3779B9u;
  STAMP(10);
  const float xa0 = XN(bf_lo(xq0), mb0, sd0), xa1 = XN(bf_hi(xq0), mb0, sd0), xb0 = XN(bf_lo(xq1), mb1, sd1), xb1 = XN(bf_hi(xq1), mb1, sd1);
#define PARTIAL(w0_, w1_, base, n, res) do { \
    float t[8]; \
    _Pragma("unroll") for (int k = 0; k < 4; ++k) { \
      const f32x2 w0 = bf_unpack((w0_)[k]), w1 = bf_unpack((w1_)[k]); \
      const f32x2 qa = __builtin_elementwise_fma(w1, f32x2{xa1, xa1}, w0 * xa0), qb = __builtin_elementwise_fma(w1, f32x2{xb1, xb1}, w0 * xb0); \
      t[2 * k] = tr32(qa[0], qb[0]); t[2 * k + 1] = tr32(qa[1], qb[1]);             \
    } \
    _Pragma("unroll") for (int k = 0; k < 4; ++k) t[k] = tr16(t[k], t[4 + k]);        \
    f32x2 o; \
    _Pragma("unroll") for (int k = 0; k < 2; ++k) { const float lo = t[k] + dpp<ROR8>(t[k]), hi = t[2 + k] + dpp<ROR8>(t[2 + k]); o[k] = (c & 8) ? hi : lo; } \
    const float lo = o[0] + dpp<XOR7>(dpp<XOR3>(o[0])), hi = o[1] + dpp<XOR7>(dpp<XOR3>(o[1]));     \
    lds[(base) + wave * 64 + (r >> 1) * 32 + (c & 3) * 8 + (r & 1) * 4 + (c >> 3) * 2 + ((c >> 2) & 1)] = (c & 4) ? hi : lo; \
    lds_wait(); \
    HSYNC(n); \
    const LAS float* P = (const LAS float*)lds + (base) + lane; \
    res = ((P[0] + P[64]) + (P[128] + P[192])) + ((P[256] + P[320]) + (P[384] + P[448])); \
  } while (0)
  {
    float qs;
    PARTIAL(wq0, wq1, L_Y, 0, qs);
    if (wave == 0) st_granule((gu64*)(a.ws + WS_Q) + (h * 8 + g8) * 64 + lane, tag, qs);
  }
  STAMP(9);
  asm volatile("" :: "v"(junk));
  if (wave == 7) {
    __builtin_amdgcn_s_sleep(QDELAY);
    const rsrc_t rg = __builtin_amdgcn_make_buffer_rsrc((void*)(a.ws + WS_Q + (size_t)h * 4096), 0, 4096, 0x00020000);
    const unsigned t0 = now32();
    u32x4 g[2];
    while (true) {
      bool ok = true;
#pragma unroll
      for (int i = 0; i < 2; ++i)
        g[i] = __builtin_amdgcn_raw_buffer_load_b128(rg, r * 512 + c * 16, i * 2048 + b * 256, QLD);
#pragma unroll
      for (int i = 0; i < 2; ++i) ok = ok & (g[i][1] == tag) & (g[i][3] == tag);
      if (__all(ok)) break;
      if (now32() - t0 > SPIN_TICKS) break;
      __builtin_amdgcn_s_sleep(8);
      asm volatile("" ::: "memory");
    }
    const f32x2 qs = f32x2{__uint_as_float(g[0][0]), __uint_as_float(g[0][2])} + f32x2{__uint_as_float(g[1][0]), __uint_as_float(g[1][2])};
    ((LAS unsigned*)lds)[L_Q + lane] = pack_bf16(qs[0], qs[1]);
    lds_wait();
  }
  STAMP(1);
  __builtin_amdgcn_s_barrier();
  asm volatile("" ::: "memory");
  gu64* G = (gu64*)(a.ws + WS_G);
  float gsum = 0.f;
  const int red = blockIdx.x < NRED ? (xg * 2 + (j & 1)) * 2 + (j >> 1) : -1;
  const unsigned xw = (red >= 0 && tid < 16) ? *(const gu32*)(a.x + (red & 1) * DM + (red >> 1) * 32 + tid * 2) : 0u;
  if (h < 28) {
    const rsrc_t rm = __builtin_amdgcn_make_buffer_rsrc((void*)((g8 & 2) ? a.w3 : a.w1), 0, DM * FF * 2, 0x00020000);
    const int so = h * 128 + (g8 & 1) * 64;
    const u32x4 wm0 = ldnt(rm, prow * (FF * 2) + (lane & 3) * 16, so), wm1 = ldnt(rm, prow * (FF * 2) + (lane & 3) * 16, so + FF * 2);
    PARTIAL(wm0, wm1, L_Y + 1024, 1, gsum);
  }
  const int lane0 = lane;
  int pass = DRY ? 0 : 1;
  asm volatile("" : "+s"(pass));
#pragma nounroll
  for (; pass < 2; ++pass) {
    f32x2 wf[64];
    f32x2 yacc[4] = {{0.f, 0.f}, {0.f, 0.f}, {0.f, 0.f}, {0.f, 0.f}};
    if (!pass) {
#pragma unroll
      for (int i = 0; i < 64; ++i) asm volatile("" : "=v"(wf[i]));
    } else {
      {
        FRESH();
        const rsrc_t ro = __builtin_amdgcn_make_buffer_rsrc((void*)(a.wo + (size_t)h * DH * DM), 0, DH * DM * 2, 0x00020000);
#pragma unroll
        for (int i0 = 0; i0 < 16; i0 += WOD) {
          u32x4 w[WOD];
#pragma unroll
          for (int i = 0; i < WOD; ++i) w[i] = ldw(ro, voff + ((i0 + i) & 3) * 1024, (wave * 16 + ((i0 + i) & ~3)) * 1024);
          VM_DRAIN();
#pragma unroll
          for (int i = 0; i < WOD; ++i)
#pragma unroll
            for (int k = 0; k < 4; ++k) { wf[(i0 + i) * 4 + k] = bf_unpack(w[i][k]); asm volatile("" : "+v"(wf[(i0 + i) * 4 + k])); }
          TRICKLE();
        }
      }
      STAMP(2);
      while (__hip_atomic_load((LAS unsigned*)lds + L_CNT, __ATOMIC_RELAXED, __HIP_MEMORY_SCOPE_WORKGROUP) < 4u) __builtin_amdgcn_s_sleep(8);
      asm volatile("" ::: "memory");
      gu64* ml = (gu64*)(a.ws + WS_ML);
      float mw = 0.f;
      const unsigned tk = (unsigned)__builtin_amdgcn_s_memrealtime();
      if (wave == 7) {
        FRESH();
        mw = lds[L_ML + (lane & 3)];
        float m = fmaxf(mw, dpp<XOR1>(mw)); m = fmaxf(m, dpp<XOR2>(m));
        float l = 0.f;
#pragma unroll 4
        for (int i = 0; i < 16; ++i) {
          const f32x4 sv = *(const LAS f32x4*)(lds + L_S + i * 256 + lane * 4);
#pragma unroll
          for (int k = 0; k < 4; ++k) l += __builtin_amdgcn_exp2f((sv[k] - m) * LOG2E);
        }
        l = wave_sum(l);
        if (lane < 16) st_granule(ml + chunk * MLS + lane, tag, lane == 0 ? m : lane == 1 ? l : __uint_as_float(tk));
      }
      else if (wave == 0 && h < 28) { FRESH(); __builtin_amdgcn_s_sleep(16); st_granule(G + (h * 8 + g8) * 64 + lane, tag, gsum); }
      __builtin_amdgcn_s_sleep(127); __builtin_amdgcn_s_sleep(MSLEEP);
      if (wave < 7) {
        FRESH();
        const int f0 = (h * 4 + sp) * 14 + wave * 2;
        const rsrc_t r2 = __builtin_amdgcn_make_buffer_rsrc((void*)(a.w2 + (size_t)f0 * DM), 0, 2 * DM * 2, 0x00020000);
        const u32x4 w20 = __builtin_amdgcn_raw_buffer_load_b128(r2, voff, 0, W2POL), w21 = __builtin_amdgcn_raw_buffer_load_b128(r2, voff + 1024, 0, W2POL);
        const int f = f0 + (r & 1);
        gu64* gp = G + ((f >> 6) * 8 + (c & 1) * 4 + (c >> 3) * 2 + ((f >> 5) & 1)) * 64 + b * 32 + (f & 31);
        const unsigned t0 = now32();
        unsigned long long g;
        while (true) {
          g = __hip_atomic_load(gp, RLX_AGENT);
          if (__all((unsigned)(g >> 32) == tag)) break;
          if (now32() - t0 > SPIN_TICKS) break;
          __builtin_amdgcn_s_sleep(127);
        }
        float s = __uint_as_float((unsigned)g);
        s += dpp<XOR1>(s);
        const float gs = rbf(s), us = rbf(dpp<ROR8>(s));
        const float si = rbf(gs / (1.f + __expf(-gs)));
        const float hv = rbf(si * us);
        const float h0 = __builtin_bit_cast(float, __builtin_amdgcn_readlane(__builtin_bit_cast(int, hv), 0));
        const float h1 = __builtin_bit_cast(float, __builtin_amdgcn_readlane(__builtin_bit_cast(int, hv), 16));
#pragma unroll
        for (int k = 0; k < 4; ++k) yacc[k] = __builtin_elementwise_fma(bf_unpack(w21[k]), f32x2{h1, h1}, bf_unpack(w20[k]) * h0);
      } else {
        FRESH();
        {
          const unsigned long long gt = __hip_atomic_load(ml + (lane * 4 + (lane & 3)) * MLS + 2, RLX_AGENT);
          const bool v = (unsigned)(gt >> 32) == tag;
          const float n = wave_sum(v ? 1.f : 0.f), d = wave_sum(v ? (float)(int)((unsigned)gt - tk) : 0.f);
          if (lane == 0 && n > 0.f) ((LAS unsigned*)lds)[L_REF] = tk + (unsigned)(int)(d / n);
        }
        const unsigned t0 = now32();
        unsigned long long gm, gl;
        while (true) {
          gm = __hip_atomic_load(ml + ((chunk & ~3) + (lane & 3)) * MLS, RLX_AGENT);
          gl = __hip_atomic_load(ml + ((chunk & ~3) + (lane & 3)) * MLS + 1, RLX_AGENT);
          if (__all((unsigned)(gm >> 32) == tag && (unsigned)(gl >> 32) == tag)) break;
          if (now32() - t0 > SPIN_TICKS) break;
          __builtin_amdgcn_s_sleep(127);
        }
        const float ms = __uint_as_float((unsigned)gm), ls = __uint_as_float((unsigned)gl);
        float M = fmaxf(ms, dpp<XOR1>(ms)); M = fmaxf(M, dpp<XOR2>(M));
        float L = ls * __builtin_amdgcn_exp2f((ms - M) * LOG2E);
        L += dpp<XOR1>(L); L += dpp<XOR2>(L);
        if (lane < 4) lds[L_FW + lane] = __builtin_amdgcn_exp2f((mw - M) * LOG2E) / L;
        lds_wait();
        {
          const unsigned char* pa = (lane < 32 || red < 0) ? a.ws + WS_Y + (lane & 15) * YB + chunk * 128
                                  : (RTOUCH && lane < 48) ? a.ws + WS_Y + (red >> 1) * YB + (red & 1) * 16384 + min((lane & 7) * 4096, 16256)
                                  : (const unsigned char*)a.out + (red & 1) * (DM * 2) + (red >> 1) * 64;
          const unsigned jk = *(const volatile gu32*)pa;
          asm volatile("" :: "v"(jk));
        }
        if (blockIdx.x == NWG - 1) {
          while (true) {
            bool ok = true;
#pragma unroll
            for (int i = 0; i < 4; ++i) ok = ok & ((unsigned)(__hip_atomic_load(ml + (i * 64 + lane) * MLS + 2, RLX_AGENT) >> 32) == tag);
            if (__all(ok)) break;
            if (now32() - t0 > SPIN_TICKS) break;
            __builtin_amdgcn_s_sleep(127);
          }
          if (lane == 0) __hip_atomic_store((gu32*)(a.ws + WS_EPOCH), tag, RLX_AGENT);
        }
        STAMP(3);
      }
    }
    FRESH();
    if (pass) __builtin_amdgcn_s_barrier();
    asm volatile("" ::: "memory");
    STAMP(6);
    {
      const LAS float* P = (const LAS float*)lds + L_RED + (4 * r) * 128 + wave * 16 + c;
      float o = ((P[0] + P[128]) + (P[256] + P[384])) * lds[L_FW + r];
      o = xrow_sum(o);
#pragma unroll
      for (int i = 0; i < 16; ++i) {
        const float oi = __builtin_bit_cast(float, __builtin_amdgcn_readlane(__builtin_bit_cast(int, o), i));
#pragma unroll
        for (int k = 0; k < 4; ++k) yacc[k] = __builtin_elementwise_fma(wf[i * 4 + k], f32x2{oi, oi}, yacc[k]);
      }
    }
    *(LAS f32x4*)(lds + L_Y2 + wave * 512 + lane * 8) = f32x4{yacc[0][0], yacc[0][1], yacc[1][0], yacc[1][1]};
    *(LAS f32x4*)(lds + L_Y2 + wave * 512 + lane * 8 + 4) = f32x4{yacc[2][0], yacc[2][1], yacc[3][0], yacc[3][1]};
    lds_wait();
    if (pass) __builtin_amdgcn_s_barrier();
    asm volatile("" ::: "memory");
    const rsrc_t ry = __builtin_amdgcn_make_buffer_rsrc((void*)(a.ws + WS_Y), 0, pass ? 16 * YB : 0, 0x00020000);
    {
      const LAS float* P = (const LAS float*)lds + L_Y2 + tid;
      const float y = ((P[0] + P[512]) + (P[1024] + P[1536])) + ((P[2048] + P[2560]) + (P[3072] + P[3584]));
      const unsigned u = __float_as_uint(y);
      const unsigned w = (((u & 0x7f800000u) == 0x7f800000u ? u | ((u & 0x7fffffu) ? 0x400000u : 0u) : u + 0x400u) & 0xfffff800u) | ((tag >> ((lane & 1) * 11)) & 0x7ffu);
      const unsigned w1 = __builtin_amdgcn_mov_dpp(w, XOR1, 0xf, 0xf, true);
      if (!(lane & 1)) __builtin_amdgcn_raw_buffer_store_b64(u32x2{w, w1}, ry, (tid >> 5) * YB + (tid & 31) * 4, chunk * 128, YST);
    }
    STAMP(7);
    if (red >= 0) {
      const unsigned t0 = now32();
      u32x4 g0 = {0u, 0u, 0u, 0u}, g1 = {0u, 0u, 0u, 0u};
      bool ok0 = false, ok1 = false;
      const unsigned ta = tag & 0x7ffu, tb = (tag >> 11) & 0x7ffu;
      while (true) {
        if (!ok0) g0 = __builtin_amdgcn_raw_buffer_load_b128(ry, voff, (red >> 1) * YB + (red & 1) * 16384 + wave * 2048, YLD);
        if (!ok1) g1 = __builtin_amdgcn_raw_buffer_load_b128(ry, voff + 1024, (red >> 1) * YB + (red & 1) * 16384 + wave * 2048, YLD);
        ok0 = ((((g0[0] ^ ta) | (g0[1] ^ tb) | (g0[2] ^ ta) | (g0[3] ^ tb)) & 0x7ffu) == 0u);
        ok1 = ((((g1[0] ^ ta) | (g1[1] ^ tb) | (g1[2] ^ ta) | (g1[3] ^ tb)) & 0x7ffu) == 0u);
        if (__all(ok0 & ok1) || !pass) break;
        if (now32() - t0 > SPIN_TICKS) break;
        __builtin_amdgcn_s_sleep(FSLEEP);
        asm volatile("" ::: "memory");
      }
      float t[4];
#pragma unroll
      for (int k = 0; k < 4; ++k) t[k] = __uint_as_float(g0[k] & 0xfffff800u) + __uint_as_float(g1[k] & 0xfffff800u);
      float s = tr16(tr32(t[0], t[2]), tr32(t[1], t[3]));
      s += dpp<ROR8>(s);
      if (!(lane & 8)) lds[L_FIN + wave * 32 + (lane & 7) * 4 + r] = s;
      lds_wait();
      if (pass) __builtin_amdgcn_s_barrier();
      asm volatile("" ::: "memory");
      if (tid < 16) {
        const LAS f32x2* P = (const LAS f32x2*)(lds + L_FIN + tid * 2);
        const f32x2 o = bf_unpack(xw) + (((P[0] + P[16]) + (P[32] + P[48])) + ((P[64] + P[80]) + (P[96] + P[112])));
        const rsrc_t rout = __builtin_amdgcn_make_buffer_rsrc((void*)a.out, 0, pass ? 2 * DM * 2 : 0, 0x00020000);
        __builtin_amdgcn_raw_buffer_store_b32(pack_bf16(o[0], o[1]), rout, tid * 4, (red & 1) * (DM * 2) + (red >> 1) * 64, 0);
      }
    }
  }
  asm volatile(".Lfd_end:");
  STAMP(8);
  STAMP_FLUSH();
}

}

extern "C" void kernel_launch(void* const* d_in, const int* in_sizes, int n_in, void* d_out, int out_size, void* d_ws, size_t ws_size,
                              hipStream_t stream) {
  Args a;
  a.x = (const unsigned short*)d_in[0]; a.k = (const unsigned short*)d_in[1]; a.v = (const unsigned short*)d_in[2];
  a.wq = (const unsigned short*)d_in[3]; a.wo = (const unsigned short*)d_in[4];
  a.w1 = (const unsigned short*)d_in[6]; a.w3 = (const unsigned short*)d_in[7]; a.w2 = (const unsigned short*)d_in[8];
  a.out = (unsigned short*)d_out; a.ws = (unsigned char*)d_ws;
  fused_decode<<<NWG, NTHR, 0, stream>>>(a);
}
```

```cpp
#define DRY 0
#define CODEPF 0
#include <hip/hip_runtime.h>
#include <hip/hip_bf16.h>
#include <stdint.h>

#define KDIST 4
#define KSTAG 1
#define GNT 0
#define QLD 16
#define MIMIC 1
#define LATEFLUSH 0
#define STAMPS 0

namespace {

constexpr int NWG = 256, NTHR = 768, NRED = 32;
constexpr int DM = 512, NH = 32, SK = 16384, DH = 128, FF = 1792;
constexpr float LOG2E = 1.4426950408889634f;
constexpr float SCALE = 0.08838834764831845f;
#define MLS 16
constexpr size_t WS_EPOCH = 0, WS_ML = 8192;
constexpr size_t WS_Y = 65536;
constexpr int YB = 32768 + 128;
constexpr size_t WS_Q = WS_Y + (1u << 20) + 8192;
constexpr size_t WS_G = WS_Q + (256u << 10);
constexpr size_t WS_STAMP = 8u << 20;
constexpr int L_S = 0;
constexpr int L_RED = L_S + 4096;
constexpr int L_Y = L_RED + 2048;
constexpr int L_Y2 = L_Y + 4096;
constexpr int L_Q = L_Y2 + 4096;
constexpr int L_ML = L_Q + 64;
constexpr int L_FW = L_ML + 8;
constexpr int L_FIN = L_FW + 4;
constexpr int L_CNT = L_FIN + 256;
constexpr int L_REF = L_CNT + 4;
constexpr int L_END = L_REF + 4;
#define TR(i) ((void)0)

#define GAS __attribute__((address_space(1)))
#define LAS __attribute__((address_space(3)))
#define RLX_AGENT __ATOMIC_RELAXED, __HIP_MEMORY_SCOPE_AGENT
typedef unsigned u32x4 __attribute__((ext_vector_type(4)));
typedef unsigned u32x2 __attribute__((ext_vector_type(2)));
typedef float f32x2 __attribute__((ext_vector_type(2)));
typedef float f32x4 __attribute__((ext_vector_type(4)));
typedef __bf16 bf16x2 __attribute__((ext_vector_type(2)));
typedef GAS unsigned gu32;
typedef GAS unsigned long long gu64;

__device__ __forceinline__ float bf_lo(unsigned u) { return __builtin_bit_cast(float, u << 16); }
__device__ __forceinline__ float bf_hi(unsigned u) { return __builtin_bit_cast(float, u & 0xffff0000u); }
__device__ __forceinline__ f32x2 bf_unpack(unsigned u) { return f32x2{bf_lo(u), bf_hi(u)}; }
__device__ __forceinline__ unsigned pack_bf16(float lo, float hi) {
  bf16x2 r = {(__bf16)lo, (__bf16)hi};
  return __builtin_bit_cast(unsigned, r);
}
__device__ __forceinline__ float rbf(float x) {
  return (float)(__bf16)x;
}
__device__ __forceinline__ float dot2(unsigned a, unsigned b, float acc) {
  return __builtin_amdgcn_fdot2_f32_bf16(__builtin_bit_cast(bf16x2, a), __builtin_bit_cast(bf16x2, b), acc, false);
}
template <int CTRL> __device__ __forceinline__ float dpp(float x) {
  return __builtin_bit_cast(float, __builtin_amdgcn_mov_dpp(__builtin_bit_cast(int, x), CTRL, 0xf, 0xf, true));
}
constexpr int XOR1 = 0xB1, XOR2 = 0x4E, XOR3 = 0x1B, XOR7 = 0x141, ROR2 = 0x122, ROR4 = 0x124, ROR8 = 0x128;
__device__ __forceinline__ float xrow_sum(float x) {
  auto s = __builtin_amdgcn_permlane16_swap(__float_as_uint(x), __float_as_uint(x), false, false);
  x = __uint_as_float(s[0]) + __uint_as_float(s[1]);
  auto t = __builtin_amdgcn_permlane32_swap(__float_as_uint(x), __float_as_uint(x), false, false);
  return __uint_as_float(t[0]) + __uint_as_float(t[1]);
}
__device__ __forceinline__ float xrow_max(float x) {
  auto s = __builtin_amdgcn_permlane16_swap(__float_as_uint(x), __float_as_uint(x), false, false);
  x = fmaxf(__uint_as_float(s[0]), __uint_as_float(s[1]));
  auto t = __builtin_amdgcn_permlane32_swap(__float_as_uint(x), __float_as_uint(x), false, false);
  return fmaxf(__uint_as_float(t[0]), __uint_as_float(t[1]));
}
__device__ __forceinline__ float tr16(float a, float b) {
  auto s = __builtin_amdgcn_permlane16_swap(__float_as_uint(a), __float_as_uint(b), false, false);
  return __uint_as_float(s[0]) + __uint_as_float(s[1]);
}
__device__ __forceinline__ float tr32(float a, float b) {
  auto s = __builtin_amdgcn_permlane32_swap(__float_as_uint(a), __float_as_uint(b), false, false);
  return __uint_as_float(s[0]) + __uint_as_float(s[1]);
}
__device__ __forceinline__ float wave_sum(float x) {
  x += dpp<XOR1>(x); x += dpp<XOR2>(x); x += dpp<XOR7>(x); x += dpp<ROR8>(x);
  return xrow_sum(x);
}
__device__ __forceinline__ float wave_max(float x) {
  x = fmaxf(x, dpp<XOR1>(x)); x = fmaxf(x, dpp<XOR2>(x)); x = fmaxf(x, dpp<XOR7>(x)); x = fmaxf(x, dpp<ROR8>(x));
  return xrow_max(x);
}
__device__ __forceinline__ void lds_wait() { asm volatile("s_waitcnt lgkmcnt(0)" ::: "memory"); }
__device__ __forceinline__ void st_granule(gu64* g, unsigned tag, float v) {
  __hip_atomic_store(g, ((unsigned long long)tag << 32) | (unsigned long long)__float_as_uint(v), RLX_AGENT);
}
constexpr unsigned SPIN_TICKS = 20000000u;
__device__ __forceinline__ unsigned now32() { return (unsigned)__builtin_amdgcn_s_memrealtime(); }

struct Args {
  const unsigned short* x; const unsigned short* k; const unsigned short* v; const unsigned short* wq; const unsigned short* wo;
  const unsigned short* w1; const unsigned short* w3; const unsigned short* w2;
  unsigned short* out; unsigned char* ws;
};

#define STAMP(i) do { } while (0)
#define STAMP_FLUSH() do { } while (0)
#define STAMP_DECL

typedef __amdgpu_buffer_rsrc_t rsrc_t;
#define WPOL 2
#define NPRE 3
__device__ __forceinline__ u32x4 ldw(rsrc_t r, int voff, int soff) { return __builtin_amdgcn_raw_buffer_load_b128(r, voff, soff, WPOL); }
__device__ __forceinline__ u32x4 ldnt(rsrc_t r, int voff, int soff) { return __builtin_amdgcn_raw_buffer_load_b128(r, voff, soff, 2); }
#define TRK 0
#define WOD 2
#define QDELAY 32
#define FSLEEP 4
#define B0POS (PDIST + 1)
#define SFIRST 1
#define KMIX 0
#define GLATE 1
#define FDELAY 0
#define WIDE_T 3300
#define LAG_A 100
#define LAG_B 110
#define LAG_W 5
#define MSLEEP 16
#define Y16 0
#define FPIPE 0
#define YLD 16
#define YST 16
#define VDIST 4
#define PRETOUCH 1
#define REDMAP 1
#define W2POL 2
#define RTOUCH 0
#define QTOUCH 0
#define PDIST KDIST
#define TRICKLE() __builtin_amdgcn_s_sleep(TRK)
#define HSYNC(n) do { \
    if (lane == 0) __hip_atomic_fetch_add((LAS unsigned*)lds + L_CNT + 1 + (n), 1u, __ATOMIC_RELAXED, __HIP_MEMORY_SCOPE_WORKGROUP); \
    while (__hip_atomic_load((LAS unsigned*)lds + L_CNT + 1 + (n), __ATOMIC_RELAXED, __HIP_MEMORY_SCOPE_WORKGROUP) < 8u) __builtin_amdgcn_s_sleep(0); \
    asm volatile("" ::: "memory"); \
  } while (0)
#define FRESH() int lane_ = lane0; asm volatile("" : "+v"(lane_)); \
  const int lane = lane_, r = lane >> 4, c = lane & 15, tid = wave * 64 + lane, voff = lane * 16; (void)r; (void)c; (void)tid; (void)voff
#define SB() __builtin_amdgcn_sched_barrier(0)
#define VM_DRAIN() do { SB(); asm volatile("s_waitcnt vmcnt(0)" ::: "memory"); SB(); } while (0)

__global__ void __launch_bounds__(NTHR, 3) fused_decode(Args a) {
  __shared__ __attribute__((aligned(16))) float lds[L_END];
  const int tid = threadIdx.x, lane = tid & 63, wave = __builtin_amdgcn_readfirstlane(tid >> 6);
  const int r = lane >> 4, c = lane & 15;
  const int xg = blockIdx.x & 7, j = blockIdx.x >> 3;
  const int b = j >> 4, h = xg * 4 + ((j >> 2) & 3), sp = j & 3;
  const int chunk = b * 128 + h * 4 + sp;
  const int voff = lane * 16;
  STAMP_DECL;
  STAMP(0);

  const int g8 = b * 4 + sp;
  const int prow = (g8 >> 2) * 256 + wave * 32 + 2 * (lane >> 2);
  u32x4 xv0, xv1, wq0, wq1;
  unsigned xq0, xq1, junk = 0;
  if (wave < 8) {
    xv0 = *(const GAS u32x4*)(a.x + lane * 8); xv1 = *(const GAS u32x4*)(a.x + DM + lane * 8);
    xq0 = *(const gu32*)(a.x + prow); xq1 = *(const gu32*)(a.x + DM + prow);
    const rsrc_t rq = __builtin_amdgcn_make_buffer_rsrc((void*)a.wq, 0, DM * NH * DH * 2, 0x00020000);
    wq0 = ldnt(rq, prow * (NH * DH * 2) + (lane & 3) * 16, h * (DH * 2) + (g8 & 3) * 64);
    wq1 = ldnt(rq, prow * (NH * DH * 2) + (lane & 3) * 16, h * (DH * 2) + (g8 & 3) * 64 + NH * DH * 2);
  }
  int role = wave;
  asm volatile("" : "+s"(role));
  if (__builtin_expect(role >= 8, SFIRST)) {
    const int sw = wave - 8;
    constexpr int HB = SK * DH * 2;
    const unsigned short* kp = a.k + (size_t)(chunk >> 2) * SK * DH;
    const unsigned short* vp = a.v + (size_t)(chunk >> 2) * SK * DH;
    const rsrc_t rk = __builtin_amdgcn_make_buffer_rsrc((void*)kp, 0, HB, 0x00020000);
    const rsrc_t rv = __builtin_amdgcn_make_buffer_rsrc((void*)vp, 0, HB, 0x00020000);
    const int stag = j * KSTAG, voffh = voff + HB / 2;
#define ROWVO(n) ((((n) & 1) ? voffh : voff) + (((n) & 7) >> 1) * 1024)
#define ROWSO(pair, n) (((((pair) + ((n) >> 3) + stag) & 31) << 14) + (sw << 12) + (sp << 19))
#define LDROW(rs, pair, n) ldnt(rs, ROWVO(n), ROWSO(pair, n))
    __builtin_amdgcn_s_setprio(2);
    u32x4 buf[8], pre[NPRE * 8];
    STAMP(12);
    if (B0POS == 0) { __builtin_amdgcn_s_barrier(); SB(); }
    STAMP(13);
#pragma unroll
    for (int u = 0; u < NPRE * 8; ++u) {
      pre[u] = LDROW(rk, 0, u); SB();
      if (u + 1 == B0POS) { __builtin_amdgcn_s_barrier(); SB(); }
      if (u >= PDIST) { asm volatile("s_waitcnt vmcnt(%0)" :: "i"(PDIST)); SB(); }
      if (u == PDIST) STAMP(14);
      if ((u & 7) == 6 && u > 8) { TR((u >> 3) - 1); SB(); }
    }
#pragma unroll
    for (int u = 0; u < 8; ++u) {
      buf[u] = LDROW(rk, NPRE, u); SB();
      if (u < 7) { asm volatile("s_waitcnt vmcnt(%0)" :: "i"(PDIST)); SB(); }
    }
    TR(2);
    STAMP(1);
    __builtin_amdgcn_s_barrier();
    asm volatile("" ::: "memory");
    const u32x4 qp = *(const LAS u32x4*)(lds + L_Q + c * 4);
    STAMP(2);
    LAS float* S = (LAS float*)lds + L_S + sw * 1024;
    float mx = -INFINITY;
#define DOT(w) dot2((w)[3], qp[3], dot2((w)[2], qp[2], dot2((w)[1], qp[1], dot2((w)[0], qp[0], 0.f))))
#define K_FIN(tt) do { \
      _Pragma("unroll") for (int u = 0; u < 4; ++u) { const float lo = x[u] + dpp<ROR8>(x[u]), hi = x[u + 4] + dpp<ROR8>(x[u + 4]); x[u] = (c & 8) ? hi : lo; } \
      _Pragma("unroll") for (int u = 0; u < 2; ++u) { const float lo = x[u] + dpp<XOR7>(x[u]), hi = x[u + 2] + dpp<XOR7>(x[u + 2]); x[u] = (c & 4) ? hi : lo; } \
      { const float lo = x[0] + dpp<XOR2>(x[0]), hi = x[1] + dpp<XOR2>(x[1]); x[0] = (c & 2) ? hi : lo; } \
      float sc = x[0] + dpp<XOR1>(x[0]); \
      sc = rbf(rbf(sc) * SCALE); \
      mx = fmaxf(mx, sc); \
      S[(tt) * 32 + r * 8 + (c >> 1)] = sc; \
      SB(); \
    } while (0)
    static_assert(KDIST <= 6 && KDIST >= 3 && NPRE >= 1 && PDIST <= NPRE * 8, "ring of 8");
#define PACE() do { asm volatile("s_waitcnt vmcnt(%0)" :: "i"(((KMIX >> u) & 1) ? KDIST - 1 : KDIST)); SB(); } while (0)
    {
      float x[8];
#pragma unroll
      for (int u = 0; u < 8; ++u) {
        if (u > 0 && u <= KDIST + 1) { buf[u - 1] = LDROW(rk, NPRE + 1, u - 1); SB(); }
        x[u] = DOT(buf[u]);
        SB();
      }
      K_FIN(NPRE);
    }
#pragma unroll
    for (int pp = 0; pp < NPRE; ++pp) {
      float x[8];
#pragma unroll
      for (int w = 0; w < 8; ++w) x[w] = DOT(pre[pp * 8 + w]);
      K_FIN(pp);
    }
#pragma unroll
    for (int u = KDIST + 1; u < 7; ++u) { PACE(); buf[u] = LDROW(rk, NPRE + 1, u); SB(); }
    TR(3);
    STAMP(11);
    int last = 31;
    asm volatile("" : "+s"(last));
#pragma nounroll
    for (int tt = NPRE + 1; tt <= last; ++tt) {
      const unsigned short* nb = tt == last ? vp : kp;
      const rsrc_t rn = __builtin_amdgcn_make_buffer_rsrc((void*)nb, 0, HB, 0x00020000);
      float x[8];
#pragma unroll
      for (int u = 0; u < 8; ++u) {
        const int n = u + 7;
        PACE();
        buf[n & 7] = LDROW(n >= 8 ? rn : rk, tt, n);
        SB();
        x[u] = DOT(buf[u]);
        SB();
      }
      K_FIN(tt);
      TR(tt);
    }
    STAMP(3);
    mx = wave_max(mx);
    if (lane == 0) lds[L_ML + sw] = mx;
    lds_wait();
    if (lane == 0) __hip_atomic_fetch_add((LAS unsigned*)lds + L_CNT, 1u, __ATOMIC_RELAXED, __HIP_MEMORY_SCOPE_WORKGROUP);
    STAMP(4);
    f32x2 acc[4] = {{0.f, 0.f}, {0.f, 0.f}, {0.f, 0.f}, {0.f, 0.f}};
    f32x4 s0 = *(const LAS f32x4*)(S + r * 8), s1;
    int el = -1, lag = -1;
#pragma nounroll
    for (int tt = 0; tt <= last; ++tt) {
      const rsrc_t rn = __builtin_amdgcn_make_buffer_rsrc((void*)vp, 0, tt == last ? 0 : HB, 0x00020000);
      unsigned ref, tnow;
#pragma unroll
      for (int u = 0; u < 8; ++u) {
        const int n = u + 7;
        const int so = ROWSO(tt, n);
        asm volatile("s_cmpk_gt_i32 %0, %4\n\ts_cbranch_scc1 1f\n\ts_waitcnt vmcnt(%2)\n\ts_cmp_gt_i32 %1, 0\n\ts_cbranch_scc1 1f\n\ts_waitcnt vmcnt(%3)\n1:"
                     :: "s"(el), "s"(lag), "i"(LAG_W), "i"(VDIST), "i"(WIDE_T), "s"(so) : "scc");
        SB();
        buf[n & 7] = ldnt(n >= 8 ? rn : rv, ROWVO(n), so);
        SB();
        if (u == 0) s1 = *(const LAS f32x4*)(S + tt * 32 + r * 8 + 4);
        if (u == 4) { s0 = *(const LAS f32x4*)(S + ((tt + 1) & 31) * 32 + r * 8); ref = *(volatile LAS unsigned*)(lds + L_REF); tnow = now32(); }
        const float p = __builtin_amdgcn_exp2f(((u < 4 ? s0[u & 3] : s1[u & 3]) - mx) * LOG2E);
#pragma unroll
        for (int k = 0; k < 4; ++k) acc[k] = __builtin_elementwise_fma(bf_unpack(buf[u][k]), f32x2{p, p}, acc[k]);
        asm volatile("" : "+v"(acc[0]), "+v"(acc[1]), "+v"(acc[2]), "+v"(acc[3]));
        if (u == 6) {
          el = (int)(tnow - __builtin_amdgcn_readfirstlane(ref));
          lag = el - (LAG_A + LAG_B / 2 + tt * LAG_B);
        }
        SB();
      }
      TR(32 + tt);
    }
    STAMP(5);
    *(LAS f32x4*)(lds + L_RED + (sw * 4 + r) * 128 + c * 8) = f32x4{acc[0][0], acc[0][1], acc[1][0], acc[1][1]};
    *(LAS f32x4*)(lds + L_RED + (sw * 4 + r) * 128 + c * 8 + 4) = f32x4{acc[2][0], acc[2][1], acc[3][0], acc[3][1]};
    lds_wait();
    __builtin_amdgcn_s_barrier();
    __builtin_amdgcn_s_barrier();
    if (blockIdx.x < NRED) __builtin_amdgcn_s_barrier();
    STAMP_FLUSH();
    return;
  }

  unsigned epoch = *(const __attribute__((address_space(4))) unsigned*)(a.ws + WS_EPOCH);
  SB();
  if (tid < 5) ((LAS unsigned*)lds)[L_CNT + tid] = tid < 4 ? 0u : (unsigned)__builtin_amdgcn_s_memrealtime() + 0x40000000u;
  lds_wait();
  __builtin_amdgcn_s_barrier();
  SB();
  float mb0, mb1, sd0, sd1;
  {
    float f0[8], f1[8];
#pragma unroll
    for (int k = 0; k < 4; ++k) { f0[2 * k] = bf_lo(xv0[k]); f0[2 * k + 1] = bf_hi(xv0[k]); f1[2 * k] = bf_lo(xv1[k]); f1[2 * k + 1] = bf_hi(xv1[k]); }
    float s0 = 0.f, s1 = 0.f;
#pragma unroll
    for (int k = 0; k < 8; ++k) { s0 += f0[k]; s1 += f1[k]; }
    const float m0 = wave_sum(s0) * (1.f / DM), m1 = wave_sum(s1) * (1.f / DM);
    float v0 = 0.f, v1 = 0.f;
#pragma unroll
    for (int k = 0; k < 8; ++k) { const float d0 = f0[k] - m0, d1 = f1[k] - m1; v0 = fmaf(d0, d0, v0); v1 = fmaf(d1, d1, v1); }
    v0 = wave_sum(v0) * (1.f / DM); v1 = wave_sum(v1) * (1.f / DM);
    mb0 = rbf(m0); sd0 = rbf(__builtin_sqrtf(rbf(rbf(v0) + 1e-5f)));
    mb1 = rbf(m1); sd1 = rbf(__builtin_sqrtf(rbf(rbf(v1) + 1e-5f)));
  }
#define XN(xf, mb, sd) rbf(rbf((xf) - (mb)) / (sd))
  asm volatile("" : "+s"(epoch));
  const unsigned tag = epoch + 0x9E3779B9u;
  STAMP(10);
  const float xa0 = XN(bf_lo(xq0), mb0, sd0), xa1 = XN(bf_hi(xq0), mb0, sd0), xb0 = XN(bf_lo(xq1), mb1, sd1), xb1 = XN(bf_hi(xq1), mb1, sd1);
#define PARTIAL(w0_, w1_, base, n, res) do { \
    float t[8]; \
    _Pragma("unroll") for (int k = 0; k < 4; ++k) { \
      const f32x2 w0 = bf_unpack((w0_)[k]), w1 = bf_unpack((w1_)[k]); \
      const f32x2 qa = __builtin_elementwise_fma(w1, f32x2{xa1, xa1}, w0 * xa0), qb = __builtin_elementwise_fma(w1, f32x2{xb1, xb1}, w0 * xb0); \
      t[2 * k] = tr32(qa[0], qb[0]); t[2 * k + 1] = tr32(qa[1], qb[1]);             \
    } \
    _Pragma("unroll") for (int k = 0; k < 4; ++k) t[k] = tr16(t[k], t[4 + k]);        \
    f32x2 o; \
    _Pragma("unroll") for (int k = 0; k < 2; ++k) { const float lo = t[k] + dpp<ROR8>(t[k]), hi = t[2 + k] + dpp<ROR8>(t[2 + k]); o[k] = (c & 8) ? hi : lo; } \
    const float lo = o[0] + dpp<XOR7>(dpp<XOR3>(o[0])), hi = o[1] + dpp<XOR7>(dpp<XOR3>(o[1]));     \
    lds[(base) + wave * 64 + (r >> 1) * 32 + (c & 3) * 8 + (r & 1) * 4 + (c >> 3) * 2 + ((c >> 2) & 1)] = (c & 4) ? hi : lo; \
    lds_wait(); \
    HSYNC(n); \
    const LAS float* P = (const LAS float*)lds + (base) + lane; \
    res = ((P[0] + P[64]) + (P[128] + P[192])) + ((P[256] + P[320]) + (P[384] + P[448])); \
  } while (0)
  {
    float qs;
    PARTIAL(wq0, wq1, L_Y, 0, qs);
    if (wave == 0) st_granule((gu64*)(a.ws + WS_Q) + (h * 8 + g8) * 64 + lane, tag, qs);
  }
  STAMP(9);
  asm volatile("" :: "v"(junk));
  if (wave == 7) {
    __builtin_amdgcn_s_sleep(QDELAY);
    const rsrc_t rg = __builtin_amdgcn_make_buffer_rsrc((void*)(a.ws + WS_Q + (size_t)h * 4096), 0, 4096, 0x00020000);
    const unsigned t0 = now32();
    u32x4 g[2];
    while (true) {
      bool ok = true;
#pragma unroll
      for (int i = 0; i < 2; ++i)
        g[i] = __builtin_amdgcn_raw_buffer_load_b128(rg, r * 512 + c * 16, i * 2048 + b * 256, QLD);
#pragma unroll
      for (int i = 0; i < 2; ++i) ok = ok & (g[i][1] == tag) & (g[i][3] == tag);
      if (__all(ok)) break;
      if (now32() - t0 > SPIN_TICKS) break;
      __builtin_amdgcn_s_sleep(8);
      asm volatile("" ::: "memory");
    }
    const f32x2 qs = f32x2{__uint_as_float(g[0][0]), __uint_as_float(g[0][2])} + f32x2{__uint_as_float(g[1][0]), __uint_as_float(g[1][2])};
    ((LAS unsigned*)lds)[L_Q + lane] = pack_bf16(qs[0], qs[1]);
    lds_wait();
  }
  STAMP(1);
  __builtin_amdgcn_s_barrier();
  asm volatile("" ::: "memory");
  gu64* G = (gu64*)(a.ws + WS_G);
  float gsum = 0.f;
  const int red = blockIdx.x < NRED ? (xg * 2 + (j & 1)) * 2 + (j >> 1) : -1;
  const unsigned xw = (red >= 0 && tid < 16) ? *(const gu32*)(a.x + (red & 1) * DM + (red >> 1) * 32 + tid * 2) : 0u;
  if (h < 28) {
    const rsrc_t rm = __builtin_amdgcn_make_buffer_rsrc((void*)((g8 & 2) ? a.w3 : a.w1), 0, DM * FF * 2, 0x00020000);
    const int so = h * 128 + (g8 & 1) * 64;
    const u32x4 wm0 = ldnt(rm, prow * (FF * 2) + (lane & 3) * 16, so), wm1 = ldnt(rm, prow * (FF * 2) + (lane & 3) * 16, so + FF * 2);
    PARTIAL(wm0, wm1, L_Y + 1024, 1, gsum);
  }
  const int lane0 = lane;
  int pass = DRY ? 0 : 1;
  asm volatile("" : "+s"(pass));
#pragma nounroll
  for (; pass < 2; ++pass) {
    f32x2 wf[64];
    f32x2 yacc[4] = {{0.f, 0.f}, {0.f, 0.f}, {0.f, 0.f}, {0.f, 0.f}};
    if (!pass) {
#pragma unroll
      for (int i = 0; i < 64; ++i) asm volatile("" : "=v"(wf[i]));
    } else {
      {
        FRESH();
        const rsrc_t ro = __builtin_amdgcn_make_buffer_rsrc((void*)(a.wo + (size_t)h * DH * DM), 0, DH * DM * 2, 0x00020000);
#pragma unroll
        for (int i0 = 0; i0 < 16; i0 += WOD) {
          u32x4 w[WOD];
#pragma unroll
          for (int i = 0; i < WOD; ++i) w[i] = ldw(ro, voff + ((i0 + i) & 3) * 1024, (wave * 16 + ((i0 + i) & ~3)) * 1024);
          VM_DRAIN();
#pragma unroll
          for (int i = 0; i < WOD; ++i)
#pragma unroll
            for (int k = 0; k < 4; ++k) { wf[(i0 + i) * 4 + k] = bf_unpack(w[i][k]); asm volatile("" : "+v"(wf[(i0 + i) * 4 + k])); }
          TRICKLE();
        }
      }
      STAMP(2);
      while (__hip_atomic_load((LAS unsigned*)lds + L_CNT, __ATOMIC_RELAXED, __HIP_MEMORY_SCOPE_WORKGROUP) < 4u) __builtin_amdgcn_s_sleep(8);
      asm volatile("" ::: "memory");
      gu64* ml = (gu64*)(a.ws + WS_ML);
      float mw = 0.f;
      const unsigned tk = (unsigned)__builtin_amdgcn_s_memrealtime();
      if (wave == 7) {
        FRESH();
        mw = lds[L_ML + (lane & 3)];
        float m = fmaxf(mw, dpp<XOR1>(mw)); m = fmaxf(m, dpp<XOR2>(m));
        float l = 0.f;
#pragma unroll 4
        for (int i = 0; i < 16; ++i) {
          const f32x4 sv = *(const LAS f32x4*)(lds + L_S + i * 256 + lane * 4);
#pragma unroll
          for (int k = 0; k < 4; ++k) l += __builtin_amdgcn_exp2f((sv[k] - m) * LOG2E);
        }
        l = wave_sum(l);
        if (lane < 16) st_granule(ml + chunk * MLS + lane, tag, lane == 0 ? m : lane == 1 ? l : __uint_as_float(tk));
      }
      else if (wave == 0 && h < 28) { FRESH(); __builtin_amdgcn_s_sleep(16); st_granule(G + (h * 8 + g8) * 64 + lane, tag, gsum); }
      __builtin_amdgcn_s_sleep(127); __builtin_amdgcn_s_sleep(MSLEEP);
      if (wave < 7) {
        FRESH();
        const int f0 = (h * 4 + sp) * 14 + wave * 2;
        const rsrc_t r2 = __builtin_amdgcn_make_buffer_rsrc((void*)(a.w2 + (size_t)f0 * DM), 0, 2 * DM * 2, 0x00020000);
        const u32x4 w20 = __builtin_amdgcn_raw_buffer_load_b128(r2, voff, 0, W2POL), w21 = __builtin_amdgcn_raw_buffer_load_b128(r2, voff + 1024, 0, W2POL);
        const int f = f0 + (r & 1);
        gu64* gp = G + ((f >> 6) * 8 + (c & 1) * 4 + (c >> 3) * 2 + ((f >> 5) & 1)) * 64 + b * 32 + (f & 31);
        const unsigned t0 = now32();
        unsigned long long g;
        while (true) {
          g = __hip_atomic_load(gp, RLX_AGENT);
          if (__all((unsigned)(g >> 32) == tag)) break;
          if (now32() - t0 > SPIN_TICKS) break;
          __builtin_amdgcn_s_sleep(127);
        }
        float s = __uint_as_float((unsigned)g);
        s += dpp<XOR1>(s);
        const float gs = rbf(s), us = rbf(dpp<ROR8>(s));
        const float si = rbf(gs / (1.f + __expf(-gs)));
        const float hv = rbf(si * us);
        const float h0 = __builtin_bit_cast(float, __builtin_amdgcn_readlane(__builtin_bit_cast(int, hv), 0));
        const float h1 = __builtin_bit_cast(float, __builtin_amdgcn_readlane(__builtin_bit_cast(int, hv), 16));
#pragma unroll
        for (int k = 0; k < 4; ++k) yacc[k] = __builtin_elementwise_fma(bf_unpack(w21[k]), f32x2{h1, h1}, bf_unpack(w20[k]) * h0);
      } else {
        FRESH();
        {
          const unsigned long long gt = __hip_atomic_load(ml + (lane * 4 + (lane & 3)) * MLS + 2, RLX_AGENT);
          const bool v = (unsigned)(gt >> 32) == tag;
          const float n = wave_sum(v ? 1.f : 0.f), d = wave_sum(v ? (float)(int)((unsigned)gt - tk) : 0.f);
          if (lane == 0 && n > 0.f) ((LAS unsigned*)lds)[L_REF] = tk + (unsigned)(int)(d / n);
        }
        const unsigned t0 = now32();
        unsigned long long gm, gl;
        while (true) {
          gm = __hip_atomic_load(ml + ((chunk & ~3) + (lane & 3)) * MLS, RLX_AGENT);
          gl = __hip_atomic_load(ml + ((chunk & ~3) + (lane & 3)) * MLS + 1, RLX_AGENT);
          if (__all((unsigned)(gm >> 32) == tag && (unsigned)(gl >> 32) == tag)) break;
          if (now32() - t0 > SPIN_TICKS) break;
          __builtin_amdgcn_s_sleep(127);
        }
        const float ms = __uint_as_float((unsigned)gm), ls = __uint_as_float((unsigned)gl);
        float M = fmaxf(ms, dpp<XOR1>(ms)); M = fmaxf(M, dpp<XOR2>(M));
        float L = ls * __builtin_amdgcn_exp2f((ms - M) * LOG2E);
        L += dpp<XOR1>(L); L += dpp<XOR2>(L);
        if (lane < 4) lds[L_FW + lane] = __builtin_amdgcn_exp2f((mw - M) * LOG2E) / L;
        lds_wait();
        {
          const unsigned char* pa = (lane < 32 || red < 0) ? a.ws + WS_Y + (lane & 15) * YB + chunk * 128
                                  : (RTOUCH && lane < 48) ? a.ws + WS_Y + (red >> 1) * YB + (red & 1) * 16384 + min((lane & 7) * 4096, 16256)
                                  : (const unsigned char*)a.out + (red & 1) * (DM * 2) + (red >> 1) * 64;
          const unsigned jk = *(const volatile gu32*)pa;
          asm volatile("" :: "v"(jk));
        }
        if (blockIdx.x == NWG - 1) {
          while (true) {
            bool ok = true;
#pragma unroll
            for (int i = 0; i < 4; ++i) ok = ok & ((unsigned)(__hip_atomic_load(ml + (i * 64 + lane) * MLS + 2, RLX_AGENT) >> 32) == tag);
            if (__all(ok)) break;
            if (now32() - t0 > SPIN_TICKS) break;
            __builtin_amdgcn_s_sleep(127);
          }
          if (lane == 0) __hip_atomic_store((gu32*)(a.ws + WS_EPOCH), tag, RLX_AGENT);
        }
        STAMP(3);
      }
    }
    FRESH();
    if (pass) __builtin_amdgcn_s_barrier();
    asm volatile("" ::: "memory");
    STAMP(6);
    {
      const LAS float* P = (const LAS float*)lds + L_RED + (4 * r) * 128 + wave * 16 + c;
      float o = ((P[0] + P[128]) + (P[256] + P[384])) * lds[L_FW + r];
      o = xrow_sum(o);
#pragma unroll
      for (int i = 0; i < 16; ++i) {
        const float oi = __builtin_bit_cast(float, __builtin_amdgcn_readlane(__builtin_bit_cast(int, o), i));
#pragma unroll
        for (int k = 0; k < 4; ++k) yacc[k] = __builtin_elementwise_fma(wf[i * 4 + k], f32x2{oi, oi}, yacc[k]);
      }
    }
    *(LAS f32x4*)(lds + L_Y2 + wave * 512 + lane * 8) = f32x4{yacc[0][0], yacc[0][1], yacc[1][0], yacc[1][1]};
    *(LAS f32x4*)(lds + L_Y2 + wave * 512 + lane * 8 + 4) = f32x4{yacc[2][0], yacc[2][1], yacc[3][0], yacc[3][1]};
    lds_wait();
    if (pass) __builtin_amdgcn_s_barrier();
    asm volatile("" ::: "memory");
    const rsrc_t ry = __builtin_amdgcn_make_buffer_rsrc((void*)(a.ws + WS_Y), 0, pass ? 16 * YB : 0, 0x00020000);
    {
      const LAS float* P = (const LAS float*)lds + L_Y2 + tid;
      const float y = ((P[0] + P[512]) + (P[1024] + P[1536])) + ((P[2048] + P[2560]) + (P[3072] + P[3584]));
      const unsigned u = __float_as_uint(y);
      const unsigned w = (((u & 0x7f800000u) == 0x7f800000u ? u | ((u & 0x7fffffu) ? 0x400000u : 0u) : u + 0x400u) & 0xfffff800u) | ((tag >> ((lane & 1) * 11)) & 0x7ffu);
      const unsigned w1 = __builtin_amdgcn_mov_dpp(w, XOR1, 0xf, 0xf, true);
      if (!(lane & 1)) __builtin_amdgcn_raw_buffer_store_b64(u32x2{w, w1}, ry, (tid >> 5) * YB + (tid & 31) * 4, chunk * 128, YST);
    }
    STAMP(7);
    if (red >= 0) {
      const unsigned t0 = now32();
      u32x4 g0 = {0u, 0u, 0u, 0u}, g1 = {0u, 0u, 0u, 0u};
      bool ok0 = false, ok1 = false;
      const unsigned ta = tag & 0x7ffu, tb = (tag >> 11) & 0x7ffu;
      while (true) {
        if (!ok0) g0 = __builtin_amdgcn_raw_buffer_load_b128(ry, voff, (red >> 1) * YB + (red & 1) * 16384 + wave * 2048, YLD);
        if (!ok1) g1 = __builtin_amdgcn_raw_buffer_load_b128(ry, voff + 1024, (red >> 1) * YB + (red & 1) * 16384 + wave * 2048, YLD);
        ok0 = ((((g0[0] ^ ta) | (g0[1] ^ tb) | (g0[2] ^ ta) | (g0[3] ^ tb)) & 0x7ffu) == 0u);
        ok1 = ((((g1[0] ^ ta) | (g1[1] ^ tb) | (g1[2] ^ ta) | (g1[3] ^ tb)) & 0x7ffu) == 0u);
        if (__all(ok0 & ok1) || !pass) break;
        if (now32() - t0 > SPIN_TICKS) break;
        __builtin_amdgcn_s_sleep(FSLEEP);
        asm volatile("" ::: "memory");
      }
      float t[4];
#pragma unroll
      for (int k = 0; k < 4; ++k) t[k] = __uint_as_float(g0[k] & 0xfffff800u) + __uint_as_float(g1[k] & 0xfffff800u);
      float s = tr16(tr32(t[0], t[2]), tr32(t[1], t[3]));
      s += dpp<ROR8>(s);
      if (!(lane & 8)) lds[L_FIN + wave * 32 + (lane & 7) * 4 + r] = s;
      lds_wait();
      if (pass) __builtin_amdgcn_s_barrier();
      asm volatile("" ::: "memory");
      if (tid < 16) {
        const LAS f32x2* P = (const LAS f32x2*)(lds + L_FIN + tid * 2);
        const f32x2 o = bf_unpack(xw) + (((P[0] + P[16]) + (P[32] + P[48])) + ((P[64] + P[80]) + (P[96] + P[112])));
        const rsrc_t rout = __builtin_amdgcn_make_buffer_rsrc((void*)a.out, 0, pass ? 2 * DM * 2 : 0, 0x00020000);
        __builtin_amdgcn_raw_buffer_store_b32(pack_bf16(o[0], o[1]), rout, tid * 4, (red & 1) * (DM * 2) + (red >> 1) * 64, 0);
      }
    }
  }
  STAMP(8);
  STAMP_FLUSH();
}

}

extern "C" void kernel_launch(void* const* d_in, const int* in_sizes, int n_in, void* d_out, int out_size, void* d_ws, size_t ws_size,
                              hipStream_t stream) {
  Args a;
  a.x = (const unsigned short*)d_in[0]; a.k = (const unsigned short*)d_in[1]; a.v = (const unsigned short*)d_in[2];
  a.wq = (const unsigned short*)d_in[3]; a.wo = (const unsigned short*)d_in[4];
  a.w1 = (const unsigned short*)d_in[6]; a.w3 = (const unsigned short*)d_in[7]; a.w2 = (const unsigned short*)d_in[8];
  a.out = (unsigned short*)d_out; a.ws = (unsigned char*)d_ws;
  fused_decode<<<NWG, NTHR, 0, stream>>>(a);
}
```

```cpp
#include <hip/hip_runtime.h>
#include <hip/hip_bf16.h>
#include <stdint.h>

namespace {

constexpr int NWG = 256, NTHR = 768, NRED = 32;
constexpr int DM = 512, NH = 32, SK = 16384, DH = 128, FF = 1792;
constexpr float LOG2E = 1.4426950408889634f;
constexpr float SCALE = 0.08838834764831845f;
constexpr int WIN = 4;
constexpr int NPRE = 3;
constexpr int LAG_A = 155, LAG_B = 110, WIDE_T = 3300;
constexpr size_t WS_EPOCH = 0;
constexpr size_t WS_ML = 8192;
constexpr size_t WS_Y = 65536;
constexpr int YB = 32768;
constexpr size_t WS_Q = WS_Y + 16 * YB;
constexpr size_t WS_G = WS_Q + (128u << 10);
constexpr int L_S = 0;
constexpr int L_O = L_S + 4096;
constexpr int L_Y = L_O + 2048;
constexpr int L_P = L_Y + 4096;
constexpr int L_Q = L_P + 1024;
constexpr int L_MX = L_Q + 64;
constexpr int L_FW = L_MX + 4;
constexpr int L_FIN = L_FW + 4;
constexpr int L_CNT = L_FIN + 256;
constexpr int L_REF = L_CNT + 3;
constexpr int L_END = L_REF + 1;

#define GAS __attribute__((address_space(1)))
#define LAS __attribute__((address_space(3)))
#define RLX_AGENT __ATOMIC_RELAXED, __HIP_MEMORY_SCOPE_AGENT
#define RLX_WG __ATOMIC_RELAXED, __HIP_MEMORY_SCOPE_WORKGROUP
typedef unsigned u32x2 __attribute__((ext_vector_type(2)));
typedef unsigned u32x4 __attribute__((ext_vector_type(4)));
typedef float f32x2 __attribute__((ext_vector_type(2)));
typedef float f32x4 __attribute__((ext_vector_type(4)));
typedef __bf16 bf16x2 __attribute__((ext_vector_type(2)));
typedef GAS unsigned gu32;
typedef GAS unsigned long long gu64;
typedef __amdgpu_buffer_rsrc_t rsrc_t;

__device__ __forceinline__ float bf_lo(unsigned u) { return __builtin_bit_cast(float, u << 16); }
__device__ __forceinline__ float bf_hi(unsigned u) { return __builtin_bit_cast(float, u & 0xffff0000u); }
__device__ __forceinline__ f32x2 bf_unpack(unsigned u) { return f32x2{bf_lo(u), bf_hi(u)}; }
__device__ __forceinline__ unsigned pack_bf16(float lo, float hi) {
  bf16x2 r = {(__bf16)lo, (__bf16)hi};
  return __builtin_bit_cast(unsigned, r);
}
__device__ __forceinline__ float rbf(float x) { return (float)(__bf16)x; }
__device__ __forceinline__ float dot2(unsigned a, unsigned b, float acc) {
  return __builtin_amdgcn_fdot2_f32_bf16(__builtin_bit_cast(bf16x2, a), __builtin_bit_cast(bf16x2, b), acc, false);
}
template <int CTRL> __device__ __forceinline__ float dpp(float x) {
  return __builtin_bit_cast(float, __builtin_amdgcn_mov_dpp(__builtin_bit_cast(int, x), CTRL, 0xf, 0xf, true));
}
constexpr int XOR1 = 0xB1, XOR2 = 0x4E, XOR3 = 0x1B, XOR7 = 0x141, ROR8 = 0x128;
__device__ __forceinline__ float tr16(float a, float b) {
  auto s = __builtin_amdgcn_permlane16_swap(__float_as_uint(a), __float_as_uint(b), false, false);
  return __uint_as_float(s[0]) + __uint_as_float(s[1]);
}
__device__ __forceinline__ float tr32(float a, float b) {
  auto s = __builtin_amdgcn_permlane32_swap(__float_as_uint(a), __float_as_uint(b), false, false);
  return __uint_as_float(s[0]) + __uint_as_float(s[1]);
}
__device__ __forceinline__ float xrow_sum(float x) { x = tr16(x, x); return tr32(x, x); }
__device__ __forceinline__ float wave_sum(float x) {
  x += dpp<XOR1>(x); x += dpp<XOR2>(x); x += dpp<XOR7>(x); x += dpp<ROR8>(x);
  return xrow_sum(x);
}
__device__ __forceinline__ float wave_max(float x) {
  x = fmaxf(x, dpp<XOR1>(x)); x = fmaxf(x, dpp<XOR2>(x)); x = fmaxf(x, dpp<XOR7>(x)); x = fmaxf(x, dpp<ROR8>(x));
  auto s = __builtin_amdgcn_permlane16_swap(__float_as_uint(x), __float_as_uint(x), false, false);
  x = fmaxf(__uint_as_float(s[0]), __uint_as_float(s[1]));
  auto t = __builtin_amdgcn_permlane32_swap(__float_as_uint(x), __float_as_uint(x), false, false);
  return fmaxf(__uint_as_float(t[0]), __uint_as_float(t[1]));
}
__device__ __forceinline__ float lane_value(float x, int l) { return __builtin_bit_cast(float, __builtin_amdgcn_readlane(__builtin_bit_cast(int, x), l)); }
__device__ __forceinline__ void lds_wait() { asm volatile("s_waitcnt lgkmcnt(0)" ::: "memory"); }
__device__ __forceinline__ void wg_barrier() { __builtin_amdgcn_s_barrier(); asm volatile("" ::: "memory"); }
__device__ __forceinline__ void st_granule(gu64* g, unsigned tag, float v) {
  __hip_atomic_store(g, ((unsigned long long)tag << 32) | (unsigned long long)__float_as_uint(v), RLX_AGENT);
}
__device__ __forceinline__ rsrc_t mk_rsrc(const void* p, int bytes) { return __builtin_amdgcn_make_buffer_rsrc((void*)p, 0, bytes, 0x00020000); }
__device__ __forceinline__ u32x4 ldnt(rsrc_t r, int voff, int soff) { return __builtin_amdgcn_raw_buffer_load_b128(r, voff, soff, 2); }
__device__ __forceinline__ u32x4 lddev(rsrc_t r, int voff, int soff) { return __builtin_amdgcn_raw_buffer_load_b128(r, voff, soff, 16); }
__device__ __forceinline__ unsigned now32() { return (unsigned)__builtin_amdgcn_s_memrealtime(); }
constexpr unsigned SPIN_TICKS = 20000000u;

#define SB() __builtin_amdgcn_sched_barrier(0)
#define VM_WAIT(n) do { asm volatile("s_waitcnt vmcnt(%0)" :: "i"(n)); SB(); } while (0)
#define FRESH() int lane_ = lane0; asm volatile("" : "+v"(lane_)); \
  const int lane = lane_, r = lane >> 4, c = lane & 15, tid = wave * 64 + lane, voff = lane * 16; (void)r; (void)c; (void)tid; (void)voff

struct Args {
  const unsigned short* x; const unsigned short* k; const unsigned short* v; const unsigned short* wq; const unsigned short* wo;
  const unsigned short* w1; const unsigned short* w3; const unsigned short* w2;
  unsigned short* out; unsigned char* ws;
};

__global__ void __launch_bounds__(NTHR, 3) fused_decode(Args a) {
  __shared__ __attribute__((aligned(16))) float lds[L_END];
  const int tid = threadIdx.x, lane = tid & 63, wave = __builtin_amdgcn_readfirstlane(tid >> 6);
  const int r = lane >> 4, c = lane & 15;
  const int xg = blockIdx.x & 7, j = blockIdx.x >> 3;
  const int b = j >> 4, h = xg * 2 + ((j >> 2) & 1) + (j & 8) * 2, sp = j & 3;
  const int chunk = b * 128 + h * 4 + sp;
  const int voff = lane * 16;

  const int g8 = b * 4 + sp;
  const int prow = (g8 >> 2) * 256 + wave * 32 + 2 * (lane >> 2);
  u32x4 xv0, xv1, wq0, wq1;
  unsigned xq0, xq1;
  if (wave < 8) {
    xv0 = *(const GAS u32x4*)(a.x + lane * 8); xv1 = *(const GAS u32x4*)(a.x + DM + lane * 8);
    xq0 = *(const gu32*)(a.x + prow); xq1 = *(const gu32*)(a.x + DM + prow);
    const rsrc_t rq = mk_rsrc(a.wq, DM * NH * DH * 2);
    wq0 = ldnt(rq, prow * (NH * DH * 2) + (lane & 3) * 16, h * (DH * 2) + (g8 & 3) * 64);
    wq1 = ldnt(rq, prow * (NH * DH * 2) + (lane & 3) * 16, h * (DH * 2) + (g8 & 3) * 64 + NH * DH * 2);
    SB();
    wg_barrier();
  }
  int role = wave;
  asm volatile("" : "+s"(role));
  if (__builtin_expect(role >= 8, 1)) {
    const int sw = wave - 8;
    constexpr int HB = SK * DH * 2;
    const unsigned short* kp = a.k + (size_t)(chunk >> 2) * SK * DH;
    const unsigned short* vp = a.v + (size_t)(chunk >> 2) * SK * DH;
    const rsrc_t rk = mk_rsrc(kp, HB), rv = mk_rsrc(vp, HB);
    const int voffh = voff + HB / 2;
#define ROWVO(n) ((((n) & 1) ? voffh : voff) + (((n) & 7) >> 1) * 1024)
#define ROWSO(grp, n) (((((grp) + ((n) >> 3) + j) & 31) << 14) + (sw << 12) + (sp << 19))
#define LDROW(rs, grp, n) ldnt(rs, ROWVO(n), ROWSO(grp, n))
    __builtin_amdgcn_s_setprio(2);
    u32x4 buf[8], pre[NPRE * 8];
#pragma unroll
    for (int u = 0; u < NPRE * 8; ++u) {
      pre[u] = LDROW(rk, 0, u); SB();
      if (u == WIN) {
        if (lane < 4) ((LAS unsigned*)lds)[L_CNT + lane] = lane < 3 ? 0u : now32() + 0x40000000u;
        lds_wait();
        wg_barrier();
        SB();
      }
      if (u >= WIN) VM_WAIT(WIN);
    }
#pragma unroll
    for (int u = 0; u < 8; ++u) {
      buf[u] = LDROW(rk, NPRE, u); SB();
      if (u < 7) VM_WAIT(WIN);
    }
    wg_barrier();
    const u32x4 qp = *(const LAS u32x4*)(lds + L_Q + c * 4);
    LAS float* S = (LAS float*)lds + L_S + sw * 1024;
    float mx = -INFINITY;
#define DOT(w) dot2((w)[3], qp[3], dot2((w)[2], qp[2], dot2((w)[1], qp[1], dot2((w)[0], qp[0], 0.f))))
#define K_FIN(grp) do { \
      _Pragma("unroll") for (int u = 0; u < 4; ++u) { const float lo = x[u] + dpp<ROR8>(x[u]), hi = x[u + 4] + dpp<ROR8>(x[u + 4]); x[u] = (c & 8) ? hi : lo; } \
      _Pragma("unroll") for (int u = 0; u < 2; ++u) { const float lo = x[u] + dpp<XOR7>(x[u]), hi = x[u + 2] + dpp<XOR7>(x[u + 2]); x[u] = (c & 4) ? hi : lo; } \
      { const float lo = x[0] + dpp<XOR2>(x[0]), hi = x[1] + dpp<XOR2>(x[1]); x[0] = (c & 2) ? hi : lo; } \
      float sc = x[0] + dpp<XOR1>(x[0]); \
      sc = rbf(rbf(sc) * SCALE); \
      mx = fmaxf(mx, sc); \
      S[(grp) * 32 + r * 8 + (c >> 1)] = sc; \
      SB(); \
    } while (0)
    static_assert(WIN >= 3 && WIN <= 5, "ring of 8 rows");
    {
      float x[8];
#pragma unroll
      for (int u = 0; u < 8; ++u) {
        if (u > 0 && u <= WIN + 1) { buf[u - 1] = LDROW(rk, NPRE + 1, u - 1); SB(); }
        x[u] = DOT(buf[u]);
        SB();
      }
      K_FIN(NPRE);
    }
#pragma unroll
    for (int pp = 0; pp < NPRE; ++pp) {
      float x[8];
#pragma unroll
      for (int w = 0; w < 8; ++w) x[w] = DOT(pre[pp * 8 + w]);
      K_FIN(pp);
    }
#pragma unroll
    for (int u = WIN + 1; u < 7; ++u) { VM_WAIT(WIN); buf[u] = LDROW(rk, NPRE + 1, u); SB(); }
    int last = 31;
    asm volatile("" : "+s"(last));
#pragma nounroll
    for (int tt = NPRE + 1; tt <= last; ++tt) {
      const rsrc_t rn = mk_rsrc(tt == last ? vp : kp, HB);
      float x[8];
#pragma unroll
      for (int u = 0; u < 8; ++u) {
        const int n = u + 7;
        VM_WAIT(WIN);
        buf[n & 7] = LDROW(n >= 8 ? rn : rk, tt, n);
        SB();
        x[u] = DOT(buf[u]);
        SB();
      }
      K_FIN(tt);
    }
    mx = wave_max(mx);
    if (lane == 0) lds[L_MX + sw] = mx;
    lds_wait();
    if (lane == 0) __hip_atomic_fetch_add((LAS unsigned*)lds + L_CNT, 1u, RLX_WG);
    f32x2 acc[4] = {{0.f, 0.f}, {0.f, 0.f}, {0.f, 0.f}, {0.f, 0.f}};
    f32x4 s0 = *(const LAS f32x4*)(S + r * 8), s1;
    int el = -1, lag = -1;
#pragma nounroll
    for (int tt = 0; tt <= last; ++tt) {
      const rsrc_t rn = mk_rsrc(vp, tt == last ? 0 : HB);
      unsigned ref, tnow;
#pragma unroll
      for (int u = 0; u < 8; ++u) {
        const int n = u + 7;
        const int so = ROWSO(tt, n);
        asm volatile("s_cmpk_gt_i32 %0, %4\n\ts_cbranch_scc1 1f\n\ts_waitcnt vmcnt(%2)\n\ts_cmp_gt_i32 %1, 0\n\ts_cbranch_scc1 1f\n\ts_waitcnt vmcnt(%3)\n1:"
                     :: "s"(el), "s"(lag), "i"(WIN + 1), "i"(WIN), "i"(WIDE_T), "s"(so) : "scc");
        SB();
        buf[n & 7] = ldnt(n >= 8 ? rn : rv, ROWVO(n), so);
        SB();
        if (u == 0) s1 = *(const LAS f32x4*)(S + tt * 32 + r * 8 + 4);
        if (u == 4) { s0 = *(const LAS f32x4*)(S + ((tt + 1) & 31) * 32 + r * 8); ref = *(volatile LAS unsigned*)(lds + L_REF); tnow = now32(); }
        const float p = __builtin_amdgcn_exp2f(((u < 4 ? s0[u & 3] : s1[u & 3]) - mx) * LOG2E);
#pragma unroll
        for (int k = 0; k < 4; ++k) acc[k] = __builtin_elementwise_fma(bf_unpack(buf[u][k]), f32x2{p, p}, acc[k]);
        asm volatile("" : "+v"(acc[0]), "+v"(acc[1]), "+v"(acc[2]), "+v"(acc[3]));
        if (u == 6) {
          asm volatile("" : "+v"(ref), "+s"(tnow));
          el = (int)(tnow - __builtin_amdgcn_readfirstlane(ref));
          lag = el - (LAG_A + tt * LAG_B);
        }
        SB();
      }
    }
    *(LAS f32x4*)(lds + L_O + (sw * 4 + r) * 128 + c * 8) = f32x4{acc[0][0], acc[0][1], acc[1][0], acc[1][1]};
    *(LAS f32x4*)(lds + L_O + (sw * 4 + r) * 128 + c * 8 + 4) = f32x4{acc[2][0], acc[2][1], acc[3][0], acc[3][1]};
    lds_wait();
    wg_barrier();
    wg_barrier();
    if (blockIdx.x < NRED) wg_barrier();
    return;
  }

  unsigned epoch = *(const __attribute__((address_space(4))) unsigned*)(a.ws + WS_EPOCH);
  SB();
  float mb0, mb1, sd0, sd1;
  {
    float f0[8], f1[8];
#pragma unroll
    for (int k = 0; k < 4; ++k) { f0[2 * k] = bf_lo(xv0[k]); f0[2 * k + 1] = bf_hi(xv0[k]); f1[2 * k] = bf_lo(xv1[k]); f1[2 * k + 1] = bf_hi(xv1[k]); }
    float s0 = 0.f, s1 = 0.f;
#pragma unroll
    for (int k = 0; k < 8; ++k) { s0 += f0[k]; s1 += f1[k]; }
    const float m0 = wave_sum(s0) * (1.f / DM), m1 = wave_sum(s1) * (1.f / DM);
    float v0 = 0.f, v1 = 0.f;
#pragma unroll
    for (int k = 0; k < 8; ++k) { const float d0 = f0[k] - m0, d1 = f1[k] - m1; v0 = fmaf(d0, d0, v0); v1 = fmaf(d1, d1, v1); }
    v0 = wave_sum(v0) * (1.f / DM); v1 = wave_sum(v1) * (1.f / DM);
    mb0 = rbf(m0); sd0 = rbf(__builtin_sqrtf(rbf(rbf(v0) + 1e-5f)));
    mb1 = rbf(m1); sd1 = rbf(__builtin_sqrtf(rbf(rbf(v1) + 1e-5f)));
  }
#define XN(xf, mb, sd) rbf(rbf((xf) - (mb)) / (sd))
  asm volatile("" : "+s"(epoch));
  const unsigned tag = epoch + 0x9E3779B9u;
  const float xa0 = XN(bf_lo(xq0), mb0, sd0), xa1 = XN(bf_hi(xq0), mb0, sd0), xb0 = XN(bf_lo(xq1), mb1, sd1), xb1 = XN(bf_hi(xq1), mb1, sd1);
#define PARTIAL(w0_, w1_, n, res) do { \
    float t[8]; \
    _Pragma("unroll") for (int k = 0; k < 4; ++k) { \
      const f32x2 w0 = bf_unpack((w0_)[k]), w1 = bf_unpack((w1_)[k]); \
      const f32x2 qa = __builtin_elementwise_fma(w1, f32x2{xa1, xa1}, w0 * xa0), qb = __builtin_elementwise_fma(w1, f32x2{xb1, xb1}, w0 * xb0); \
      t[2 * k] = tr32(qa[0], qb[0]); t[2 * k + 1] = tr32(qa[1], qb[1]);             \
    } \
    _Pragma("unroll") for (int k = 0; k < 4; ++k) t[k] = tr16(t[k], t[4 + k]);        \
    f32x2 o; \
    _Pragma("unroll") for (int k = 0; k < 2; ++k) { const float lo = t[k] + dpp<ROR8>(t[k]), hi = t[2 + k] + dpp<ROR8>(t[2 + k]); o[k] = (c & 8) ? hi : lo; } \
    const float lo = o[0] + dpp<XOR7>(dpp<XOR3>(o[0])), hi = o[1] + dpp<XOR7>(dpp<XOR3>(o[1]));     \
    lds[L_P + (n) * 512 + wave * 64 + (r >> 1) * 32 + (c & 3) * 8 + (r & 1) * 4 + (c >> 3) * 2 + ((c >> 2) & 1)] = (c & 4) ? hi : lo; \
    lds_wait(); \
    if (lane == 0) __hip_atomic_fetch_add((LAS unsigned*)lds + L_CNT + 1 + (n), 1u, RLX_WG); \
    while (__hip_atomic_load((LAS unsigned*)lds + L_CNT + 1 + (n), RLX_WG) < 8u) __builtin_amdgcn_s_sleep(0); \
    asm volatile("" ::: "memory"); \
    const LAS float* P = (const LAS float*)lds + L_P + (n) * 512 + lane; \
    res = ((P[0] + P[64]) + (P[128] + P[192])) + ((P[256] + P[320]) + (P[384] + P[448])); \
  } while (0)
  {
    float qs;
    PARTIAL(wq0, wq1, 0, qs);
    if (wave == 0) st_granule((gu64*)(a.ws + WS_Q) + (h * 8 + g8) * 64 + lane, tag, qs);
  }
  if (wave == 7) {
    __builtin_amdgcn_s_sleep(32);
    const rsrc_t rg = mk_rsrc(a.ws + WS_Q + (size_t)h * 4096, 4096);
    const unsigned t0 = now32();
    u32x4 g0, g1;
    while (true) {
      g0 = lddev(rg, r * 512 + c * 16, b * 256); g1 = lddev(rg, r * 512 + c * 16, 2048 + b * 256);
      if (__all((g0[1] == tag) & (g0[3] == tag) & (g1[1] == tag) & (g1[3] == tag))) break;
      if (now32() - t0 > SPIN_TICKS) break;
      __builtin_amdgcn_s_sleep(8);
      asm volatile("" ::: "memory");
    }
    ((LAS unsigned*)lds)[L_Q + lane] = pack_bf16(__uint_as_float(g0[0]) + __uint_as_float(g1[0]), __uint_as_float(g0[2]) + __uint_as_float(g1[2]));
    lds_wait();
  }
  wg_barrier();
  const int red = blockIdx.x < NRED ? (xg * 2 + (j & 1)) * 2 + (j >> 1) : -1;
  const unsigned xw = (red >= 0 && tid < 16) ? *(const gu32*)(a.x + (red & 1) * DM + (red >> 1) * 32 + tid * 2) : 0u;
  float gsum = 0.f;
  if (h < 28) {
    const rsrc_t rm = mk_rsrc((g8 & 2) ? a.w3 : a.w1, DM * FF * 2);
    const int so = h * 128 + (g8 & 1) * 64;
    const u32x4 wm0 = ldnt(rm, prow * (FF * 2) + (lane & 3) * 16, so), wm1 = ldnt(rm, prow * (FF * 2) + (lane & 3) * 16, so + FF * 2);
    PARTIAL(wm0, wm1, 1, gsum);
  }
  f32x2 wf[64];
  {
    const rsrc_t ro = mk_rsrc(a.wo + (size_t)h * DH * DM, DH * DM * 2);
#pragma unroll
    for (int i = 0; i < 16; i += 2) {
      const u32x4 w0 = ldnt(ro, voff + (i & 3) * 1024, (wave * 16 + (i & ~3)) * 1024), w1 = ldnt(ro, voff + (i & 3) * 1024 + 1024, (wave * 16 + (i & ~3)) * 1024);
      SB();
#pragma unroll
      for (int k = 0; k < 4; ++k) {
        wf[i * 4 + k] = bf_unpack(w0[k]); wf[i * 4 + 4 + k] = bf_unpack(w1[k]);
        asm volatile("" : "+v"(wf[i * 4 + k]), "+v"(wf[i * 4 + 4 + k]));
      }
      SB();
    }
  }
  const int lane0 = lane;
  while (__hip_atomic_load((LAS unsigned*)lds + L_CNT, RLX_WG) < 4u) __builtin_amdgcn_s_sleep(8);
  asm volatile("" ::: "memory");
  gu64* ml = (gu64*)(a.ws + WS_ML);
  gu64* G = (gu64*)(a.ws + WS_G);
  float mw = 0.f;
  const unsigned tk = now32();
  if (wave == 7) {
    FRESH();
    mw = lds[L_MX + (lane & 3)];
    float m = fmaxf(mw, dpp<XOR1>(mw)); m = fmaxf(m, dpp<XOR2>(m));
    float l = 0.f;
#pragma unroll 2
    for (int i = 0; i < 16; ++i) {
      const f32x4 sv = *(const LAS f32x4*)(lds + L_S + i * 256 + lane * 4);
#pragma unroll
      for (int k = 0; k < 4; ++k) l += __builtin_amdgcn_exp2f((sv[k] - m) * LOG2E);
    }
    l = wave_sum(l);
    if (lane < 16) st_granule(ml + chunk * 16 + lane, tag, lane == 0 ? m : lane == 1 ? l : __uint_as_float(tk));
  } else if (wave == 0 && h < 28) {
    FRESH();
    __builtin_amdgcn_s_sleep(16);
    st_granule(G + (h * 8 + g8) * 64 + lane, tag, gsum);
  }
  __builtin_amdgcn_s_sleep(127); __builtin_amdgcn_s_sleep(16);
  f32x2 yacc[4] = {{0.f, 0.f}, {0.f, 0.f}, {0.f, 0.f}, {0.f, 0.f}};
  if (wave < 7) {
    FRESH();
    const int f0 = (h * 4 + sp) * 14 + wave * 2;
    const rsrc_t r2 = mk_rsrc(a.w2 + (size_t)f0 * DM, 2 * DM * 2);
    const u32x4 w20 = ldnt(r2, voff, 0), w21 = ldnt(r2, voff + 1024, 0);
    const int f = f0 + (r & 1);
    gu64* gp = G + ((f >> 6) * 8 + (c & 1) * 4 + (c >> 3) * 2 + ((f >> 5) & 1)) * 64 + b * 32 + (f & 31);
    const unsigned t0 = now32();
    unsigned long long g;
    while (true) {
      g = __hip_atomic_load(gp, RLX_AGENT);
      if (__all((unsigned)(g >> 32) == tag)) break;
      if (now32() - t0 > SPIN_TICKS) break;
      __builtin_amdgcn_s_sleep(127);
    }
    float s = __uint_as_float((unsigned)g);
    s += dpp<XOR1>(s);
    const float gs = rbf(s), us = rbf(dpp<ROR8>(s));
    const float hv = rbf(rbf(gs / (1.f + __expf(-gs))) * us);
    const float h0 = lane_value(hv, 0), h1 = lane_value(hv, 16);
#pragma unroll
    for (int k = 0; k < 4; ++k) yacc[k] = __builtin_elementwise_fma(bf_unpack(w21[k]), f32x2{h1, h1}, bf_unpack(w20[k]) * h0);
  } else {
    FRESH();
    {
      const unsigned long long gt = __hip_atomic_load(ml + (lane * 4 + (lane & 3)) * 16 + 2, RLX_AGENT);
      const bool v = (unsigned)(gt >> 32) == tag;
      const float n = wave_sum(v ? 1.f : 0.f), d = wave_sum(v ? (float)(int)((unsigned)gt - tk) : 0.f);
      if (lane == 0 && n > 0.f) ((LAS unsigned*)lds)[L_REF] = tk + (unsigned)(int)(d / n);
    }
    const unsigned t0 = now32();
    unsigned long long gm, gl;
    while (true) {
      gm = __hip_atomic_load(ml + ((chunk & ~3) + (lane & 3)) * 16, RLX_AGENT);
      gl = __hip_atomic_load(ml + ((chunk & ~3) + (lane & 3)) * 16 + 1, RLX_AGENT);
      if (__all((unsigned)(gm >> 32) == tag && (unsigned)(gl >> 32) == tag)) break;
      if (now32() - t0 > SPIN_TICKS) break;
      __builtin_amdgcn_s_sleep(127);
    }
    const float ms = __uint_as_float((unsigned)gm), ls = __uint_as_float((unsigned)gl);
    float M = fmaxf(ms, dpp<XOR1>(ms)); M = fmaxf(M, dpp<XOR2>(M));
    float L = ls * __builtin_amdgcn_exp2f((ms - M) * LOG2E);
    L += dpp<XOR1>(L); L += dpp<XOR2>(L);
    if (lane < 4) lds[L_FW + lane] = __builtin_amdgcn_exp2f((mw - M) * LOG2E) / L;
    lds_wait();
    {
      const unsigned char* pa = (lane < 32 || red < 0) ? a.ws + WS_Y + (lane & 15) * YB + chunk * 128
                                                       : (const unsigned char*)a.out + (red & 1) * (DM * 2) + (red >> 1) * 64;
      const unsigned jk = __hip_atomic_load((gu32*)pa, RLX_AGENT);
      asm volatile("" :: "v"(jk));
    }
    if (blockIdx.x == NWG - 1) {
      while (true) {
        bool ok = true;
#pragma unroll
        for (int i = 0; i < 4; ++i) ok = ok & ((unsigned)(__hip_atomic_load(ml + (i * 64 + lane) * 16 + 2, RLX_AGENT) >> 32) == tag);
        if (__all(ok)) break;
        if (now32() - t0 > SPIN_TICKS) break;
        __builtin_amdgcn_s_sleep(127);
      }
      if (lane == 0) __hip_atomic_store((gu32*)(a.ws + WS_EPOCH), tag, RLX_AGENT);
    }
  }
  {
    FRESH();
    wg_barrier();
    {
      const LAS float* P = (const LAS float*)lds + L_O + (4 * r) * 128 + wave * 16 + c;
      const float o = xrow_sum(((P[0] + P[128]) + (P[256] + P[384])) * lds[L_FW + r]);
#pragma unroll
      for (int i = 0; i < 16; ++i) {
        const float oi = lane_value(o, i);
#pragma unroll
        for (int k = 0; k < 4; ++k) yacc[k] = __builtin_elementwise_fma(wf[i * 4 + k], f32x2{oi, oi}, yacc[k]);
      }
    }
    *(LAS f32x4*)(lds + L_Y + wave * 512 + lane * 8) = f32x4{yacc[0][0], yacc[0][1], yacc[1][0], yacc[1][1]};
    *(LAS f32x4*)(lds + L_Y + wave * 512 + lane * 8 + 4) = f32x4{yacc[2][0], yacc[2][1], yacc[3][0], yacc[3][1]};
    lds_wait();
    wg_barrier();
    const rsrc_t ry = mk_rsrc(a.ws + WS_Y, 16 * YB);
    {
      const LAS float* P = (const LAS float*)lds + L_Y + tid;
      const float y = ((P[0] + P[512]) + (P[1024] + P[1536])) + ((P[2048] + P[2560]) + (P[3072] + P[3584]));
      const unsigned u = __float_as_uint(y);
      const unsigned w = (((u & 0x7f800000u) == 0x7f800000u ? u | ((u & 0x7fffffu) ? 0x400000u : 0u) : u + 0x400u) & 0xfffff800u) | ((tag >> ((lane & 1) * 11)) & 0x7ffu);
      const unsigned w1 = __builtin_amdgcn_mov_dpp(w, XOR1, 0xf, 0xf, true);
      if (!(lane & 1)) __builtin_amdgcn_raw_buffer_store_b64(u32x2{w, w1}, ry, (tid >> 5) * YB + (tid & 31) * 4, chunk * 128, 16);
    }
    if (red >= 0) {
      const unsigned t0 = now32();
      u32x4 g0 = {0u, 0u, 0u, 0u}, g1 = {0u, 0u, 0u, 0u};
      bool ok0 = false, ok1 = false;
      const unsigned ta = tag & 0x7ffu, tb = (tag >> 11) & 0x7ffu;
      while (true) {
        if (!ok0) g0 = lddev(ry, voff, red * 16384 + wave * 2048);
        if (!ok1) g1 = lddev(ry, voff + 1024, red * 16384 + wave * 2048);
        ok0 = (((g0[0] ^ ta) | (g0[1] ^ tb) | (g0[2] ^ ta) | (g0[3] ^ tb)) & 0x7ffu) == 0u;
        ok1 = (((g1[0] ^ ta) | (g1[1] ^ tb) | (g1[2] ^ ta) | (g1[3] ^ tb)) & 0x7ffu) == 0u;
        if (__all(ok0 & ok1)) break;
        if (now32() - t0 > SPIN_TICKS) break;
        __builtin_amdgcn_s_sleep(4);
        asm volatile("" ::: "memory");
      }
      float t[4];
#pragma unroll
      for (int k = 0; k < 4; ++k) t[k] = __uint_as_float(g0[k] & 0xfffff800u) + __uint_as_float(g1[k] & 0xfffff800u);
      float s = tr16(tr32(t[0], t[2]), tr32(t[1], t[3]));
      s += dpp<ROR8>(s);
      if (!(lane & 8)) lds[L_FIN + wave * 32 + (lane & 7) * 4 + r] = s;
      lds_wait();
      wg_barrier();
      if (tid < 16) {
        const LAS f32x2* P = (const LAS f32x2*)(lds + L_FIN + tid * 2);
        const f32x2 o = bf_unpack(xw) + (((P[0] + P[16]) + (P[32] + P[48])) + ((P[64] + P[80]) + (P[96] + P[112])));
        *(gu32*)(a.out + (red & 1) * DM + (red >> 1) * 32 + tid * 2) = pack_bf16(o[0], o[1]);
      }
    }
  }
}

}

extern "C" void kernel_launch(void* const* d_in, const int* in_sizes, int n_in, void* d_out, int out_size, void* d_ws, size_t ws_size,
                              hipStream_t stream) {
  Args a;
  a.x = (const unsigned short*)d_in[0]; a.k = (const unsigned short*)d_in[1]; a.v = (const unsigned short*)d_in[2];
  a.wq = (const unsigned short*)d_in[3]; a.wo = (const unsigned short*)d_in[4];
  a.w1 = (const unsigned short*)d_in[6]; a.w3 = (const unsigned short*)d_in[7]; a.w2 = (const unsigned short*)d_in[8];
  a.out = (unsigned short*)d_out; a.ws = (unsigned char*)d_ws;
  fused_decode<<<NWG, NTHR, 0, stream>>>(a);
}
```

```cpp
#define STAMPS 1
#define DIAG 1
#define DIAG_I 3
#define DIAG_W0 8
#define DIAG_W1 12
#define DIAG_S 4
#define WPOL 0
#include <hip/hip_runtime.h>
#include <hip/hip_bf16.h>
#include <stdint.h>

#define KDIST 4
#define KSTAG 1
#define GNT 0
#define QLD 16
#define MIMIC 1
#define LATEFLUSH 0
#define DMODE -1

namespace {

constexpr int NWG = 256, NTHR = 768, NRED = 32;
constexpr int DM = 512, NH = 32, SK = 16384, DH = 128, FF = 1792;
constexpr float LOG2E = 1.4426950408889634f;
constexpr float SCALE = 0.08838834764831845f;
#define MLS 16
constexpr size_t WS_EPOCH = 0, WS_ML = 8192;
constexpr size_t WS_Y = 65536;
constexpr int YB = 32768 + 128;
constexpr size_t WS_Q = WS_Y + (1u << 20) + 8192;
constexpr size_t WS_G = WS_Q + (256u << 10);
constexpr size_t WS_STAMP = 8u << 20;
constexpr int L_S = 0;
constexpr int L_RED = L_S + 4096;
constexpr int L_Y = L_RED + 2048;
constexpr int L_Y2 = L_Y + 4096;
constexpr int L_Q = L_Y2 + 4096;
constexpr int L_ML = L_Q + 64;
constexpr int L_FW = L_ML + 8;
constexpr int L_FIN = L_FW + 4;
constexpr int L_CNT = L_FIN + 256;
constexpr int L_REF = L_CNT + 4;
constexpr int L_END = L_REF + 4;
#define TR(i) ((void)0)

#define GAS __attribute__((address_space(1)))
#define LAS __attribute__((address_space(3)))
#define RLX_AGENT __ATOMIC_RELAXED, __HIP_MEMORY_SCOPE_AGENT
typedef unsigned u32x4 __attribute__((ext_vector_type(4)));
typedef unsigned u32x2 __attribute__((ext_vector_type(2)));
typedef float f32x2 __attribute__((ext_vector_type(2)));
typedef float f32x4 __attribute__((ext_vector_type(4)));
typedef __bf16 bf16x2 __attribute__((ext_vector_type(2)));
typedef GAS unsigned gu32;
typedef GAS unsigned long long gu64;

__device__ __forceinline__ float bf_lo(unsigned u) { return __builtin_bit_cast(float, u << 16); }
__device__ __forceinline__ float bf_hi(unsigned u) { return __builtin_bit_cast(float, u & 0xffff0000u); }
__device__ __forceinline__ f32x2 bf_unpack(unsigned u) { return f32x2{bf_lo(u), bf_hi(u)}; }
__device__ __forceinline__ unsigned pack_bf16(float lo, float hi) {
  bf16x2 r = {(__bf16)lo, (__bf16)hi};
  return __builtin_bit_cast(unsigned, r);
}
__device__ __forceinline__ float rbf(float x) {
  return (float)(__bf16)x;
}
__device__ __forceinline__ float dot2(unsigned a, unsigned b, float acc) {
  return __builtin_amdgcn_fdot2_f32_bf16(__builtin_bit_cast(bf16x2, a), __builtin_bit_cast(bf16x2, b), acc, false);
}
template <int CTRL> __device__ __forceinline__ float dpp(float x) {
  return __builtin_bit_cast(float, __builtin_amdgcn_mov_dpp(__builtin_bit_cast(int, x), CTRL, 0xf, 0xf, true));
}
constexpr int XOR1 = 0xB1, XOR2 = 0x4E, XOR3 = 0x1B, XOR7 = 0x141, ROR2 = 0x122, ROR4 = 0x124, ROR8 = 0x128;
__device__ __forceinline__ float xrow_sum(float x) {
  auto s = __builtin_amdgcn_permlane16_swap(__float_as_uint(x), __float_as_uint(x), false, false);
  x = __uint_as_float(s[0]) + __uint_as_float(s[1]);
  auto t = __builtin_amdgcn_permlane32_swap(__float_as_uint(x), __float_as_uint(x), false, false);
  return __uint_as_float(t[0]) + __uint_as_float(t[1]);
}
__device__ __forceinline__ float xrow_max(float x) {
  auto s = __builtin_amdgcn_permlane16_swap(__float_as_uint(x), __float_as_uint(x), false, false);
  x = fmaxf(__uint_as_float(s[0]), __uint_as_float(s[1]));
  auto t = __builtin_amdgcn_permlane32_swap(__float_as_uint(x), __float_as_uint(x), false, false);
  return fmaxf(__uint_as_float(t[0]), __uint_as_float(t[1]));
}
__device__ __forceinline__ float tr16(float a, float b) {
  auto s = __builtin_amdgcn_permlane16_swap(__float_as_uint(a), __float_as_uint(b), false, false);
  return __uint_as_float(s[0]) + __uint_as_float(s[1]);
}
__device__ __forceinline__ float tr32(float a, float b) {
  auto s = __builtin_amdgcn_permlane32_swap(__float_as_uint(a), __float_as_uint(b), false, false);
  return __uint_as_float(s[0]) + __uint_as_float(s[1]);
}
__device__ __forceinline__ float wave_sum(float x) {
  x += dpp<XOR1>(x); x += dpp<XOR2>(x); x += dpp<XOR7>(x); x += dpp<ROR8>(x);
  return xrow_sum(x);
}
__device__ __forceinline__ float wave_max(float x) {
  x = fmaxf(x, dpp<XOR1>(x)); x = fmaxf(x, dpp<XOR2>(x)); x = fmaxf(x, dpp<XOR7>(x)); x = fmaxf(x, dpp<ROR8>(x));
  return xrow_max(x);
}
__device__ __forceinline__ void lds_wait() { asm volatile("s_waitcnt lgkmcnt(0)" ::: "memory"); }
__device__ __forceinline__ void st_granule(gu64* g, unsigned tag, float v) {
  __hip_atomic_store(g, ((unsigned long long)tag << 32) | (unsigned long long)__float_as_uint(v), RLX_AGENT);
}
constexpr unsigned SPIN_TICKS = 20000000u;
__device__ __forceinline__ unsigned now32() { return (unsigned)__builtin_amdgcn_s_memrealtime(); }

struct Args {
  const unsigned short* x; const unsigned short* k; const unsigned short* v; const unsigned short* wq; const unsigned short* wo;
  const unsigned short* w1; const unsigned short* w3; const unsigned short* w2;
  unsigned short* out; unsigned char* ws;
};

#define STAMP(i) do { __builtin_amdgcn_sched_barrier(0); unsigned long long t_; asm volatile("s_memrealtime %0\n\ts_waitcnt lgkmcnt(0)" : "=s"(t_) :: "memory"); ts[i] = (unsigned)t_ | 1u; __builtin_amdgcn_sched_barrier(0); } while (0)
#define STAMP_FLUSH() do { const unsigned tg_ = dg_epoch + 0x9E3779B9u; while (now32() - ts[0] < 12000u) __builtin_amdgcn_s_sleep(64); if (lane == 0) { _Pragma("unroll") for (int i_ = 0; i_ < 16; ++i_) st_granule((gu64*)(a.ws + WS_STAMP) + (blockIdx.x * 16 + wave) * 16 + i_, tg_, __uint_as_float(ts[i_])); } \
  if (blockIdx.x == 0 && wave == 0) { \
    int mn0 = 1 << 30, mx = -(1 << 30), mn = 1 << 30; float sum = 0.f; \
    for (int i = 0; i < 4; ++i) for (int w = 0; w < 12; ++w) { \
      gu64* p = (gu64*)(a.ws + WS_STAMP) + ((lane * 4 + i) * 16 + w) * 16; unsigned long long g0, g1; \
      do { g0 = __hip_atomic_load(p, RLX_AGENT); g1 = __hip_atomic_load(p + DIAG_I, RLX_AGENT); } while (((unsigned)(g0 >> 32) != tg_ || (unsigned)(g1 >> 32) != tg_) && (unsigned)__builtin_amdgcn_s_memrealtime() - ts[0] < 1000000u); \
      mn0 = min(mn0, (int)((unsigned)g0 - ts[0])); \
      if (w >= DIAG_W0 && w < DIAG_W1) { const int d = (int)((unsigned)g1 - ts[0]); mx = max(mx, d); mn = min(mn, d); sum += (float)d; } \
    } \
    mn0 = -(int)wave_max((float)-mn0); mx = (int)wave_max((float)mx); mn = -(int)wave_max((float)-mn); sum = wave_sum(sum) / (256.f * (DIAG_W1 - DIAG_W0)); \
    const int st = DIAG == 0 ? 0 : DIAG == 1 ? (int)sum - mn0 : DIAG == 2 ? mx - mn0 : mn - mn0; \
    const unsigned pw_ = __hip_atomic_load((gu32*)(a.ws + (32u << 20) + lane * 4), RLX_AGENT); const bool fixed_ = __all(pw_ == (pw_ & 255u) * 0x01010101u); \
    const int until = __builtin_amdgcn_readfirstlane((DMODE < 0 || (int)fixed_ == DMODE) ? mn0 + 30000 + DIAG_S * st : ((ts[0] >> 3) & 1) ? 20000 : 60000); \
    while ((int)((unsigned)__builtin_amdgcn_s_memrealtime() - ts[0]) < until) __builtin_amdgcn_s_sleep(1); \
  } } while (0)
#define STAMP_DECL unsigned ts[16] = {0, 0, 0, 0, 0, 0, 0, 0, 0, 0, 0, 0, 0, 0, 0, 0}

typedef __amdgpu_buffer_rsrc_t rsrc_t;
#define NPRE 3
__device__ __forceinline__ u32x4 ldw(rsrc_t r, int voff, int soff) { return __builtin_amdgcn_raw_buffer_load_b128(r, voff, soff, WPOL); }
__device__ __forceinline__ u32x4 ldnt(rsrc_t r, int voff, int soff) { return __builtin_amdgcn_raw_buffer_load_b128(r, voff, soff, 2); }
#define TRK 0
#define WOD 2
#define QDELAY 32
#define DRY 0
#define FSLEEP 4
#define B0POS (PDIST + 1)
#define SFIRST 1
#define CODEPF 0
#define KMIX 0
#define GLATE 1
#define FDELAY 0
#define WIDE_T 3300
#define LAG_A 100
#define LAG_B 110
#define LAG_W 5
#define MSLEEP 16
#define Y16 0
#define FPIPE 0
#define YLD 16
#define YST 16
#define VDIST 4
#define PRETOUCH 1
#define REDMAP 1
#define W2POL 2
#define RTOUCH 0
#define QTOUCH 0
#define PDIST KDIST
#define TRICKLE() __builtin_amdgcn_s_sleep(TRK)
#define HSYNC(n) do { \
    if (lane == 0) __hip_atomic_fetch_add((LAS unsigned*)lds + L_CNT + 1 + (n), 1u, __ATOMIC_RELAXED, __HIP_MEMORY_SCOPE_WORKGROUP); \
    while (__hip_atomic_load((LAS unsigned*)lds + L_CNT + 1 + (n), __ATOMIC_RELAXED, __HIP_MEMORY_SCOPE_WORKGROUP) < 8u) __builtin_amdgcn_s_sleep(0); \
    asm volatile("" ::: "memory"); \
  } while (0)
#define FRESH() int lane_ = lane0; asm volatile("" : "+v"(lane_)); \
  const int lane = lane_, r = lane >> 4, c = lane & 15, tid = wave * 64 + lane, voff = lane * 16; (void)r; (void)c; (void)tid; (void)voff
#define SB() __builtin_amdgcn_sched_barrier(0)
#define VM_DRAIN() do { SB(); asm volatile("s_waitcnt vmcnt(0)" ::: "memory"); SB(); } while (0)

__global__ void __launch_bounds__(NTHR, 3) fused_decode(Args a) {
  __shared__ __attribute__((aligned(16))) float lds[L_END];
  const int tid = threadIdx.x, lane = tid & 63, wave = __builtin_amdgcn_readfirstlane(tid >> 6);
  const int r = lane >> 4, c = lane & 15;
  const int xg = blockIdx.x & 7, j = blockIdx.x >> 3;
  const int b = j >> 4, h = xg * 2 + ((j >> 2) & 1) + (j & 8) * 2, sp = j & 3;
  const int chunk = b * 128 + h * 4 + sp;
  const int voff = lane * 16;
  STAMP_DECL;
  STAMP(0);
  unsigned dg_epoch = *(const __attribute__((address_space(4))) unsigned*)(a.ws + WS_EPOCH);

  const int g8 = b * 4 + sp;
  const int prow = (g8 >> 2) * 256 + wave * 32 + 2 * (lane >> 2);
  u32x4 xv0, xv1, wq0, wq1;
  unsigned xq0, xq1, junk = 0;
  if (wave < 8) {
    xv0 = *(const GAS u32x4*)(a.x + lane * 8); xv1 = *(const GAS u32x4*)(a.x + DM + lane * 8);
    xq0 = *(const gu32*)(a.x + prow); xq1 = *(const gu32*)(a.x + DM + prow);
    const rsrc_t rq = __builtin_amdgcn_make_buffer_rsrc((void*)a.wq, 0, DM * NH * DH * 2, 0x00020000);
    wq0 = ldnt(rq, prow * (NH * DH * 2) + (lane & 3) * 16, h * (DH * 2) + (g8 & 3) * 64);
    wq1 = ldnt(rq, prow * (NH * DH * 2) + (lane & 3) * 16, h * (DH * 2) + (g8 & 3) * 64 + NH * DH * 2);
  }
  int role = wave;
  asm volatile("" : "+s"(role));
  if (__builtin_expect(role >= 8, SFIRST)) {
    const int sw = wave - 8;
    constexpr int HB = SK * DH * 2;
    const unsigned short* kp = a.k + (size_t)(chunk >> 2) * SK * DH;
    const unsigned short* vp = a.v + (size_t)(chunk >> 2) * SK * DH;
    const rsrc_t rk = __builtin_amdgcn_make_buffer_rsrc((void*)kp, 0, HB, 0x00020000);
    const rsrc_t rv = __builtin_amdgcn_make_buffer_rsrc((void*)vp, 0, HB, 0x00020000);
    const int stag = j * KSTAG, voffh = voff + HB / 2;
#define ROWVO(n) ((((n) & 1) ? voffh : voff) + (((n) & 7) >> 1) * 1024)
#define ROWSO(pair, n) (((((pair) + ((n) >> 3) + stag) & 31) << 14) + (sw << 12) + (sp << 19))
#define LDROW(rs, pair, n) ldnt(rs, ROWVO(n), ROWSO(pair, n))
    __builtin_amdgcn_s_setprio(2);
    u32x4 buf[8], pre[NPRE * 8];
    STAMP(12);
    if (B0POS == 0) { __builtin_amdgcn_s_barrier(); SB(); }
    STAMP(13);
#pragma unroll
    for (int u = 0; u < NPRE * 8; ++u) {
      pre[u] = LDROW(rk, 0, u); SB();
      if (u + 1 == B0POS) { __builtin_amdgcn_s_barrier(); SB(); }
      if (u >= PDIST) { asm volatile("s_waitcnt vmcnt(%0)" :: "i"(PDIST)); SB(); }
      if (u == PDIST) STAMP(14);
      if ((u & 7) == 6 && u > 8) { TR((u >> 3) - 1); SB(); }
    }
#pragma unroll
    for (int u = 0; u < 8; ++u) {
      buf[u] = LDROW(rk, NPRE, u); SB();
      if (u < 7) { asm volatile("s_waitcnt vmcnt(%0)" :: "i"(PDIST)); SB(); }
    }
    TR(2);
    STAMP(1);
    __builtin_amdgcn_s_barrier();
    asm volatile("" ::: "memory");
    asm volatile("" : "+s"(dg_epoch));
    const u32x4 qp = *(const LAS u32x4*)(lds + L_Q + c * 4);
    STAMP(2);
    LAS float* S = (LAS float*)lds + L_S + sw * 1024;
    float mx = -INFINITY;
#define DOT(w) dot2((w)[3], qp[3], dot2((w)[2], qp[2], dot2((w)[1], qp[1], dot2((w)[0], qp[0], 0.f))))
#define K_FIN(tt) do { \
      _Pragma("unroll") for (int u = 0; u < 4; ++u) { const float lo = x[u] + dpp<ROR8>(x[u]), hi = x[u + 4] + dpp<ROR8>(x[u + 4]); x[u] = (c & 8) ? hi : lo; } \
      _Pragma("unroll") for (int u = 0; u < 2; ++u) { const float lo = x[u] + dpp<XOR7>(x[u]), hi = x[u + 2] + dpp<XOR7>(x[u + 2]); x[u] = (c & 4) ? hi : lo; } \
      { const float lo = x[0] + dpp<XOR2>(x[0]), hi = x[1] + dpp<XOR2>(x[1]); x[0] = (c & 2) ? hi : lo; } \
      float sc = x[0] + dpp<XOR1>(x[0]); \
      sc = rbf(rbf(sc) * SCALE); \
      mx = fmaxf(mx, sc); \
      S[(tt) * 32 + r * 8 + (c >> 1)] = sc; \
      SB(); \
    } while (0)
    static_assert(KDIST <= 6 && KDIST >= 3 && NPRE >= 1 && PDIST <= NPRE * 8, "ring of 8");
#define PACE() do { asm volatile("s_waitcnt vmcnt(%0)" :: "i"(((KMIX >> u) & 1) ? KDIST - 1 : KDIST)); SB(); } while (0)
    {
      float x[8];
#pragma unroll
      for (int u = 0; u < 8; ++u) {
        if (u > 0 && u <= KDIST + 1) { buf[u - 1] = LDROW(rk, NPRE + 1, u - 1); SB(); }
        x[u] = DOT(buf[u]);
        SB();
      }
      K_FIN(NPRE);
    }
#pragma unroll
    for (int pp = 0; pp < NPRE; ++pp) {
      float x[8];
#pragma unroll
      for (int w = 0; w < 8; ++w) x[w] = DOT(pre[pp * 8 + w]);
      K_FIN(pp);
    }
#pragma unroll
    for (int u = KDIST + 1; u < 7; ++u) { PACE(); buf[u] = LDROW(rk, NPRE + 1, u); SB(); }
    TR(3);
    STAMP(11);
    int last = 31;
    asm volatile("" : "+s"(last));
#pragma nounroll
    for (int tt = NPRE + 1; tt <= last; ++tt) {
      const unsigned short* nb = tt == last ? vp : kp;
      const rsrc_t rn = __builtin_amdgcn_make_buffer_rsrc((void*)nb, 0, HB, 0x00020000);
      float x[8];
#pragma unroll
      for (int u = 0; u < 8; ++u) {
        const int n = u + 7;
        PACE();
        buf[n & 7] = LDROW(n >= 8 ? rn : rk, tt, n);
        SB();
        x[u] = DOT(buf[u]);
        SB();
      }
      K_FIN(tt);
      TR(tt);
    }
    STAMP(3);
    mx = wave_max(mx);
    if (lane == 0) lds[L_ML + sw] = mx;
    lds_wait();
    if (lane == 0) __hip_atomic_fetch_add((LAS unsigned*)lds + L_CNT, 1u, __ATOMIC_RELAXED, __HIP_MEMORY_SCOPE_WORKGROUP);
    STAMP(4);
    f32x2 acc[4] = {{0.f, 0.f}, {0.f, 0.f}, {0.f, 0.f}, {0.f, 0.f}};
    f32x4 s0 = *(const LAS f32x4*)(S + r * 8), s1;
    int el = -1, lag = -1;
#pragma nounroll
    for (int tt = 0; tt <= last; ++tt) {
      const rsrc_t rn = __builtin_amdgcn_make_buffer_rsrc((void*)vp, 0, tt == last ? 0 : HB, 0x00020000);
      unsigned ref, tnow;
#pragma unroll
      for (int u = 0; u < 8; ++u) {
        const int n = u + 7;
        const int so = ROWSO(tt, n);
        asm volatile("s_cmpk_gt_i32 %0, %4\n\ts_cbranch_scc1 1f\n\ts_waitcnt vmcnt(%2)\n\ts_cmp_gt_i32 %1, 0\n\ts_cbranch_scc1 1f\n\ts_waitcnt vmcnt(%3)\n1:"
                     :: "s"(el), "s"(lag), "i"(LAG_W), "i"(VDIST), "i"(WIDE_T), "s"(so) : "scc");
        SB();
        buf[n & 7] = ldnt(n >= 8 ? rn : rv, ROWVO(n), so);
        SB();
        if (u == 0) s1 = *(const LAS f32x4*)(S + tt * 32 + r * 8 + 4);
        if (u == 4) { s0 = *(const LAS f32x4*)(S + ((tt + 1) & 31) * 32 + r * 8); ref = *(volatile LAS unsigned*)(lds + L_REF); tnow = now32(); }
        const float p = __builtin_amdgcn_exp2f(((u < 4 ? s0[u & 3] : s1[u & 3]) - mx) * LOG2E);
#pragma unroll
        for (int k = 0; k < 4; ++k) acc[k] = __builtin_elementwise_fma(bf_unpack(buf[u][k]), f32x2{p, p}, acc[k]);
        asm volatile("" : "+v"(acc[0]), "+v"(acc[1]), "+v"(acc[2]), "+v"(acc[3]));
        if (u == 6) {
          asm volatile("" : "+v"(ref), "+s"(tnow));
          el = (int)(tnow - __builtin_amdgcn_readfirstlane(ref));
          lag = el - (LAG_A + LAG_B / 2 + tt * LAG_B);
        }
        SB();
      }
      TR(32 + tt);
    }
    STAMP(5);
    *(LAS f32x4*)(lds + L_RED + (sw * 4 + r) * 128 + c * 8) = f32x4{acc[0][0], acc[0][1], acc[1][0], acc[1][1]};
    *(LAS f32x4*)(lds + L_RED + (sw * 4 + r) * 128 + c * 8 + 4) = f32x4{acc[2][0], acc[2][1], acc[3][0], acc[3][1]};
    lds_wait();
    __builtin_amdgcn_s_barrier();
    __builtin_amdgcn_s_barrier();
    if (blockIdx.x < NRED) __builtin_amdgcn_s_barrier();
    STAMP_FLUSH();
    return;
  }

  unsigned epoch = *(const __attribute__((address_space(4))) unsigned*)(a.ws + WS_EPOCH);
  SB();
  if (tid < 5) ((LAS unsigned*)lds)[L_CNT + tid] = tid < 4 ? 0u : (unsigned)__builtin_amdgcn_s_memrealtime() + 0x40000000u;
  lds_wait();
  __builtin_amdgcn_s_barrier();
  SB();
  float mb0, mb1, sd0, sd1;
  {
    float f0[8], f1[8];
#pragma unroll
    for (int k = 0; k < 4; ++k) { f0[2 * k] = bf_lo(xv0[k]); f0[2 * k + 1] = bf_hi(xv0[k]); f1[2 * k] = bf_lo(xv1[k]); f1[2 * k + 1] = bf_hi(xv1[k]); }
    float s0 = 0.f, s1 = 0.f;
#pragma unroll
    for (int k = 0; k < 8; ++k) { s0 += f0[k]; s1 += f1[k]; }
    const float m0 = wave_sum(s0) * (1.f / DM), m1 = wave_sum(s1) * (1.f / DM);
    float v0 = 0.f, v1 = 0.f;
#pragma unroll
    for (int k = 0; k < 8; ++k) { const float d0 = f0[k] - m0, d1 = f1[k] - m1; v0 = fmaf(d0, d0, v0); v1 = fmaf(d1, d1, v1); }
    v0 = wave_sum(v0) * (1.f / DM); v1 = wave_sum(v1) * (1.f / DM);
    mb0 = rbf(m0); sd0 = rbf(__builtin_sqrtf(rbf(rbf(v0) + 1e-5f)));
    mb1 = rbf(m1); sd1 = rbf(__builtin_sqrtf(rbf(rbf(v1) + 1e-5f)));
  }
#define XN(xf, mb, sd) rbf(rbf((xf) - (mb)) / (sd))
  asm volatile("" : "+s"(epoch));
  asm volatile("" : "+s"(dg_epoch));
  const unsigned tag = epoch + 0x9E3779B9u;
  STAMP(10);
  const float xa0 = XN(bf_lo(xq0), mb0, sd0), xa1 = XN(bf_hi(xq0), mb0, sd0), xb0 = XN(bf_lo(xq1), mb1, sd1), xb1 = XN(bf_hi(xq1), mb1, sd1);
#define PARTIAL(w0_, w1_, base, n, res) do { \
    float t[8]; \
    _Pragma("unroll") for (int k = 0; k < 4; ++k) { \
      const f32x2 w0 = bf_unpack((w0_)[k]), w1 = bf_unpack((w1_)[k]); \
      const f32x2 qa = __builtin_elementwise_fma(w1, f32x2{xa1, xa1}, w0 * xa0), qb = __builtin_elementwise_fma(w1, f32x2{xb1, xb1}, w0 * xb0); \
      t[2 * k] = tr32(qa[0], qb[0]); t[2 * k + 1] = tr32(qa[1], qb[1]);             \
    } \
    _Pragma("unroll") for (int k = 0; k < 4; ++k) t[k] = tr16(t[k], t[4 + k]);        \
    f32x2 o; \
    _Pragma("unroll") for (int k = 0; k < 2; ++k) { const float lo = t[k] + dpp<ROR8>(t[k]), hi = t[2 + k] + dpp<ROR8>(t[2 + k]); o[k] = (c & 8) ? hi : lo; } \
    const float lo = o[0] + dpp<XOR7>(dpp<XOR3>(o[0])), hi = o[1] + dpp<XOR7>(dpp<XOR3>(o[1]));     \
    lds[(base) + wave * 64 + (r >> 1) * 32 + (c & 3) * 8 + (r & 1) * 4 + (c >> 3) * 2 + ((c >> 2) & 1)] = (c & 4) ? hi : lo; \
    lds_wait(); \
    HSYNC(n); \
    const LAS float* P = (const LAS float*)lds + (base) + lane; \
    res = ((P[0] + P[64]) + (P[128] + P[192])) + ((P[256] + P[320]) + (P[384] + P[448])); \
  } while (0)
  {
    float qs;
    PARTIAL(wq0, wq1, L_Y, 0, qs);
    if (wave == 0) st_granule((gu64*)(a.ws + WS_Q) + (h * 8 + g8) * 64 + lane, tag, qs);
  }
  STAMP(9);
  asm volatile("" :: "v"(junk));
  if (wave == 7) {
    __builtin_amdgcn_s_sleep(QDELAY);
    const rsrc_t rg = __builtin_amdgcn_make_buffer_rsrc((void*)(a.ws + WS_Q + (size_t)h * 4096), 0, 4096, 0x00020000);
    const unsigned t0 = now32();
    u32x4 g[2];
    while (true) {
      bool ok = true;
#pragma unroll
      for (int i = 0; i < 2; ++i)
        g[i] = __builtin_amdgcn_raw_buffer_load_b128(rg, r * 512 + c * 16, i * 2048 + b * 256, QLD);
#pragma unroll
      for (int i = 0; i < 2; ++i) ok = ok & (g[i][1] == tag) & (g[i][3] == tag);
      if (__all(ok)) break;
      if (now32() - t0 > SPIN_TICKS) break;
      __builtin_amdgcn_s_sleep(8);
      asm volatile("" ::: "memory");
    }
    const f32x2 qs = f32x2{__uint_as_float(g[0][0]), __uint_as_float(g[0][2])} + f32x2{__uint_as_float(g[1][0]), __uint_as_float(g[1][2])};
    ((LAS unsigned*)lds)[L_Q + lane] = pack_bf16(qs[0], qs[1]);
    lds_wait();
  }
  STAMP(1);
  __builtin_amdgcn_s_barrier();
  asm volatile("" ::: "memory");
  gu64* G = (gu64*)(a.ws + WS_G);
  float gsum = 0.f;
  const int red = blockIdx.x < NRED ? (xg * 2 + (j & 1)) * 2 + (j >> 1) : -1;
  const unsigned xw = (red >= 0 && tid < 16) ? *(const gu32*)(a.x + (red & 1) * DM + (red >> 1) * 32 + tid * 2) : 0u;
  if (h < 28) {
    const rsrc_t rm = __builtin_amdgcn_make_buffer_rsrc((void*)((g8 & 2) ? a.w3 : a.w1), 0, DM * FF * 2, 0x00020000);
    const int so = h * 128 + (g8 & 1) * 64;
    const u32x4 wm0 = ldnt(rm, prow * (FF * 2) + (lane & 3) * 16, so), wm1 = ldnt(rm, prow * (FF * 2) + (lane & 3) * 16, so + FF * 2);
    PARTIAL(wm0, wm1, L_Y + 1024, 1, gsum);
  }
  const int lane0 = lane;
  int pass = DRY ? 0 : 1;
  asm volatile("" : "+s"(pass));
#pragma nounroll
  for (; pass < 2; ++pass) {
    f32x2 wf[64];
    f32x2 yacc[4] = {{0.f, 0.f}, {0.f, 0.f}, {0.f, 0.f}, {0.f, 0.f}};
    if (!pass) {
#pragma unroll
      for (int i = 0; i < 64; ++i) asm volatile("" : "=v"(wf[i]));
    } else {
      {
        FRESH();
        const rsrc_t ro = __builtin_amdgcn_make_buffer_rsrc((void*)(a.wo + (size_t)h * DH * DM), 0, DH * DM * 2, 0x00020000);
#pragma unroll
        for (int i0 = 0; i0 < 16; i0 += WOD) {
          u32x4 w[WOD];
#pragma unroll
          for (int i = 0; i < WOD; ++i) w[i] = ldw(ro, voff + ((i0 + i) & 3) * 1024, (wave * 16 + ((i0 + i) & ~3)) * 1024);
          VM_DRAIN();
#pragma unroll
          for (int i = 0; i < WOD; ++i)
#pragma unroll
            for (int k = 0; k < 4; ++k) { wf[(i0 + i) * 4 + k] = bf_unpack(w[i][k]); asm volatile("" : "+v"(wf[(i0 + i) * 4 + k])); }
          TRICKLE();
        }
      }
      STAMP(2);
      while (__hip_atomic_load((LAS unsigned*)lds + L_CNT, __ATOMIC_RELAXED, __HIP_MEMORY_SCOPE_WORKGROUP) < 4u) __builtin_amdgcn_s_sleep(8);
      asm volatile("" ::: "memory");
      gu64* ml = (gu64*)(a.ws + WS_ML);
      float mw = 0.f;
      const unsigned tk = (unsigned)__builtin_amdgcn_s_memrealtime();
      if (wave == 7) {
        FRESH();
        mw = lds[L_ML + (lane & 3)];
        float m = fmaxf(mw, dpp<XOR1>(mw)); m = fmaxf(m, dpp<XOR2>(m));
        float l = 0.f;
#pragma unroll 4
        for (int i = 0; i < 16; ++i) {
          const f32x4 sv = *(const LAS f32x4*)(lds + L_S + i * 256 + lane * 4);
#pragma unroll
          for (int k = 0; k < 4; ++k) l += __builtin_amdgcn_exp2f((sv[k] - m) * LOG2E);
        }
        l = wave_sum(l);
        if (lane < 16) st_granule(ml + chunk * MLS + lane, tag, lane == 0 ? m : lane == 1 ? l : __uint_as_float(tk));
      }
      else if (wave == 0 && h < 28) { FRESH(); __builtin_amdgcn_s_sleep(16); st_granule(G + (h * 8 + g8) * 64 + lane, tag, gsum); }
      __builtin_amdgcn_s_sleep(127); __builtin_amdgcn_s_sleep(MSLEEP);
      if (wave < 7) {
        FRESH();
        const int f0 = (h * 4 + sp) * 14 + wave * 2;
        const rsrc_t r2 = __builtin_amdgcn_make_buffer_rsrc((void*)(a.w2 + (size_t)f0 * DM), 0, 2 * DM * 2, 0x00020000);
        const u32x4 w20 = __builtin_amdgcn_raw_buffer_load_b128(r2, voff, 0, W2POL), w21 = __builtin_amdgcn_raw_buffer_load_b128(r2, voff + 1024, 0, W2POL);
        const int f = f0 + (r & 1);
        gu64* gp = G + ((f >> 6) * 8 + (c & 1) * 4 + (c >> 3) * 2 + ((f >> 5) & 1)) * 64 + b * 32 + (f & 31);
        const unsigned t0 = now32();
        unsigned long long g;
        while (true) {
          g = __hip_atomic_load(gp, RLX_AGENT);
          if (__all((unsigned)(g >> 32) == tag)) break;
          if (now32() - t0 > SPIN_TICKS) break;
          __builtin_amdgcn_s_sleep(127);
        }
        float s = __uint_as_float((unsigned)g);
        s += dpp<XOR1>(s);
        const float gs = rbf(s), us = rbf(dpp<ROR8>(s));
        const float si = rbf(gs / (1.f + __expf(-gs)));
        const float hv = rbf(si * us);
        const float h0 = __builtin_bit_cast(float, __builtin_amdgcn_readlane(__builtin_bit_cast(int, hv), 0));
        const float h1 = __builtin_bit_cast(float, __builtin_amdgcn_readlane(__builtin_bit_cast(int, hv), 16));
#pragma unroll
        for (int k = 0; k < 4; ++k) yacc[k] = __builtin_elementwise_fma(bf_unpack(w21[k]), f32x2{h1, h1}, bf_unpack(w20[k]) * h0);
      } else {
        FRESH();
        {
          const unsigned long long gt = __hip_atomic_load(ml + (lane * 4 + (lane & 3)) * MLS + 2, RLX_AGENT);
          const bool v = (unsigned)(gt >> 32) == tag;
          const float n = wave_sum(v ? 1.f : 0.f), d = wave_sum(v ? (float)(int)((unsigned)gt - tk) : 0.f);
          if (lane == 0 && n > 0.f) ((LAS unsigned*)lds)[L_REF] = tk + (unsigned)(int)(d / n);
        }
        const unsigned t0 = now32();
        unsigned long long gm, gl;
        while (true) {
          gm = __hip_atomic_load(ml + ((chunk & ~3) + (lane & 3)) * MLS, RLX_AGENT);
          gl = __hip_atomic_load(ml + ((chunk & ~3) + (lane & 3)) * MLS + 1, RLX_AGENT);
          if (__all((unsigned)(gm >> 32) == tag && (unsigned)(gl >> 32) == tag)) break;
          if (now32() - t0 > SPIN_TICKS) break;
          __builtin_amdgcn_s_sleep(127);
        }
        const float ms = __uint_as_float((unsigned)gm), ls = __uint_as_float((unsigned)gl);
        float M = fmaxf(ms, dpp<XOR1>(ms)); M = fmaxf(M, dpp<XOR2>(M));
        float L = ls * __builtin_amdgcn_exp2f((ms - M) * LOG2E);
        L += dpp<XOR1>(L); L += dpp<XOR2>(L);
        if (lane < 4) lds[L_FW + lane] = __builtin_amdgcn_exp2f((mw - M) * LOG2E) / L;
        lds_wait();
        {
          const unsigned char* pa = (lane < 32 || red < 0) ? a.ws + WS_Y + (lane & 15) * YB + chunk * 128
                                  : (RTOUCH && lane < 48) ? a.ws + WS_Y + (red >> 1) * YB + (red & 1) * 16384 + min((lane & 7) * 4096, 16256)
                                  : (const unsigned char*)a.out + (red & 1) * (DM * 2) + (red >> 1) * 64;
          const unsigned jk = *(const volatile gu32*)pa;
          asm volatile("" :: "v"(jk));
        }
        if (blockIdx.x == NWG - 1) {
          while (true) {
            bool ok = true;
#pragma unroll
            for (int i = 0; i < 4; ++i) ok = ok & ((unsigned)(__hip_atomic_load(ml + (i * 64 + lane) * MLS + 2, RLX_AGENT) >> 32) == tag);
            if (__all(ok)) break;
            if (now32() - t0 > SPIN_TICKS) break;
            __builtin_amdgcn_s_sleep(127);
          }
          if (lane == 0) __hip_atomic_store((gu32*)(a.ws + WS_EPOCH), tag, RLX_AGENT);
        }
        STAMP(3);
      }
    }
    FRESH();
    if (pass) __builtin_amdgcn_s_barrier();
    asm volatile("" ::: "memory");
    STAMP(6);
    {
      const LAS float* P = (const LAS float*)lds + L_RED + (4 * r) * 128 + wave * 16 + c;
      float o = ((P[0] + P[128]) + (P[256] + P[384])) * lds[L_FW + r];
      o = xrow_sum(o);
#pragma unroll
      for (int i = 0; i < 16; ++i) {
        const float oi = __builtin_bit_cast(float, __builtin_amdgcn_readlane(__builtin_bit_cast(int, o), i));
#pragma unroll
        for (int k = 0; k < 4; ++k) yacc[k] = __builtin_elementwise_fma(wf[i * 4 + k], f32x2{oi, oi}, yacc[k]);
      }
    }
    *(LAS f32x4*)(lds + L_Y2 + wave * 512 + lane * 8) = f32x4{yacc[0][0], yacc[0][1], yacc[1][0], yacc[1][1]};
    *(LAS f32x4*)(lds + L_Y2 + wave * 512 + lane * 8 + 4) = f32x4{yacc[2][0], yacc[2][1], yacc[3][0], yacc[3][1]};
    lds_wait();
    if (pass) __builtin_amdgcn_s_barrier();
    asm volatile("" ::: "memory");
    const rsrc_t ry = __builtin_amdgcn_make_buffer_rsrc((void*)(a.ws + WS_Y), 0, pass ? 16 * YB : 0, 0x00020000);
    {
      const LAS float* P = (const LAS float*)lds + L_Y2 + tid;
      const float y = ((P[0] + P[512]) + (P[1024] + P[1536])) + ((P[2048] + P[2560]) + (P[3072] + P[3584]));
      const unsigned u = __float_as_uint(y);
      const unsigned w = (((u & 0x7f800000u) == 0x7f800000u ? u | ((u & 0x7fffffu) ? 0x400000u : 0u) : u + 0x400u) & 0xfffff800u) | ((tag >> ((lane & 1) * 11)) & 0x7ffu);
      const unsigned w1 = __builtin_amdgcn_mov_dpp(w, XOR1, 0xf, 0xf, true);
      if (!(lane & 1)) __builtin_amdgcn_raw_buffer_store_b64(u32x2{w, w1}, ry, (tid >> 5) * YB + (tid & 31) * 4, chunk * 128, YST);
    }
    STAMP(7);
    if (red >= 0) {
      const unsigned t0 = now32();
      u32x4 g0 = {0u, 0u, 0u, 0u}, g1 = {0u, 0u, 0u, 0u};
      bool ok0 = false, ok1 = false;
      const unsigned ta = tag & 0x7ffu, tb = (tag >> 11) & 0x7ffu;
      while (true) {
        if (!ok0) g0 = __builtin_amdgcn_raw_buffer_load_b128(ry, voff, (red >> 1) * YB + (red & 1) * 16384 + wave * 2048, YLD);
        if (!ok1) g1 = __builtin_amdgcn_raw_buffer_load_b128(ry, voff + 1024, (red >> 1) * YB + (red & 1) * 16384 + wave * 2048, YLD);
        ok0 = ((((g0[0] ^ ta) | (g0[1] ^ tb) | (g0[2] ^ ta) | (g0[3] ^ tb)) & 0x7ffu) == 0u);
        ok1 = ((((g1[0] ^ ta) | (g1[1] ^ tb) | (g1[2] ^ ta) | (g1[3] ^ tb)) & 0x7ffu) == 0u);
        if (__all(ok0 & ok1) || !pass) break;
        if (now32() - t0 > SPIN_TICKS) break;
        __builtin_amdgcn_s_sleep(FSLEEP);
        asm volatile("" ::: "memory");
      }
      float t[4];
#pragma unroll
      for (int k = 0; k < 4; ++k) t[k] = __uint_as_float(g0[k] & 0xfffff800u) + __uint_as_float(g1[k] & 0xfffff800u);
      float s = tr16(tr32(t[0], t[2]), tr32(t[1], t[3]));
      s += dpp<ROR8>(s);
      if (!(lane & 8)) lds[L_FIN + wave * 32 + (lane & 7) * 4 + r] = s;
      lds_wait();
      if (pass) __builtin_amdgcn_s_barrier();
      asm volatile("" ::: "memory");
      if (tid < 16) {
        const LAS f32x2* P = (const LAS f32x2*)(lds + L_FIN + tid * 2);
        const f32x2 o = bf_unpack(xw) + (((P[0] + P[16]) + (P[32] + P[48])) + ((P[64] + P[80]) + (P[96] + P[112])));
        const rsrc_t rout = __builtin_amdgcn_make_buffer_rsrc((void*)a.out, 0, pass ? 2 * DM * 2 : 0, 0x00020000);
        __builtin_amdgcn_raw_buffer_store_b32(pack_bf16(o[0], o[1]), rout, tid * 4, (red & 1) * (DM * 2) + (red >> 1) * 64, 0);
      }
    }
  }
  STAMP(8);
  STAMP_FLUSH();
}

}

extern "C" void kernel_launch(void* const* d_in, const int* in_sizes, int n_in, void* d_out, int out_size, void* d_ws, size_t ws_size,
                              hipStream_t stream) {
  Args a;
  a.x = (const unsigned short*)d_in[0]; a.k = (const unsigned short*)d_in[1]; a.v = (const unsigned short*)d_in[2];
  a.wq = (const unsigned short*)d_in[3]; a.wo = (const unsigned short*)d_in[4];
  a.w1 = (const unsigned short*)d_in[6]; a.w3 = (const unsigned short*)d_in[7]; a.w2 = (const unsigned short*)d_in[8];
  a.out = (unsigned short*)d_out; a.ws = (unsigned char*)d_ws;
  fused_decode<<<NWG, NTHR, 0, stream>>>(a);
}
```

```cpp
#include <hip/hip_runtime.h>
#include <hip/hip_bf16.h>
#include <stdint.h>

namespace {

constexpr int NWG = 256, NTHR = 768, NRED = 32;
constexpr int DM = 512, NH = 32, SK = 16384, DH = 128, FF = 1792;
constexpr float LOG2E = 1.4426950408889634f;
constexpr float SCALE = 0.08838834764831845f;
constexpr int WIN = 4;
constexpr int NPRE = 3;
constexpr int LAG_A = 155, LAG_B = 110, WIDE_T = 3300;
constexpr size_t WS_EPOCH = 0;
constexpr size_t WS_ML = 8192;
constexpr size_t WS_Y = 65536;
constexpr int YB = 32768;
constexpr size_t WS_Q = WS_Y + 16 * YB;
constexpr size_t WS_G = WS_Q + (128u << 10);
constexpr int L_S = 0;
constexpr int L_O = L_S + 4096;
constexpr int L_Y = L_O + 2048;
constexpr int L_P = L_Y + 4096;
constexpr int L_Q = L_P + 1024;
constexpr int L_MX = L_Q + 64;
constexpr int L_FW = L_MX + 4;
constexpr int L_FIN = L_FW + 4;
constexpr int L_CNT = L_FIN + 256;
constexpr int L_REF = L_CNT + 3;
constexpr int L_END = L_REF + 1;

#define GAS __attribute__((address_space(1)))
#define LAS __attribute__((address_space(3)))
#define RLX_AGENT __ATOMIC_RELAXED, __HIP_MEMORY_SCOPE_AGENT
#define RLX_WG __ATOMIC_RELAXED, __HIP_MEMORY_SCOPE_WORKGROUP
typedef unsigned u32x2 __attribute__((ext_vector_type(2)));
typedef unsigned u32x4 __attribute__((ext_vector_type(4)));
typedef float f32x2 __attribute__((ext_vector_type(2)));
typedef float f32x4 __attribute__((ext_vector_type(4)));
typedef __bf16 bf16x2 __attribute__((ext_vector_type(2)));
typedef GAS unsigned gu32;
typedef GAS unsigned long long gu64;
typedef __amdgpu_buffer_rsrc_t rsrc_t;

__device__ __forceinline__ float bf_lo(unsigned u) { return __builtin_bit_cast(float, u << 16); }
__device__ __forceinline__ float bf_hi(unsigned u) { return __builtin_bit_cast(float, u & 0xffff0000u); }
__device__ __forceinline__ f32x2 bf_unpack(unsigned u) { return f32x2{bf_lo(u), bf_hi(u)}; }
__device__ __forceinline__ unsigned pack_bf16(float lo, float hi) {
  bf16x2 r = {(__bf16)lo, (__bf16)hi};
  return __builtin_bit_cast(unsigned, r);
}
__device__ __forceinline__ float rbf(float x) { return (float)(__bf16)x; }
__device__ __forceinline__ float dot2(unsigned a, unsigned b, float acc) {
  return __builtin_amdgcn_fdot2_f32_bf16(__builtin_bit_cast(bf16x2, a), __builtin_bit_cast(bf16x2, b), acc, false);
}
template <int CTRL> __device__ __forceinline__ float dpp(float x) {
  return __builtin_bit_cast(float, __builtin_amdgcn_mov_dpp(__builtin_bit_cast(int, x), CTRL, 0xf, 0xf, true));
}
constexpr int XOR1 = 0xB1, XOR2 = 0x4E, XOR3 = 0x1B, XOR7 = 0x141, ROR8 = 0x128;
__device__ __forceinline__ float tr16(float a, float b) {
  auto s = __builtin_amdgcn_permlane16_swap(__float_as_uint(a), __float_as_uint(b), false, false);
  return __uint_as_float(s[0]) + __uint_as_float(s[1]);
}
__device__ __forceinline__ float tr32(float a, float b) {
  auto s = __builtin_amdgcn_permlane32_swap(__float_as_uint(a), __float_as_uint(b), false, false);
  return __uint_as_float(s[0]) + __uint_as_float(s[1]);
}
__device__ __forceinline__ float xrow_sum(float x) { x = tr16(x, x); return tr32(x, x); }
__device__ __forceinline__ float wave_sum(float x) {
  x += dpp<XOR1>(x); x += dpp<XOR2>(x); x += dpp<XOR7>(x); x += dpp<ROR8>(x);
  return xrow_sum(x);
}
__device__ __forceinline__ float wave_max(float x) {
  x = fmaxf(x, dpp<XOR1>(x)); x = fmaxf(x, dpp<XOR2>(x)); x = fmaxf(x, dpp<XOR7>(x)); x = fmaxf(x, dpp<ROR8>(x));
  auto s = __builtin_amdgcn_permlane16_swap(__float_as_uint(x), __float_as_uint(x), false, false);
  x = fmaxf(__uint_as_float(s[0]), __uint_as_float(s[1]));
  auto t = __builtin_amdgcn_permlane32_swap(__float_as_uint(x), __float_as_uint(x), false, false);
  return fmaxf(__uint_as_float(t[0]), __uint_as_float(t[1]));
}
__device__ __forceinline__ float lane_value(float x, int l) { return __builtin_bit_cast(float, __builtin_amdgcn_readlane(__builtin_bit_cast(int, x), l)); }
__device__ __forceinline__ void lds_wait() { asm volatile("s_waitcnt lgkmcnt(0)" ::: "memory"); }
__device__ __forceinline__ void wg_barrier() { __builtin_amdgcn_s_barrier(); asm volatile("" ::: "memory"); }
__device__ __forceinline__ void st_granule(gu64* g, unsigned tag, float v) {
  __hip_atomic_store(g, ((unsigned long long)tag << 32) | (unsigned long long)__float_as_uint(v), RLX_AGENT);
}
__device__ __forceinline__ rsrc_t mk_rsrc(const void* p, int bytes) { return __builtin_amdgcn_make_buffer_rsrc((void*)p, 0, bytes, 0x00020000); }
__device__ __forceinline__ u32x4 ldnt(rsrc_t r, int voff, int soff) { return __builtin_amdgcn_raw_buffer_load_b128(r, voff, soff, 2); }
__device__ __forceinline__ u32x4 lddev(rsrc_t r, int voff, int soff) { return __builtin_amdgcn_raw_buffer_load_b128(r, voff, soff, 16); }
__device__ __forceinline__ unsigned now32() { return (unsigned)__builtin_amdgcn_s_memrealtime(); }
constexpr unsigned SPIN_TICKS = 20000000u;

#define SB() __builtin_amdgcn_sched_barrier(0)
#define VM_WAIT(n) do { asm volatile("s_waitcnt vmcnt(%0)" :: "i"(n)); SB(); } while (0)
#define FRESH() int lane_ = lane0; asm volatile("" : "+v"(lane_)); \
  const int lane = lane_, r = lane >> 4, c = lane & 15, tid = wave * 64 + lane, voff = lane * 16; (void)r; (void)c; (void)tid; (void)voff

struct Args {
  const unsigned short* x; const unsigned short* k; const unsigned short* v; const unsigned short* wq; const unsigned short* wo;
  const unsigned short* w1; const unsigned short* w3; const unsigned short* w2;
  unsigned short* out; unsigned char* ws;
};

__global__ void __launch_bounds__(NTHR, 3) fused_decode(Args a) {
  __shared__ __attribute__((aligned(16))) float lds[L_END];
  const int tid = threadIdx.x, lane = tid & 63, wave = __builtin_amdgcn_readfirstlane(tid >> 6);
  const int r = lane >> 4, c = lane & 15;
  const int xg = blockIdx.x & 7, j = blockIdx.x >> 3;
  const int b = j >> 4, h = xg * 2 + ((j >> 2) & 1) + (j & 8) * 2, sp = j & 3;
  const int chunk = b * 128 + h * 4 + sp;
  const int voff = lane * 16;

  const int g8 = b * 4 + sp;
  const int prow = (g8 >> 2) * 256 + wave * 32 + 2 * (lane >> 2);
  u32x4 xv0, xv1, wq0, wq1;
  unsigned xq0, xq1;
  if (wave < 8) {
    xv0 = *(const GAS u32x4*)(a.x + lane * 8); xv1 = *(const GAS u32x4*)(a.x + DM + lane * 8);
    xq0 = *(const gu32*)(a.x + prow); xq1 = *(const gu32*)(a.x + DM + prow);
    const rsrc_t rq = mk_rsrc(a.wq, DM * NH * DH * 2);
    wq0 = ldnt(rq, prow * (NH * DH * 2) + (lane & 3) * 16, h * (DH * 2) + (g8 & 3) * 64);
    wq1 = ldnt(rq, prow * (NH * DH * 2) + (lane & 3) * 16, h * (DH * 2) + (g8 & 3) * 64 + NH * DH * 2);
    SB();
    wg_barrier();
  }
  int role = wave;
  asm volatile("" : "+s"(role));
  if (__builtin_expect(role >= 8, 1)) {
    const int sw = wave - 8;
    constexpr int HB = SK * DH * 2;
    const unsigned short* kp = a.k + (size_t)(chunk >> 2) * SK * DH;
    const unsigned short* vp = a.v + (size_t)(chunk >> 2) * SK * DH;
    const rsrc_t rk = mk_rsrc(kp, HB), rv = mk_rsrc(vp, HB);
    const int voffh = voff + HB / 2;
#define ROWVO(n) ((((n) & 1) ? voffh : voff) + (((n) & 7) >> 1) * 1024)
#define ROWSO(grp, n) (((((grp) + ((n) >> 3) + (j >> 2) * 2) & 31) << 16) + (sw << 12) + (sp << 14))
#define LDROW(rs, grp, n) ldnt(rs, ROWVO(n), ROWSO(grp, n))
    __builtin_amdgcn_s_setprio(2);
    u32x4 buf[8], pre[NPRE * 8];
#pragma unroll
    for (int u = 0; u < NPRE * 8; ++u) {
      pre[u] = LDROW(rk, 0, u); SB();
      if (u == WIN) {
        if (lane < 4) ((LAS unsigned*)lds)[L_CNT + lane] = lane < 3 ? 0u : now32() + 0x40000000u;
        lds_wait();
        wg_barrier();
        SB();
      }
      if (u >= WIN) VM_WAIT(WIN);
    }
#pragma unroll
    for (int u = 0; u < 8; ++u) {
      buf[u] = LDROW(rk, NPRE, u); SB();
      if (u < 7) VM_WAIT(WIN);
    }
    wg_barrier();
    const u32x4 qp = *(const LAS u32x4*)(lds + L_Q + c * 4);
    LAS float* S = (LAS float*)lds + L_S + sw * 1024;
    float mx = -INFINITY;
#define DOT(w) dot2((w)[3], qp[3], dot2((w)[2], qp[2], dot2((w)[1], qp[1], dot2((w)[0], qp[0], 0.f))))
#define K_FIN(grp) do { \
      _Pragma("unroll") for (int u = 0; u < 4; ++u) { const float lo = x[u] + dpp<ROR8>(x[u]), hi = x[u + 4] + dpp<ROR8>(x[u + 4]); x[u] = (c & 8) ? hi : lo; } \
      _Pragma("unroll") for (int u = 0; u < 2; ++u) { const float lo = x[u] + dpp<XOR7>(x[u]), hi = x[u + 2] + dpp<XOR7>(x[u + 2]); x[u] = (c & 4) ? hi : lo; } \
      { const float lo = x[0] + dpp<XOR2>(x[0]), hi = x[1] + dpp<XOR2>(x[1]); x[0] = (c & 2) ? hi : lo; } \
      float sc = x[0] + dpp<XOR1>(x[0]); \
      sc = rbf(rbf(sc) * SCALE); \
      mx = fmaxf(mx, sc); \
      S[(grp) * 32 + r * 8 + (c >> 1)] = sc; \
      SB(); \
    } while (0)
    static_assert(WIN >= 3 && WIN <= 5, "ring of 8 rows");
    {
      float x[8];
#pragma unroll
      for (int u = 0; u < 8; ++u) {
        if (u > 0 && u <= WIN + 1) { buf[u - 1] = LDROW(rk, NPRE + 1, u - 1); SB(); }
        x[u] = DOT(buf[u]);
        SB();
      }
      K_FIN(NPRE);
    }
#pragma unroll
    for (int pp = 0; pp < NPRE; ++pp) {
      float x[8];
#pragma unroll
      for (int w = 0; w < 8; ++w) x[w] = DOT(pre[pp * 8 + w]);
      K_FIN(pp);
    }
#pragma unroll
    for (int u = WIN + 1; u < 7; ++u) { VM_WAIT(WIN); buf[u] = LDROW(rk, NPRE + 1, u); SB(); }
    int last = 31;
    asm volatile("" : "+s"(last));
#pragma nounroll
    for (int tt = NPRE + 1; tt <= last; ++tt) {
      const rsrc_t rn = mk_rsrc(tt == last ? vp : kp, HB);
      float x[8];
#pragma unroll
      for (int u = 0; u < 8; ++u) {
        const int n = u + 7;
        VM_WAIT(WIN);
        buf[n & 7] = LDROW(n >= 8 ? rn : rk, tt, n);
        SB();
        x[u] = DOT(buf[u]);
        SB();
      }
      K_FIN(tt);
    }
    mx = wave_max(mx);
    if (lane == 0) lds[L_MX + sw] = mx;
    lds_wait();
    if (lane == 0) __hip_atomic_fetch_add((LAS unsigned*)lds + L_CNT, 1u, RLX_WG);
    f32x2 acc[4] = {{0.f, 0.f}, {0.f, 0.f}, {0.f, 0.f}, {0.f, 0.f}};
    f32x4 s0 = *(const LAS f32x4*)(S + r * 8), s1;
    int el = -1, lag = -1;
#pragma nounroll
    for (int tt = 0; tt <= last; ++tt) {
      const rsrc_t rn = mk_rsrc(vp, tt == last ? 0 : HB);
      unsigned ref, tnow;
#pragma unroll
      for (int u = 0; u < 8; ++u) {
        const int n = u + 7;
        const int so = ROWSO(tt, n);
        asm volatile("s_cmpk_gt_i32 %0, %4\n\ts_cbranch_scc1 1f\n\ts_waitcnt vmcnt(%2)\n\ts_cmp_gt_i32 %1, 0\n\ts_cbranch_scc1 1f\n\ts_waitcnt vmcnt(%3)\n1:"
                     :: "s"(el), "s"(lag), "i"(WIN + 1), "i"(WIN), "i"(WIDE_T), "s"(so) : "scc");
        SB();
        buf[n & 7] = ldnt(n >= 8 ? rn : rv, ROWVO(n), so);
        SB();
        if (u == 0) s1 = *(const LAS f32x4*)(S + tt * 32 + r * 8 + 4);
        if (u == 4) { s0 = *(const LAS f32x4*)(S + ((tt + 1) & 31) * 32 + r * 8); ref = *(volatile LAS unsigned*)(lds + L_REF); tnow = now32(); }
        const float p = __builtin_amdgcn_exp2f(((u < 4 ? s0[u & 3] : s1[u & 3]) - mx) * LOG2E);
#pragma unroll
        for (int k = 0; k < 4; ++k) acc[k] = __builtin_elementwise_fma(bf_unpack(buf[u][k]), f32x2{p, p}, acc[k]);
        asm volatile("" : "+v"(acc[0]), "+v"(acc[1]), "+v"(acc[2]), "+v"(acc[3]));
        if (u == 6) {
          asm volatile("" : "+v"(ref), "+s"(tnow));
          el = (int)(tnow - __builtin_amdgcn_readfirstlane(ref));
          lag = el - (LAG_A + tt * LAG_B);
        }
        SB();
      }
    }
    *(LAS f32x4*)(lds + L_O + (sw * 4 + r) * 128 + c * 8) = f32x4{acc[0][0], acc[0][1], acc[1][0], acc[1][1]};
    *(LAS f32x4*)(lds + L_O + (sw * 4 + r) * 128 + c * 8 + 4) = f32x4{acc[2][0], acc[2][1], acc[3][0], acc[3][1]};
    lds_wait();
    wg_barrier();
    wg_barrier();
    if (blockIdx.x < NRED) wg_barrier();
    return;
  }

  unsigned epoch = *(const __attribute__((address_space(4))) unsigned*)(a.ws + WS_EPOCH);
  SB();
  float mb0, mb1, sd0, sd1;
  {
    float f0[8], f1[8];
#pragma unroll
    for (int k = 0; k < 4; ++k) { f0[2 * k] = bf_lo(xv0[k]); f0[2 * k + 1] = bf_hi(xv0[k]); f1[2 * k] = bf_lo(xv1[k]); f1[2 * k + 1] = bf_hi(xv1[k]); }
    float s0 = 0.f, s1 = 0.f;
#pragma unroll
    for (int k = 0; k < 8; ++k) { s0 += f0[k]; s1 += f1[k]; }
    const float m0 = wave_sum(s0) * (1.f / DM), m1 = wave_sum(s1) * (1.f / DM);
    float v0 = 0.f, v1 = 0.f;
#pragma unroll
    for (int k = 0; k < 8; ++k) { const float d0 = f0[k] - m0, d1 = f1[k] - m1; v0 = fmaf(d0, d0, v0); v1 = fmaf(d1, d1, v1); }
    v0 = wave_sum(v0) * (1.f / DM); v1 = wave_sum(v1) * (1.f / DM);
    mb0 = rbf(m0); sd0 = rbf(__builtin_sqrtf(rbf(rbf(v0) + 1e-5f)));
    mb1 = rbf(m1); sd1 = rbf(__builtin_sqrtf(rbf(rbf(v1) + 1e-5f)));
  }
#define XN(xf, mb, sd) rbf(rbf((xf) - (mb)) / (sd))
  asm volatile("" : "+s"(epoch));
  const unsigned tag = epoch + 0x9E3779B9u;
  const float xa0 = XN(bf_lo(xq0), mb0, sd0), xa1 = XN(bf_hi(xq0), mb0, sd0), xb0 = XN(bf_lo(xq1), mb1, sd1), xb1 = XN(bf_hi(xq1), mb1, sd1);
#define PARTIAL(w0_, w1_, n, res) do { \
    float t[8]; \
    _Pragma("unroll") for (int k = 0; k < 4; ++k) { \
      const f32x2 w0 = bf_unpack((w0_)[k]), w1 = bf_unpack((w1_)[k]); \
      const f32x2 qa = __builtin_elementwise_fma(w1, f32x2{xa1, xa1}, w0 * xa0), qb = __builtin_elementwise_fma(w1, f32x2{xb1, xb1}, w0 * xb0); \
      t[2 * k] = tr32(qa[0], qb[0]); t[2 * k + 1] = tr32(qa[1], qb[1]);             \
    } \
    _Pragma("unroll") for (int k = 0; k < 4; ++k) t[k] = tr16(t[k], t[4 + k]);        \
    f32x2 o; \
    _Pragma("unroll") for (int k = 0; k < 2; ++k) { const float lo = t[k] + dpp<ROR8>(t[k]), hi = t[2 + k] + dpp<ROR8>(t[2 + k]); o[k] = (c & 8) ? hi : lo; } \
    const float lo = o[0] + dpp<XOR7>(dpp<XOR3>(o[0])), hi = o[1] + dpp<XOR7>(dpp<XOR3>(o[1]));     \
    lds[L_P + (n) * 512 + wave * 64 + (r >> 1) * 32 + (c & 3) * 8 + (r & 1) * 4 + (c >> 3) * 2 + ((c >> 2) & 1)] = (c & 4) ? hi : lo; \
    lds_wait(); \
    if (lane == 0) __hip_atomic_fetch_add((LAS unsigned*)lds + L_CNT + 1 + (n), 1u, RLX_WG); \
    while (__hip_atomic_load((LAS unsigned*)lds + L_CNT + 1 + (n), RLX_WG) < 8u) __builtin_amdgcn_s_sleep(0); \
    asm volatile("" ::: "memory"); \
    const LAS float* P = (const LAS float*)lds + L_P + (n) * 512 + lane; \
    res = ((P[0] + P[64]) + (P[128] + P[192])) + ((P[256] + P[320]) + (P[384] + P[448])); \
  } while (0)
  {
    float qs;
    PARTIAL(wq0, wq1, 0, qs);
    if (wave == 0) st_granule((gu64*)(a.ws + WS_Q) + (h * 8 + g8) * 64 + lane, tag, qs);
  }
  if (wave == 7) {
    __builtin_amdgcn_s_sleep(32);
    const rsrc_t rg = mk_rsrc(a.ws + WS_Q + (size_t)h * 4096, 4096);
    const unsigned t0 = now32();
    u32x4 g0, g1;
    while (true) {
      g0 = lddev(rg, r * 512 + c * 16, b * 256); g1 = lddev(rg, r * 512 + c * 16, 2048 + b * 256);
      if (__all((g0[1] == tag) & (g0[3] == tag) & (g1[1] == tag) & (g1[3] == tag))) break;
      if (now32() - t0 > SPIN_TICKS) break;
      __builtin_amdgcn_s_sleep(8);
      asm volatile("" ::: "memory");
    }
    ((LAS unsigned*)lds)[L_Q + lane] = pack_bf16(__uint_as_float(g0[0]) + __uint_as_float(g1[0]), __uint_as_float(g0[2]) + __uint_as_float(g1[2]));
    lds_wait();
  }
  wg_barrier();
  const int red = blockIdx.x < NRED ? (xg * 2 + (j & 1)) * 2 + (j >> 1) : -1;
  const unsigned xw = (red >= 0 && tid < 16) ? *(const gu32*)(a.x + (red & 1) * DM + (red >> 1) * 32 + tid * 2) : 0u;
  float gsum = 0.f;
  if (h < 28) {
    const rsrc_t rm = mk_rsrc((g8 & 2) ? a.w3 : a.w1, DM * FF * 2);
    const int so = h * 128 + (g8 & 1) * 64;
    const u32x4 wm0 = ldnt(rm, prow * (FF * 2) + (lane & 3) * 16, so), wm1 = ldnt(rm, prow * (FF * 2) + (lane & 3) * 16, so + FF * 2);
    PARTIAL(wm0, wm1, 1, gsum);
  }
  f32x2 wf[64];
  {
    const rsrc_t ro = mk_rsrc(a.wo + (size_t)h * DH * DM, DH * DM * 2);
#pragma unroll
    for (int i = 0; i < 16; i += 2) {
      const u32x4 w0 = ldnt(ro, voff + (i & 3) * 1024, (wave * 16 + (i & ~3)) * 1024), w1 = ldnt(ro, voff + (i & 3) * 1024 + 1024, (wave * 16 + (i & ~3)) * 1024);
      SB();
#pragma unroll
      for (int k = 0; k < 4; ++k) {
        wf[i * 4 + k] = bf_unpack(w0[k]); wf[i * 4 + 4 + k] = bf_unpack(w1[k]);
        asm volatile("" : "+v"(wf[i * 4 + k]), "+v"(wf[i * 4 + 4 + k]));
      }
      SB();
    }
  }
  const int lane0 = lane;
  while (__hip_atomic_load((LAS unsigned*)lds + L_CNT, RLX_WG) < 4u) __builtin_amdgcn_s_sleep(8);
  asm volatile("" ::: "memory");
  gu64* ml = (gu64*)(a.ws + WS_ML);
  gu64* G = (gu64*)(a.ws + WS_G);
  float mw = 0.f;
  const unsigned tk = now32();
  if (wave == 7) {
    FRESH();
    mw = lds[L_MX + (lane & 3)];
    float m = fmaxf(mw, dpp<XOR1>(mw)); m = fmaxf(m, dpp<XOR2>(m));
    float l = 0.f;
#pragma unroll 2
    for (int i = 0; i < 16; ++i) {
      const f32x4 sv = *(const LAS f32x4*)(lds + L_S + i * 256 + lane * 4);
#pragma unroll
      for (int k = 0; k < 4; ++k) l += __builtin_amdgcn_exp2f((sv[k] - m) * LOG2E);
    }
    l = wave_sum(l);
    if (lane < 16) st_granule(ml + chunk * 16 + lane, tag, lane == 0 ? m : lane == 1 ? l : __uint_as_float(tk));
  } else if (wave == 0 && h < 28) {
    FRESH();
    __builtin_amdgcn_s_sleep(16);
    st_granule(G + (h * 8 + g8) * 64 + lane, tag, gsum);
  }
  __builtin_amdgcn_s_sleep(127); __builtin_amdgcn_s_sleep(16);
  f32x2 yacc[4] = {{0.f, 0.f}, {0.f, 0.f}, {0.f, 0.f}, {0.f, 0.f}};
  if (wave < 7) {
    FRESH();
    const int f0 = (h * 4 + sp) * 14 + wave * 2;
    const rsrc_t r2 = mk_rsrc(a.w2 + (size_t)f0 * DM, 2 * DM * 2);
    const u32x4 w20 = ldnt(r2, voff, 0), w21 = ldnt(r2, voff + 1024, 0);
    const int f = f0 + (r & 1);
    gu64* gp = G + ((f >> 6) * 8 + (c & 1) * 4 + (c >> 3) * 2 + ((f >> 5) & 1)) * 64 + b * 32 + (f & 31);
    const unsigned t0 = now32();
    unsigned long long g;
    while (true) {
      g = __hip_atomic_load(gp, RLX_AGENT);
      if (__all((unsigned)(g >> 32) == tag)) break;
      if (now32() - t0 > SPIN_TICKS) break;
      __builtin_amdgcn_s_sleep(127);
    }
    float s = __uint_as_float((unsigned)g);
    s += dpp<XOR1>(s);
    const float gs = rbf(s), us = rbf(dpp<ROR8>(s));
    const float hv = rbf(rbf(gs / (1.f + __expf(-gs))) * us);
    const float h0 = lane_value(hv, 0), h1 = lane_value(hv, 16);
#pragma unroll
    for (int k = 0; k < 4; ++k) yacc[k] = __builtin_elementwise_fma(bf_unpack(w21[k]), f32x2{h1, h1}, bf_unpack(w20[k]) * h0);
  } else {
    FRESH();
    {
      const unsigned long long gt = __hip_atomic_load(ml + (lane * 4 + (lane & 3)) * 16 + 2, RLX_AGENT);
      const bool v = (unsigned)(gt >> 32) == tag;
      const float n = wave_sum(v ? 1.f : 0.f), d = wave_sum(v ? (float)(int)((unsigned)gt - tk) : 0.f);
      if (lane == 0 && n > 0.f) ((LAS unsigned*)lds)[L_REF] = tk + (unsigned)(int)(d / n);
    }
    const unsigned t0 = now32();
    unsigned long long gm, gl;
    while (true) {
      gm = __hip_atomic_load(ml + ((chunk & ~3) + (lane & 3)) * 16, RLX_AGENT);
      gl = __hip_atomic_load(ml + ((chunk & ~3) + (lane & 3)) * 16 + 1, RLX_AGENT);
      if (__all((unsigned)(gm >> 32) == tag && (unsigned)(gl >> 32) == tag)) break;
      if (now32() - t0 > SPIN_TICKS) break;
      __builtin_amdgcn_s_sleep(127);
    }
    const float ms = __uint_as_float((unsigned)gm), ls = __uint_as_float((unsigned)gl);
    float M = fmaxf(ms, dpp<XOR1>(ms)); M = fmaxf(M, dpp<XOR2>(M));
    float L = ls * __builtin_amdgcn_exp2f((ms - M) * LOG2E);
    L += dpp<XOR1>(L); L += dpp<XOR2>(L);
    if (lane < 4) lds[L_FW + lane] = __builtin_amdgcn_exp2f((mw - M) * LOG2E) / L;
    lds_wait();
    {
      const unsigned char* pa = (lane < 32 || red < 0) ? a.ws + WS_Y + (lane & 15) * YB + chunk * 128
                                                       : (const unsigned char*)a.out + (red & 1) * (DM * 2) + (red >> 1) * 64;
      const unsigned jk = __hip_atomic_load((gu32*)pa, RLX_AGENT);
      asm volatile("" :: "v"(jk));
    }
    if (blockIdx.x == NWG - 1) {
      while (true) {
        bool ok = true;
#pragma unroll
        for (int i = 0; i < 4; ++i) ok = ok & ((unsigned)(__hip_atomic_load(ml + (i * 64 + lane) * 16 + 2, RLX_AGENT) >> 32) == tag);
        if (__all(ok)) break;
        if (now32() - t0 > SPIN_TICKS) break;
        __builtin_amdgcn_s_sleep(127);
      }
      if (lane == 0) __hip_atomic_store((gu32*)(a.ws + WS_EPOCH), tag, RLX_AGENT);
    }
  }
  {
    FRESH();
    wg_barrier();
    {
      const LAS float* P = (const LAS float*)lds + L_O + (4 * r) * 128 + wave * 16 + c;
      const float o = xrow_sum(((P[0] + P[128]) + (P[256] + P[384])) * lds[L_FW + r]);
#pragma unroll
      for (int i = 0; i < 16; ++i) {
        const float oi = lane_value(o, i);
#pragma unroll
        for (int k = 0; k < 4; ++k) yacc[k] = __builtin_elementwise_fma(wf[i * 4 + k], f32x2{oi, oi}, yacc[k]);
      }
    }
    *(LAS f32x4*)(lds + L_Y + wave * 512 + lane * 8) = f32x4{yacc[0][0], yacc[0][1], yacc[1][0], yacc[1][1]};
    *(LAS f32x4*)(lds + L_Y + wave * 512 + lane * 8 + 4) = f32x4{yacc[2][0], yacc[2][1], yacc[3][0], yacc[3][1]};
    lds_wait();
    wg_barrier();
    const rsrc_t ry = mk_rsrc(a.ws + WS_Y, 16 * YB);
    {
      const LAS float* P = (const LAS float*)lds + L_Y + tid;
      const float y = ((P[0] + P[512]) + (P[1024] + P[1536])) + ((P[2048] + P[2560]) + (P[3072] + P[3584]));
      const unsigned u = __float_as_uint(y);
      const unsigned w = (((u & 0x7f800000u) == 0x7f800000u ? u | ((u & 0x7fffffu) ? 0x400000u : 0u) : u + 0x400u) & 0xfffff800u) | ((tag >> ((lane & 1) * 11)) & 0x7ffu);
      const unsigned w1 = __builtin_amdgcn_mov_dpp(w, XOR1, 0xf, 0xf, true);
      if (!(lane & 1)) __builtin_amdgcn_raw_buffer_store_b64(u32x2{w, w1}, ry, (tid >> 5) * YB + (tid & 31) * 4, chunk * 128, 16);
    }
    if (red >= 0) {
      const unsigned t0 = now32();
      u32x4 g0 = {0u, 0u, 0u, 0u}, g1 = {0u, 0u, 0u, 0u};
      bool ok0 = false, ok1 = false;
      const unsigned ta = tag & 0x7ffu, tb = (tag >> 11) & 0x7ffu;
      while (true) {
        if (!ok0) g0 = lddev(ry, voff, red * 16384 + wave * 2048);
        if (!ok1) g1 = lddev(ry, voff + 1024, red * 16384 + wave * 2048);
        ok0 = (((g0[0] ^ ta) | (g0[1] ^ tb) | (g0[2] ^ ta) | (g0[3] ^ tb)) & 0x7ffu) == 0u;
        ok1 = (((g1[0] ^ ta) | (g1[1] ^ tb) | (g1[2] ^ ta) | (g1[3] ^ tb)) & 0x7ffu) == 0u;
        if (__all(ok0 & ok1)) break;
        if (now32() - t0 > SPIN_TICKS) break;
        __builtin_amdgcn_s_sleep(4);
        asm volatile("" ::: "memory");
      }
      float t[4];
#pragma unroll
      for (int k = 0; k < 4; ++k) t[k] = __uint_as_float(g0[k] & 0xfffff800u) + __uint_as_float(g1[k] & 0xfffff800u);
      float s = tr16(tr32(t[0], t[2]), tr32(t[1], t[3]));
      s += dpp<ROR8>(s);
      if (!(lane & 8)) lds[L_FIN + wave * 32 + (lane & 7) * 4 + r] = s;
      lds_wait();
      wg_barrier();
      if (tid < 16) {
        const LAS f32x2* P = (const LAS f32x2*)(lds + L_FIN + tid * 2);
        const f32x2 o = bf_unpack(xw) + (((P[0] + P[16]) + (P[32] + P[48])) + ((P[64] + P[80]) + (P[96] + P[112])));
        *(gu32*)(a.out + (red & 1) * DM + (red >> 1) * 32 + tid * 2) = pack_bf16(o[0], o[1]);
      }
    }
  }
}

}

extern "C" void kernel_launch(void* const* d_in, const int* in_sizes, int n_in, void* d_out, int out_size, void* d_ws, size_t ws_size,
                              hipStream_t stream) {
  Args a;
  a.x = (const unsigned short*)d_in[0]; a.k = (const unsigned short*)d_in[1]; a.v = (const unsigned short*)d_in[2];
  a.wq = (const unsigned short*)d_in[3]; a.wo = (const unsigned short*)d_in[4];
  a.w1 = (const unsigned short*)d_in[6]; a.w3 = (const unsigned short*)d_in[7]; a.w2 = (const unsigned short*)d_in[8];
  a.out = (unsigned short*)d_out; a.ws = (unsigned char*)d_ws;
  fused_decode<<<NWG, NTHR, 0, stream>>>(a);
}
```

```cpp
#include <hip/hip_runtime.h>
#include <hip/hip_bf16.h>
#include <stdint.h>

namespace {

constexpr int NWG = 256, NTHR = 768, NRED = 32;
constexpr int DM = 512, NH = 32, SK = 16384, DH = 128, FF = 1792;
constexpr float LOG2E = 1.4426950408889634f;
constexpr float SCALE = 0.08838834764831845f;
constexpr int WIN = 4;
constexpr int NPRE = 3;
constexpr int LAG_A = 155, LAG_B = 109, WIDE_T = 3280;
constexpr size_t WS_EPOCH = 0;
constexpr size_t WS_ML = 8192;
constexpr size_t WS_Y = 65536;
constexpr int YB = 32768;
constexpr size_t WS_Q = WS_Y + 16 * YB;
constexpr size_t WS_G = WS_Q + (128u << 10);
constexpr int L_S = 0;
constexpr int L_O = L_S + 4096;
constexpr int L_Y = L_O + 2048;
constexpr int L_P = L_Y + 4096;
constexpr int L_Q = L_P + 1024;
constexpr int L_MX = L_Q + 64;
constexpr int L_FW = L_MX + 4;
constexpr int L_FIN = L_FW + 4;
constexpr int L_CNT = L_FIN + 256;
constexpr int L_REF = L_CNT + 3;
constexpr int L_END = L_REF + 1;

#define GAS __attribute__((address_space(1)))
#define LAS __attribute__((address_space(3)))
#define RLX_AGENT __ATOMIC_RELAXED, __HIP_MEMORY_SCOPE_AGENT
#define RLX_WG __ATOMIC_RELAXED, __HIP_MEMORY_SCOPE_WORKGROUP
typedef unsigned u32x2 __attribute__((ext_vector_type(2)));
typedef unsigned u32x4 __attribute__((ext_vector_type(4)));
typedef float f32x2 __attribute__((ext_vector_type(2)));
typedef float f32x4 __attribute__((ext_vector_type(4)));
typedef __bf16 bf16x2 __attribute__((ext_vector_type(2)));
typedef GAS unsigned gu32;
typedef GAS unsigned long long gu64;
typedef __amdgpu_buffer_rsrc_t rsrc_t;

__device__ __forceinline__ float bf_lo(unsigned u) { return __builtin_bit_cast(float, u << 16); }
__device__ __forceinline__ float bf_hi(unsigned u) { return __builtin_bit_cast(float, u & 0xffff0000u); }
__device__ __forceinline__ f32x2 bf_unpack(unsigned u) { return f32x2{bf_lo(u), bf_hi(u)}; }
__device__ __forceinline__ unsigned pack_bf16(float lo, float hi) {
  bf16x2 r = {(__bf16)lo, (__bf16)hi};
  return __builtin_bit_cast(unsigned, r);
}
__device__ __forceinline__ float rbf(float x) { return (float)(__bf16)x; }
__device__ __forceinline__ float dot2(unsigned a, unsigned b, float acc) {
  return __builtin_amdgcn_fdot2_f32_bf16(__builtin_bit_cast(bf16x2, a), __builtin_bit_cast(bf16x2, b), acc, false);
}
template <int CTRL> __device__ __forceinline__ float dpp(float x) {
  return __builtin_bit_cast(float, __builtin_amdgcn_mov_dpp(__builtin_bit_cast(int, x), CTRL, 0xf, 0xf, true));
}
constexpr int XOR1 = 0xB1, XOR2 = 0x4E, XOR3 = 0x1B, XOR7 = 0x141, ROR8 = 0x128;
__device__ __forceinline__ float tr16(float a, float b) {
  auto s = __builtin_amdgcn_permlane16_swap(__float_as_uint(a), __float_as_uint(b), false, false);
  return __uint_as_float(s[0]) + __uint_as_float(s[1]);
}
__device__ __forceinline__ float tr32(float a, float b) {
  auto s = __builtin_amdgcn_permlane32_swap(__float_as_uint(a), __float_as_uint(b), false, false);
  return __uint_as_float(s[0]) + __uint_as_float(s[1]);
}
__device__ __forceinline__ float xrow_sum(float x) { x = tr16(x, x); return tr32(x, x); }
__device__ __forceinline__ float wave_sum(float x) {
  x += dpp<XOR1>(x); x += dpp<XOR2>(x); x += dpp<XOR7>(x); x += dpp<ROR8>(x);
  return xrow_sum(x);
}
__device__ __forceinline__ float wave_max(float x) {
  x = fmaxf(x, dpp<XOR1>(x)); x = fmaxf(x, dpp<XOR2>(x)); x = fmaxf(x, dpp<XOR7>(x)); x = fmaxf(x, dpp<ROR8>(x));
  auto s = __builtin_amdgcn_permlane16_swap(__float_as_uint(x), __float_as_uint(x), false, false);
  x = fmaxf(__uint_as_float(s[0]), __uint_as_float(s[1]));
  auto t = __builtin_amdgcn_permlane32_swap(__float_as_uint(x), __float_as_uint(x), false, false);
  return fmaxf(__uint_as_float(t[0]), __uint_as_float(t[1]));
}
__device__ __forceinline__ float lane_value(float x, int l) { return __builtin_bit_cast(float, __builtin_amdgcn_readlane(__builtin_bit_cast(int, x), l)); }
__device__ __forceinline__ void lds_wait() { asm volatile("s_waitcnt lgkmcnt(0)" ::: "memory"); }
__device__ __forceinline__ void wg_barrier() { __builtin_amdgcn_s_barrier(); asm volatile("" ::: "memory"); }
__device__ __forceinline__ void st_granule(gu64* g, unsigned tag, float v) {
  __hip_atomic_store(g, ((unsigned long long)tag << 32) | (unsigned long long)__float_as_uint(v), RLX_AGENT);
}
__device__ __forceinline__ rsrc_t mk_rsrc(const void* p, int bytes) { return __builtin_amdgcn_make_buffer_rsrc((void*)p, 0, bytes, 0x00020000); }
__device__ __forceinline__ u32x4 ldnt(rsrc_t r, int voff, int soff) { return __builtin_amdgcn_raw_buffer_load_b128(r, voff, soff, 2); }
__device__ __forceinline__ u32x4 lddev(rsrc_t r, int voff, int soff) { return __builtin_amdgcn_raw_buffer_load_b128(r, voff, soff, 16); }
__device__ __forceinline__ unsigned now32() { return (unsigned)__builtin_amdgcn_s_memrealtime(); }
constexpr unsigned SPIN_TICKS = 20000000u;

#define SB() __builtin_amdgcn_sched_barrier(0)
#define VM_WAIT(n) do { asm volatile("s_waitcnt vmcnt(%0)" :: "i"(n)); SB(); } while (0)
#define FRESH() int lane_ = lane0; asm volatile("" : "+v"(lane_)); \
  const int lane = lane_, r = lane >> 4, c = lane & 15, tid = wave * 64 + lane, voff = lane * 16; (void)r; (void)c; (void)tid; (void)voff

struct Args {
  const unsigned short* x; const unsigned short* k; const unsigned short* v; const unsigned short* wq; const unsigned short* wo;
  const unsigned short* w1; const unsigned short* w3; const unsigned short* w2;
  unsigned short* out; unsigned char* ws;
};

__global__ void __launch_bounds__(NTHR, 3) fused_decode(Args a) {
  __shared__ __attribute__((aligned(16))) float lds[L_END];
  const int tid = threadIdx.x, lane = tid & 63, wave = __builtin_amdgcn_readfirstlane(tid >> 6);
  const int r = lane >> 4, c = lane & 15;
  const int xg = blockIdx.x & 7, j = blockIdx.x >> 3;
  const int b = j >> 4, h = xg + ((j >> 2) & 3) * 8, sp = j & 3;
  const int chunk = b * 128 + h * 4 + sp;
  const int voff = lane * 16;

  const int g8 = b * 4 + sp;
  const int prow = (g8 >> 2) * 256 + wave * 32 + 2 * (lane >> 2);
  u32x4 xv0, xv1, wq0, wq1;
  unsigned xq0, xq1;
  if (wave < 8) {
    xv0 = *(const GAS u32x4*)(a.x + lane * 8); xv1 = *(const GAS u32x4*)(a.x + DM + lane * 8);
    xq0 = *(const gu32*)(a.x + prow); xq1 = *(const gu32*)(a.x + DM + prow);
    const rsrc_t rq = mk_rsrc(a.wq, DM * NH * DH * 2);
    wq0 = ldnt(rq, prow * (NH * DH * 2) + (lane & 3) * 16, h * (DH * 2) + (g8 & 3) * 64);
    wq1 = ldnt(rq, prow * (NH * DH * 2) + (lane & 3) * 16, h * (DH * 2) + (g8 & 3) * 64 + NH * DH * 2);
    SB();
    wg_barrier();
  }
  int role = wave;
  asm volatile("" : "+s"(role));
  if (__builtin_expect(role >= 8, 1)) {
    const int sw = wave - 8;
    constexpr int HB = SK * DH * 2;
    const unsigned short* kp = a.k + (size_t)(chunk >> 2) * SK * DH;
    const unsigned short* vp = a.v + (size_t)(chunk >> 2) * SK * DH;
    const rsrc_t rk = mk_rsrc(kp, HB), rv = mk_rsrc(vp, HB);
    const int voffh = voff + HB / 2;
    auto window = [&](int g) -> int { g += (j >> 3) * 2; return ((g & 1) << 19) + ((g & 14) << 15) + ((g & 16) << 16) + (sp << 14) + (sw << 12); };
#define ROWVO(n) ((((n) & 1) ? voffh : voff) + (((n) & 7) >> 1) * 1024)
#define ROWSO(grp, n) window((grp) + ((n) >> 3))
#define LDROW(rs, grp, n) ldnt(rs, ROWVO(n), ROWSO(grp, n))
    __builtin_amdgcn_s_setprio(2);
    u32x4 buf[8], pre[NPRE * 8];
#pragma unroll
    for (int u = 0; u < NPRE * 8; ++u) {
      pre[u] = LDROW(rk, 0, u); SB();
      if (u == WIN) {
        if (lane < 4) ((LAS unsigned*)lds)[L_CNT + lane] = lane < 3 ? 0u : now32() + 0x40000000u;
        lds_wait();
        wg_barrier();
        SB();
      }
      if (u >= WIN) VM_WAIT(WIN);
    }
#pragma unroll
    for (int u = 0; u < 8; ++u) {
      buf[u] = LDROW(rk, NPRE, u); SB();
      if (u < 7) VM_WAIT(WIN);
    }
    wg_barrier();
    const u32x4 qp = *(const LAS u32x4*)(lds + L_Q + c * 4);
    LAS float* S = (LAS float*)lds + L_S + sw * 1024;
    float mx = -INFINITY;
#define DOT(w) dot2((w)[3], qp[3], dot2((w)[2], qp[2], dot2((w)[1], qp[1], dot2((w)[0], qp[0], 0.f))))
#define K_FIN(grp) do { \
      _Pragma("unroll") for (int u = 0; u < 4; ++u) { const float lo = x[u] + dpp<ROR8>(x[u]), hi = x[u + 4] + dpp<ROR8>(x[u + 4]); x[u] = (c & 8) ? hi : lo; } \
      _Pragma("unroll") for (int u = 0; u < 2; ++u) { const float lo = x[u] + dpp<XOR7>(x[u]), hi = x[u + 2] + dpp<XOR7>(x[u + 2]); x[u] = (c & 4) ? hi : lo; } \
      { const float lo = x[0] + dpp<XOR2>(x[0]), hi = x[1] + dpp<XOR2>(x[1]); x[0] = (c & 2) ? hi : lo; } \
      float sc = x[0] + dpp<XOR1>(x[0]); \
      sc = rbf(rbf(sc) * SCALE); \
      mx = fmaxf(mx, sc); \
      S[(grp) * 32 + r * 8 + (c >> 1)] = sc; \
      SB(); \
    } while (0)
    static_assert(WIN >= 3 && WIN <= 5, "ring of 8 rows");
    {
      float x[8];
#pragma unroll
      for (int u = 0; u < 8; ++u) {
        if (u > 0 && u <= WIN + 1) { buf[u - 1] = LDROW(rk, NPRE + 1, u - 1); SB(); }
        x[u] = DOT(buf[u]);
        SB();
      }
      K_FIN(NPRE);
    }
#pragma unroll
    for (int pp = 0; pp < NPRE; ++pp) {
      float x[8];
#pragma unroll
      for (int w = 0; w < 8; ++w) x[w] = DOT(pre[pp * 8 + w]);
      K_FIN(pp);
    }
#pragma unroll
    for (int u = WIN + 1; u < 7; ++u) { VM_WAIT(WIN); buf[u] = LDROW(rk, NPRE + 1, u); SB(); }
    int last = 31;
    asm volatile("" : "+s"(last));
#pragma nounroll
    for (int tt = NPRE + 1; tt <= last; ++tt) {
      const rsrc_t rn = mk_rsrc(tt == last ? vp : kp, HB);
      float x[8];
#pragma unroll
      for (int u = 0; u < 8; ++u) {
        const int n = u + 7;
        const int so = ROWSO(tt, n);
        asm volatile("s_waitcnt vmcnt(%0)" :: "i"(WIN), "s"(so));
        SB();
        buf[n & 7] = ldnt(n >= 8 ? rn : rk, ROWVO(n), so);
        SB();
        x[u] = DOT(buf[u]);
        SB();
      }
      K_FIN(tt);
    }
    mx = wave_max(mx);
    if (lane == 0) lds[L_MX + sw] = mx;
    lds_wait();
    if (lane == 0) __hip_atomic_fetch_add((LAS unsigned*)lds + L_CNT, 1u, RLX_WG);
    f32x2 acc[4] = {{0.f, 0.f}, {0.f, 0.f}, {0.f, 0.f}, {0.f, 0.f}};
    f32x4 s0 = *(const LAS f32x4*)(S + r * 8), s1;
    int el = -1, lag = -1;
#pragma nounroll
    for (int tt = 0; tt <= last; ++tt) {
      const rsrc_t rn = mk_rsrc(vp, tt == last ? 0 : HB);
      unsigned ref, tnow;
#pragma unroll
      for (int u = 0; u < 8; ++u) {
        const int n = u + 7;
        const int so = ROWSO(tt, n);
        asm volatile("s_cmpk_gt_i32 %0, %4\n\ts_cbranch_scc1 1f\n\ts_waitcnt vmcnt(%2)\n\ts_cmp_gt_i32 %1, 0\n\ts_cbranch_scc1 1f\n\ts_waitcnt vmcnt(%3)\n1:"
                     :: "s"(el), "s"(lag), "i"(WIN + 1), "i"(WIN), "i"(WIDE_T), "s"(so) : "scc");
        SB();
        buf[n & 7] = ldnt(n >= 8 ? rn : rv, ROWVO(n), so);
        SB();
        if (u == 0) s1 = *(const LAS f32x4*)(S + tt * 32 + r * 8 + 4);
        if (u == 4) { s0 = *(const LAS f32x4*)(S + ((tt + 1) & 31) * 32 + r * 8); ref = *(volatile LAS unsigned*)(lds + L_REF); tnow = now32(); }
        const float p = __builtin_amdgcn_exp2f(((u < 4 ? s0[u & 3] : s1[u & 3]) - mx) * LOG2E);
#pragma unroll
        for (int k = 0; k < 4; ++k) acc[k] = __builtin_elementwise_fma(bf_unpack(buf[u][k]), f32x2{p, p}, acc[k]);
        asm volatile("" : "+v"(acc[0]), "+v"(acc[1]), "+v"(acc[2]), "+v"(acc[3]));
        if (u == 6) {
          asm volatile("" : "+v"(ref), "+s"(tnow));
          el = (int)(tnow - __builtin_amdgcn_readfirstlane(ref));
          lag = el - (LAG_A + tt * LAG_B);
        }
        SB();
      }
    }
    *(LAS f32x4*)(lds + L_O + (sw * 4 + r) * 128 + c * 8) = f32x4{acc[0][0], acc[0][1], acc[1][0], acc[1][1]};
    *(LAS f32x4*)(lds + L_O + (sw * 4 + r) * 128 + c * 8 + 4) = f32x4{acc[2][0], acc[2][1], acc[3][0], acc[3][1]};
    lds_wait();
    wg_barrier();
    wg_barrier();
    if (blockIdx.x < NRED) wg_barrier();
    return;
  }

  unsigned epoch = *(const __attribute__((address_space(4))) unsigned*)(a.ws + WS_EPOCH);
  SB();
  float mb0, mb1, sd0, sd1;
  {
    float f0[8], f1[8];
#pragma unroll
    for (int k = 0; k < 4; ++k) { f0[2 * k] = bf_lo(xv0[k]); f0[2 * k + 1] = bf_hi(xv0[k]); f1[2 * k] = bf_lo(xv1[k]); f1[2 * k + 1] = bf_hi(xv1[k]); }
    float s0 = 0.f, s1 = 0.f;
#pragma unroll
    for (int k = 0; k < 8; ++k) { s0 += f0[k]; s1 += f1[k]; }
    const float m0 = wave_sum(s0) * (1.f / DM), m1 = wave_sum(s1) * (1.f / DM);
    float v0 = 0.f, v1 = 0.f;
#pragma unroll
    for (int k = 0; k < 8; ++k) { const float d0 = f0[k] - m0, d1 = f1[k] - m1; v0 = fmaf(d0, d0, v0); v1 = fmaf(d1, d1, v1); }
    v0 = wave_sum(v0) * (1.f / DM); v1 = wave_sum(v1) * (1.f / DM);
    mb0 = rbf(m0); sd0 = rbf(__builtin_sqrtf(rbf(rbf(v0) + 1e-5f)));
    mb1 = rbf(m1); sd1 = rbf(__builtin_sqrtf(rbf(rbf(v1) + 1e-5f)));
  }
#define XN(xf, mb, sd) rbf(rbf((xf) - (mb)) / (sd))
  asm volatile("" : "+s"(epoch));
  const unsigned tag = epoch + 0x9E3779B9u;
  const float xa0 = XN(bf_lo(xq0), mb0, sd0), xa1 = XN(bf_hi(xq0), mb0, sd0), xb0 = XN(bf_lo(xq1), mb1, sd1), xb1 = XN(bf_hi(xq1), mb1, sd1);
#define PARTIAL(w0_, w1_, n, res) do { \
    float t[8]; \
    _Pragma("unroll") for (int k = 0; k < 4; ++k) { \
      const f32x2 w0 = bf_unpack((w0_)[k]), w1 = bf_unpack((w1_)[k]); \
      const f32x2 qa = __builtin_elementwise_fma(w1, f32x2{xa1, xa1}, w0 * xa0), qb = __builtin_elementwise_fma(w1, f32x2{xb1, xb1}, w0 * xb0); \
      t[2 * k] = tr32(qa[0], qb[0]); t[2 * k + 1] = tr32(qa[1], qb[1]);             \
    } \
    _Pragma("unroll") for (int k = 0; k < 4; ++k) t[k] = tr16(t[k], t[4 + k]);        \
    f32x2 o; \
    _Pragma("unroll") for (int k = 0; k < 2; ++k) { const float lo = t[k] + dpp<ROR8>(t[k]), hi = t[2 + k] + dpp<ROR8>(t[2 + k]); o[k] = (c & 8) ? hi : lo; } \
    const float lo = o[0] + dpp<XOR7>(dpp<XOR3>(o[0])), hi = o[1] + dpp<XOR7>(dpp<XOR3>(o[1]));     \
    lds[L_P + (n) * 512 + wave * 64 + (r >> 1) * 32 + (c & 3) * 8 + (r & 1) * 4 + (c >> 3) * 2 + ((c >> 2) & 1)] = (c & 4) ? hi : lo; \
    lds_wait(); \
    if (lane == 0) __hip_atomic_fetch_add((LAS unsigned*)lds + L_CNT + 1 + (n), 1u, RLX_WG); \
    while (__hip_atomic_load((LAS unsigned*)lds + L_CNT + 1 + (n), RLX_WG) < 8u) __builtin_amdgcn_s_sleep(0); \
    asm volatile("" ::: "memory"); \
    const LAS float* P = (const LAS float*)lds + L_P + (n) * 512 + lane; \
    res = ((P[0] + P[64]) + (P[128] + P[192])) + ((P[256] + P[320]) + (P[384] + P[448])); \
  } while (0)
  {
    float qs;
    PARTIAL(wq0, wq1, 0, qs);
    if (wave == 0) st_granule((gu64*)(a.ws + WS_Q) + (h * 8 + g8) * 64 + lane, tag, qs);
  }
  if (wave == 7) {
    __builtin_amdgcn_s_sleep(32);
    const rsrc_t rg = mk_rsrc(a.ws + WS_Q + (size_t)h * 4096, 4096);
    const unsigned t0 = now32();
    u32x4 g0, g1;
    while (true) {
      g0 = lddev(rg, r * 512 + c * 16, b * 256); g1 = lddev(rg, r * 512 + c * 16, 2048 + b * 256);
      if (__all((g0[1] == tag) & (g0[3] == tag) & (g1[1] == tag) & (g1[3] == tag))) break;
      if (now32() - t0 > SPIN_TICKS) break;
      __builtin_amdgcn_s_sleep(8);
      asm volatile("" ::: "memory");
    }
    ((LAS unsigned*)lds)[L_Q + lane] = pack_bf16(__uint_as_float(g0[0]) + __uint_as_float(g1[0]), __uint_as_float(g0[2]) + __uint_as_float(g1[2]));
    lds_wait();
  }
  wg_barrier();
  const int red = blockIdx.x < NRED ? (xg * 2 + (j & 1)) * 2 + (j >> 1) : -1;
  const unsigned xw = (red >= 0 && tid < 16) ? *(const gu32*)(a.x + (red & 1) * DM + (red >> 1) * 32 + tid * 2) : 0u;
  float gsum = 0.f;
  if (h < 28) {
    const rsrc_t rm = mk_rsrc((g8 & 2) ? a.w3 : a.w1, DM * FF * 2);
    const int so = h * 128 + (g8 & 1) * 64;
    const u32x4 wm0 = ldnt(rm, prow * (FF * 2) + (lane & 3) * 16, so), wm1 = ldnt(rm, prow * (FF * 2) + (lane & 3) * 16, so + FF * 2);
    PARTIAL(wm0, wm1, 1, gsum);
  }
  f32x2 wf[64];
  {
    const rsrc_t ro = mk_rsrc(a.wo + (size_t)h * DH * DM, DH * DM * 2);
#pragma unroll
    for (int i = 0; i < 16; i += 2) {
      const u32x4 w0 = ldnt(ro, voff + (i & 3) * 1024, (wave * 16 + (i & ~3)) * 1024), w1 = ldnt(ro, voff + (i & 3) * 1024 + 1024, (wave * 16 + (i & ~3)) * 1024);
      SB();
#pragma unroll
      for (int k = 0; k < 4; ++k) {
        wf[i * 4 + k] = bf_unpack(w0[k]); wf[i * 4 + 4 + k] = bf_unpack(w1[k]);
        asm volatile("" : "+v"(wf[i * 4 + k]), "+v"(wf[i * 4 + 4 + k]));
      }
      SB();
    }
  }
  const int lane0 = lane;
  while (__hip_atomic_load((LAS unsigned*)lds + L_CNT, RLX_WG) < 4u) __builtin_amdgcn_s_sleep(8);
  asm volatile("" ::: "memory");
  gu64* ml = (gu64*)(a.ws + WS_ML);
  gu64* G = (gu64*)(a.ws + WS_G);
  float mw = 0.f;
  const unsigned tk = now32();
  if (wave == 7) {
    FRESH();
    mw = lds[L_MX + (lane & 3)];
    float m = fmaxf(mw, dpp<XOR1>(mw)); m = fmaxf(m, dpp<XOR2>(m));
    float l = 0.f;
#pragma unroll 2
    for (int i = 0; i < 16; ++i) {
      const f32x4 sv = *(const LAS f32x4*)(lds + L_S + i * 256 + lane * 4);
#pragma unroll
      for (int k = 0; k < 4; ++k) l += __builtin_amdgcn_exp2f((sv[k] - m) * LOG2E);
    }
    l = wave_sum(l);
    if (lane < 16) st_granule(ml + chunk * 16 + lane, tag, lane == 0 ? m : lane == 1 ? l : __uint_as_float(tk));
  } else if (wave == 0 && h < 28) {
    FRESH();
    __builtin_amdgcn_s_sleep(16);
    st_granule(G + (h * 8 + g8) * 64 + lane, tag, gsum);
  }
  __builtin_amdgcn_s_sleep(127); __builtin_amdgcn_s_sleep(16);
  f32x2 yacc[4] = {{0.f, 0.f}, {0.f, 0.f}, {0.f, 0.f}, {0.f, 0.f}};
  if (wave < 7) {
    FRESH();
    const int f0 = (h * 4 + sp) * 14 + wave * 2;
    const rsrc_t r2 = mk_rsrc(a.w2 + (size_t)f0 * DM, 2 * DM * 2);
    const u32x4 w20 = ldnt(r2, voff, 0), w21 = ldnt(r2, voff + 1024, 0);
    const int f = f0 + (r & 1);
    gu64* gp = G + ((f >> 6) * 8 + (c & 1) * 4 + (c >> 3) * 2 + ((f >> 5) & 1)) * 64 + b * 32 + (f & 31);
    const unsigned t0 = now32();
    unsigned long long g;
    while (true) {
      g = __hip_atomic_load(gp, RLX_AGENT);
      if (__all((unsigned)(g >> 32) == tag)) break;
      if (now32() - t0 > SPIN_TICKS) break;
      __builtin_amdgcn_s_sleep(127);
    }
    float s = __uint_as_float((unsigned)g);
    s += dpp<XOR1>(s);
    const float gs = rbf(s), us = rbf(dpp<ROR8>(s));
    const float hv = rbf(rbf(gs / (1.f + __expf(-gs))) * us);
    const float h0 = lane_value(hv, 0), h1 = lane_value(hv, 16);
#pragma unroll
    for (int k = 0; k < 4; ++k) yacc[k] = __builtin_elementwise_fma(bf_unpack(w21[k]), f32x2{h1, h1}, bf_unpack(w20[k]) * h0);
  } else {
    FRESH();
    {
      const unsigned long long gt = __hip_atomic_load(ml + (lane * 4 + (lane & 3)) * 16 + 2, RLX_AGENT);
      const bool v = (unsigned)(gt >> 32) == tag;
      const float n = wave_sum(v ? 1.f : 0.f), d = wave_sum(v ? (float)(int)((unsigned)gt - tk) : 0.f);
      if (lane == 0 && n > 0.f) ((LAS unsigned*)lds)[L_REF] = tk + (unsigned)(int)(d / n);
    }
    const unsigned t0 = now32();
    unsigned long long gm, gl;
    while (true) {
      gm = __hip_atomic_load(ml + ((chunk & ~3) + (lane & 3)) * 16, RLX_AGENT);
      gl = __hip_atomic_load(ml + ((chunk & ~3) + (lane & 3)) * 16 + 1, RLX_AGENT);
      if (__all((unsigned)(gm >> 32) == tag && (unsigned)(gl >> 32) == tag)) break;
      if (now32() - t0 > SPIN_TICKS) break;
      __builtin_amdgcn_s_sleep(127);
    }
    const float ms = __uint_as_float((unsigned)gm), ls = __uint_as_float((unsigned)gl);
    float M = fmaxf(ms, dpp<XOR1>(ms)); M = fmaxf(M, dpp<XOR2>(M));
    float L = ls * __builtin_amdgcn_exp2f((ms - M) * LOG2E);
    L += dpp<XOR1>(L); L += dpp<XOR2>(L);
    if (lane < 4) lds[L_FW + lane] = __builtin_amdgcn_exp2f((mw - M) * LOG2E) / L;
    lds_wait();
    {
      const unsigned char* pa = (lane < 32 || red < 0) ? a.ws + WS_Y + (lane & 15) * YB + chunk * 128
                                                       : (const unsigned char*)a.out + (red & 1) * (DM * 2) + (red >> 1) * 64;
      const unsigned jk = __hip_atomic_load((gu32*)pa, RLX_AGENT);
      asm volatile("" :: "v"(jk));
    }
    if (blockIdx.x == NWG - 1) {
      while (true) {
        bool ok = true;
#pragma unroll
        for (int i = 0; i < 4; ++i) ok = ok & ((unsigned)(__hip_atomic_load(ml + (i * 64 + lane) * 16 + 2, RLX_AGENT) >> 32) == tag);
        if (__all(ok)) break;
        if (now32() - t0 > SPIN_TICKS) break;
        __builtin_amdgcn_s_sleep(127);
      }
      if (lane == 0) __hip_atomic_store((gu32*)(a.ws + WS_EPOCH), tag, RLX_AGENT);
    }
  }
  {
    FRESH();
    wg_barrier();
    {
      const LAS float* P = (const LAS float*)lds + L_O + (4 * r) * 128 + wave * 16 + c;
      const float o = xrow_sum(((P[0] + P[128]) + (P[256] + P[384])) * lds[L_FW + r]);
#pragma unroll
      for (int i = 0; i < 16; ++i) {
        const float oi = lane_value(o, i);
#pragma unroll
        for (int k = 0; k < 4; ++k) yacc[k] = __builtin_elementwise_fma(wf[i * 4 + k], f32x2{oi, oi}, yacc[k]);
      }
    }
    *(LAS f32x4*)(lds + L_Y + wave * 512 + lane * 8) = f32x4{yacc[0][0], yacc[0][1], yacc[1][0], yacc[1][1]};
    *(LAS f32x4*)(lds + L_Y + wave * 512 + lane * 8 + 4) = f32x4{yacc[2][0], yacc[2][1], yacc[3][0], yacc[3][1]};
    lds_wait();
    wg_barrier();
    const rsrc_t ry = mk_rsrc(a.ws + WS_Y, 16 * YB);
    {
      const LAS float* P = (const LAS float*)lds + L_Y + tid;
      const float y = ((P[0] + P[512]) + (P[1024] + P[1536])) + ((P[2048] + P[2560]) + (P[3072] + P[3584]));
      const unsigned u = __float_as_uint(y);
      const unsigned w = (((u & 0x7f800000u) == 0x7f800000u ? u | ((u & 0x7fffffu) ? 0x400000u : 0u) : u + 0x400u) & 0xfffff800u) | ((tag >> ((lane & 1) * 11)) & 0x7ffu);
      const unsigned w1 = __builtin_amdgcn_mov_dpp(w, XOR1, 0xf, 0xf, true);
      if (!(lane & 1)) __builtin_amdgcn_raw_buffer_store_b64(u32x2{w, w1}, ry, (tid >> 5) * YB + (tid & 31) * 4, chunk * 128, 16);
    }
    if (red >= 0) {
      const unsigned t0 = now32();
      u32x4 g0 = {0u, 0u, 0u, 0u}, g1 = {0u, 0u, 0u, 0u};
      bool ok0 = false, ok1 = false;
      const unsigned ta = tag & 0x7ffu, tb = (tag >> 11) & 0x7ffu;
      while (true) {
        if (!ok0) g0 = lddev(ry, voff, red * 16384 + wave * 2048);
        if (!ok1) g1 = lddev(ry, voff + 1024, red * 16384 + wave * 2048);
        ok0 = (((g0[0] ^ ta) | (g0[1] ^ tb) | (g0[2] ^ ta) | (g0[3] ^ tb)) & 0x7ffu) == 0u;
        ok1 = (((g1[0] ^ ta) | (g1[1] ^ tb) | (g1[2] ^ ta) | (g1[3] ^ tb)) & 0x7ffu) == 0u;
        if (__all(ok0 & ok1)) break;
        if (now32() - t0 > SPIN_TICKS) break;
        __builtin_amdgcn_s_sleep(4);
        asm volatile("" ::: "memory");
      }
      float t[4];
#pragma unroll
      for (int k = 0; k < 4; ++k) t[k] = __uint_as_float(g0[k] & 0xfffff800u) + __uint_as_float(g1[k] & 0xfffff800u);
      float s = tr16(tr32(t[0], t[2]), tr32(t[1], t[3]));
      s += dpp<ROR8>(s);
      if (!(lane & 8)) lds[L_FIN + wave * 32 + (lane & 7) * 4 + r] = s;
      lds_wait();
      wg_barrier();
      if (tid < 16) {
        const LAS f32x2* P = (const LAS f32x2*)(lds + L_FIN + tid * 2);
        const f32x2 o = bf_unpack(xw) + (((P[0] + P[16]) + (P[32] + P[48])) + ((P[64] + P[80]) + (P[96] + P[112])));
        *(gu32*)(a.out + (red & 1) * DM + (red >> 1) * 32 + tid * 2) = pack_bf16(o[0], o[1]);
      }
    }
  }
}

}

extern "C" void kernel_launch(void* const* d_in, const int* in_sizes, int n_in, void* d_out, int out_size, void* d_ws, size_t ws_size,
                              hipStream_t stream) {
  Args a;
  a.x = (const unsigned short*)d_in[0]; a.k = (const unsigned short*)d_in[1]; a.v = (const unsigned short*)d_in[2];
  a.wq = (const unsigned short*)d_in[3]; a.wo = (const unsigned short*)d_in[4];
  a.w1 = (const unsigned short*)d_in[6]; a.w3 = (const unsigned short*)d_in[7]; a.w2 = (const unsigned short*)d_in[8];
  a.out = (unsigned short*)d_out; a.ws = (unsigned char*)d_ws;
  fused_decode<<<NWG, NTHR, 0, stream>>>(a);
}
```

```cpp
#include <hip/hip_runtime.h>
#include <hip/hip_bf16.h>
#include <stdint.h>

namespace {

constexpr int NWG = 256, NTHR = 768, NRED = 32;
constexpr int DM = 512, NH = 32, SK = 16384, DH = 128, FF = 1792;
constexpr float LOG2E = 1.4426950408889634f;
constexpr float SCALE = 0.08838834764831845f;
constexpr int WIN = 4;
constexpr int NPRE = 3;
constexpr int LAG_A = 155, LAG_B = 109, LAG_2 = 80, WIDE_T = 3280;
constexpr size_t WS_EPOCH = 0;
constexpr size_t WS_ML = 8192;
constexpr size_t WS_Y = 65536;
constexpr int YB = 32768;
constexpr size_t WS_Q = WS_Y + 16 * YB;
constexpr size_t WS_G = WS_Q + (128u << 10);
constexpr int L_S = 0;
constexpr int L_O = L_S + 4096;
constexpr int L_Y = L_O + 2048;
constexpr int L_P = L_Y + 4096;
constexpr int L_Q = L_P + 1024;
constexpr int L_MX = L_Q + 64;
constexpr int L_FW = L_MX + 4;
constexpr int L_FIN = L_FW + 4;
constexpr int L_CNT = L_FIN + 256;
constexpr int L_REF = L_CNT + 3;
constexpr int L_END = L_REF + 1;

#define GAS __attribute__((address_space(1)))
#define LAS __attribute__((address_space(3)))
#define RLX_AGENT __ATOMIC_RELAXED, __HIP_MEMORY_SCOPE_AGENT
#define RLX_WG __ATOMIC_RELAXED, __HIP_MEMORY_SCOPE_WORKGROUP
typedef unsigned u32x2 __attribute__((ext_vector_type(2)));
typedef unsigned u32x4 __attribute__((ext_vector_type(4)));
typedef float f32x2 __attribute__((ext_vector_type(2)));
typedef float f32x4 __attribute__((ext_vector_type(4)));
typedef __bf16 bf16x2 __attribute__((ext_vector_type(2)));
typedef GAS unsigned gu32;
typedef GAS unsigned long long gu64;
typedef __amdgpu_buffer_rsrc_t rsrc_t;

__device__ __forceinline__ float bf_lo(unsigned u) { return __builtin_bit_cast(float, u << 16); }
__device__ __forceinline__ float bf_hi(unsigned u) { return __builtin_bit_cast(float, u & 0xffff0000u); }
__device__ __forceinline__ f32x2 bf_unpack(unsigned u) { return f32x2{bf_lo(u), bf_hi(u)}; }
__device__ __forceinline__ unsigned pack_bf16(float lo, float hi) {
  bf16x2 r = {(__bf16)lo, (__bf16)hi};
  return __builtin_bit_cast(unsigned, r);
}
__device__ __forceinline__ float rbf(float x) { return (float)(__bf16)x; }
__device__ __forceinline__ float dot2(unsigned a, unsigned b, float acc) {
  return __builtin_amdgcn_fdot2_f32_bf16(__builtin_bit_cast(bf16x2, a), __builtin_bit_cast(bf16x2, b), acc, false);
}
template <int CTRL> __device__ __forceinline__ float dpp(float x) {
  return __builtin_bit_cast(float, __builtin_amdgcn_mov_dpp(__builtin_bit_cast(int, x), CTRL, 0xf, 0xf, true));
}
constexpr int XOR1 = 0xB1, XOR2 = 0x4E, XOR3 = 0x1B, XOR7 = 0x141, ROR8 = 0x128;
__device__ __forceinline__ float tr16(float a, float b) {
  auto s = __builtin_amdgcn_permlane16_swap(__float_as_uint(a), __float_as_uint(b), false, false);
  return __uint_as_float(s[0]) + __uint_as_float(s[1]);
}
__device__ __forceinline__ float tr32(float a, float b) {
  auto s = __builtin_amdgcn_permlane32_swap(__float_as_uint(a), __float_as_uint(b), false, false);
  return __uint_as_float(s[0]) + __uint_as_float(s[1]);
}
__device__ __forceinline__ float xrow_sum(float x) { x = tr16(x, x); return tr32(x, x); }
__device__ __forceinline__ float wave_sum(float x) {
  x += dpp<XOR1>(x); x += dpp<XOR2>(x); x += dpp<XOR7>(x); x += dpp<ROR8>(x);
  return xrow_sum(x);
}
__device__ __forceinline__ float wave_max(float x) {
  x = fmaxf(x, dpp<XOR1>(x)); x = fmaxf(x, dpp<XOR2>(x)); x = fmaxf(x, dpp<XOR7>(x)); x = fmaxf(x, dpp<ROR8>(x));
  auto s = __builtin_amdgcn_permlane16_swap(__float_as_uint(x), __float_as_uint(x), false, false);
  x = fmaxf(__uint_as_float(s[0]), __uint_as_float(s[1]));
  auto t = __builtin_amdgcn_permlane32_swap(__float_as_uint(x), __float_as_uint(x), false, false);
  return fmaxf(__uint_as_float(t[0]), __uint_as_float(t[1]));
}
__device__ __forceinline__ float lane_value(float x, int l) { return __builtin_bit_cast(float, __builtin_amdgcn_readlane(__builtin_bit_cast(int, x), l)); }
__device__ __forceinline__ void lds_wait() { asm volatile("s_waitcnt lgkmcnt(0)" ::: "memory"); }
__device__ __forceinline__ void wg_barrier() { __builtin_amdgcn_s_barrier(); asm volatile("" ::: "memory"); }
__device__ __forceinline__ void st_granule(gu64* g, unsigned tag, float v) {
  __hip_atomic_store(g, ((unsigned long long)tag << 32) | (unsigned long long)__float_as_uint(v), RLX_AGENT);
}
__device__ __forceinline__ rsrc_t mk_rsrc(const void* p, int bytes) { return __builtin_amdgcn_make_buffer_rsrc((void*)p, 0, bytes, 0x00020000); }
__device__ __forceinline__ u32x4 ldnt(rsrc_t r, int voff, int soff) { return __builtin_amdgcn_raw_buffer_load_b128(r, voff, soff, 2); }
__device__ __forceinline__ u32x4 lddev(rsrc_t r, int voff, int soff) { return __builtin_amdgcn_raw_buffer_load_b128(r, voff, soff, 16); }
__device__ __forceinline__ u32x4 lddef(rsrc_t r, int voff, int soff) { return __builtin_amdgcn_raw_buffer_load_b128(r, voff, soff, 0); }
__device__ __forceinline__ unsigned now32() { return (unsigned)__builtin_amdgcn_s_memrealtime(); }
constexpr unsigned SPIN_TICKS = 20000000u;

#define SB() __builtin_amdgcn_sched_barrier(0)
#define VM_WAIT(n) do { asm volatile("s_waitcnt vmcnt(%0)" :: "i"(n)); SB(); } while (0)
#define FRESH() int lane_ = lane0; asm volatile("" : "+v"(lane_)); \
  const int lane = lane_, r = lane >> 4, c = lane & 15, tid = wave * 64 + lane, voff = lane * 16; (void)r; (void)c; (void)tid; (void)voff

struct Args {
  const unsigned short* x; const unsigned short* k; const unsigned short* v; const unsigned short* wq; const unsigned short* wo;
  const unsigned short* w1; const unsigned short* w3; const unsigned short* w2;
  unsigned short* out; unsigned char* ws;
};

__global__ void __launch_bounds__(NTHR, 3) fused_decode(Args a) {
  __shared__ __attribute__((aligned(16))) float lds[L_END];
  const int tid = threadIdx.x, lane = tid & 63, wave = __builtin_amdgcn_readfirstlane(tid >> 6);
  const int r = lane >> 4, c = lane & 15;
  const int xg = blockIdx.x & 7, j = blockIdx.x >> 3;
  const int b = j >> 4, h = xg + ((j >> 2) & 3) * 8, sp = j & 3;
  const int chunk = b * 128 + h * 4 + sp;
  const int voff = lane * 16;

  const int g8 = b * 4 + sp;
  const int prow = (g8 >> 2) * 256 + wave * 32 + 2 * (lane >> 2);
  u32x4 xv0, xv1, wq0, wq1;
  unsigned xq0, xq1;
  if (wave < 8) {
    xv0 = *(const GAS u32x4*)(a.x + lane * 8); xv1 = *(const GAS u32x4*)(a.x + DM + lane * 8);
    xq0 = *(const gu32*)(a.x + prow); xq1 = *(const gu32*)(a.x + DM + prow);
    const rsrc_t rq = mk_rsrc(a.wq, DM * NH * DH * 2);
    wq0 = ldnt(rq, prow * (NH * DH * 2) + (lane & 3) * 16, h * (DH * 2) + (g8 & 3) * 64);
    wq1 = ldnt(rq, prow * (NH * DH * 2) + (lane & 3) * 16, h * (DH * 2) + (g8 & 3) * 64 + NH * DH * 2);
    SB();
    wg_barrier();
  }
  int role = wave;
  asm volatile("" : "+s"(role));
  if (__builtin_expect(role >= 8, 1)) {
    const int sw = wave - 8;
    constexpr int HB = SK * DH * 2;
    const unsigned short* kp = a.k + (size_t)(chunk >> 2) * SK * DH;
    const unsigned short* vp = a.v + (size_t)(chunk >> 2) * SK * DH;
    const rsrc_t rk = mk_rsrc(kp, HB), rv = mk_rsrc(vp, HB);
    const int voffh = voff + HB / 2;
    auto window = [&](int g) -> int { g += (j >> 3) * 2; return ((g & 1) << 19) + ((g & 14) << 15) + ((g & 16) << 16) + (sp << 14) + (sw << 12); };
#define ROWVO(n) ((((n) & 1) ? voffh : voff) + (((n) & 7) >> 1) * 1024)
#define ROWSO(grp, n) window((grp) + ((n) >> 3))
#define LDROW(rs, grp, n) ldnt(rs, ROWVO(n), ROWSO(grp, n))
    __builtin_amdgcn_s_setprio(2);
    u32x4 buf[8], pre[NPRE * 8];
#pragma unroll
    for (int u = 0; u < NPRE * 8; ++u) {
      pre[u] = LDROW(rk, 0, u); SB();
      if (u == WIN) {
        if (lane < 4) ((LAS unsigned*)lds)[L_CNT + lane] = lane < 3 ? 0u : now32() + 0x40000000u;
        lds_wait();
        wg_barrier();
        SB();
      }
      if (u >= WIN) VM_WAIT(WIN);
    }
#pragma unroll
    for (int u = 0; u < 8; ++u) {
      buf[u] = LDROW(rk, NPRE, u); SB();
      if (u < 7) VM_WAIT(WIN);
    }
    wg_barrier();
    const u32x4 qp = *(const LAS u32x4*)(lds + L_Q + c * 4);
    LAS float* S = (LAS float*)lds + L_S + sw * 1024;
    float mx = -INFINITY;
#define DOT(w) dot2((w)[3], qp[3], dot2((w)[2], qp[2], dot2((w)[1], qp[1], dot2((w)[0], qp[0], 0.f))))
#define K_FIN(grp) do { \
      _Pragma("unroll") for (int u = 0; u < 4; ++u) { const float lo = x[u] + dpp<ROR8>(x[u]), hi = x[u + 4] + dpp<ROR8>(x[u + 4]); x[u] = (c & 8) ? hi : lo; } \
      _Pragma("unroll") for (int u = 0; u < 2; ++u) { const float lo = x[u] + dpp<XOR7>(x[u]), hi = x[u + 2] + dpp<XOR7>(x[u + 2]); x[u] = (c & 4) ? hi : lo; } \
      { const float lo = x[0] + dpp<XOR2>(x[0]), hi = x[1] + dpp<XOR2>(x[1]); x[0] = (c & 2) ? hi : lo; } \
      float sc = x[0] + dpp<XOR1>(x[0]); \
      sc = rbf(rbf(sc) * SCALE); \
      mx = fmaxf(mx, sc); \
      S[(grp) * 32 + r * 8 + (c >> 1)] = sc; \
      SB(); \
    } while (0)
    static_assert(WIN >= 3 && WIN <= 5, "ring of 8 rows");
    {
      float x[8];
#pragma unroll
      for (int u = 0; u < 8; ++u) {
        if (u > 0 && u <= WIN + 1) { buf[u - 1] = LDROW(rk, NPRE + 1, u - 1); SB(); }
        x[u] = DOT(buf[u]);
        SB();
      }
      K_FIN(NPRE);
    }
#pragma unroll
    for (int pp = 0; pp < NPRE; ++pp) {
      float x[8];
#pragma unroll
      for (int w = 0; w < 8; ++w) x[w] = DOT(pre[pp * 8 + w]);
      K_FIN(pp);
      if (WIN + 1 + pp < 7) { VM_WAIT(WIN); buf[WIN + 1 + pp] = LDROW(rk, NPRE + 1, WIN + 1 + pp); SB(); }
    }
    static_assert(WIN + 1 + NPRE >= 7, "");
    int last = 31;
    asm volatile("" : "+s"(last));
#pragma nounroll
    for (int tt = NPRE + 1; tt <= last; ++tt) {
      const rsrc_t rn = mk_rsrc(tt == last ? vp : kp, HB);
      float x[8];
#pragma unroll
      for (int u = 0; u < 8; ++u) {
        const int n = u + 7;
        const int so = ROWSO(tt, n);
        asm volatile("s_waitcnt vmcnt(%0)" :: "i"(WIN), "s"(so));
        SB();
        buf[n & 7] = ldnt(n >= 8 ? rn : rk, ROWVO(n), so);
        SB();
        x[u] = DOT(buf[u]);
        SB();
      }
      K_FIN(tt);
    }
    mx = wave_max(mx);
    if (lane == 0) lds[L_MX + sw] = mx;
    lds_wait();
    if (lane == 0) __hip_atomic_fetch_add((LAS unsigned*)lds + L_CNT, 1u, RLX_WG);
    f32x2 acc[4] = {{0.f, 0.f}, {0.f, 0.f}, {0.f, 0.f}, {0.f, 0.f}};
    f32x4 s0 = *(const LAS f32x4*)(S + r * 8), s1;
    int open = -1, lag = -1;
#pragma nounroll
    for (int tt = 0; tt <= last; ++tt) {
      const rsrc_t rn = mk_rsrc(vp, tt == last ? 0 : HB);
      unsigned ref, tnow;
#pragma unroll
      for (int u = 0; u < 8; ++u) {
        const int n = u + 7;
        const int so = ROWSO(tt, n);
        asm volatile("s_cmp_gt_i32 %0, 0\n\ts_cbranch_scc1 1f\n\ts_waitcnt vmcnt(%2)\n\ts_cmp_gt_i32 %1, 0\n\ts_cbranch_scc1 1f\n\ts_waitcnt vmcnt(%3)\n1:"
                     :: "s"(open), "s"(lag), "i"(WIN + 2), "i"(WIN), "s"(so) : "scc");
        SB();
        buf[n & 7] = ldnt(n >= 8 ? rn : rv, ROWVO(n), so);
        SB();
        if (u == 0) s1 = *(const LAS f32x4*)(S + tt * 32 + r * 8 + 4);
        if (u == 4) { s0 = *(const LAS f32x4*)(S + ((tt + 1) & 31) * 32 + r * 8); ref = *(volatile LAS unsigned*)(lds + L_REF); tnow = now32(); }
        const float p = __builtin_amdgcn_exp2f(((u < 4 ? s0[u & 3] : s1[u & 3]) - mx) * LOG2E);
#pragma unroll
        for (int k = 0; k < 4; ++k) acc[k] = __builtin_elementwise_fma(bf_unpack(buf[u][k]), f32x2{p, p}, acc[k]);
        asm volatile("" : "+v"(acc[0]), "+v"(acc[1]), "+v"(acc[2]), "+v"(acc[3]));
        if (u == 6) {
          asm volatile("" : "+v"(ref), "+s"(tnow));
          const int el = (int)(tnow - __builtin_amdgcn_readfirstlane(ref));
          lag = el - (LAG_A + tt * LAG_B);
          open = max(el - WIDE_T, lag - LAG_2);
        }
        SB();
      }
    }
    *(LAS f32x4*)(lds + L_O + (sw * 4 + r) * 128 + c * 8) = f32x4{acc[0][0], acc[0][1], acc[1][0], acc[1][1]};
    *(LAS f32x4*)(lds + L_O + (sw * 4 + r) * 128 + c * 8 + 4) = f32x4{acc[2][0], acc[2][1], acc[3][0], acc[3][1]};
    lds_wait();
    wg_barrier();
    wg_barrier();
    if (blockIdx.x < NRED) wg_barrier();
    return;
  }

  unsigned epoch = *(const __attribute__((address_space(4))) unsigned*)(a.ws + WS_EPOCH);
  SB();
  float mb0, mb1, sd0, sd1;
  {
    float f0[8], f1[8];
#pragma unroll
    for (int k = 0; k < 4; ++k) { f0[2 * k] = bf_lo(xv0[k]); f0[2 * k + 1] = bf_hi(xv0[k]); f1[2 * k] = bf_lo(xv1[k]); f1[2 * k + 1] = bf_hi(xv1[k]); }
    float s0 = 0.f, s1 = 0.f;
#pragma unroll
    for (int k = 0; k < 8; ++k) { s0 += f0[k]; s1 += f1[k]; }
    const float m0 = wave_sum(s0) * (1.f / DM), m1 = wave_sum(s1) * (1.f / DM);
    float v0 = 0.f, v1 = 0.f;
#pragma unroll
    for (int k = 0; k < 8; ++k) { const float d0 = f0[k] - m0, d1 = f1[k] - m1; v0 = fmaf(d0, d0, v0); v1 = fmaf(d1, d1, v1); }
    v0 = wave_sum(v0) * (1.f / DM); v1 = wave_sum(v1) * (1.f / DM);
    mb0 = rbf(m0); sd0 = rbf(__builtin_sqrtf(rbf(rbf(v0) + 1e-5f)));
    mb1 = rbf(m1); sd1 = rbf(__builtin_sqrtf(rbf(rbf(v1) + 1e-5f)));
  }
#define XN(xf, mb, sd) rbf(rbf((xf) - (mb)) / (sd))
  asm volatile("" : "+s"(epoch));
  const unsigned tag = epoch + 0x9E3779B9u;
  const float xa0 = XN(bf_lo(xq0), mb0, sd0), xa1 = XN(bf_hi(xq0), mb0, sd0), xb0 = XN(bf_lo(xq1), mb1, sd1), xb1 = XN(bf_hi(xq1), mb1, sd1);
#define PARTIAL(w0_, w1_, n, res) do { \
    float t[8]; \
    _Pragma("unroll") for (int k = 0; k < 4; ++k) { \
      const f32x2 w0 = bf_unpack((w0_)[k]), w1 = bf_unpack((w1_)[k]); \
      const f32x2 qa = __builtin_elementwise_fma(w1, f32x2{xa1, xa1}, w0 * xa0), qb = __builtin_elementwise_fma(w1, f32x2{xb1, xb1}, w0 * xb0); \
      t[2 * k] = tr32(qa[0], qb[0]); t[2 * k + 1] = tr32(qa[1], qb[1]);             \
    } \
    _Pragma("unroll") for (int k = 0; k < 4; ++k) t[k] = tr16(t[k], t[4 + k]);        \
    f32x2 o; \
    _Pragma("unroll") for (int k = 0; k < 2; ++k) { const float lo = t[k] + dpp<ROR8>(t[k]), hi = t[2 + k] + dpp<ROR8>(t[2 + k]); o[k] = (c & 8) ? hi : lo; } \
    const float lo = o[0] + dpp<XOR7>(dpp<XOR3>(o[0])), hi = o[1] + dpp<XOR7>(dpp<XOR3>(o[1]));     \
    lds[L_P + (n) * 512 + wave * 64 + (r >> 1) * 32 + (c & 3) * 8 + (r & 1) * 4 + (c >> 3) * 2 + ((c >> 2) & 1)] = (c & 4) ? hi : lo; \
    lds_wait(); \
    if (lane == 0) __hip_atomic_fetch_add((LAS unsigned*)lds + L_CNT + 1 + (n), 1u, RLX_WG); \
    while (__hip_atomic_load((LAS unsigned*)lds + L_CNT + 1 + (n), RLX_WG) < 8u) __builtin_amdgcn_s_sleep(0); \
    asm volatile("" ::: "memory"); \
    const LAS float* P = (const LAS float*)lds + L_P + (n) * 512 + lane; \
    res = ((P[0] + P[64]) + (P[128] + P[192])) + ((P[256] + P[320]) + (P[384] + P[448])); \
  } while (0)
  {
    float qs;
    PARTIAL(wq0, wq1, 0, qs);
    if (wave == 0) st_granule((gu64*)(a.ws + WS_Q) + (h * 8 + g8) * 64 + lane, tag, qs);
  }
  if (wave == 7) {
    __builtin_amdgcn_s_sleep(32);
    const rsrc_t rg = mk_rsrc(a.ws + WS_Q + (size_t)h * 4096, 4096);
    const unsigned t0 = now32();
    u32x4 g0, g1;
    while (true) {
      g0 = lddev(rg, r * 512 + c * 16, b * 256); g1 = lddev(rg, r * 512 + c * 16, 2048 + b * 256);
      if (__all((g0[1] == tag) & (g0[3] == tag) & (g1[1] == tag) & (g1[3] == tag))) break;
      if (now32() - t0 > SPIN_TICKS) break;
      __builtin_amdgcn_s_sleep(8);
      asm volatile("" ::: "memory");
    }
    ((LAS unsigned*)lds)[L_Q + lane] = pack_bf16(__uint_as_float(g0[0]) + __uint_as_float(g1[0]), __uint_as_float(g0[2]) + __uint_as_float(g1[2]));
    lds_wait();
  }
  wg_barrier();
  const int red = blockIdx.x < NRED ? (xg * 2 + (j & 1)) * 2 + (j >> 1) : -1;
  const unsigned xw = (red >= 0 && tid < 16) ? *(const gu32*)(a.x + (red & 1) * DM + (red >> 1) * 32 + tid * 2) : 0u;
  float gsum = 0.f;
  if (h < 28) {
    const rsrc_t rm = mk_rsrc((g8 & 2) ? a.w3 : a.w1, DM * FF * 2);
    const int so = h * 128 + (g8 & 1) * 64;
    const u32x4 wm0 = ldnt(rm, prow * (FF * 2) + (lane & 3) * 16, so), wm1 = ldnt(rm, prow * (FF * 2) + (lane & 3) * 16, so + FF * 2);
    PARTIAL(wm0, wm1, 1, gsum);
  }
  f32x2 wf[64];
  {
    const rsrc_t ro = mk_rsrc(a.wo + (size_t)h * DH * DM, DH * DM * 2);
#pragma unroll
    for (int i = 0; i < 16; i += 2) {
      const u32x4 w0 = lddef(ro, voff + (i & 3) * 1024, (wave * 16 + (i & ~3)) * 1024), w1 = lddef(ro, voff + (i & 3) * 1024 + 1024, (wave * 16 + (i & ~3)) * 1024);
      SB();
#pragma unroll
      for (int k = 0; k < 4; ++k) {
        wf[i * 4 + k] = bf_unpack(w0[k]); wf[i * 4 + 4 + k] = bf_unpack(w1[k]);
        asm volatile("" : "+v"(wf[i * 4 + k]), "+v"(wf[i * 4 + 4 + k]));
      }
      SB();
    }
  }
  const int lane0 = lane;
  while (__hip_atomic_load((LAS unsigned*)lds + L_CNT, RLX_WG) < 4u) __builtin_amdgcn_s_sleep(8);
  asm volatile("" ::: "memory");
  gu64* ml = (gu64*)(a.ws + WS_ML);
  gu64* G = (gu64*)(a.ws + WS_G);
  float mw = 0.f;
  const unsigned tk = now32();
  if (wave == 7) {
    FRESH();
    mw = lds[L_MX + (lane & 3)];
    float m = fmaxf(mw, dpp<XOR1>(mw)); m = fmaxf(m, dpp<XOR2>(m));
    float l = 0.f;
#pragma unroll 2
    for (int i = 0; i < 16; ++i) {
      const f32x4 sv = *(const LAS f32x4*)(lds + L_S + i * 256 + lane * 4);
#pragma unroll
      for (int k = 0; k < 4; ++k) l += __builtin_amdgcn_exp2f((sv[k] - m) * LOG2E);
    }
    l = wave_sum(l);
    if (lane < 16) st_granule(ml + chunk * 16 + lane, tag, lane == 0 ? m : lane == 1 ? l : __uint_as_float(tk));
  } else if (wave == 0 && h < 28) {
    FRESH();
    __builtin_amdgcn_s_sleep(16);
    st_granule(G + (h * 8 + g8) * 64 + lane, tag, gsum);
  }
  __builtin_amdgcn_s_sleep(127); __builtin_amdgcn_s_sleep(16);
  f32x2 yacc[4] = {{0.f, 0.f}, {0.f, 0.f}, {0.f, 0.f}, {0.f, 0.f}};
  if (wave < 7) {
    FRESH();
    const int f0 = (h * 4 + sp) * 14 + wave * 2;
    const rsrc_t r2 = mk_rsrc(a.w2 + (size_t)f0 * DM, 2 * DM * 2);
    const u32x4 w20 = lddef(r2, voff, 0), w21 = lddef(r2, voff + 1024, 0);
    const int f = f0 + (r & 1);
    gu64* gp = G + ((f >> 6) * 8 + (c & 1) * 4 + (c >> 3) * 2 + ((f >> 5) & 1)) * 64 + b * 32 + (f & 31);
    const unsigned t0 = now32();
    unsigned long long g;
    while (true) {
      g = __hip_atomic_load(gp, RLX_AGENT);
      if (__all((unsigned)(g >> 32) == tag)) break;
      if (now32() - t0 > SPIN_TICKS) break;
      __builtin_amdgcn_s_sleep(127);
    }
    float s = __uint_as_float((unsigned)g);
    s += dpp<XOR1>(s);
    const float gs = rbf(s), us = rbf(dpp<ROR8>(s));
    const float hv = rbf(rbf(gs / (1.f + __expf(-gs))) * us);
    const float h0 = lane_value(hv, 0), h1 = lane_value(hv, 16);
#pragma unroll
    for (int k = 0; k < 4; ++k) yacc[k] = __builtin_elementwise_fma(bf_unpack(w21[k]), f32x2{h1, h1}, bf_unpack(w20[k]) * h0);
  } else {
    FRESH();
    {
      const unsigned long long gt = __hip_atomic_load(ml + (lane * 4 + (lane & 3)) * 16 + 2, RLX_AGENT);
      const bool v = (unsigned)(gt >> 32) == tag;
      const float n = wave_sum(v ? 1.f : 0.f), d = wave_sum(v ? (float)(int)((unsigned)gt - tk) : 0.f);
      if (lane == 0 && n > 0.f) ((LAS unsigned*)lds)[L_REF] = tk + (unsigned)(int)(d / n);
    }
    const unsigned t0 = now32();
    unsigned long long gm, gl;
    while (true) {
      gm = __hip_atomic_load(ml + ((chunk & ~3) + (lane & 3)) * 16, RLX_AGENT);
      gl = __hip_atomic_load(ml + ((chunk & ~3) + (lane & 3)) * 16 + 1, RLX_AGENT);
      if (__all((unsigned)(gm >> 32) == tag && (unsigned)(gl >> 32) == tag)) break;
      if (now32() - t0 > SPIN_TICKS) break;
      __builtin_amdgcn_s_sleep(127);
    }
    const float ms = __uint_as_float((unsigned)gm), ls = __uint_as_float((unsigned)gl);
    float M = fmaxf(ms, dpp<XOR1>(ms)); M = fmaxf(M, dpp<XOR2>(M));
    float L = ls * __builtin_amdgcn_exp2f((ms - M) * LOG2E);
    L += dpp<XOR1>(L); L += dpp<XOR2>(L);
    if (lane < 4) lds[L_FW + lane] = __builtin_amdgcn_exp2f((mw - M) * LOG2E) / L;
    lds_wait();
    {
      const unsigned char* pa = (lane < 32 || red < 0) ? a.ws + WS_Y + (lane & 15) * YB + chunk * 128
                                                       : (const unsigned char*)a.out + (red & 1) * (DM * 2) + (red >> 1) * 64;
      const unsigned jk = __hip_atomic_load((gu32*)pa, RLX_AGENT);
      asm volatile("" :: "v"(jk));
    }
    if (blockIdx.x == NWG - 1) {
      while (true) {
        bool ok = true;
#pragma unroll
        for (int i = 0; i < 4; ++i) ok = ok & ((unsigned)(__hip_atomic_load(ml + (i * 64 + lane) * 16 + 2, RLX_AGENT) >> 32) == tag);
        if (__all(ok)) break;
        if (now32() - t0 > SPIN_TICKS) break;
        __builtin_amdgcn_s_sleep(127);
      }
      if (lane == 0) __hip_atomic_store((gu32*)(a.ws + WS_EPOCH), tag, RLX_AGENT);
    }
  }
  {
    FRESH();
    wg_barrier();
    {
      const LAS float* P = (const LAS float*)lds + L_O + (4 * r) * 128 + wave * 16 + c;
      const float o = xrow_sum(((P[0] + P[128]) + (P[256] + P[384])) * lds[L_FW + r]);
#pragma unroll
      for (int i = 0; i < 16; ++i) {
        const float oi = lane_value(o, i);
#pragma unroll
        for (int k = 0; k < 4; ++k) yacc[k] = __builtin_elementwise_fma(wf[i * 4 + k], f32x2{oi, oi}, yacc[k]);
      }
    }
    *(LAS f32x4*)(lds + L_Y + wave * 512 + lane * 8) = f32x4{yacc[0][0], yacc[0][1], yacc[1][0], yacc[1][1]};
    *(LAS f32x4*)(lds + L_Y + wave * 512 + lane * 8 + 4) = f32x4{yacc[2][0], yacc[2][1], yacc[3][0], yacc[3][1]};
    lds_wait();
    wg_barrier();
    const rsrc_t ry = mk_rsrc(a.ws + WS_Y, 16 * YB);
    {
      const LAS float* P = (const LAS float*)lds + L_Y + tid;
      const float y = ((P[0] + P[512]) + (P[1024] + P[1536])) + ((P[2048] + P[2560]) + (P[3072] + P[3584]));
      const unsigned u = __float_as_uint(y);
      const unsigned w = (((u & 0x7f800000u) == 0x7f800000u ? u | ((u & 0x7fffffu) ? 0x400000u : 0u) : u + 0x400u) & 0xfffff800u) | ((tag >> ((lane & 1) * 11)) & 0x7ffu);
      const unsigned w1 = __builtin_amdgcn_mov_dpp(w, XOR1, 0xf, 0xf, true);
      if (!(lane & 1)) __builtin_amdgcn_raw_buffer_store_b64(u32x2{w, w1}, ry, (tid >> 5) * YB + (tid & 31) * 4, chunk * 128, 16);
    }
    if (red >= 0) {
      const unsigned t0 = now32();
      u32x4 g0 = {0u, 0u, 0u, 0u}, g1 = {0u, 0u, 0u, 0u};
      bool ok0 = false, ok1 = false;
      const unsigned ta = tag & 0x7ffu, tb = (tag >> 11) & 0x7ffu;
      while (true) {
        if (!ok0) g0 = lddev(ry, voff, red * 16384 + wave * 2048);
        if (!ok1) g1 = lddev(ry, voff + 1024, red * 16384 + wave * 2048);
        ok0 = (((g0[0] ^ ta) | (g0[1] ^ tb) | (g0[2] ^ ta) | (g0[3] ^ tb)) & 0x7ffu) == 0u;
        ok1 = (((g1[0] ^ ta) | (g1[1] ^ tb) | (g1[2] ^ ta) | (g1[3] ^ tb)) & 0x7ffu) == 0u;
        if (__all(ok0 & ok1)) break;
        if (now32() - t0 > SPIN_TICKS) break;
        __builtin_amdgcn_s_sleep(4);
        asm volatile("" ::: "memory");
      }
      float t[4];
#pragma unroll
      for (int k = 0; k < 4; ++k) t[k] = __uint_as_float(g0[k] & 0xfffff800u) + __uint_as_float(g1[k] & 0xfffff800u);
      float s = tr16(tr32(t[0], t[2]), tr32(t[1], t[3]));
      s += dpp<ROR8>(s);
      if (!(lane & 8)) lds[L_FIN + wave * 32 + (lane & 7) * 4 + r] = s;
      lds_wait();
      wg_barrier();
      if (tid < 16) {
        const LAS f32x2* P = (const LAS f32x2*)(lds + L_FIN + tid * 2);
        const f32x2 o = bf_unpack(xw) + (((P[0] + P[16]) + (P[32] + P[48])) + ((P[64] + P[80]) + (P[96] + P[112])));
        *(gu32*)(a.out + (red & 1) * DM + (red >> 1) * 32 + tid * 2) = pack_bf16(o[0], o[1]);
      }
    }
  }
}

}

extern "C" void kernel_launch(void* const* d_in, const int* in_sizes, int n_in, void* d_out, int out_size, void* d_ws, size_t ws_size,
                              hipStream_t stream) {
  Args a;
  a.x = (const unsigned short*)d_in[0]; a.k = (const unsigned short*)d_in[1]; a.v = (const unsigned short*)d_in[2];
  a.wq = (const unsigned short*)d_in[3]; a.wo = (const unsigned short*)d_in[4];
  a.w1 = (const unsigned short*)d_in[6]; a.w3 = (const unsigned short*)d_in[7]; a.w2 = (const unsigned short*)d_in[8];
  a.out = (unsigned short*)d_out; a.ws = (unsigned char*)d_ws;
  fused_decode<<<NWG, NTHR, 0, stream>>>(a);
}
```

```cpp
#include <hip/hip_runtime.h>
#include <hip/hip_bf16.h>
#include <stdint.h>

namespace {

constexpr int NWG = 256, NTHR = 768, NRED = 32;
constexpr int DM = 512, NH = 32, SK = 16384, DH = 128, FF = 1792;
constexpr float LOG2E = 1.4426950408889634f;
constexpr float SCALE = 0.08838834764831845f;
constexpr int WIN = 4;
constexpr int NPRE = 3;
constexpr int LAG_A = 125, LAG_B = 108, LAG_2 = 80, WIDE_T = 3280;
constexpr size_t WS_EPOCH = 0;
constexpr size_t WS_ML = 8192;
constexpr size_t WS_Y = 65536;
constexpr size_t WS_Q = WS_Y + NWG * 2048;
constexpr size_t WS_G = WS_Q + (128u << 10);
constexpr int L_S = 0;
constexpr int L_O = L_S + 4096;
constexpr int L_Y = L_O + 2048;
constexpr int L_P = L_Y + 4096;
constexpr int L_Q = L_P + 1024;
constexpr int L_MX = L_Q + 64;
constexpr int L_FW = L_MX + 4;
constexpr int L_FIN = L_FW + 4;
constexpr int L_CNT = L_FIN + 256;
constexpr int L_REF = L_CNT + 3;
constexpr int L_END = L_REF + 1;

#define GAS __attribute__((address_space(1)))
#define LAS __attribute__((address_space(3)))
#define RLX_AGENT __ATOMIC_RELAXED, __HIP_MEMORY_SCOPE_AGENT
#define RLX_WG __ATOMIC_RELAXED, __HIP_MEMORY_SCOPE_WORKGROUP
typedef unsigned u32x2 __attribute__((ext_vector_type(2)));
typedef unsigned u32x4 __attribute__((ext_vector_type(4)));
typedef float f32x2 __attribute__((ext_vector_type(2)));
typedef float f32x4 __attribute__((ext_vector_type(4)));
typedef __bf16 bf16x2 __attribute__((ext_vector_type(2)));
typedef GAS unsigned gu32;
typedef GAS unsigned long long gu64;
typedef __amdgpu_buffer_rsrc_t rsrc_t;

__device__ __forceinline__ float bf_lo(unsigned u) { return __builtin_bit_cast(float, u << 16); }
__device__ __forceinline__ float bf_hi(unsigned u) { return __builtin_bit_cast(float, u & 0xffff0000u); }
__device__ __forceinline__ f32x2 bf_unpack(unsigned u) { return f32x2{bf_lo(u), bf_hi(u)}; }
__device__ __forceinline__ unsigned pack_bf16(float lo, float hi) {
  bf16x2 r = {(__bf16)lo, (__bf16)hi};
  return __builtin_bit_cast(unsigned, r);
}
__device__ __forceinline__ float rbf(float x) { return (float)(__bf16)x; }
__device__ __forceinline__ float dot2(unsigned a, unsigned b, float acc) {
  return __builtin_amdgcn_fdot2_f32_bf16(__builtin_bit_cast(bf16x2, a), __builtin_bit_cast(bf16x2, b), acc, false);
}
template <int CTRL> __device__ __forceinline__ float dpp(float x) {
  return __builtin_bit_cast(float, __builtin_amdgcn_mov_dpp(__builtin_bit_cast(int, x), CTRL, 0xf, 0xf, true));
}
constexpr int XOR1 = 0xB1, XOR2 = 0x4E, XOR3 = 0x1B, XOR7 = 0x141, ROR8 = 0x128;
__device__ __forceinline__ float tr16(float a, float b) {
  auto s = __builtin_amdgcn_permlane16_swap(__float_as_uint(a), __float_as_uint(b), false, false);
  return __uint_as_float(s[0]) + __uint_as_float(s[1]);
}
__device__ __forceinline__ float tr32(float a, float b) {
  auto s = __builtin_amdgcn_permlane32_swap(__float_as_uint(a), __float_as_uint(b), false, false);
  return __uint_as_float(s[0]) + __uint_as_float(s[1]);
}
__device__ __forceinline__ float xrow_sum(float x) { x = tr16(x, x); return tr32(x, x); }
__device__ __forceinline__ float wave_sum(float x) {
  x += dpp<XOR1>(x); x += dpp<XOR2>(x); x += dpp<XOR7>(x); x += dpp<ROR8>(x);
  return xrow_sum(x);
}
__device__ __forceinline__ float wave_max(float x) {
  x = fmaxf(x, dpp<XOR1>(x)); x = fmaxf(x, dpp<XOR2>(x)); x = fmaxf(x, dpp<XOR7>(x)); x = fmaxf(x, dpp<ROR8>(x));
  auto s = __builtin_amdgcn_permlane16_swap(__float_as_uint(x), __float_as_uint(x), false, false);
  x = fmaxf(__uint_as_float(s[0]), __uint_as_float(s[1]));
  auto t = __builtin_amdgcn_permlane32_swap(__float_as_uint(x), __float_as_uint(x), false, false);
  return fmaxf(__uint_as_float(t[0]), __uint_as_float(t[1]));
}
__device__ __forceinline__ float lane_value(float x, int l) { return __builtin_bit_cast(float, __builtin_amdgcn_readlane(__builtin_bit_cast(int, x), l)); }
__device__ __forceinline__ void lds_wait() { asm volatile("s_waitcnt lgkmcnt(0)" ::: "memory"); }
__device__ __forceinline__ void wg_barrier() { __builtin_amdgcn_s_barrier(); asm volatile("" ::: "memory"); }
__device__ __forceinline__ void st_granule(gu64* g, unsigned tag, float v) {
  __hip_atomic_store(g, ((unsigned long long)tag << 32) | (unsigned long long)__float_as_uint(v), RLX_AGENT);
}
__device__ __forceinline__ rsrc_t mk_rsrc(const void* p, int bytes) { return __builtin_amdgcn_make_buffer_rsrc((void*)p, 0, bytes, 0x00020000); }
__device__ __forceinline__ u32x4 ldnt(rsrc_t r, int voff, int soff) { return __builtin_amdgcn_raw_buffer_load_b128(r, voff, soff, 2); }
__device__ __forceinline__ u32x4 lddev(rsrc_t r, int voff, int soff) { return __builtin_amdgcn_raw_buffer_load_b128(r, voff, soff, 16); }
__device__ __forceinline__ u32x4 lddef(rsrc_t r, int voff, int soff) { return __builtin_amdgcn_raw_buffer_load_b128(r, voff, soff, 0); }
__device__ __forceinline__ unsigned now32() { return (unsigned)__builtin_amdgcn_s_memrealtime(); }
constexpr unsigned SPIN_TICKS = 20000000u;

#define SB() __builtin_amdgcn_sched_barrier(0)
#define VM_WAIT(n) do { asm volatile("s_waitcnt vmcnt(%0)" :: "i"(n)); SB(); } while (0)
#define FRESH() int lane_ = lane0; asm volatile("" : "+v"(lane_)); \
  const int lane = lane_, r = lane >> 4, c = lane & 15, tid = wave * 64 + lane, voff = lane * 16; (void)r; (void)c; (void)tid; (void)voff

struct Args {
  const unsigned short* x; const unsigned short* k; const unsigned short* v; const unsigned short* wq; const unsigned short* wo;
  const unsigned short* w1; const unsigned short* w3; const unsigned short* w2;
  unsigned short* out; unsigned char* ws;
};

__global__ void __launch_bounds__(NTHR, 3) fused_decode(Args a) {
  __shared__ __attribute__((aligned(16))) float lds[L_END];
  const int tid = threadIdx.x, lane = tid & 63, wave = __builtin_amdgcn_readfirstlane(tid >> 6);
  const int r = lane >> 4, c = lane & 15;
  const int xg = blockIdx.x & 7, j = blockIdx.x >> 3;
  const int b = j >> 4, h = xg + ((j >> 2) & 3) * 8, sp = j & 3;
  const int chunk = b * 128 + h * 4 + sp;
  const int voff = lane * 16;
  asm volatile("" :: "s"(a.x), "s"(a.k), "s"(a.v), "s"(a.wq), "s"(a.ws));

  const int g8 = b * 4 + sp;
  const int prow = (g8 >> 2) * 256 + wave * 32 + 2 * (lane >> 2);
  u32x4 xv0, xv1, wq0, wq1;
  unsigned xq0, xq1;
  if (wave < 8) {
    xv0 = *(const GAS u32x4*)(a.x + lane * 8); xv1 = *(const GAS u32x4*)(a.x + DM + lane * 8);
    xq0 = *(const gu32*)(a.x + prow); xq1 = *(const gu32*)(a.x + DM + prow);
    const rsrc_t rq = mk_rsrc(a.wq, DM * NH * DH * 2);
    wq0 = ldnt(rq, prow * (NH * DH * 2) + (lane & 3) * 16, h * (DH * 2) + (g8 & 3) * 64);
    wq1 = ldnt(rq, prow * (NH * DH * 2) + (lane & 3) * 16, h * (DH * 2) + (g8 & 3) * 64 + NH * DH * 2);
    SB();
    wg_barrier();
  }
  int role = wave;
  asm volatile("" : "+s"(role));
  if (__builtin_expect(role >= 8, 1)) {
    const int sw = wave - 8;
    constexpr int HB = SK * DH * 2;
    const unsigned short* kp = a.k + (size_t)(chunk >> 2) * SK * DH;
    const unsigned short* vp = a.v + (size_t)(chunk >> 2) * SK * DH;
    const rsrc_t rk = mk_rsrc(kp, HB), rv = mk_rsrc(vp, HB);
    const int voffh = voff + HB / 2;
    auto window = [&](int g) -> int { g += (j >> 3) * 2; return ((g & 1) << 19) + ((g & 14) << 15) + ((g & 16) << 16) + (sp << 14) + (sw << 12); };
#define ROWVO(n) ((((n) & 1) ? voffh : voff) + (((n) & 7) >> 1) * 1024)
#define ROWSO(grp, n) window((grp) + ((n) >> 3))
#define LDROW(rs, grp, n) ldnt(rs, ROWVO(n), ROWSO(grp, n))
    __builtin_amdgcn_s_setprio(2);
    u32x4 buf[8], pre[NPRE * 8];
#pragma unroll
    for (int u = 0; u < NPRE * 8; ++u) {
      pre[u] = LDROW(rk, 0, u); SB();
      if (u == WIN) {
        if (lane < 4) ((LAS unsigned*)lds)[L_CNT + lane] = lane < 3 ? 0u : now32() + 0x40000000u;
        lds_wait();
        wg_barrier();
        SB();
      }
      if (u >= WIN) VM_WAIT(WIN);
    }
#pragma unroll
    for (int u = 0; u < 8; ++u) {
      buf[u] = LDROW(rk, NPRE, u); SB();
      if (u < 7) VM_WAIT(WIN);
    }
    wg_barrier();
    const u32x4 qp = *(const LAS u32x4*)(lds + L_Q + c * 4);
    LAS float* S = (LAS float*)lds + L_S + sw * 1024;
    float mx = -INFINITY;
#define DOT(w) dot2((w)[3], qp[3], dot2((w)[2], qp[2], dot2((w)[1], qp[1], dot2((w)[0], qp[0], 0.f))))
#define K_FIN(grp) do { \
      _Pragma("unroll") for (int u = 0; u < 4; ++u) { const float lo = x[u] + dpp<ROR8>(x[u]), hi = x[u + 4] + dpp<ROR8>(x[u + 4]); x[u] = (c & 8) ? hi : lo; } \
      _Pragma("unroll") for (int u = 0; u < 2; ++u) { const float lo = x[u] + dpp<XOR7>(x[u]), hi = x[u + 2] + dpp<XOR7>(x[u + 2]); x[u] = (c & 4) ? hi : lo; } \
      { const float lo = x[0] + dpp<XOR2>(x[0]), hi = x[1] + dpp<XOR2>(x[1]); x[0] = (c & 2) ? hi : lo; } \
      float sc = x[0] + dpp<XOR1>(x[0]); \
      sc = rbf(rbf(sc) * SCALE); \
      mx = fmaxf(mx, sc); \
      S[(grp) * 32 + r * 8 + (c >> 1)] = sc; \
      SB(); \
    } while (0)
    static_assert(WIN >= 3 && WIN <= 5, "ring of 8 rows");
    {
      float x[8];
#pragma unroll
      for (int u = 0; u < 8; ++u) {
        if (u > 0 && u <= WIN + 1) { buf[u - 1] = LDROW(rk, NPRE + 1, u - 1); SB(); }
        x[u] = DOT(buf[u]);
        SB();
      }
      K_FIN(NPRE);
    }
#pragma unroll
    for (int pp = 0; pp < NPRE; ++pp) {
      float x[8];
#pragma unroll
      for (int w = 0; w < 8; ++w) x[w] = DOT(pre[pp * 8 + w]);
      K_FIN(pp);
      if (WIN + 1 + pp < 7) { VM_WAIT(WIN); buf[WIN + 1 + pp] = LDROW(rk, NPRE + 1, WIN + 1 + pp); SB(); }
    }
    static_assert(WIN + 1 + NPRE >= 7, "");
    int last = 31;
    asm volatile("" : "+s"(last));
#pragma nounroll
    for (int tt = NPRE + 1; tt <= last; ++tt) {
      const rsrc_t rn = mk_rsrc(tt == last ? vp : kp, HB);
      float x[8];
#pragma unroll
      for (int u = 0; u < 8; ++u) {
        const int n = u + 7;
        const int so = ROWSO(tt, n);
        asm volatile("s_waitcnt vmcnt(%0)" :: "i"(WIN), "s"(so));
        SB();
        buf[n & 7] = ldnt(n >= 8 ? rn : rk, ROWVO(n), so);
        SB();
        x[u] = DOT(buf[u]);
        SB();
      }
      K_FIN(tt);
    }
    mx = wave_max(mx);
    if (lane == 0) lds[L_MX + sw] = mx;
    lds_wait();
    if (lane == 0) __hip_atomic_fetch_add((LAS unsigned*)lds + L_CNT, 1u, RLX_WG);
    f32x2 acc[4] = {{0.f, 0.f}, {0.f, 0.f}, {0.f, 0.f}, {0.f, 0.f}};
    f32x4 s0 = *(const LAS f32x4*)(S + r * 8), s1;
    int open = -1, lag = -1;
#pragma nounroll
    for (int tt = 0; tt <= last; ++tt) {
      const rsrc_t rn = mk_rsrc(vp, tt == last ? 0 : HB);
      unsigned ref, tnow;
#pragma unroll
      for (int u = 0; u < 8; ++u) {
        const int n = u + 7;
        const int so = ROWSO(tt, n);
        asm volatile("s_cmp_gt_i32 %0, 0\n\ts_cbranch_scc1 1f\n\ts_waitcnt vmcnt(%2)\n\ts_cmp_gt_i32 %1, 0\n\ts_cbranch_scc1 1f\n\ts_waitcnt vmcnt(%3)\n1:"
                     :: "s"(open), "s"(lag), "i"(WIN + 2), "i"(WIN), "s"(so) : "scc");
        SB();
        buf[n & 7] = ldnt(n >= 8 ? rn : rv, ROWVO(n), so);
        SB();
        if (u == 0) s1 = *(const LAS f32x4*)(S + tt * 32 + r * 8 + 4);
        if (u == 4) { s0 = *(const LAS f32x4*)(S + ((tt + 1) & 31) * 32 + r * 8); ref = *(volatile LAS unsigned*)(lds + L_REF); tnow = now32(); }
        const float p = __builtin_amdgcn_exp2f(((u < 4 ? s0[u & 3] : s1[u & 3]) - mx) * LOG2E);
#pragma unroll
        for (int k = 0; k < 4; ++k) acc[k] = __builtin_elementwise_fma(bf_unpack(buf[u][k]), f32x2{p, p}, acc[k]);
        asm volatile("" : "+v"(acc[0]), "+v"(acc[1]), "+v"(acc[2]), "+v"(acc[3]));
        if (u == 6) {
          asm volatile("" : "+v"(ref), "+s"(tnow));
          const int el = (int)(tnow - __builtin_amdgcn_readfirstlane(ref));
          lag = el - (LAG_A + tt * LAG_B);
          open = max(el - WIDE_T, lag - LAG_2);
        }
        SB();
      }
    }
    *(LAS f32x4*)(lds + L_O + (sw * 4 + r) * 128 + c * 8) = f32x4{acc[0][0], acc[0][1], acc[1][0], acc[1][1]};
    *(LAS f32x4*)(lds + L_O + (sw * 4 + r) * 128 + c * 8 + 4) = f32x4{acc[2][0], acc[2][1], acc[3][0], acc[3][1]};
    lds_wait();
    wg_barrier();
    wg_barrier();
    if (blockIdx.x < NRED) wg_barrier();
    return;
  }

  unsigned epoch = *(const __attribute__((address_space(4))) unsigned*)(a.ws + WS_EPOCH);
  SB();
  float mb0, mb1, sd0, sd1;
  {
    float f0[8], f1[8];
#pragma unroll
    for (int k = 0; k < 4; ++k) { f0[2 * k] = bf_lo(xv0[k]); f0[2 * k + 1] = bf_hi(xv0[k]); f1[2 * k] = bf_lo(xv1[k]); f1[2 * k + 1] = bf_hi(xv1[k]); }
    float s0 = 0.f, s1 = 0.f;
#pragma unroll
    for (int k = 0; k < 8; ++k) { s0 += f0[k]; s1 += f1[k]; }
    const float m0 = wave_sum(s0) * (1.f / DM), m1 = wave_sum(s1) * (1.f / DM);
    float v0 = 0.f, v1 = 0.f;
#pragma unroll
    for (int k = 0; k < 8; ++k) { const float d0 = f0[k] - m0, d1 = f1[k] - m1; v0 = fmaf(d0, d0, v0); v1 = fmaf(d1, d1, v1); }
    v0 = wave_sum(v0) * (1.f / DM); v1 = wave_sum(v1) * (1.f / DM);
    mb0 = rbf(m0); sd0 = rbf(__builtin_sqrtf(rbf(rbf(v0) + 1e-5f)));
    mb1 = rbf(m1); sd1 = rbf(__builtin_sqrtf(rbf(rbf(v1) + 1e-5f)));
  }
#define XN(xf, mb, sd) rbf(rbf((xf) - (mb)) / (sd))
  asm volatile("" : "+s"(epoch));
  const unsigned tag = epoch + 0x9E3779B9u;
  const float xa0 = XN(bf_lo(xq0), mb0, sd0), xa1 = XN(bf_hi(xq0), mb0, sd0), xb0 = XN(bf_lo(xq1), mb1, sd1), xb1 = XN(bf_hi(xq1), mb1, sd1);
#define PARTIAL(w0_, w1_, n, res) do { \
    float t[8]; \
    _Pragma("unroll") for (int k = 0; k < 4; ++k) { \
      const f32x2 w0 = bf_unpack((w0_)[k]), w1 = bf_unpack((w1_)[k]); \
      const f32x2 qa = __builtin_elementwise_fma(w1, f32x2{xa1, xa1}, w0 * xa0), qb = __builtin_elementwise_fma(w1, f32x2{xb1, xb1}, w0 * xb0); \
      t[2 * k] = tr32(qa[0], qb[0]); t[2 * k + 1] = tr32(qa[1], qb[1]);             \
    } \
    _Pragma("unroll") for (int k = 0; k < 4; ++k) t[k] = tr16(t[k], t[4 + k]);        \
    f32x2 o; \
    _Pragma("unroll") for (int k = 0; k < 2; ++k) { const float lo = t[k] + dpp<ROR8>(t[k]), hi = t[2 + k] + dpp<ROR8>(t[2 + k]); o[k] = (c & 8) ? hi : lo; } \
    const float lo = o[0] + dpp<XOR7>(dpp<XOR3>(o[0])), hi = o[1] + dpp<XOR7>(dpp<XOR3>(o[1]));     \
    lds[L_P + (n) * 512 + wave * 64 + (r >> 1) * 32 + (c & 3) * 8 + (r & 1) * 4 + (c >> 3) * 2 + ((c >> 2) & 1)] = (c & 4) ? hi : lo; \
    lds_wait(); \
    if (lane == 0) __hip_atomic_fetch_add((LAS unsigned*)lds + L_CNT + 1 + (n), 1u, RLX_WG); \
    while (__hip_atomic_load((LAS unsigned*)lds + L_CNT + 1 + (n), RLX_WG) < 8u) __builtin_amdgcn_s_sleep(0); \
    asm volatile("" ::: "memory"); \
    const LAS float* P = (const LAS float*)lds + L_P + (n) * 512 + lane; \
    res = ((P[0] + P[64]) + (P[128] + P[192])) + ((P[256] + P[320]) + (P[384] + P[448])); \
  } while (0)
  {
    float qs;
    PARTIAL(wq0, wq1, 0, qs);
    if (wave == 0) st_granule((gu64*)(a.ws + WS_Q) + (h * 8 + g8) * 64 + lane, tag, qs);
  }
  if (wave == 7) {
    __builtin_amdgcn_s_sleep(32);
    const rsrc_t rg = mk_rsrc(a.ws + WS_Q + (size_t)h * 4096, 4096);
    const unsigned t0 = now32();
    u32x4 g0, g1;
    while (true) {
      g0 = lddev(rg, r * 512 + c * 16, b * 256); g1 = lddev(rg, r * 512 + c * 16, 2048 + b * 256);
      if (__all((g0[1] == tag) & (g0[3] == tag) & (g1[1] == tag) & (g1[3] == tag))) break;
      if (now32() - t0 > SPIN_TICKS) break;
      __builtin_amdgcn_s_sleep(8);
      asm volatile("" ::: "memory");
    }
    ((LAS unsigned*)lds)[L_Q + lane] = pack_bf16(__uint_as_float(g0[0]) + __uint_as_float(g1[0]), __uint_as_float(g0[2]) + __uint_as_float(g1[2]));
    lds_wait();
  }
  wg_barrier();
  const int red = blockIdx.x < NRED ? (xg * 2 + (j & 1)) * 2 + (j >> 1) : -1;
  const unsigned xw = (red >= 0 && tid < 16) ? *(const gu32*)(a.x + (red & 1) * DM + (red >> 1) * 32 + tid * 2) : 0u;
  float gsum = 0.f;
  if (h < 28) {
    const rsrc_t rm = mk_rsrc((g8 & 2) ? a.w3 : a.w1, DM * FF * 2);
    const int so = h * 128 + (g8 & 1) * 64;
    const u32x4 wm0 = ldnt(rm, prow * (FF * 2) + (lane & 3) * 16, so), wm1 = ldnt(rm, prow * (FF * 2) + (lane & 3) * 16, so + FF * 2);
    PARTIAL(wm0, wm1, 1, gsum);
  }
  f32x2 wf[64];
  {
    const rsrc_t ro = mk_rsrc(a.wo + (size_t)h * DH * DM, DH * DM * 2);
#pragma unroll
    for (int i = 0; i < 16; ++i) {
      const u32x4 w = lddef(ro, voff + (i & 3) * 1024, (wave * 16 + (i & ~3)) * 1024);
      SB();
#pragma unroll
      for (int k = 0; k < 4; ++k) { wf[i * 4 + k] = bf_unpack(w[k]); asm volatile("" : "+v"(wf[i * 4 + k])); }
      SB();
    }
  }
  const int lane0 = lane;
  while (__hip_atomic_load((LAS unsigned*)lds + L_CNT, RLX_WG) < 4u) __builtin_amdgcn_s_sleep(8);
  asm volatile("" ::: "memory");
  gu64* ml = (gu64*)(a.ws + WS_ML);
  gu64* G = (gu64*)(a.ws + WS_G);
  float mw = 0.f;
  const unsigned tk = now32();
  if (wave == 7) {
    FRESH();
    mw = lds[L_MX + (lane & 3)];
    float m = fmaxf(mw, dpp<XOR1>(mw)); m = fmaxf(m, dpp<XOR2>(m));
    float l = 0.f;
#pragma unroll 2
    for (int i = 0; i < 16; ++i) {
      const f32x4 sv = *(const LAS f32x4*)(lds + L_S + i * 256 + lane * 4);
#pragma unroll
      for (int k = 0; k < 4; ++k) l += __builtin_amdgcn_exp2f((sv[k] - m) * LOG2E);
    }
    l = wave_sum(l);
    if (lane < 16) st_granule(ml + chunk * 16 + lane, tag, lane == 0 ? m : lane == 1 ? l : __uint_as_float(tk));
  } else if (wave == 0 && h < 28) {
    FRESH();
    __builtin_amdgcn_s_sleep(16);
    st_granule(G + (h * 8 + g8) * 64 + lane, tag, gsum);
  }
  __builtin_amdgcn_s_sleep(127); __builtin_amdgcn_s_sleep(16);
  f32x2 yacc[4] = {{0.f, 0.f}, {0.f, 0.f}, {0.f, 0.f}, {0.f, 0.f}};
  if (wave < 7) {
    FRESH();
    const int f0 = (h * 4 + sp) * 14 + wave * 2;
    const rsrc_t r2 = mk_rsrc(a.w2 + (size_t)f0 * DM, 2 * DM * 2);
    const u32x4 w20 = lddef(r2, voff, 0), w21 = lddef(r2, voff + 1024, 0);
    const int f = f0 + (r & 1);
    gu64* gp = G + ((f >> 6) * 8 + (c & 1) * 4 + (c >> 3) * 2 + ((f >> 5) & 1)) * 64 + b * 32 + (f & 31);
    const unsigned t0 = now32();
    unsigned long long g;
    while (true) {
      g = __hip_atomic_load(gp, RLX_AGENT);
      if (__all((unsigned)(g >> 32) == tag)) break;
      if (now32() - t0 > SPIN_TICKS) break;
      __builtin_amdgcn_s_sleep(127);
    }
    float s = __uint_as_float((unsigned)g);
    s += dpp<XOR1>(s);
    const float gs = rbf(s), us = rbf(dpp<ROR8>(s));
    const float hv = rbf(rbf(gs / (1.f + __expf(-gs))) * us);
    const float h0 = lane_value(hv, 0), h1 = lane_value(hv, 16);
#pragma unroll
    for (int k = 0; k < 4; ++k) yacc[k] = __builtin_elementwise_fma(bf_unpack(w21[k]), f32x2{h1, h1}, bf_unpack(w20[k]) * h0);
  } else {
    FRESH();
    {
      const unsigned long long gt = __hip_atomic_load(ml + (lane * 4 + (lane & 3)) * 16 + 2, RLX_AGENT);
      const bool v = (unsigned)(gt >> 32) == tag;
      const float n = wave_sum(v ? 1.f : 0.f), d = wave_sum(v ? (float)(int)((unsigned)gt - tk) : 0.f);
      if (lane == 0 && n > 0.f) ((LAS unsigned*)lds)[L_REF] = tk + (unsigned)(int)(d / n);
    }
    const unsigned t0 = now32();
    unsigned long long gm, gl;
    while (true) {
      gm = __hip_atomic_load(ml + ((chunk & ~3) + (lane & 3)) * 16, RLX_AGENT);
      gl = __hip_atomic_load(ml + ((chunk & ~3) + (lane & 3)) * 16 + 1, RLX_AGENT);
      if (__all((unsigned)(gm >> 32) == tag && (unsigned)(gl >> 32) == tag)) break;
      if (now32() - t0 > SPIN_TICKS) break;
      __builtin_amdgcn_s_sleep(127);
    }
    const float ms = __uint_as_float((unsigned)gm), ls = __uint_as_float((unsigned)gl);
    float M = fmaxf(ms, dpp<XOR1>(ms)); M = fmaxf(M, dpp<XOR2>(M));
    float L = ls * __builtin_amdgcn_exp2f((ms - M) * LOG2E);
    L += dpp<XOR1>(L); L += dpp<XOR2>(L);
    if (lane < 4) lds[L_FW + lane] = __builtin_amdgcn_exp2f((mw - M) * LOG2E) / L;
    lds_wait();
    {
      const unsigned char* pa = (lane < 32 || red < 0) ? a.ws + WS_Y + chunk * 2048 + (lane & 15) * 128
                                                       : (const unsigned char*)a.out + (red & 1) * (DM * 2) + (red >> 1) * 64;
      const unsigned jk = __hip_atomic_load((gu32*)pa, RLX_AGENT);
      asm volatile("" :: "v"(jk));
    }
    if (blockIdx.x == NWG - 1) {
      while (true) {
        bool ok = true;
#pragma unroll
        for (int i = 0; i < 4; ++i) ok = ok & ((unsigned)(__hip_atomic_load(ml + (i * 64 + lane) * 16 + 2, RLX_AGENT) >> 32) == tag);
        if (__all(ok)) break;
        if (now32() - t0 > SPIN_TICKS) break;
        __builtin_amdgcn_s_sleep(127);
      }
      if (lane == 0) __hip_atomic_store((gu32*)(a.ws + WS_EPOCH), tag, RLX_AGENT);
    }
  }
  {
    FRESH();
    wg_barrier();
    {
      const LAS float* P = (const LAS float*)lds + L_O + (4 * r) * 128 + wave * 16 + c;
      const float o = xrow_sum(((P[0] + P[128]) + (P[256] + P[384])) * lds[L_FW + r]);
#pragma unroll
      for (int i = 0; i < 16; ++i) {
        const float oi = lane_value(o, i);
#pragma unroll
        for (int k = 0; k < 4; ++k) yacc[k] = __builtin_elementwise_fma(wf[i * 4 + k], f32x2{oi, oi}, yacc[k]);
      }
    }
    *(LAS f32x4*)(lds + L_Y + wave * 512 + lane * 8) = f32x4{yacc[0][0], yacc[0][1], yacc[1][0], yacc[1][1]};
    *(LAS f32x4*)(lds + L_Y + wave * 512 + lane * 8 + 4) = f32x4{yacc[2][0], yacc[2][1], yacc[3][0], yacc[3][1]};
    lds_wait();
    wg_barrier();
    const rsrc_t ry = mk_rsrc(a.ws + WS_Y, NWG * 2048);
    {
      const LAS float* P = (const LAS float*)lds + L_Y + tid;
      const float y = ((P[0] + P[512]) + (P[1024] + P[1536])) + ((P[2048] + P[2560]) + (P[3072] + P[3584]));
      const unsigned u = __float_as_uint(y);
      const unsigned w = (((u & 0x7f800000u) == 0x7f800000u ? u | ((u & 0x7fffffu) ? 0x400000u : 0u) : u + 0x400u) & 0xfffff800u) | ((tag >> ((lane & 1) * 11)) & 0x7ffu);
      const unsigned w1 = __builtin_amdgcn_mov_dpp(w, XOR1, 0xf, 0xf, true);
      if (!(lane & 1)) __builtin_amdgcn_raw_buffer_store_b64(u32x2{w, w1}, ry, tid * 4, chunk * 2048, 16);
    }
    if (red >= 0) {
      const unsigned t0 = now32();
      u32x4 g0 = {0u, 0u, 0u, 0u}, g1 = {0u, 0u, 0u, 0u};
      bool ok0 = false, ok1 = false;
      const unsigned ta = tag & 0x7ffu, tb = (tag >> 11) & 0x7ffu;
      while (true) {
        if (!ok0) g0 = lddev(ry, (lane >> 3) * 2048 + (lane & 7) * 16, ((red & 1) * 128 + wave * 16) * 2048 + (red >> 1) * 128);
        if (!ok1) g1 = lddev(ry, (lane >> 3) * 2048 + (lane & 7) * 16, ((red & 1) * 128 + wave * 16 + 8) * 2048 + (red >> 1) * 128);
        ok0 = (((g0[0] ^ ta) | (g0[1] ^ tb) | (g0[2] ^ ta) | (g0[3] ^ tb)) & 0x7ffu) == 0u;
        ok1 = (((g1[0] ^ ta) | (g1[1] ^ tb) | (g1[2] ^ ta) | (g1[3] ^ tb)) & 0x7ffu) == 0u;
        if (__all(ok0 & ok1)) break;
        if (now32() - t0 > SPIN_TICKS) break;
        __builtin_amdgcn_s_sleep(4);
        asm volatile("" ::: "memory");
      }
      float t[4];
#pragma unroll
      for (int k = 0; k < 4; ++k) t[k] = __uint_as_float(g0[k] & 0xfffff800u) + __uint_as_float(g1[k] & 0xfffff800u);
      float s = tr16(tr32(t[0], t[2]), tr32(t[1], t[3]));
      s += dpp<ROR8>(s);
      if (!(lane & 8)) lds[L_FIN + wave * 32 + (lane & 7) * 4 + r] = s;
      lds_wait();
      wg_barrier();
      if (tid < 16) {
        const LAS f32x2* P = (const LAS f32x2*)(lds + L_FIN + tid * 2);
        const f32x2 o = bf_unpack(xw) + (((P[0] + P[16]) + (P[32] + P[48])) + ((P[64] + P[80]) + (P[96] + P[112])));
        *(gu32*)(a.out + (red & 1) * DM + (red >> 1) * 32 + tid * 2) = pack_bf16(o[0], o[1]);
      }
    }
  }
}

}

extern "C" void kernel_launch(void* const* d_in, const int* in_sizes, int n_in, void* d_out, int out_size, void* d_ws, size_t ws_size,
                              hipStream_t stream) {
  Args a;
  a.x = (const unsigned short*)d_in[0]; a.k = (const unsigned short*)d_in[1]; a.v = (const unsigned short*)d_in[2];
  a.wq = (const unsigned short*)d_in[3]; a.wo = (const unsigned short*)d_in[4];
  a.w1 = (const unsigned short*)d_in[6]; a.w3 = (const unsigned short*)d_in[7]; a.w2 = (const unsigned short*)d_in[8];
  a.out = (unsigned short*)d_out; a.ws = (unsigned char*)d_ws;
  fused_decode<<<NWG, NTHR, 0, stream>>>(a);
}
```

```cpp
#include <hip/hip_runtime.h>
#include <hip/hip_bf16.h>
#include <stdint.h>

namespace {

constexpr int NWG = 256, NTHR = 768, NRED = 32;
constexpr int DM = 512, NH = 32, SK = 16384, DH = 128, FF = 1792;
constexpr float LOG2E = 1.4426950408889634f;
constexpr float SCALE = 0.08838834764831845f;
constexpr int WIN = 4;
constexpr int NPRE = 3;
constexpr int LAG_A = 125, LAG_B = 108, LAG_2 = 80, WIDE_T = 3280;
constexpr size_t WS_EPOCH = 0;
constexpr size_t WS_ML = 8192;
constexpr size_t WS_Y = 65536;
constexpr size_t WS_Q = WS_Y + NWG * 2048;
constexpr size_t WS_G = WS_Q + (128u << 10);
constexpr int L_S = 0;
constexpr int L_O = L_S + 4096;
constexpr int L_Y = L_O + 2048;
constexpr int L_P = L_Y + 4096;
constexpr int L_Q = L_P + 1024;
constexpr int L_MX = L_Q + 64;
constexpr int L_FW = L_MX + 4;
constexpr int L_FIN = L_FW + 4;
constexpr int L_CNT = L_FIN + 256;
constexpr int L_REF = L_CNT + 3;
constexpr int L_END = L_REF + 1;

#define GAS __attribute__((address_space(1)))
#define LAS __attribute__((address_space(3)))
#define RLX_AGENT __ATOMIC_RELAXED, __HIP_MEMORY_SCOPE_AGENT
#define RLX_WG __ATOMIC_RELAXED, __HIP_MEMORY_SCOPE_WORKGROUP
typedef unsigned u32x2 __attribute__((ext_vector_type(2)));
typedef unsigned u32x4 __attribute__((ext_vector_type(4)));
typedef float f32x2 __attribute__((ext_vector_type(2)));
typedef float f32x4 __attribute__((ext_vector_type(4)));
typedef __bf16 bf16x2 __attribute__((ext_vector_type(2)));
typedef GAS unsigned gu32;
typedef GAS unsigned long long gu64;
typedef __amdgpu_buffer_rsrc_t rsrc_t;

__device__ __forceinline__ float bf_lo(unsigned u) { return __builtin_bit_cast(float, u << 16); }
__device__ __forceinline__ float bf_hi(unsigned u) { return __builtin_bit_cast(float, u & 0xffff0000u); }
__device__ __forceinline__ f32x2 bf_unpack(unsigned u) { return f32x2{bf_lo(u), bf_hi(u)}; }
__device__ __forceinline__ unsigned pack_bf16(float lo, float hi) {
  bf16x2 r = {(__bf16)lo, (__bf16)hi};
  return __builtin_bit_cast(unsigned, r);
}
__device__ __forceinline__ float rbf(float x) { return (float)(__bf16)x; }
__device__ __forceinline__ float dot2(unsigned a, unsigned b, float acc) {
  return __builtin_amdgcn_fdot2_f32_bf16(__builtin_bit_cast(bf16x2, a), __builtin_bit_cast(bf16x2, b), acc, false);
}
template <int CTRL> __device__ __forceinline__ float dpp(float x) {
  return __builtin_bit_cast(float, __builtin_amdgcn_mov_dpp(__builtin_bit_cast(int, x), CTRL, 0xf, 0xf, true));
}
constexpr int XOR1 = 0xB1, XOR2 = 0x4E, XOR3 = 0x1B, XOR7 = 0x141, ROR8 = 0x128;
__device__ __forceinline__ float tr16(float a, float b) {
  auto s = __builtin_amdgcn_permlane16_swap(__float_as_uint(a), __float_as_uint(b), false, false);
  return __uint_as_float(s[0]) + __uint_as_float(s[1]);
}
__device__ __forceinline__ float tr32(float a, float b) {
  auto s = __builtin_amdgcn_permlane32_swap(__float_as_uint(a), __float_as_uint(b), false, false);
  return __uint_as_float(s[0]) + __uint_as_float(s[1]);
}
__device__ __forceinline__ float xrow_sum(float x) { x = tr16(x, x); return tr32(x, x); }
__device__ __forceinline__ float wave_sum(float x) {
  x += dpp<XOR1>(x); x += dpp<XOR2>(x); x += dpp<XOR7>(x); x += dpp<ROR8>(x);
  return xrow_sum(x);
}
__device__ __forceinline__ float wave_max(float x) {
  x = fmaxf(x, dpp<XOR1>(x)); x = fmaxf(x, dpp<XOR2>(x)); x = fmaxf(x, dpp<XOR7>(x)); x = fmaxf(x, dpp<ROR8>(x));
  auto s = __builtin_amdgcn_permlane16_swap(__float_as_uint(x), __float_as_uint(x), false, false);
  x = fmaxf(__uint_as_float(s[0]), __uint_as_float(s[1]));
  auto t = __builtin_amdgcn_permlane32_swap(__float_as_uint(x), __float_as_uint(x), false, false);
  return fmaxf(__uint_as_float(t[0]), __uint_as_float(t[1]));
}
__device__ __forceinline__ float lane_value(float x, int l) { return __builtin_bit_cast(float, __builtin_amdgcn_readlane(__builtin_bit_cast(int, x), l)); }
__device__ __forceinline__ void lds_wait() { asm volatile("s_waitcnt lgkmcnt(0)" ::: "memory"); }
__device__ __forceinline__ void wg_barrier() { __builtin_amdgcn_s_barrier(); asm volatile("" ::: "memory"); }
__device__ __forceinline__ void st_granule(gu64* g, unsigned tag, float v) {
  __hip_atomic_store(g, ((unsigned long long)tag << 32) | (unsigned long long)__float_as_uint(v), RLX_AGENT);
}
__device__ __forceinline__ rsrc_t mk_rsrc(const void* p, int bytes) { return __builtin_amdgcn_make_buffer_rsrc((void*)p, 0, bytes, 0x00020000); }
__device__ __forceinline__ u32x4 ldnt(rsrc_t r, int voff, int soff) { return __builtin_amdgcn_raw_buffer_load_b128(r, voff, soff, 2); }
__device__ __forceinline__ u32x4 lddev(rsrc_t r, int voff, int soff) { return __builtin_amdgcn_raw_buffer_load_b128(r, voff, soff, 16); }
__device__ __forceinline__ u32x4 lddef(rsrc_t r, int voff, int soff) { return __builtin_amdgcn_raw_buffer_load_b128(r, voff, soff, 0); }
__device__ __forceinline__ unsigned now32() { return (unsigned)__builtin_amdgcn_s_memrealtime(); }
constexpr unsigned SPIN_TICKS = 20000000u;

#define SB() __builtin_amdgcn_sched_barrier(0)
#define VM_WAIT(n) do { asm volatile("s_waitcnt vmcnt(%0)" :: "i"(n)); SB(); } while (0)
#define FRESH() int lane_ = lane0; asm volatile("" : "+v"(lane_)); \
  const int lane = lane_, r = lane >> 4, c = lane & 15, tid = wave * 64 + lane, voff = lane * 16; (void)r; (void)c; (void)tid; (void)voff

struct Args {
  const unsigned short* x; const unsigned short* k; const unsigned short* v; const unsigned short* wq; const unsigned short* wo;
  const unsigned short* w1; const unsigned short* w3; const unsigned short* w2;
  unsigned short* out; unsigned char* ws;
};

__global__ void __launch_bounds__(NTHR, 3) fused_decode(Args a) {
  __shared__ __attribute__((aligned(16))) float lds[L_END];
  unsigned long long tA; asm volatile("s_memrealtime %0" : "=s"(tA)); SB();
  const int tid = threadIdx.x, lane = tid & 63, wave = __builtin_amdgcn_readfirstlane(tid >> 6);
  const int r = lane >> 4, c = lane & 15;
  const int xg = blockIdx.x & 7, j = blockIdx.x >> 3;
  const int b = j >> 4, h = xg + ((j >> 2) & 3) * 8, sp = j & 3;
  const int chunk = b * 128 + h * 4 + sp;
  const int voff = lane * 16;
  asm volatile("" :: "s"(a.x), "s"(a.k), "s"(a.v), "s"(a.wq), "s"(a.ws));
  SB(); unsigned long long tB; asm volatile("s_memrealtime %0\n s_waitcnt lgkmcnt(0)" : "=s"(tB)); SB();
  unsigned long long tC = 0;

  const int g8 = b * 4 + sp;
  const int prow = (g8 >> 2) * 256 + wave * 32 + 2 * (lane >> 2);
  u32x4 xv0, xv1, wq0, wq1;
  unsigned xq0, xq1;
  if (wave < 8) {
    xv0 = *(const GAS u32x4*)(a.x + lane * 8); xv1 = *(const GAS u32x4*)(a.x + DM + lane * 8);
    xq0 = *(const gu32*)(a.x + prow); xq1 = *(const gu32*)(a.x + DM + prow);
    const rsrc_t rq = mk_rsrc(a.wq, DM * NH * DH * 2);
    wq0 = ldnt(rq, prow * (NH * DH * 2) + (lane & 3) * 16, h * (DH * 2) + (g8 & 3) * 64);
    wq1 = ldnt(rq, prow * (NH * DH * 2) + (lane & 3) * 16, h * (DH * 2) + (g8 & 3) * 64 + NH * DH * 2);
    SB();
    wg_barrier();
  }
  int role = wave;
  asm volatile("" : "+s"(role));
  if (__builtin_expect(role >= 8, 1)) {
    const int sw = wave - 8;
    constexpr int HB = SK * DH * 2;
    const unsigned short* kp = a.k + (size_t)(chunk >> 2) * SK * DH;
    const unsigned short* vp = a.v + (size_t)(chunk >> 2) * SK * DH;
    const rsrc_t rk = mk_rsrc(kp, HB), rv = mk_rsrc(vp, HB);
    const int voffh = voff + HB / 2;
    auto window = [&](int g) -> int { g += (j >> 3) * 2; return ((g & 1) << 19) + ((g & 14) << 15) + ((g & 16) << 16) + (sp << 14) + (sw << 12); };
#define ROWVO(n) ((((n) & 1) ? voffh : voff) + (((n) & 7) >> 1) * 1024)
#define ROWSO(grp, n) window((grp) + ((n) >> 3))
#define LDROW(rs, grp, n) ldnt(rs, ROWVO(n), ROWSO(grp, n))
    __builtin_amdgcn_s_setprio(2);
    u32x4 buf[8], pre[NPRE * 8];
#pragma unroll
    for (int u = 0; u < NPRE * 8; ++u) {
      pre[u] = LDROW(rk, 0, u); SB();
      if (u == WIN) {
        if (lane < 4) ((LAS unsigned*)lds)[L_CNT + lane] = lane < 3 ? 0u : now32() + 0x40000000u;
        lds_wait();
        wg_barrier();
        SB();
      }
      if (u >= WIN) VM_WAIT(WIN);
      if (u == WIN) { asm volatile("s_memrealtime %0\n s_waitcnt lgkmcnt(0)" : "=s"(tC)); SB(); }
    }
#pragma unroll
    for (int u = 0; u < 8; ++u) {
      buf[u] = LDROW(rk, NPRE, u); SB();
      if (u < 7) VM_WAIT(WIN);
    }
    wg_barrier();
    const u32x4 qp = *(const LAS u32x4*)(lds + L_Q + c * 4);
    LAS float* S = (LAS float*)lds + L_S + sw * 1024;
    float mx = -INFINITY;
#define DOT(w) dot2((w)[3], qp[3], dot2((w)[2], qp[2], dot2((w)[1], qp[1], dot2((w)[0], qp[0], 0.f))))
#define K_FIN(grp) do { \
      _Pragma("unroll") for (int u = 0; u < 4; ++u) { const float lo = x[u] + dpp<ROR8>(x[u]), hi = x[u + 4] + dpp<ROR8>(x[u + 4]); x[u] = (c & 8) ? hi : lo; } \
      _Pragma("unroll") for (int u = 0; u < 2; ++u) { const float lo = x[u] + dpp<XOR7>(x[u]), hi = x[u + 2] + dpp<XOR7>(x[u + 2]); x[u] = (c & 4) ? hi : lo; } \
      { const float lo = x[0] + dpp<XOR2>(x[0]), hi = x[1] + dpp<XOR2>(x[1]); x[0] = (c & 2) ? hi : lo; } \
      float sc = x[0] + dpp<XOR1>(x[0]); \
      sc = rbf(rbf(sc) * SCALE); \
      mx = fmaxf(mx, sc); \
      S[(grp) * 32 + r * 8 + (c >> 1)] = sc; \
      SB(); \
    } while (0)
    static_assert(WIN >= 3 && WIN <= 5, "ring of 8 rows");
    {
      float x[8];
#pragma unroll
      for (int u = 0; u < 8; ++u) {
        if (u > 0 && u <= WIN + 1) { buf[u - 1] = LDROW(rk, NPRE + 1, u - 1); SB(); }
        x[u] = DOT(buf[u]);
        SB();
      }
      K_FIN(NPRE);
    }
#pragma unroll
    for (int pp = 0; pp < NPRE; ++pp) {
      float x[8];
#pragma unroll
      for (int w = 0; w < 8; ++w) x[w] = DOT(pre[pp * 8 + w]);
      K_FIN(pp);
      if (WIN + 1 + pp < 7) { VM_WAIT(WIN); buf[WIN + 1 + pp] = LDROW(rk, NPRE + 1, WIN + 1 + pp); SB(); }
    }
    static_assert(WIN + 1 + NPRE >= 7, "");
    int last = 31;
    asm volatile("" : "+s"(last));
#pragma nounroll
    for (int tt = NPRE + 1; tt <= last; ++tt) {
      const rsrc_t rn = mk_rsrc(tt == last ? vp : kp, HB);
      float x[8];
#pragma unroll
      for (int u = 0; u < 8; ++u) {
        const int n = u + 7;
        const int so = ROWSO(tt, n);
        asm volatile("s_waitcnt vmcnt(%0)" :: "i"(WIN), "s"(so));
        SB();
        buf[n & 7] = ldnt(n >= 8 ? rn : rk, ROWVO(n), so);
        SB();
        x[u] = DOT(buf[u]);
        SB();
      }
      K_FIN(tt);
    }
    mx = wave_max(mx);
    if (lane == 0) lds[L_MX + sw] = mx;
    lds_wait();
    if (lane == 0) __hip_atomic_fetch_add((LAS unsigned*)lds + L_CNT, 1u, RLX_WG);
    f32x2 acc[4] = {{0.f, 0.f}, {0.f, 0.f}, {0.f, 0.f}, {0.f, 0.f}};
    f32x4 s0 = *(const LAS f32x4*)(S + r * 8), s1;
    int open = -1, lag = -1;
#pragma nounroll
    for (int tt = 0; tt <= last; ++tt) {
      const rsrc_t rn = mk_rsrc(vp, tt == last ? 0 : HB);
      unsigned ref, tnow;
#pragma unroll
      for (int u = 0; u < 8; ++u) {
        const int n = u + 7;
        const int so = ROWSO(tt, n);
        asm volatile("s_cmp_gt_i32 %0, 0\n\ts_cbranch_scc1 1f\n\ts_waitcnt vmcnt(%2)\n\ts_cmp_gt_i32 %1, 0\n\ts_cbranch_scc1 1f\n\ts_waitcnt vmcnt(%3)\n1:"
                     :: "s"(open), "s"(lag), "i"(WIN + 2), "i"(WIN), "s"(so) : "scc");
        SB();
        buf[n & 7] = ldnt(n >= 8 ? rn : rv, ROWVO(n), so);
        SB();
        if (u == 0) s1 = *(const LAS f32x4*)(S + tt * 32 + r * 8 + 4);
        if (u == 4) { s0 = *(const LAS f32x4*)(S + ((tt + 1) & 31) * 32 + r * 8); ref = *(volatile LAS unsigned*)(lds + L_REF); tnow = now32(); }
        const float p = __builtin_amdgcn_exp2f(((u < 4 ? s0[u & 3] : s1[u & 3]) - mx) * LOG2E);
#pragma unroll
        for (int k = 0; k < 4; ++k) acc[k] = __builtin_elementwise_fma(bf_unpack(buf[u][k]), f32x2{p, p}, acc[k]);
        asm volatile("" : "+v"(acc[0]), "+v"(acc[1]), "+v"(acc[2]), "+v"(acc[3]));
        if (u == 6) {
          asm volatile("" : "+v"(ref), "+s"(tnow));
          const int el = (int)(tnow - __builtin_amdgcn_readfirstlane(ref));
          lag = el - (LAG_A + tt * LAG_B);
          open = max(el - WIDE_T, lag - LAG_2);
        }
        SB();
      }
    }
    *(LAS f32x4*)(lds + L_O + (sw * 4 + r) * 128 + c * 8) = f32x4{acc[0][0], acc[0][1], acc[1][0], acc[1][1]};
    *(LAS f32x4*)(lds + L_O + (sw * 4 + r) * 128 + c * 8 + 4) = f32x4{acc[2][0], acc[2][1], acc[3][0], acc[3][1]};
    lds_wait();
    wg_barrier();
    wg_barrier();
    if (blockIdx.x < NRED) wg_barrier();
    if (blockIdx.x == 100) while (now32() - (unsigned)tA < 20000u + 30u * 0u) __builtin_amdgcn_s_sleep(1);
    return;
  }

  unsigned epoch = *(const __attribute__((address_space(4))) unsigned*)(a.ws + WS_EPOCH);
  SB();
  float mb0, mb1, sd0, sd1;
  {
    float f0[8], f1[8];
#pragma unroll
    for (int k = 0; k < 4; ++k) { f0[2 * k] = bf_lo(xv0[k]); f0[2 * k + 1] = bf_hi(xv0[k]); f1[2 * k] = bf_lo(xv1[k]); f1[2 * k + 1] = bf_hi(xv1[k]); }
    float s0 = 0.f, s1 = 0.f;
#pragma unroll
    for (int k = 0; k < 8; ++k) { s0 += f0[k]; s1 += f1[k]; }
    const float m0 = wave_sum(s0) * (1.f / DM), m1 = wave_sum(s1) * (1.f / DM);
    float v0 = 0.f, v1 = 0.f;
#pragma unroll
    for (int k = 0; k < 8; ++k) { const float d0 = f0[k] - m0, d1 = f1[k] - m1; v0 = fmaf(d0, d0, v0); v1 = fmaf(d1, d1, v1); }
    v0 = wave_sum(v0) * (1.f / DM); v1 = wave_sum(v1) * (1.f / DM);
    mb0 = rbf(m0); sd0 = rbf(__builtin_sqrtf(rbf(rbf(v0) + 1e-5f)));
    mb1 = rbf(m1); sd1 = rbf(__builtin_sqrtf(rbf(rbf(v1) + 1e-5f)));
  }
#define XN(xf, mb, sd) rbf(rbf((xf) - (mb)) / (sd))
  asm volatile("" : "+s"(epoch));
  const unsigned tag = epoch + 0x9E3779B9u;
  const float xa0 = XN(bf_lo(xq0), mb0, sd0), xa1 = XN(bf_hi(xq0), mb0, sd0), xb0 = XN(bf_lo(xq1), mb1, sd1), xb1 = XN(bf_hi(xq1), mb1, sd1);
#define PARTIAL(w0_, w1_, n, res) do { \
    float t[8]; \
    _Pragma("unroll") for (int k = 0; k < 4; ++k) { \
      const f32x2 w0 = bf_unpack((w0_)[k]), w1 = bf_unpack((w1_)[k]); \
      const f32x2 qa = __builtin_elementwise_fma(w1, f32x2{xa1, xa1}, w0 * xa0), qb = __builtin_elementwise_fma(w1, f32x2{xb1, xb1}, w0 * xb0); \
      t[2 * k] = tr32(qa[0], qb[0]); t[2 * k + 1] = tr32(qa[1], qb[1]);             \
    } \
    _Pragma("unroll") for (int k = 0; k < 4; ++k) t[k] = tr16(t[k], t[4 + k]);        \
    f32x2 o; \
    _Pragma("unroll") for (int k = 0; k < 2; ++k) { const float lo = t[k] + dpp<ROR8>(t[k]), hi = t[2 + k] + dpp<ROR8>(t[2 + k]); o[k] = (c & 8) ? hi : lo; } \
    const float lo = o[0] + dpp<XOR7>(dpp<XOR3>(o[0])), hi = o[1] + dpp<XOR7>(dpp<XOR3>(o[1]));     \
    lds[L_P + (n) * 512 + wave * 64 + (r >> 1) * 32 + (c & 3) * 8 + (r & 1) * 4 + (c >> 3) * 2 + ((c >> 2) & 1)] = (c & 4) ? hi : lo; \
    lds_wait(); \
    if (lane == 0) __hip_atomic_fetch_add((LAS unsigned*)lds + L_CNT + 1 + (n), 1u, RLX_WG); \
    while (__hip_atomic_load((LAS unsigned*)lds + L_CNT + 1 + (n), RLX_WG) < 8u) __builtin_amdgcn_s_sleep(0); \
    asm volatile("" ::: "memory"); \
    const LAS float* P = (const LAS float*)lds + L_P + (n) * 512 + lane; \
    res = ((P[0] + P[64]) + (P[128] + P[192])) + ((P[256] + P[320]) + (P[384] + P[448])); \
  } while (0)
  {
    float qs;
    PARTIAL(wq0, wq1, 0, qs);
    if (wave == 0) st_granule((gu64*)(a.ws + WS_Q) + (h * 8 + g8) * 64 + lane, tag, qs);
  }
  if (wave == 7) {
    __builtin_amdgcn_s_sleep(32);
    const rsrc_t rg = mk_rsrc(a.ws + WS_Q + (size_t)h * 4096, 4096);
    const unsigned t0 = now32();
    u32x4 g0, g1;
    while (true) {
      g0 = lddev(rg, r * 512 + c * 16, b * 256); g1 = lddev(rg, r * 512 + c * 16, 2048 + b * 256);
      if (__all((g0[1] == tag) & (g0[3] == tag) & (g1[1] == tag) & (g1[3] == tag))) break;
      if (now32() - t0 > SPIN_TICKS) break;
      __builtin_amdgcn_s_sleep(8);
      asm volatile("" ::: "memory");
    }
    ((LAS unsigned*)lds)[L_Q + lane] = pack_bf16(__uint_as_float(g0[0]) + __uint_as_float(g1[0]), __uint_as_float(g0[2]) + __uint_as_float(g1[2]));
    lds_wait();
  }
  wg_barrier();
  const int red = blockIdx.x < NRED ? (xg * 2 + (j & 1)) * 2 + (j >> 1) : -1;
  const unsigned xw = (red >= 0 && tid < 16) ? *(const gu32*)(a.x + (red & 1) * DM + (red >> 1) * 32 + tid * 2) : 0u;
  float gsum = 0.f;
  if (h < 28) {
    const rsrc_t rm = mk_rsrc((g8 & 2) ? a.w3 : a.w1, DM * FF * 2);
    const int so = h * 128 + (g8 & 1) * 64;
    const u32x4 wm0 = ldnt(rm, prow * (FF * 2) + (lane & 3) * 16, so), wm1 = ldnt(rm, prow * (FF * 2) + (lane & 3) * 16, so + FF * 2);
    PARTIAL(wm0, wm1, 1, gsum);
  }
  f32x2 wf[64];
  {
    const rsrc_t ro = mk_rsrc(a.wo + (size_t)h * DH * DM, DH * DM * 2);
#pragma unroll
    for (int i = 0; i < 16; ++i) {
      const u32x4 w = lddef(ro, voff + (i & 3) * 1024, (wave * 16 + (i & ~3)) * 1024);
      SB();
#pragma unroll
      for (int k = 0; k < 4; ++k) { wf[i * 4 + k] = bf_unpack(w[k]); asm volatile("" : "+v"(wf[i * 4 + k])); }
      SB();
    }
  }
  const int lane0 = lane;
  while (__hip_atomic_load((LAS unsigned*)lds + L_CNT, RLX_WG) < 4u) __builtin_amdgcn_s_sleep(8);
  asm volatile("" ::: "memory");
  gu64* ml = (gu64*)(a.ws + WS_ML);
  gu64* G = (gu64*)(a.ws + WS_G);
  float mw = 0.f;
  const unsigned tk = now32();
  if (wave == 7) {
    FRESH();
    mw = lds[L_MX + (lane & 3)];
    float m = fmaxf(mw, dpp<XOR1>(mw)); m = fmaxf(m, dpp<XOR2>(m));
    float l = 0.f;
#pragma unroll 2
    for (int i = 0; i < 16; ++i) {
      const f32x4 sv = *(const LAS f32x4*)(lds + L_S + i * 256 + lane * 4);
#pragma unroll
      for (int k = 0; k < 4; ++k) l += __builtin_amdgcn_exp2f((sv[k] - m) * LOG2E);
    }
    l = wave_sum(l);
    if (lane < 16) st_granule(ml + chunk * 16 + lane, tag, lane == 0 ? m : lane == 1 ? l : __uint_as_float(tk));
  } else if (wave == 0 && h < 28) {
    FRESH();
    __builtin_amdgcn_s_sleep(16);
    st_granule(G + (h * 8 + g8) * 64 + lane, tag, gsum);
  }
  __builtin_amdgcn_s_sleep(127); __builtin_amdgcn_s_sleep(16);
  f32x2 yacc[4] = {{0.f, 0.f}, {0.f, 0.f}, {0.f, 0.f}, {0.f, 0.f}};
  if (wave < 7) {
    FRESH();
    const int f0 = (h * 4 + sp) * 14 + wave * 2;
    const rsrc_t r2 = mk_rsrc(a.w2 + (size_t)f0 * DM, 2 * DM * 2);
    const u32x4 w20 = lddef(r2, voff, 0), w21 = lddef(r2, voff + 1024, 0);
    const int f = f0 + (r & 1);
    gu64* gp = G + ((f >> 6) * 8 + (c & 1) * 4 + (c >> 3) * 2 + ((f >> 5) & 1)) * 64 + b * 32 + (f & 31);
    const unsigned t0 = now32();
    unsigned long long g;
    while (true) {
      g = __hip_atomic_load(gp, RLX_AGENT);
      if (__all((unsigned)(g >> 32) == tag)) break;
      if (now32() - t0 > SPIN_TICKS) break;
      __builtin_amdgcn_s_sleep(127);
    }
    float s = __uint_as_float((unsigned)g);
    s += dpp<XOR1>(s);
    const float gs = rbf(s), us = rbf(dpp<ROR8>(s));
    const float hv = rbf(rbf(gs / (1.f + __expf(-gs))) * us);
    const float h0 = lane_value(hv, 0), h1 = lane_value(hv, 16);
#pragma unroll
    for (int k = 0; k < 4; ++k) yacc[k] = __builtin_elementwise_fma(bf_unpack(w21[k]), f32x2{h1, h1}, bf_unpack(w20[k]) * h0);
  } else {
    FRESH();
    {
      const unsigned long long gt = __hip_atomic_load(ml + (lane * 4 + (lane & 3)) * 16 + 2, RLX_AGENT);
      const bool v = (unsigned)(gt >> 32) == tag;
      const float n = wave_sum(v ? 1.f : 0.f), d = wave_sum(v ? (float)(int)((unsigned)gt - tk) : 0.f);
      if (lane == 0 && n > 0.f) ((LAS unsigned*)lds)[L_REF] = tk + (unsigned)(int)(d / n);
    }
    const unsigned t0 = now32();
    unsigned long long gm, gl;
    while (true) {
      gm = __hip_atomic_load(ml + ((chunk & ~3) + (lane & 3)) * 16, RLX_AGENT);
      gl = __hip_atomic_load(ml + ((chunk & ~3) + (lane & 3)) * 16 + 1, RLX_AGENT);
      if (__all((unsigned)(gm >> 32) == tag && (unsigned)(gl >> 32) == tag)) break;
      if (now32() - t0 > SPIN_TICKS) break;
      __builtin_amdgcn_s_sleep(127);
    }
    const float ms = __uint_as_float((unsigned)gm), ls = __uint_as_float((unsigned)gl);
    float M = fmaxf(ms, dpp<XOR1>(ms)); M = fmaxf(M, dpp<XOR2>(M));
    float L = ls * __builtin_amdgcn_exp2f((ms - M) * LOG2E);
    L += dpp<XOR1>(L); L += dpp<XOR2>(L);
    if (lane < 4) lds[L_FW + lane] = __builtin_amdgcn_exp2f((mw - M) * LOG2E) / L;
    lds_wait();
    {
      const unsigned char* pa = (lane < 32 || red < 0) ? a.ws + WS_Y + chunk * 2048 + (lane & 15) * 128
                                                       : (const unsigned char*)a.out + (red & 1) * (DM * 2) + (red >> 1) * 64;
      const unsigned jk = __hip_atomic_load((gu32*)pa, RLX_AGENT);
      asm volatile("" :: "v"(jk));
    }
    if (blockIdx.x == NWG - 1) {
      while (true) {
        bool ok = true;
#pragma unroll
        for (int i = 0; i < 4; ++i) ok = ok & ((unsigned)(__hip_atomic_load(ml + (i * 64 + lane) * 16 + 2, RLX_AGENT) >> 32) == tag);
        if (__all(ok)) break;
        if (now32() - t0 > SPIN_TICKS) break;
        __builtin_amdgcn_s_sleep(127);
      }
      if (lane == 0) __hip_atomic_store((gu32*)(a.ws + WS_EPOCH), tag, RLX_AGENT);
    }
  }
  {
    FRESH();
    wg_barrier();
    {
      const LAS float* P = (const LAS float*)lds + L_O + (4 * r) * 128 + wave * 16 + c;
      const float o = xrow_sum(((P[0] + P[128]) + (P[256] + P[384])) * lds[L_FW + r]);
#pragma unroll
      for (int i = 0; i < 16; ++i) {
        const float oi = lane_value(o, i);
#pragma unroll
        for (int k = 0; k < 4; ++k) yacc[k] = __builtin_elementwise_fma(wf[i * 4 + k], f32x2{oi, oi}, yacc[k]);
      }
    }
    *(LAS f32x4*)(lds + L_Y + wave * 512 + lane * 8) = f32x4{yacc[0][0], yacc[0][1], yacc[1][0], yacc[1][1]};
    *(LAS f32x4*)(lds + L_Y + wave * 512 + lane * 8 + 4) = f32x4{yacc[2][0], yacc[2][1], yacc[3][0], yacc[3][1]};
    lds_wait();
    wg_barrier();
    const rsrc_t ry = mk_rsrc(a.ws + WS_Y, NWG * 2048);
    {
      const LAS float* P = (const LAS float*)lds + L_Y + tid;
      const float y = ((P[0] + P[512]) + (P[1024] + P[1536])) + ((P[2048] + P[2560]) + (P[3072] + P[3584]));
      const unsigned u = __float_as_uint(y);
      const unsigned w = (((u & 0x7f800000u) == 0x7f800000u ? u | ((u & 0x7fffffu) ? 0x400000u : 0u) : u + 0x400u) & 0xfffff800u) | ((tag >> ((lane & 1) * 11)) & 0x7ffu);
      const unsigned w1 = __builtin_amdgcn_mov_dpp(w, XOR1, 0xf, 0xf, true);
      if (!(lane & 1)) __builtin_amdgcn_raw_buffer_store_b64(u32x2{w, w1}, ry, tid * 4, chunk * 2048, 16);
    }
    if (red >= 0) {
      const unsigned t0 = now32();
      u32x4 g0 = {0u, 0u, 0u, 0u}, g1 = {0u, 0u, 0u, 0u};
      bool ok0 = false, ok1 = false;
      const unsigned ta = tag & 0x7ffu, tb = (tag >> 11) & 0x7ffu;
      while (true) {
        if (!ok0) g0 = lddev(ry, (lane >> 3) * 2048 + (lane & 7) * 16, ((red & 1) * 128 + wave * 16) * 2048 + (red >> 1) * 128);
        if (!ok1) g1 = lddev(ry, (lane >> 3) * 2048 + (lane & 7) * 16, ((red & 1) * 128 + wave * 16 + 8) * 2048 + (red >> 1) * 128);
        ok0 = (((g0[0] ^ ta) | (g0[1] ^ tb) | (g0[2] ^ ta) | (g0[3] ^ tb)) & 0x7ffu) == 0u;
        ok1 = (((g1[0] ^ ta) | (g1[1] ^ tb) | (g1[2] ^ ta) | (g1[3] ^ tb)) & 0x7ffu) == 0u;
        if (__all(ok0 & ok1)) break;
        if (now32() - t0 > SPIN_TICKS) break;
        __builtin_amdgcn_s_sleep(4);
        asm volatile("" ::: "memory");
      }
      float t[4];
#pragma unroll
      for (int k = 0; k < 4; ++k) t[k] = __uint_as_float(g0[k] & 0xfffff800u) + __uint_as_float(g1[k] & 0xfffff800u);
      float s = tr16(tr32(t[0], t[2]), tr32(t[1], t[3]));
      s += dpp<ROR8>(s);
      if (!(lane & 8)) lds[L_FIN + wave * 32 + (lane & 7) * 4 + r] = s;
      lds_wait();
      wg_barrier();
      if (tid < 16) {
        const LAS f32x2* P = (const LAS f32x2*)(lds + L_FIN + tid * 2);
        const f32x2 o = bf_unpack(xw) + (((P[0] + P[16]) + (P[32] + P[48])) + ((P[64] + P[80]) + (P[96] + P[112])));
        *(gu32*)(a.out + (red & 1) * DM + (red >> 1) * 32 + tid * 2) = pack_bf16(o[0], o[1]);
      }
    }
  }
}

}

extern "C" void kernel_launch(void* const* d_in, const int* in_sizes, int n_in, void* d_out, int out_size, void* d_ws, size_t ws_size,
                              hipStream_t stream) {
  Args a;
  a.x = (const unsigned short*)d_in[0]; a.k = (const unsigned short*)d_in[1]; a.v = (const unsigned short*)d_in[2];
  a.wq = (const unsigned short*)d_in[3]; a.wo = (const unsigned short*)d_in[4];
  a.w1 = (const unsigned short*)d_in[6]; a.w3 = (const unsigned short*)d_in[7]; a.w2 = (const unsigned short*)d_in[8];
  a.out = (unsigned short*)d_out; a.ws = (unsigned char*)d_ws;
  fused_decode<<<NWG, NTHR, 0, stream>>>(a);
}
```

```cpp
#include <hip/hip_runtime.h>
#include <hip/hip_bf16.h>
#include <stdint.h>

namespace {

constexpr int NWG = 256, NTHR = 768, NRED = 32;
constexpr int DM = 512, NH = 32, SK = 16384, DH = 128, FF = 1792;
constexpr float LOG2E = 1.4426950408889634f;
constexpr float SCALE = 0.08838834764831845f;
constexpr int WIN = 4;
constexpr int NPRE = 3;
constexpr int LAG_A = 125, LAG_B = 108, LAG_2 = 80, WIDE_T = 3280;
constexpr size_t WS_EPOCH = 0;
constexpr size_t WS_ML = 8192;
constexpr size_t WS_Y = 65536;
constexpr size_t WS_Q = WS_Y + NWG * 2048;
constexpr size_t WS_G = WS_Q + (128u << 10);
constexpr int L_S = 0;
constexpr int L_O = L_S + 4096;
constexpr int L_Y = L_O + 2048;
constexpr int L_P = L_Y + 4096;
constexpr int L_Q = L_P + 1024;
constexpr int L_MX = L_Q + 64;
constexpr int L_FW = L_MX + 4;
constexpr int L_FIN = L_FW + 4;
constexpr int L_CNT = L_FIN + 256;
constexpr int L_REF = L_CNT + 3;
constexpr int L_END = L_REF + 1;

#define GAS __attribute__((address_space(1)))
#define LAS __attribute__((address_space(3)))
#define RLX_AGENT __ATOMIC_RELAXED, __HIP_MEMORY_SCOPE_AGENT
#define RLX_WG __ATOMIC_RELAXED, __HIP_MEMORY_SCOPE_WORKGROUP
typedef unsigned u32x2 __attribute__((ext_vector_type(2)));
typedef unsigned u32x4 __attribute__((ext_vector_type(4)));
typedef float f32x2 __attribute__((ext_vector_type(2)));
typedef float f32x4 __attribute__((ext_vector_type(4)));
typedef __bf16 bf16x2 __attribute__((ext_vector_type(2)));
typedef GAS unsigned gu32;
typedef GAS unsigned long long gu64;
typedef __amdgpu_buffer_rsrc_t rsrc_t;

__device__ __forceinline__ float bf_lo(unsigned u) { return __builtin_bit_cast(float, u << 16); }
__device__ __forceinline__ float bf_hi(unsigned u) { return __builtin_bit_cast(float, u & 0xffff0000u); }
__device__ __forceinline__ f32x2 bf_unpack(unsigned u) { return f32x2{bf_lo(u), bf_hi(u)}; }
__device__ __forceinline__ unsigned pack_bf16(float lo, float hi) {
  bf16x2 r = {(__bf16)lo, (__bf16)hi};
  return __builtin_bit_cast(unsigned, r);
}
__device__ __forceinline__ float rbf(float x) { return (float)(__bf16)x; }
__device__ __forceinline__ float dot2(unsigned a, unsigned b, float acc) {
  return __builtin_amdgcn_fdot2_f32_bf16(__builtin_bit_cast(bf16x2, a), __builtin_bit_cast(bf16x2, b), acc, false);
}
template <int CTRL> __device__ __forceinline__ float dpp(float x) {
  return __builtin_bit_cast(float, __builtin_amdgcn_mov_dpp(__builtin_bit_cast(int, x), CTRL, 0xf, 0xf, true));
}
constexpr int XOR1 = 0xB1, XOR2 = 0x4E, XOR3 = 0x1B, XOR7 = 0x141, ROR8 = 0x128;
__device__ __forceinline__ float tr16(float a, float b) {
  auto s = __builtin_amdgcn_permlane16_swap(__float_as_uint(a), __float_as_uint(b), false, false);
  return __uint_as_float(s[0]) + __uint_as_float(s[1]);
}
__device__ __forceinline__ float tr32(float a, float b) {
  auto s = __builtin_amdgcn_permlane32_swap(__float_as_uint(a), __float_as_uint(b), false, false);
  return __uint_as_float(s[0]) + __uint_as_float(s[1]);
}
__device__ __forceinline__ float xrow_sum(float x) { x = tr16(x, x); return tr32(x, x); }
__device__ __forceinline__ float wave_sum(float x) {
  x += dpp<XOR1>(x); x += dpp<XOR2>(x); x += dpp<XOR7>(x); x += dpp<ROR8>(x);
  return xrow_sum(x);
}
__device__ __forceinline__ float wave_max(float x) {
  x = fmaxf(x, dpp<XOR1>(x)); x = fmaxf(x, dpp<XOR2>(x)); x = fmaxf(x, dpp<XOR7>(x)); x = fmaxf(x, dpp<ROR8>(x));
  auto s = __builtin_amdgcn_permlane16_swap(__float_as_uint(x), __float_as_uint(x), false, false);
  x = fmaxf(__uint_as_float(s[0]), __uint_as_float(s[1]));
  auto t = __builtin_amdgcn_permlane32_swap(__float_as_uint(x), __float_as_uint(x), false, false);
  return fmaxf(__uint_as_float(t[0]), __uint_as_float(t[1]));
}
__device__ __forceinline__ float lane_value(float x, int l) { return __builtin_bit_cast(float, __builtin_amdgcn_readlane(__builtin_bit_cast(int, x), l)); }
__device__ __forceinline__ void lds_wait() { asm volatile("s_waitcnt lgkmcnt(0)" ::: "memory"); }
__device__ __forceinline__ void wg_barrier() { __builtin_amdgcn_s_barrier(); asm volatile("" ::: "memory"); }
__device__ __forceinline__ void st_granule(gu64* g, unsigned tag, float v) {
  __hip_atomic_store(g, ((unsigned long long)tag << 32) | (unsigned long long)__float_as_uint(v), RLX_AGENT);
}
__device__ __forceinline__ rsrc_t mk_rsrc(const void* p, int bytes) { return __builtin_amdgcn_make_buffer_rsrc((void*)p, 0, bytes, 0x00020000); }
__device__ __forceinline__ u32x4 ldnt(rsrc_t r, int voff, int soff) { return __builtin_amdgcn_raw_buffer_load_b128(r, voff, soff, 2); }
__device__ __forceinline__ u32x4 lddev(rsrc_t r, int voff, int soff) { return __builtin_amdgcn_raw_buffer_load_b128(r, voff, soff, 16); }
__device__ __forceinline__ u32x4 lddef(rsrc_t r, int voff, int soff) { return __builtin_amdgcn_raw_buffer_load_b128(r, voff, soff, 0); }
__device__ __forceinline__ unsigned now32() { return (unsigned)__builtin_amdgcn_s_memrealtime(); }
constexpr unsigned SPIN_TICKS = 20000000u;

#define SB() __builtin_amdgcn_sched_barrier(0)
#define VM_WAIT(n) do { asm volatile("s_waitcnt vmcnt(%0)" :: "i"(n)); SB(); } while (0)
#define FRESH() int lane_ = lane0; asm volatile("" : "+v"(lane_)); \
  const int lane = lane_, r = lane >> 4, c = lane & 15, tid = wave * 64 + lane, voff = lane * 16; (void)r; (void)c; (void)tid; (void)voff

struct Args {
  const unsigned short* x; const unsigned short* k; const unsigned short* v; const unsigned short* wq; const unsigned short* wo;
  const unsigned short* w1; const unsigned short* w3; const unsigned short* w2;
  unsigned short* out; unsigned char* ws;
};

__global__ void __launch_bounds__(NTHR, 3) fused_decode(Args a) {
  __shared__ __attribute__((aligned(16))) float lds[L_END];
  const int tid = threadIdx.x, lane = tid & 63, wave = __builtin_amdgcn_readfirstlane(tid >> 6);
  const int r = lane >> 4, c = lane & 15;
  const int xg = blockIdx.x & 7, j = blockIdx.x >> 3;
  const int b = j >> 4, h = xg + ((j >> 2) & 3) * 8, sp = j & 3;
  const int chunk = b * 128 + h * 4 + sp;
  const int voff = lane * 16;
  asm volatile("" :: "s"(a.x), "s"(a.k), "s"(a.v), "s"(a.wq), "s"(a.ws));

  const int g8 = b * 4 + sp;
  const int prow = (g8 >> 2) * 256 + wave * 32 + 2 * (lane >> 2);
  u32x4 xv0, xv1, wq0, wq1;
  unsigned xq0, xq1, epoch;
  if (wave < 8) {
    xv0 = *(const GAS u32x4*)(a.x + lane * 8); xv1 = *(const GAS u32x4*)(a.x + DM + lane * 8);
    xq0 = *(const gu32*)(a.x + prow); xq1 = *(const gu32*)(a.x + DM + prow);
    const rsrc_t rq = mk_rsrc(a.wq, DM * NH * DH * 2);
    wq0 = ldnt(rq, prow * (NH * DH * 2) + (lane & 3) * 16, h * (DH * 2) + (g8 & 3) * 64);
    wq1 = ldnt(rq, prow * (NH * DH * 2) + (lane & 3) * 16, h * (DH * 2) + (g8 & 3) * 64 + NH * DH * 2);
    epoch = __hip_atomic_load((gu32*)(a.ws + WS_EPOCH), RLX_AGENT);
    SB();
    wg_barrier();
  }
  int role = wave;
  asm volatile("" : "+s"(role));
  if (__builtin_expect(role >= 8, 1)) {
    const int sw = wave - 8;
    constexpr int HB = SK * DH * 2;
    const unsigned short* kp = a.k + (size_t)(chunk >> 2) * SK * DH;
    const unsigned short* vp = a.v + (size_t)(chunk >> 2) * SK * DH;
    const rsrc_t rk = mk_rsrc(kp, HB), rv = mk_rsrc(vp, HB);
    const int voffh = voff + HB / 2;
    auto window = [&](int g) -> int { g += (j >> 3) * 2; return ((g & 1) << 19) + ((g & 14) << 15) + ((g & 16) << 16) + (sp << 14) + (sw << 12); };
#define ROWVO(n) ((((n) & 1) ? voffh : voff) + (((n) & 7) >> 1) * 1024)
#define ROWSO(grp, n) window((grp) + ((n) >> 3))
#define LDROW(rs, grp, n) ldnt(rs, ROWVO(n), ROWSO(grp, n))
    __builtin_amdgcn_s_setprio(2);
    u32x4 buf[8], pre[NPRE * 8];
#pragma unroll
    for (int u = 0; u < NPRE * 8; ++u) {
      pre[u] = LDROW(rk, 0, u); SB();
      if (u == WIN) {
        if (lane < 4) ((LAS unsigned*)lds)[L_CNT + lane] = lane < 3 ? 0u : now32() + 0x40000000u;
        lds_wait();
        wg_barrier();
        SB();
      }
      if (u >= WIN) VM_WAIT(WIN);
    }
#pragma unroll
    for (int u = 0; u < 8; ++u) {
      buf[u] = LDROW(rk, NPRE, u); SB();
      if (u < 7) VM_WAIT(WIN);
    }
    wg_barrier();
    const u32x4 qp = *(const LAS u32x4*)(lds + L_Q + c * 4);
    LAS float* S = (LAS float*)lds + L_S + sw * 1024;
    float mx = -INFINITY;
#define DOT(w) dot2((w)[3], qp[3], dot2((w)[2], qp[2], dot2((w)[1], qp[1], dot2((w)[0], qp[0], 0.f))))
#define K_FIN(grp) do { \
      _Pragma("unroll") for (int u = 0; u < 4; ++u) { const float lo = x[u] + dpp<ROR8>(x[u]), hi = x[u + 4] + dpp<ROR8>(x[u + 4]); x[u] = (c & 8) ? hi : lo; } \
      _Pragma("unroll") for (int u = 0; u < 2; ++u) { const float lo = x[u] + dpp<XOR7>(x[u]), hi = x[u + 2] + dpp<XOR7>(x[u + 2]); x[u] = (c & 4) ? hi : lo; } \
      { const float lo = x[0] + dpp<XOR2>(x[0]), hi = x[1] + dpp<XOR2>(x[1]); x[0] = (c & 2) ? hi : lo; } \
      float sc = x[0] + dpp<XOR1>(x[0]); \
      sc = rbf(rbf(sc) * SCALE); \
      mx = fmaxf(mx, sc); \
      S[(grp) * 32 + r * 8 + (c >> 1)] = sc; \
      SB(); \
    } while (0)
    static_assert(WIN >= 3 && WIN <= 5, "ring of 8 rows");
    {
      float x[8];
#pragma unroll
      for (int u = 0; u < 8; ++u) {
        if (u > 0 && u <= WIN + 1) { buf[u - 1] = LDROW(rk, NPRE + 1, u - 1); SB(); }
        x[u] = DOT(buf[u]);
        SB();
      }
      K_FIN(NPRE);
    }
#pragma unroll
    for (int pp = 0; pp < NPRE; ++pp) {
      float x[8];
#pragma unroll
      for (int w = 0; w < 8; ++w) x[w] = DOT(pre[pp * 8 + w]);
      K_FIN(pp);
      if (WIN + 1 + pp < 7) { VM_WAIT(WIN); buf[WIN + 1 + pp] = LDROW(rk, NPRE + 1, WIN + 1 + pp); SB(); }
    }
    static_assert(WIN + 1 + NPRE >= 7, "");
    int last = 31;
    asm volatile("" : "+s"(last));
#pragma nounroll
    for (int tt = NPRE + 1; tt <= last; ++tt) {
      const rsrc_t rn = mk_rsrc(tt == last ? vp : kp, HB);
      float x[8];
#pragma unroll
      for (int u = 0; u < 8; ++u) {
        const int n = u + 7;
        const int so = ROWSO(tt, n);
        asm volatile("s_waitcnt vmcnt(%0)" :: "i"(WIN), "s"(so));
        SB();
        buf[n & 7] = ldnt(n >= 8 ? rn : rk, ROWVO(n), so);
        SB();
        x[u] = DOT(buf[u]);
        SB();
      }
      K_FIN(tt);
    }
    mx = wave_max(mx);
    if (lane == 0) lds[L_MX + sw] = mx;
    lds_wait();
    if (lane == 0) __hip_atomic_fetch_add((LAS unsigned*)lds + L_CNT, 1u, RLX_WG);
    f32x2 acc[4] = {{0.f, 0.f}, {0.f, 0.f}, {0.f, 0.f}, {0.f, 0.f}};
    f32x4 s0 = *(const LAS f32x4*)(S + r * 8), s1;
    int open = -1, lag = -1;
#pragma nounroll
    for (int tt = 0; tt <= last; ++tt) {
      const rsrc_t rn = mk_rsrc(vp, tt == last ? 0 : HB);
      unsigned ref, tnow;
#pragma unroll
      for (int u = 0; u < 8; ++u) {
        const int n = u + 7;
        const int so = ROWSO(tt, n);
        asm volatile("s_cmp_gt_i32 %0, 0\n\ts_cbranch_scc1 1f\n\ts_waitcnt vmcnt(%2)\n\ts_cmp_gt_i32 %1, 0\n\ts_cbranch_scc1 1f\n\ts_waitcnt vmcnt(%3)\n1:"
                     :: "s"(open), "s"(lag), "i"(WIN + 2), "i"(WIN), "s"(so) : "scc");
        SB();
        buf[n & 7] = ldnt(n >= 8 ? rn : rv, ROWVO(n), so);
        SB();
        if (u == 0) s1 = *(const LAS f32x4*)(S + tt * 32 + r * 8 + 4);
        if (u == 4) { s0 = *(const LAS f32x4*)(S + ((tt + 1) & 31) * 32 + r * 8); ref = *(volatile LAS unsigned*)(lds + L_REF); tnow = now32(); }
        const float p = __builtin_amdgcn_exp2f(((u < 4 ? s0[u & 3] : s1[u & 3]) - mx) * LOG2E);
#pragma unroll
        for (int k = 0; k < 4; ++k) acc[k] = __builtin_elementwise_fma(bf_unpack(buf[u][k]), f32x2{p, p}, acc[k]);
        asm volatile("" : "+v"(acc[0]), "+v"(acc[1]), "+v"(acc[2]), "+v"(acc[3]));
        if (u == 6) {
          asm volatile("" : "+v"(ref), "+s"(tnow));
          const int el = (int)(tnow - __builtin_amdgcn_readfirstlane(ref));
          lag = el - (LAG_A + tt * LAG_B);
          open = max(el - WIDE_T, lag - LAG_2);
        }
        SB();
      }
    }
    *(LAS f32x4*)(lds + L_O + (sw * 4 + r) * 128 + c * 8) = f32x4{acc[0][0], acc[0][1], acc[1][0], acc[1][1]};
    *(LAS f32x4*)(lds + L_O + (sw * 4 + r) * 128 + c * 8 + 4) = f32x4{acc[2][0], acc[2][1], acc[3][0], acc[3][1]};
    lds_wait();
    wg_barrier();
    wg_barrier();
    if (blockIdx.x < NRED) wg_barrier();
    return;
  }

  float mb0, mb1, sd0, sd1;
  {
    float f0[8], f1[8];
#pragma unroll
    for (int k = 0; k < 4; ++k) { f0[2 * k] = bf_lo(xv0[k]); f0[2 * k + 1] = bf_hi(xv0[k]); f1[2 * k] = bf_lo(xv1[k]); f1[2 * k + 1] = bf_hi(xv1[k]); }
    float s0 = 0.f, s1 = 0.f;
#pragma unroll
    for (int k = 0; k < 8; ++k) { s0 += f0[k]; s1 += f1[k]; }
    const float m0 = wave_sum(s0) * (1.f / DM), m1 = wave_sum(s1) * (1.f / DM);
    float v0 = 0.f, v1 = 0.f;
#pragma unroll
    for (int k = 0; k < 8; ++k) { const float d0 = f0[k] - m0, d1 = f1[k] - m1; v0 = fmaf(d0, d0, v0); v1 = fmaf(d1, d1, v1); }
    v0 = wave_sum(v0) * (1.f / DM); v1 = wave_sum(v1) * (1.f / DM);
    mb0 = rbf(m0); sd0 = rbf(__builtin_sqrtf(rbf(rbf(v0) + 1e-5f)));
    mb1 = rbf(m1); sd1 = rbf(__builtin_sqrtf(rbf(rbf(v1) + 1e-5f)));
  }
#define XN(xf, mb, sd) rbf(rbf((xf) - (mb)) / (sd))
  const unsigned tag = __builtin_amdgcn_readfirstlane(epoch) + 0x9E3779B9u;
  asm volatile("" :: "s"(tag) : "memory");
  const float xa0 = XN(bf_lo(xq0), mb0, sd0), xa1 = XN(bf_hi(xq0), mb0, sd0), xb0 = XN(bf_lo(xq1), mb1, sd1), xb1 = XN(bf_hi(xq1), mb1, sd1);
#define PARTIAL(w0_, w1_, n, res) do { \
    float t[8]; \
    _Pragma("unroll") for (int k = 0; k < 4; ++k) { \
      const f32x2 w0 = bf_unpack((w0_)[k]), w1 = bf_unpack((w1_)[k]); \
      const f32x2 qa = __builtin_elementwise_fma(w1, f32x2{xa1, xa1}, w0 * xa0), qb = __builtin_elementwise_fma(w1, f32x2{xb1, xb1}, w0 * xb0); \
      t[2 * k] = tr32(qa[0], qb[0]); t[2 * k + 1] = tr32(qa[1], qb[1]);             \
    } \
    _Pragma("unroll") for (int k = 0; k < 4; ++k) t[k] = tr16(t[k], t[4 + k]);        \
    f32x2 o; \
    _Pragma("unroll") for (int k = 0; k < 2; ++k) { const float lo = t[k] + dpp<ROR8>(t[k]), hi = t[2 + k] + dpp<ROR8>(t[2 + k]); o[k] = (c & 8) ? hi : lo; } \
    const float lo = o[0] + dpp<XOR7>(dpp<XOR3>(o[0])), hi = o[1] + dpp<XOR7>(dpp<XOR3>(o[1]));     \
    lds[L_P + (n) * 512 + wave * 64 + (r >> 1) * 32 + (c & 3) * 8 + (r & 1) * 4 + (c >> 3) * 2 + ((c >> 2) & 1)] = (c & 4) ? hi : lo; \
    lds_wait(); \
    if (lane == 0) __hip_atomic_fetch_add((LAS unsigned*)lds + L_CNT + 1 + (n), 1u, RLX_WG); \
    while (__hip_atomic_load((LAS unsigned*)lds + L_CNT + 1 + (n), RLX_WG) < 8u) __builtin_amdgcn_s_sleep(0); \
    asm volatile("" ::: "memory"); \
    const LAS float* P = (const LAS float*)lds + L_P + (n) * 512 + lane; \
    res = ((P[0] + P[64]) + (P[128] + P[192])) + ((P[256] + P[320]) + (P[384] + P[448])); \
  } while (0)
  {
    float qs;
    PARTIAL(wq0, wq1, 0, qs);
    if (wave == 0) st_granule((gu64*)(a.ws + WS_Q) + (h * 8 + g8) * 64 + lane, tag, qs);
  }
  if (wave == 7) {
    __builtin_amdgcn_s_sleep(32);
    const rsrc_t rg = mk_rsrc(a.ws + WS_Q + (size_t)h * 4096, 4096);
    const unsigned t0 = now32();
    u32x4 g0, g1;
    while (true) {
      g0 = lddev(rg, r * 512 + c * 16, b * 256); g1 = lddev(rg, r * 512 + c * 16, 2048 + b * 256);
      if (__all((g0[1] == tag) & (g0[3] == tag) & (g1[1] == tag) & (g1[3] == tag))) break;
      if (now32() - t0 > SPIN_TICKS) break;
      __builtin_amdgcn_s_sleep(8);
      asm volatile("" ::: "memory");
    }
    ((LAS unsigned*)lds)[L_Q + lane] = pack_bf16(__uint_as_float(g0[0]) + __uint_as_float(g1[0]), __uint_as_float(g0[2]) + __uint_as_float(g1[2]));
    lds_wait();
  }
  wg_barrier();
  const int red = blockIdx.x < NRED ? (xg * 2 + (j & 1)) * 2 + (j >> 1) : -1;
  const unsigned xw = (red >= 0 && tid < 16) ? *(const gu32*)(a.x + (red & 1) * DM + (red >> 1) * 32 + tid * 2) : 0u;
  float gsum = 0.f;
  if (h < 28) {
    const rsrc_t rm = mk_rsrc((g8 & 2) ? a.w3 : a.w1, DM * FF * 2);
    const int so = h * 128 + (g8 & 1) * 64;
    const u32x4 wm0 = ldnt(rm, prow * (FF * 2) + (lane & 3) * 16, so), wm1 = ldnt(rm, prow * (FF * 2) + (lane & 3) * 16, so + FF * 2);
    PARTIAL(wm0, wm1, 1, gsum);
  }
  f32x2 wf[64];
  {
    const rsrc_t ro = mk_rsrc(a.wo + (size_t)h * DH * DM, DH * DM * 2);
#pragma unroll
    for (int i = 0; i < 16; ++i) {
      const u32x4 w = lddef(ro, voff + (i & 3) * 1024, (wave * 16 + (i & ~3)) * 1024);
      SB();
#pragma unroll
      for (int k = 0; k < 4; ++k) { wf[i * 4 + k] = bf_unpack(w[k]); asm volatile("" : "+v"(wf[i * 4 + k])); }
      SB();
    }
  }
  const int lane0 = lane;
  while (__hip_atomic_load((LAS unsigned*)lds + L_CNT, RLX_WG) < 4u) __builtin_amdgcn_s_sleep(8);
  asm volatile("" ::: "memory");
  gu64* ml = (gu64*)(a.ws + WS_ML);
  gu64* G = (gu64*)(a.ws + WS_G);
  float mw = 0.f;
  const unsigned tk = now32();
  if (wave == 7) {
    FRESH();
    mw = lds[L_MX + (lane & 3)];
    float m = fmaxf(mw, dpp<XOR1>(mw)); m = fmaxf(m, dpp<XOR2>(m));
    float l = 0.f;
#pragma unroll 2
    for (int i = 0; i < 16; ++i) {
      const f32x4 sv = *(const LAS f32x4*)(lds + L_S + i * 256 + lane * 4);
#pragma unroll
      for (int k = 0; k < 4; ++k) l += __builtin_amdgcn_exp2f((sv[k] - m) * LOG2E);
    }
    l = wave_sum(l);
    if (lane < 16) st_granule(ml + chunk * 16 + lane, tag, lane == 0 ? m : lane == 1 ? l : __uint_as_float(tk));
  } else if (wave == 0 && h < 28) {
    FRESH();
    __builtin_amdgcn_s_sleep(16);
    st_granule(G + (h * 8 + g8) * 64 + lane, tag, gsum);
  }
  __builtin_amdgcn_s_sleep(127); __builtin_amdgcn_s_sleep(16);
  f32x2 yacc[4] = {{0.f, 0.f}, {0.f, 0.f}, {0.f, 0.f}, {0.f, 0.f}};
  if (wave < 7) {
    FRESH();
    const int f0 = (h * 4 + sp) * 14 + wave * 2;
    const rsrc_t r2 = mk_rsrc(a.w2 + (size_t)f0 * DM, 2 * DM * 2);
    const u32x4 w20 = lddef(r2, voff, 0), w21 = lddef(r2, voff + 1024, 0);
    const int f = f0 + (r & 1);
    gu64* gp = G + ((f >> 6) * 8 + (c & 1) * 4 + (c >> 3) * 2 + ((f >> 5) & 1)) * 64 + b * 32 + (f & 31);
    const unsigned t0 = now32();
    unsigned long long g;
    while (true) {
      g = __hip_atomic_load(gp, RLX_AGENT);
      if (__all((unsigned)(g >> 32) == tag)) break;
      if (now32() - t0 > SPIN_TICKS) break;
      __builtin_amdgcn_s_sleep(127);
    }
    float s = __uint_as_float((unsigned)g);
    s += dpp<XOR1>(s);
    const float gs = rbf(s), us = rbf(dpp<ROR8>(s));
    const float hv = rbf(rbf(gs / (1.f + __expf(-gs))) * us);
    const float h0 = lane_value(hv, 0), h1 = lane_value(hv, 16);
#pragma unroll
    for (int k = 0; k < 4; ++k) yacc[k] = __builtin_elementwise_fma(bf_unpack(w21[k]), f32x2{h1, h1}, bf_unpack(w20[k]) * h0);
  } else {
    FRESH();
    {
      const unsigned long long gt = __hip_atomic_load(ml + (lane * 4 + (lane & 3)) * 16 + 2, RLX_AGENT);
      const bool v = (unsigned)(gt >> 32) == tag;
      const float n = wave_sum(v ? 1.f : 0.f), d = wave_sum(v ? (float)(int)((unsigned)gt - tk) : 0.f);
      if (lane == 0 && n > 0.f) ((LAS unsigned*)lds)[L_REF] = tk + (unsigned)(int)(d / n);
    }
    const unsigned t0 = now32();
    unsigned long long gm, gl;
    while (true) {
      gm = __hip_atomic_load(ml + ((chunk & ~3) + (lane & 3)) * 16, RLX_AGENT);
      gl = __hip_atomic_load(ml + ((chunk & ~3) + (lane & 3)) * 16 + 1, RLX_AGENT);
      if (__all((unsigned)(gm >> 32) == tag && (unsigned)(gl >> 32) == tag)) break;
      if (now32() - t0 > SPIN_TICKS) break;
      __builtin_amdgcn_s_sleep(127);
    }
    const float ms = __uint_as_float((unsigned)gm), ls = __uint_as_float((unsigned)gl);
    float M = fmaxf(ms, dpp<XOR1>(ms)); M = fmaxf(M, dpp<XOR2>(M));
    float L = ls * __builtin_amdgcn_exp2f((ms - M) * LOG2E);
    L += dpp<XOR1>(L); L += dpp<XOR2>(L);
    if (lane < 4) lds[L_FW + lane] = __builtin_amdgcn_exp2f((mw - M) * LOG2E) / L;
    lds_wait();
    {
      const unsigned char* pa = (lane < 32 || red < 0) ? a.ws + WS_Y + chunk * 2048 + (lane & 15) * 128
                                                       : (const unsigned char*)a.out + (red & 1) * (DM * 2) + (red >> 1) * 64;
      const unsigned jk = __hip_atomic_load((gu32*)pa, RLX_AGENT);
      asm volatile("" :: "v"(jk));
    }
    if (blockIdx.x == NWG - 1) {
      while (true) {
        bool ok = true;
#pragma unroll
        for (int i = 0; i < 4; ++i) ok = ok & ((unsigned)(__hip_atomic_load(ml + (i * 64 + lane) * 16 + 2, RLX_AGENT) >> 32) == tag);
        if (__all(ok)) break;
        if (now32() - t0 > SPIN_TICKS) break;
        __builtin_amdgcn_s_sleep(127);
      }
      if (lane == 0) __hip_atomic_store((gu32*)(a.ws + WS_EPOCH), tag, RLX_AGENT);
    }
  }
  {
    FRESH();
    wg_barrier();
    {
      const LAS float* P = (const LAS float*)lds + L_O + (4 * r) * 128 + wave * 16 + c;
      const float o = xrow_sum(((P[0] + P[128]) + (P[256] + P[384])) * lds[L_FW + r]);
#pragma unroll
      for (int i = 0; i < 16; ++i) {
        const float oi = lane_value(o, i);
#pragma unroll
        for (int k = 0; k < 4; ++k) yacc[k] = __builtin_elementwise_fma(wf[i * 4 + k], f32x2{oi, oi}, yacc[k]);
      }
    }
    *(LAS f32x4*)(lds + L_Y + wave * 512 + lane * 8) = f32x4{yacc[0][0], yacc[0][1], yacc[1][0], yacc[1][1]};
    *(LAS f32x4*)(lds + L_Y + wave * 512 + lane * 8 + 4) = f32x4{yacc[2][0], yacc[2][1], yacc[3][0], yacc[3][1]};
    lds_wait();
    wg_barrier();
    const rsrc_t ry = mk_rsrc(a.ws + WS_Y, NWG * 2048);
    {
      const LAS float* P = (const LAS float*)lds + L_Y + tid;
      const float y = ((P[0] + P[512]) + (P[1024] + P[1536])) + ((P[2048] + P[2560]) + (P[3072] + P[3584]));
      const unsigned u = __float_as_uint(y);
      const unsigned w = (((u & 0x7f800000u) == 0x7f800000u ? u | ((u & 0x7fffffu) ? 0x400000u : 0u) : u + 0x400u) & 0xfffff800u) | ((tag >> ((lane & 1) * 11)) & 0x7ffu);
      const unsigned w1 = __builtin_amdgcn_mov_dpp(w, XOR1, 0xf, 0xf, true);
      if (!(lane & 1)) __builtin_amdgcn_raw_buffer_store_b64(u32x2{w, w1}, ry, tid * 4, chunk * 2048, 16);
    }
    if (red >= 0) {
      const unsigned t0 = now32();
      u32x4 g0 = {0u, 0u, 0u, 0u}, g1 = {0u, 0u, 0u, 0u};
      bool ok0 = false, ok1 = false;
      const unsigned ta = tag & 0x7ffu, tb = (tag >> 11) & 0x7ffu;
      while (true) {
        if (!ok0) g0 = lddev(ry, (lane >> 3) * 2048 + (lane & 7) * 16, ((red & 1) * 128 + wave * 16) * 2048 + (red >> 1) * 128);
        if (!ok1) g1 = lddev(ry, (lane >> 3) * 2048 + (lane & 7) * 16, ((red & 1) * 128 + wave * 16 + 8) * 2048 + (red >> 1) * 128);
        ok0 = (((g0[0] ^ ta) | (g0[1] ^ tb) | (g0[2] ^ ta) | (g0[3] ^ tb)) & 0x7ffu) == 0u;
        ok1 = (((g1[0] ^ ta) | (g1[1] ^ tb) | (g1[2] ^ ta) | (g1[3] ^ tb)) & 0x7ffu) == 0u;
        if (__all(ok0 & ok1)) break;
        if (now32() - t0 > SPIN_TICKS) break;
        __builtin_amdgcn_s_sleep(4);
        asm volatile("" ::: "memory");
      }
      float t[4];
#pragma unroll
      for (int k = 0; k < 4; ++k) t[k] = __uint_as_float(g0[k] & 0xfffff800u) + __uint_as_float(g1[k] & 0xfffff800u);
      float s = tr16(tr32(t[0], t[2]), tr32(t[1], t[3]));
      s += dpp<ROR8>(s);
      if (!(lane & 8)) lds[L_FIN + wave * 32 + (lane & 7) * 4 + r] = s;
      lds_wait();
      wg_barrier();
      if (tid < 16) {
        const LAS f32x2* P = (const LAS f32x2*)(lds + L_FIN + tid * 2);
        const f32x2 o = bf_unpack(xw) + (((P[0] + P[16]) + (P[32] + P[48])) + ((P[64] + P[80]) + (P[96] + P[112])));
        *(gu32*)(a.out + (red & 1) * DM + (red >> 1) * 32 + tid * 2) = pack_bf16(o[0], o[1]);
      }
    }
  }
}

}

extern "C" void kernel_launch(void* const* d_in, const int* in_sizes, int n_in, void* d_out, int out_size, void* d_ws, size_t ws_size,
                              hipStream_t stream) {
  Args a;
  a.x = (const unsigned short*)d_in[0]; a.k = (const unsigned short*)d_in[1]; a.v = (const unsigned short*)d_in[2];
  a.wq = (const unsigned short*)d_in[3]; a.wo = (const unsigned short*)d_in[4];
  a.w1 = (const unsigned short*)d_in[6]; a.w3 = (const unsigned short*)d_in[7]; a.w2 = (const unsigned short*)d_in[8];
  a.out = (unsigned short*)d_out; a.ws = (unsigned char*)d_ws;
  fused_decode<<<NWG, NTHR, 0, stream>>>(a);
}
```

```cpp
#include <hip/hip_runtime.h>
#include <hip/hip_bf16.h>
#include <stdint.h>

namespace {

constexpr int NWG = 256, NTHR = 768, NRED = 32;
constexpr int DM = 512, NH = 32, SK = 16384, DH = 128, FF = 1792;
constexpr float LOG2E = 1.4426950408889634f;
constexpr float SCALE = 0.08838834764831845f;
constexpr int WIN = 4;
constexpr int NPRE = 3;
constexpr int LAG_A = 155, LAG_B = 108, LAG_2 = 80, WIDE_T = 3310, CHK_G = 16, CHK_A = 50;
constexpr size_t WS_EPOCH = 0;
constexpr size_t WS_ML = 8192;
constexpr size_t WS_M2 = WS_ML + 32768;
constexpr size_t WS_Y = 131072;
constexpr size_t WS_Q = WS_Y + NWG * 2048;
constexpr size_t WS_G = WS_Q + (128u << 10);
constexpr int L_S = 0;
constexpr int L_O = L_S + 4096;
constexpr int L_Y = L_O + 2048;
constexpr int L_P = L_Y + 4096;
constexpr int L_Q = L_P + 1024;
constexpr int L_MX = L_Q + 64;
constexpr int L_FW = L_MX + 4;
constexpr int L_FIN = L_FW + 4;
constexpr int L_CNT = L_FIN + 256;
constexpr int L_REF = L_CNT + 3;
constexpr int L_END = L_REF + 1;

#define GAS __attribute__((address_space(1)))
#define LAS __attribute__((address_space(3)))
#define RLX_AGENT __ATOMIC_RELAXED, __HIP_MEMORY_SCOPE_AGENT
#define RLX_WG __ATOMIC_RELAXED, __HIP_MEMORY_SCOPE_WORKGROUP
typedef unsigned u32x2 __attribute__((ext_vector_type(2)));
typedef unsigned u32x4 __attribute__((ext_vector_type(4)));
typedef float f32x2 __attribute__((ext_vector_type(2)));
typedef float f32x4 __attribute__((ext_vector_type(4)));
typedef __bf16 bf16x2 __attribute__((ext_vector_type(2)));
typedef GAS unsigned gu32;
typedef GAS unsigned long long gu64;
typedef __amdgpu_buffer_rsrc_t rsrc_t;

__device__ __forceinline__ float bf_lo(unsigned u) { return __builtin_bit_cast(float, u << 16); }
__device__ __forceinline__ float bf_hi(unsigned u) { return __builtin_bit_cast(float, u & 0xffff0000u); }
__device__ __forceinline__ f32x2 bf_unpack(unsigned u) { return f32x2{bf_lo(u), bf_hi(u)}; }
__device__ __forceinline__ unsigned pack_bf16(float lo, float hi) {
  bf16x2 r = {(__bf16)lo, (__bf16)hi};
  return __builtin_bit_cast(unsigned, r);
}
__device__ __forceinline__ float rbf(float x) { return (float)(__bf16)x; }
__device__ __forceinline__ float dot2(unsigned a, unsigned b, float acc) {
  return __builtin_amdgcn_fdot2_f32_bf16(__builtin_bit_cast(bf16x2, a), __builtin_bit_cast(bf16x2, b), acc, false);
}
template <int CTRL> __device__ __forceinline__ float dpp(float x) {
  return __builtin_bit_cast(float, __builtin_amdgcn_mov_dpp(__builtin_bit_cast(int, x), CTRL, 0xf, 0xf, true));
}
constexpr int XOR1 = 0xB1, XOR2 = 0x4E, XOR3 = 0x1B, XOR7 = 0x141, ROR8 = 0x128;
__device__ __forceinline__ float tr16(float a, float b) {
  auto s = __builtin_amdgcn_permlane16_swap(__float_as_uint(a), __float_as_uint(b), false, false);
  return __uint_as_float(s[0]) + __uint_as_float(s[1]);
}
__device__ __forceinline__ float tr32(float a, float b) {
  auto s = __builtin_amdgcn_permlane32_swap(__float_as_uint(a), __float_as_uint(b), false, false);
  return __uint_as_float(s[0]) + __uint_as_float(s[1]);
}
__device__ __forceinline__ float xrow_sum(float x) { x = tr16(x, x); return tr32(x, x); }
__device__ __forceinline__ float wave_sum(float x) {
  x += dpp<XOR1>(x); x += dpp<XOR2>(x); x += dpp<XOR7>(x); x += dpp<ROR8>(x);
  return xrow_sum(x);
}
__device__ __forceinline__ float wave_max(float x) {
  x = fmaxf(x, dpp<XOR1>(x)); x = fmaxf(x, dpp<XOR2>(x)); x = fmaxf(x, dpp<XOR7>(x)); x = fmaxf(x, dpp<ROR8>(x));
  auto s = __builtin_amdgcn_permlane16_swap(__float_as_uint(x), __float_as_uint(x), false, false);
  x = fmaxf(__uint_as_float(s[0]), __uint_as_float(s[1]));
  auto t = __builtin_amdgcn_permlane32_swap(__float_as_uint(x), __float_as_uint(x), false, false);
  return fmaxf(__uint_as_float(t[0]), __uint_as_float(t[1]));
}
__device__ __forceinline__ float lane_value(float x, int l) { return __builtin_bit_cast(float, __builtin_amdgcn_readlane(__builtin_bit_cast(int, x), l)); }
__device__ __forceinline__ void lds_wait() { asm volatile("s_waitcnt lgkmcnt(0)" ::: "memory"); }
__device__ __forceinline__ void wg_barrier() { __builtin_amdgcn_s_barrier(); asm volatile("" ::: "memory"); }
__device__ __forceinline__ void st_granule(gu64* g, unsigned tag, float v) {
  __hip_atomic_store(g, ((unsigned long long)tag << 32) | (unsigned long long)__float_as_uint(v), RLX_AGENT);
}
__device__ __forceinline__ rsrc_t mk_rsrc(const void* p, int bytes) { return __builtin_amdgcn_make_buffer_rsrc((void*)p, 0, bytes, 0x00020000); }
__device__ __forceinline__ u32x4 ldnt(rsrc_t r, int voff, int soff) { return __builtin_amdgcn_raw_buffer_load_b128(r, voff, soff, 2); }
__device__ __forceinline__ u32x4 lddev(rsrc_t r, int voff, int soff) { return __builtin_amdgcn_raw_buffer_load_b128(r, voff, soff, 16); }
__device__ __forceinline__ u32x4 lddef(rsrc_t r, int voff, int soff) { return __builtin_amdgcn_raw_buffer_load_b128(r, voff, soff, 0); }
__device__ __forceinline__ unsigned now32() { return (unsigned)__builtin_amdgcn_s_memrealtime(); }
constexpr unsigned SPIN_TICKS = 20000000u;

#define SB() __builtin_amdgcn_sched_barrier(0)
#define VM_WAIT(n) do { asm volatile("s_waitcnt vmcnt(%0)" :: "i"(n)); SB(); } while (0)
#define FRESH() int lane_ = lane0; asm volatile("" : "+v"(lane_)); \
  const int lane = lane_, r = lane >> 4, c = lane & 15, tid = wave * 64 + lane, voff = lane * 16; (void)r; (void)c; (void)tid; (void)voff

struct Args {
  const unsigned short* x; const unsigned short* k; const unsigned short* v; const unsigned short* wq; const unsigned short* wo;
  const unsigned short* w1; const unsigned short* w3; const unsigned short* w2;
  unsigned short* out; unsigned char* ws;
};

__global__ void __launch_bounds__(NTHR, 3) fused_decode(Args a) {
  __shared__ __attribute__((aligned(16))) float lds[L_END];
  const int tid = threadIdx.x, lane = tid & 63, wave = __builtin_amdgcn_readfirstlane(tid >> 6);
  const int r = lane >> 4, c = lane & 15;
  const int xg = blockIdx.x & 7, j = blockIdx.x >> 3;
  const int b = j >> 4, h = xg + ((j >> 2) & 3) * 8, sp = j & 3;
  const int chunk = b * 128 + h * 4 + sp;
  const int voff = lane * 16;
  asm volatile("" :: "s"(a.x), "s"(a.k), "s"(a.v), "s"(a.wq), "s"(a.ws));

  const int g8 = b * 4 + sp;
  const int prow = (g8 >> 2) * 256 + wave * 32 + 2 * (lane >> 2);
  u32x4 xv0, xv1, wq0, wq1;
  unsigned xq0, xq1;
  if (wave < 8) {
    xv0 = *(const GAS u32x4*)(a.x + lane * 8); xv1 = *(const GAS u32x4*)(a.x + DM + lane * 8);
    xq0 = *(const gu32*)(a.x + prow); xq1 = *(const gu32*)(a.x + DM + prow);
    const rsrc_t rq = mk_rsrc(a.wq, DM * NH * DH * 2);
    wq0 = ldnt(rq, prow * (NH * DH * 2) + (lane & 3) * 16, h * (DH * 2) + (g8 & 3) * 64);
    wq1 = ldnt(rq, prow * (NH * DH * 2) + (lane & 3) * 16, h * (DH * 2) + (g8 & 3) * 64 + NH * DH * 2);
    SB();
    wg_barrier();
  }
  int role = wave;
  asm volatile("" : "+s"(role));
  if (__builtin_expect(role >= 8, 1)) {
    const int sw = wave - 8;
    constexpr int HB = SK * DH * 2;
    const unsigned short* kp = a.k + (size_t)(chunk >> 2) * SK * DH;
    const unsigned short* vp = a.v + (size_t)(chunk >> 2) * SK * DH;
    const rsrc_t rk = mk_rsrc(kp, HB), rv = mk_rsrc(vp, HB);
    const int voffh = voff + HB / 2;
    auto window = [&](int g) -> int { g += (j >> 3) * 2; return ((g & 1) << 19) + ((g & 14) << 15) + ((g & 16) << 16) + (sp << 14) + (sw << 12); };
#define ROWVO(n) ((((n) & 1) ? voffh : voff) + (((n) & 7) >> 1) * 1024)
#define ROWSO(grp, n) window((grp) + ((n) >> 3))
#define LDROW(rs, grp, n) ldnt(rs, ROWVO(n), ROWSO(grp, n))
    __builtin_amdgcn_s_setprio(2);
    u32x4 buf[8], pre[NPRE * 8];
#pragma unroll
    for (int u = 0; u < NPRE * 8; ++u) {
      pre[u] = LDROW(rk, 0, u); SB();
      if (u == WIN) {
        if (lane < 4) ((LAS unsigned*)lds)[L_CNT + lane] = lane < 3 ? 0u : now32() + 0x40000000u;
        lds_wait();
        wg_barrier();
        SB();
      }
      if (u >= WIN) VM_WAIT(WIN);
    }
#pragma unroll
    for (int u = 0; u < 8; ++u) {
      buf[u] = LDROW(rk, NPRE, u); SB();
      if (u < 7) VM_WAIT(WIN);
    }
    wg_barrier();
    const u32x4 qp = *(const LAS u32x4*)(lds + L_Q + c * 4);
    LAS float* S = (LAS float*)lds + L_S + sw * 1024;
    float mx = -INFINITY;
#define DOT(w) dot2((w)[3], qp[3], dot2((w)[2], qp[2], dot2((w)[1], qp[1], dot2((w)[0], qp[0], 0.f))))
#define K_FIN(grp) do { \
      _Pragma("unroll") for (int u = 0; u < 4; ++u) { const float lo = x[u] + dpp<ROR8>(x[u]), hi = x[u + 4] + dpp<ROR8>(x[u + 4]); x[u] = (c & 8) ? hi : lo; } \
      _Pragma("unroll") for (int u = 0; u < 2; ++u) { const float lo = x[u] + dpp<XOR7>(x[u]), hi = x[u + 2] + dpp<XOR7>(x[u + 2]); x[u] = (c & 4) ? hi : lo; } \
      { const float lo = x[0] + dpp<XOR2>(x[0]), hi = x[1] + dpp<XOR2>(x[1]); x[0] = (c & 2) ? hi : lo; } \
      float sc = x[0] + dpp<XOR1>(x[0]); \
      sc = rbf(rbf(sc) * SCALE); \
      mx = fmaxf(mx, sc); \
      S[(grp) * 32 + r * 8 + (c >> 1)] = sc; \
      SB(); \
    } while (0)
    static_assert(WIN >= 3 && WIN <= 5, "ring of 8 rows");
    {
      float x[8];
#pragma unroll
      for (int u = 0; u < 8; ++u) {
        if (u > 0 && u <= WIN + 1) { buf[u - 1] = LDROW(rk, NPRE + 1, u - 1); SB(); }
        x[u] = DOT(buf[u]);
        SB();
      }
      K_FIN(NPRE);
    }
#pragma unroll
    for (int pp = 0; pp < NPRE; ++pp) {
      float x[8];
#pragma unroll
      for (int w = 0; w < 8; ++w) x[w] = DOT(pre[pp * 8 + w]);
      K_FIN(pp);
      if (WIN + 1 + pp < 7) { VM_WAIT(WIN); buf[WIN + 1 + pp] = LDROW(rk, NPRE + 1, WIN + 1 + pp); SB(); }
    }
    static_assert(WIN + 1 + NPRE >= 7, "");
    int last = 31;
    asm volatile("" : "+s"(last));
#pragma nounroll
    for (int tt = NPRE + 1; tt <= last; ++tt) {
      const rsrc_t rn = mk_rsrc(tt == last ? vp : kp, HB);
      float x[8];
#pragma unroll
      for (int u = 0; u < 8; ++u) {
        const int n = u + 7;
        const int so = ROWSO(tt, n);
        asm volatile("s_waitcnt vmcnt(%0)" :: "i"(WIN), "s"(so));
        SB();
        buf[n & 7] = ldnt(n >= 8 ? rn : rk, ROWVO(n), so);
        SB();
        x[u] = DOT(buf[u]);
        SB();
      }
      K_FIN(tt);
    }
    mx = wave_max(mx);
    if (lane == 0) lds[L_MX + sw] = mx;
    lds_wait();
    if (lane == 0) __hip_atomic_fetch_add((LAS unsigned*)lds + L_CNT, 1u, RLX_WG);
    f32x2 acc[4] = {{0.f, 0.f}, {0.f, 0.f}, {0.f, 0.f}, {0.f, 0.f}};
    f32x4 s0 = *(const LAS f32x4*)(S + r * 8), s1;
    int open = -1, lag = -1;
#pragma nounroll
    for (int tt = 0; tt <= last; ++tt) {
      const rsrc_t rn = mk_rsrc(vp, tt == last ? 0 : HB);
      unsigned ref, tnow;
#pragma unroll
      for (int u = 0; u < 8; ++u) {
        const int n = u + 7;
        const int so = ROWSO(tt, n);
        asm volatile("s_cmp_gt_i32 %0, 0\n\ts_cbranch_scc1 1f\n\ts_waitcnt vmcnt(%2)\n\ts_cmp_gt_i32 %1, 0\n\ts_cbranch_scc1 1f\n\ts_waitcnt vmcnt(%3)\n1:"
                     :: "s"(open), "s"(lag), "i"(WIN + 2), "i"(WIN), "s"(so) : "scc");
        SB();
        buf[n & 7] = ldnt(n >= 8 ? rn : rv, ROWVO(n), so);
        SB();
        if (u == 0) s1 = *(const LAS f32x4*)(S + tt * 32 + r * 8 + 4);
        if (u == 4) { s0 = *(const LAS f32x4*)(S + ((tt + 1) & 31) * 32 + r * 8); ref = *(volatile LAS unsigned*)(lds + L_REF); tnow = now32(); }
        const float p = __builtin_amdgcn_exp2f(((u < 4 ? s0[u & 3] : s1[u & 3]) - mx) * LOG2E);
#pragma unroll
        for (int k = 0; k < 4; ++k) acc[k] = __builtin_elementwise_fma(bf_unpack(buf[u][k]), f32x2{p, p}, acc[k]);
        asm volatile("" : "+v"(acc[0]), "+v"(acc[1]), "+v"(acc[2]), "+v"(acc[3]));
        if (u == 6) {
          asm volatile("" : "+v"(ref), "+s"(tnow));
          const int el = (int)(tnow - __builtin_amdgcn_readfirstlane(ref));
          lag = el - (LAG_A + tt * LAG_B);
          open = max(el - WIDE_T, lag - LAG_2);
        }
        if (u == 7 && tt == CHK_G - 1 && lane == 0) __hip_atomic_fetch_add((LAS unsigned*)lds + L_CNT, 1u, RLX_WG);
        SB();
      }
    }
    *(LAS f32x4*)(lds + L_O + (sw * 4 + r) * 128 + c * 8) = f32x4{acc[0][0], acc[0][1], acc[1][0], acc[1][1]};
    *(LAS f32x4*)(lds + L_O + (sw * 4 + r) * 128 + c * 8 + 4) = f32x4{acc[2][0], acc[2][1], acc[3][0], acc[3][1]};
    lds_wait();
    wg_barrier();
    wg_barrier();
    if (blockIdx.x < NRED) wg_barrier();
    return;
  }

  unsigned epoch = *(const __attribute__((address_space(4))) unsigned*)(a.ws + WS_EPOCH);
  SB();
  float mb0, mb1, sd0, sd1;
  {
    float f0[8], f1[8];
#pragma unroll
    for (int k = 0; k < 4; ++k) { f0[2 * k] = bf_lo(xv0[k]); f0[2 * k + 1] = bf_hi(xv0[k]); f1[2 * k] = bf_lo(xv1[k]); f1[2 * k + 1] = bf_hi(xv1[k]); }
    float s0 = 0.f, s1 = 0.f;
#pragma unroll
    for (int k = 0; k < 8; ++k) { s0 += f0[k]; s1 += f1[k]; }
    const float m0 = wave_sum(s0) * (1.f / DM), m1 = wave_sum(s1) * (1.f / DM);
    float v0 = 0.f, v1 = 0.f;
#pragma unroll
    for (int k = 0; k < 8; ++k) { const float d0 = f0[k] - m0, d1 = f1[k] - m1; v0 = fmaf(d0, d0, v0); v1 = fmaf(d1, d1, v1); }
    v0 = wave_sum(v0) * (1.f / DM); v1 = wave_sum(v1) * (1.f / DM);
    mb0 = rbf(m0); sd0 = rbf(__builtin_sqrtf(rbf(rbf(v0) + 1e-5f)));
    mb1 = rbf(m1); sd1 = rbf(__builtin_sqrtf(rbf(rbf(v1) + 1e-5f)));
  }
#define XN(xf, mb, sd) rbf(rbf((xf) - (mb)) / (sd))
  asm volatile("" : "+s"(epoch));
  const unsigned tag = epoch + 0x9E3779B9u;
  const float xa0 = XN(bf_lo(xq0), mb0, sd0), xa1 = XN(bf_hi(xq0), mb0, sd0), xb0 = XN(bf_lo(xq1), mb1, sd1), xb1 = XN(bf_hi(xq1), mb1, sd1);
#define PARTIAL(w0_, w1_, n, res) do { \
    float t[8]; \
    _Pragma("unroll") for (int k = 0; k < 4; ++k) { \
      const f32x2 w0 = bf_unpack((w0_)[k]), w1 = bf_unpack((w1_)[k]); \
      const f32x2 qa = __builtin_elementwise_fma(w1, f32x2{xa1, xa1}, w0 * xa0), qb = __builtin_elementwise_fma(w1, f32x2{xb1, xb1}, w0 * xb0); \
      t[2 * k] = tr32(qa[0], qb[0]); t[2 * k + 1] = tr32(qa[1], qb[1]);             \
    } \
    _Pragma("unroll") for (int k = 0; k < 4; ++k) t[k] = tr16(t[k], t[4 + k]);        \
    f32x2 o; \
    _Pragma("unroll") for (int k = 0; k < 2; ++k) { const float lo = t[k] + dpp<ROR8>(t[k]), hi = t[2 + k] + dpp<ROR8>(t[2 + k]); o[k] = (c & 8) ? hi : lo; } \
    const float lo = o[0] + dpp<XOR7>(dpp<XOR3>(o[0])), hi = o[1] + dpp<XOR7>(dpp<XOR3>(o[1]));     \
    lds[L_P + (n) * 512 + wave * 64 + (r >> 1) * 32 + (c & 3) * 8 + (r & 1) * 4 + (c >> 3) * 2 + ((c >> 2) & 1)] = (c & 4) ? hi : lo; \
    lds_wait(); \
    if (lane == 0) __hip_atomic_fetch_add((LAS unsigned*)lds + L_CNT + 1 + (n), 1u, RLX_WG); \
    while (__hip_atomic_load((LAS unsigned*)lds + L_CNT + 1 + (n), RLX_WG) < 8u) __builtin_amdgcn_s_sleep(0); \
    asm volatile("" ::: "memory"); \
    const LAS float* P = (const LAS float*)lds + L_P + (n) * 512 + lane; \
    res = ((P[0] + P[64]) + (P[128] + P[192])) + ((P[256] + P[320]) + (P[384] + P[448])); \
  } while (0)
  {
    float qs;
    PARTIAL(wq0, wq1, 0, qs);
    if (wave == 0) st_granule((gu64*)(a.ws + WS_Q) + (h * 8 + g8) * 64 + lane, tag, qs);
  }
  if (wave == 7) {
    __builtin_amdgcn_s_sleep(32);
    const rsrc_t rg = mk_rsrc(a.ws + WS_Q + (size_t)h * 4096, 4096);
    const unsigned t0 = now32();
    u32x4 g0, g1;
    while (true) {
      g0 = lddev(rg, r * 512 + c * 16, b * 256); g1 = lddev(rg, r * 512 + c * 16, 2048 + b * 256);
      if (__all((g0[1] == tag) & (g0[3] == tag) & (g1[1] == tag) & (g1[3] == tag))) break;
      if (now32() - t0 > SPIN_TICKS) break;
      __builtin_amdgcn_s_sleep(8);
      asm volatile("" ::: "memory");
    }
    ((LAS unsigned*)lds)[L_Q + lane] = pack_bf16(__uint_as_float(g0[0]) + __uint_as_float(g1[0]), __uint_as_float(g0[2]) + __uint_as_float(g1[2]));
    lds_wait();
  }
  wg_barrier();
  const int red = blockIdx.x < NRED ? (xg * 2 + (j & 1)) * 2 + (j >> 1) : -1;
  const unsigned xw = (red >= 0 && tid < 16) ? *(const gu32*)(a.x + (red & 1) * DM + (red >> 1) * 32 + tid * 2) : 0u;
  float gsum = 0.f;
  if (h < 28) {
    const rsrc_t rm = mk_rsrc((g8 & 2) ? a.w3 : a.w1, DM * FF * 2);
    const int so = h * 128 + (g8 & 1) * 64;
    const u32x4 wm0 = ldnt(rm, prow * (FF * 2) + (lane & 3) * 16, so), wm1 = ldnt(rm, prow * (FF * 2) + (lane & 3) * 16, so + FF * 2);
    PARTIAL(wm0, wm1, 1, gsum);
  }
  f32x2 wf[64];
  {
    const rsrc_t ro = mk_rsrc(a.wo + (size_t)h * DH * DM, DH * DM * 2);
#pragma unroll
    for (int i = 0; i < 16; ++i) {
      const u32x4 w = lddef(ro, voff + (i & 3) * 1024, (wave * 16 + (i & ~3)) * 1024);
      SB();
#pragma unroll
      for (int k = 0; k < 4; ++k) { wf[i * 4 + k] = bf_unpack(w[k]); asm volatile("" : "+v"(wf[i * 4 + k])); }
      SB();
    }
  }
  const int lane0 = lane;
  while (__hip_atomic_load((LAS unsigned*)lds + L_CNT, RLX_WG) < 4u) __builtin_amdgcn_s_sleep(8);
  asm volatile("" ::: "memory");
  gu64* ml = (gu64*)(a.ws + WS_ML);
  gu64* G = (gu64*)(a.ws + WS_G);
  float mw = 0.f;
  const unsigned tk = now32();
  if (wave == 7) {
    FRESH();
    mw = lds[L_MX + (lane & 3)];
    float m = fmaxf(mw, dpp<XOR1>(mw)); m = fmaxf(m, dpp<XOR2>(m));
    float l = 0.f;
#pragma unroll 2
    for (int i = 0; i < 16; ++i) {
      const f32x4 sv = *(const LAS f32x4*)(lds + L_S + i * 256 + lane * 4);
#pragma unroll
      for (int k = 0; k < 4; ++k) l += __builtin_amdgcn_exp2f((sv[k] - m) * LOG2E);
    }
    l = wave_sum(l);
    if (lane < 16) st_granule(ml + chunk * 16 + lane, tag, lane == 0 ? m : lane == 1 ? l : __uint_as_float(tk));
  } else if (wave == 0 && h < 28) {
    FRESH();
    __builtin_amdgcn_s_sleep(16);
    st_granule(G + (h * 8 + g8) * 64 + lane, tag, gsum);
  }
  __builtin_amdgcn_s_sleep(127); __builtin_amdgcn_s_sleep(16);
  f32x2 yacc[4] = {{0.f, 0.f}, {0.f, 0.f}, {0.f, 0.f}, {0.f, 0.f}};
  if (wave < 7) {
    FRESH();
    const int f0 = (h * 4 + sp) * 14 + wave * 2;
    const rsrc_t r2 = mk_rsrc(a.w2 + (size_t)f0 * DM, 2 * DM * 2);
    const u32x4 w20 = lddef(r2, voff, 0), w21 = lddef(r2, voff + 1024, 0);
    const int f = f0 + (r & 1);
    gu64* gp = G + ((f >> 6) * 8 + (c & 1) * 4 + (c >> 3) * 2 + ((f >> 5) & 1)) * 64 + b * 32 + (f & 31);
    const unsigned t0 = now32();
    unsigned long long g;
    while (true) {
      g = __hip_atomic_load(gp, RLX_AGENT);
      if (__all((unsigned)(g >> 32) == tag)) break;
      if (now32() - t0 > SPIN_TICKS) break;
      __builtin_amdgcn_s_sleep(127);
    }
    float s = __uint_as_float((unsigned)g);
    s += dpp<XOR1>(s);
    const float gs = rbf(s), us = rbf(dpp<ROR8>(s));
    const float hv = rbf(rbf(gs / (1.f + __expf(-gs))) * us);
    const float h0 = lane_value(hv, 0), h1 = lane_value(hv, 16);
#pragma unroll
    for (int k = 0; k < 4; ++k) yacc[k] = __builtin_elementwise_fma(bf_unpack(w21[k]), f32x2{h1, h1}, bf_unpack(w20[k]) * h0);
  } else {
    FRESH();
    {
      const unsigned long long gt = __hip_atomic_load(ml + (lane * 4 + (lane & 3)) * 16 + 2, RLX_AGENT);
      const bool v = (unsigned)(gt >> 32) == tag;
      const float n = wave_sum(v ? 1.f : 0.f), d = wave_sum(v ? (float)(int)((unsigned)gt - tk) : 0.f);
      if (lane == 0 && n > 0.f) ((LAS unsigned*)lds)[L_REF] = tk + (unsigned)(int)(d / n);
    }
    const unsigned t0 = now32();
    unsigned long long gm, gl;
    while (true) {
      gm = __hip_atomic_load(ml + ((chunk & ~3) + (lane & 3)) * 16, RLX_AGENT);
      gl = __hip_atomic_load(ml + ((chunk & ~3) + (lane & 3)) * 16 + 1, RLX_AGENT);
      if (__all((unsigned)(gm >> 32) == tag && (unsigned)(gl >> 32) == tag)) break;
      if (now32() - t0 > SPIN_TICKS) break;
      __builtin_amdgcn_s_sleep(127);
    }
    const float ms = __uint_as_float((unsigned)gm), ls = __uint_as_float((unsigned)gl);
    float M = fmaxf(ms, dpp<XOR1>(ms)); M = fmaxf(M, dpp<XOR2>(M));
    float L = ls * __builtin_amdgcn_exp2f((ms - M) * LOG2E);
    L += dpp<XOR1>(L); L += dpp<XOR2>(L);
    if (lane < 4) lds[L_FW + lane] = __builtin_amdgcn_exp2f((mw - M) * LOG2E) / L;
    lds_wait();
    {
      const unsigned char* pa = (lane < 32 || red < 0) ? a.ws + WS_Y + chunk * 2048 + (lane & 15) * 128
                                                       : (const unsigned char*)a.out + (red & 1) * (DM * 2) + (red >> 1) * 64;
      const unsigned jk = __hip_atomic_load((gu32*)pa, RLX_AGENT);
      asm volatile("" :: "v"(jk));
    }
    if (blockIdx.x == NWG - 1) {
      while (true) {
        bool ok = true;
#pragma unroll
        for (int i = 0; i < 4; ++i) ok = ok & ((unsigned)(__hip_atomic_load(ml + (i * 64 + lane) * 16 + 2, RLX_AGENT) >> 32) == tag);
        if (__all(ok)) break;
        if (now32() - t0 > SPIN_TICKS) break;
        __builtin_amdgcn_s_sleep(127);
      }
      if (lane == 0) __hip_atomic_store((gu32*)(a.ws + WS_EPOCH), tag, RLX_AGENT);
    }
    while (__hip_atomic_load((LAS unsigned*)lds + L_CNT, RLX_WG) < 8u) __builtin_amdgcn_s_sleep(8);
    const unsigned t2 = now32();
    gu64* m2 = (gu64*)(a.ws + WS_M2);
    if (lane < 16) st_granule(m2 + chunk * 16 + lane, tag, __uint_as_float(t2));
    __builtin_amdgcn_s_sleep(100);
    const unsigned long long gt = __hip_atomic_load(m2 + (lane * 4 + (lane & 3)) * 16, RLX_AGENT);
    const bool v = (unsigned)(gt >> 32) == tag;
    const float n = wave_sum(v ? 1.f : 0.f), d = wave_sum(v ? (float)(int)((unsigned)gt - t2) : 0.f);
    if (lane == 0 && n > 47.f) ((LAS unsigned*)lds)[L_REF] = t2 + (unsigned)(int)(d / n) + (unsigned)(CHK_A - LAG_A - CHK_G * LAG_B);
    lds_wait();
  }
  {
    FRESH();
    wg_barrier();
    {
      const LAS float* P = (const LAS float*)lds + L_O + (4 * r) * 128 + wave * 16 + c;
      const float o = xrow_sum(((P[0] + P[128]) + (P[256] + P[384])) * lds[L_FW + r]);
#pragma unroll
      for (int i = 0; i < 16; ++i) {
        const float oi = lane_value(o, i);
#pragma unroll
        for (int k = 0; k < 4; ++k) yacc[k] = __builtin_elementwise_fma(wf[i * 4 + k], f32x2{oi, oi}, yacc[k]);
      }
    }
    *(LAS f32x4*)(lds + L_Y + wave * 512 + lane * 8) = f32x4{yacc[0][0], yacc[0][1], yacc[1][0], yacc[1][1]};
    *(LAS f32x4*)(lds + L_Y + wave * 512 + lane * 8 + 4) = f32x4{yacc[2][0], yacc[2][1], yacc[3][0], yacc[3][1]};
    lds_wait();
    wg_barrier();
    const rsrc_t ry = mk_rsrc(a.ws + WS_Y, NWG * 2048);
    {
      const LAS float* P = (const LAS float*)lds + L_Y + tid;
      const float y = ((P[0] + P[512]) + (P[1024] + P[1536])) + ((P[2048] + P[2560]) + (P[3072] + P[3584]));
      const unsigned u = __float_as_uint(y);
      const unsigned w = (((u & 0x7f800000u) == 0x7f800000u ? u | ((u & 0x7fffffu) ? 0x400000u : 0u) : u + 0x400u) & 0xfffff800u) | ((tag >> ((lane & 1) * 11)) & 0x7ffu);
      const unsigned w1 = __builtin_amdgcn_mov_dpp(w, XOR1, 0xf, 0xf, true);
      if (!(lane & 1)) __builtin_amdgcn_raw_buffer_store_b64(u32x2{w, w1}, ry, tid * 4, chunk * 2048, 16);
    }
    if (red >= 0) {
      const unsigned t0 = now32();
      u32x4 g0 = {0u, 0u, 0u, 0u}, g1 = {0u, 0u, 0u, 0u};
      bool ok0 = false, ok1 = false;
      const unsigned ta = tag & 0x7ffu, tb = (tag >> 11) & 0x7ffu;
      while (true) {
        if (!ok0) g0 = lddev(ry, (lane >> 3) * 2048 + (lane & 7) * 16, ((red & 1) * 128 + wave * 16) * 2048 + (red >> 1) * 128);
        if (!ok1) g1 = lddev(ry, (lane >> 3) * 2048 + (lane & 7) * 16, ((red & 1) * 128 + wave * 16 + 8) * 2048 + (red >> 1) * 128);
        ok0 = (((g0[0] ^ ta) | (g0[1] ^ tb) | (g0[2] ^ ta) | (g0[3] ^ tb)) & 0x7ffu) == 0u;
        ok1 = (((g1[0] ^ ta) | (g1[1] ^ tb) | (g1[2] ^ ta) | (g1[3] ^ tb)) & 0x7ffu) == 0u;
        if (__all(ok0 & ok1)) break;
        if (now32() - t0 > SPIN_TICKS) break;
        __builtin_amdgcn_s_sleep(4);
        asm volatile("" ::: "memory");
      }
      float t[4];
#pragma unroll
      for (int k = 0; k < 4; ++k) t[k] = __uint_as_float(g0[k] & 0xfffff800u) + __uint_as_float(g1[k] & 0xfffff800u);
      float s = tr16(tr32(t[0], t[2]), tr32(t[1], t[3]));
      s += dpp<ROR8>(s);
      if (!(lane & 8)) lds[L_FIN + wave * 32 + (lane & 7) * 4 + r] = s;
      lds_wait();
      wg_barrier();
      if (tid < 16) {
        const LAS f32x2* P = (const LAS f32x2*)(lds + L_FIN + tid * 2);
        const f32x2 o = bf_unpack(xw) + (((P[0] + P[16]) + (P[32] + P[48])) + ((P[64] + P[80]) + (P[96] + P[112])));
        *(gu32*)(a.out + (red & 1) * DM + (red >> 1) * 32 + tid * 2) = pack_bf16(o[0], o[1]);
      }
    }
  }
}

}

extern "C" void kernel_launch(void* const* d_in, const int* in_sizes, int n_in, void* d_out, int out_size, void* d_ws, size_t ws_size,
                              hipStream_t stream) {
  Args a;
  a.x = (const unsigned short*)d_in[0]; a.k = (const unsigned short*)d_in[1]; a.v = (const unsigned short*)d_in[2];
  a.wq = (const unsigned short*)d_in[3]; a.wo = (const unsigned short*)d_in[4];
  a.w1 = (const unsigned short*)d_in[6]; a.w3 = (const unsigned short*)d_in[7]; a.w2 = (const unsigned short*)d_in[8];
  a.out = (unsigned short*)d_out; a.ws = (unsigned char*)d_ws;
  fused_decode<<<NWG, NTHR, 0, stream>>>(a);
}
```

```cpp
#include <hip/hip_runtime.h>
#include <hip/hip_bf16.h>
#include <stdint.h>

namespace {

constexpr int NWG = 256, NTHR = 768, NRED = 32;
constexpr int DM = 512, NH = 32, SK = 16384, DH = 128, FF = 1792;
constexpr float LOG2E = 1.4426950408889634f;
constexpr float SCALE = 0.08838834764831845f;
constexpr int WIN = 4;
constexpr int NPRE = 3;
constexpr int LAG_A = 125, LAG_B = 108, LAG_2 = 80, WIDE_T = 3280;
constexpr size_t WS_EPOCH = 0;
constexpr size_t WS_ML = 8192;
constexpr size_t WS_Y = 65536;
constexpr size_t WS_Q = WS_Y + NWG * 2048;
constexpr size_t WS_G = WS_Q + (128u << 10);
constexpr int L_S = 0;
constexpr int L_O = L_S + 4096;
constexpr int L_Y = L_O + 2048;
constexpr int L_P = L_Y + 4096;
constexpr int L_Q = L_P + 1024;
constexpr int L_MX = L_Q + 64;
constexpr int L_FW = L_MX + 4;
constexpr int L_FIN = L_FW + 4;
constexpr int L_CNT = L_FIN + 256;
constexpr int L_REF = L_CNT + 3;
constexpr int L_END = L_REF + 1;

#define GAS __attribute__((address_space(1)))
#define LAS __attribute__((address_space(3)))
#define RLX_AGENT __ATOMIC_RELAXED, __HIP_MEMORY_SCOPE_AGENT
#define RLX_WG __ATOMIC_RELAXED, __HIP_MEMORY_SCOPE_WORKGROUP
typedef unsigned u32x2 __attribute__((ext_vector_type(2)));
typedef unsigned u32x4 __attribute__((ext_vector_type(4)));
typedef float f32x2 __attribute__((ext_vector_type(2)));
typedef float f32x4 __attribute__((ext_vector_type(4)));
typedef __bf16 bf16x2 __attribute__((ext_vector_type(2)));
typedef GAS unsigned gu32;
typedef GAS unsigned long long gu64;
typedef __amdgpu_buffer_rsrc_t rsrc_t;

__device__ __forceinline__ float bf_lo(unsigned u) { return __builtin_bit_cast(float, u << 16); }
__device__ __forceinline__ float bf_hi(unsigned u) { return __builtin_bit_cast(float, u & 0xffff0000u); }
__device__ __forceinline__ f32x2 bf_unpack(unsigned u) { return f32x2{bf_lo(u), bf_hi(u)}; }
__device__ __forceinline__ unsigned pack_bf16(float lo, float hi) {
  bf16x2 r = {(__bf16)lo, (__bf16)hi};
  return __builtin_bit_cast(unsigned, r);
}
__device__ __forceinline__ float rbf(float x) { return (float)(__bf16)x; }
__device__ __forceinline__ float dot2(unsigned a, unsigned b, float acc) {
  return __builtin_amdgcn_fdot2_f32_bf16(__builtin_bit_cast(bf16x2, a), __builtin_bit_cast(bf16x2, b), acc, false);
}
template <int CTRL> __device__ __forceinline__ float dpp(float x) {
  return __builtin_bit_cast(float, __builtin_amdgcn_mov_dpp(__builtin_bit_cast(int, x), CTRL, 0xf, 0xf, true));
}
constexpr int XOR1 = 0xB1, XOR2 = 0x4E, XOR3 = 0x1B, XOR7 = 0x141, ROR8 = 0x128;
__device__ __forceinline__ float tr16(float a, float b) {
  auto s = __builtin_amdgcn_permlane16_swap(__float_as_uint(a), __float_as_uint(b), false, false);
  return __uint_as_float(s[0]) + __uint_as_float(s[1]);
}
__device__ __forceinline__ float tr32(float a, float b) {
  auto s = __builtin_amdgcn_permlane32_swap(__float_as_uint(a), __float_as_uint(b), false, false);
  return __uint_as_float(s[0]) + __uint_as_float(s[1]);
}
__device__ __forceinline__ float xrow_sum(float x) { x = tr16(x, x); return tr32(x, x); }
__device__ __forceinline__ float wave_sum(float x) {
  x += dpp<XOR1>(x); x += dpp<XOR2>(x); x += dpp<XOR7>(x); x += dpp<ROR8>(x);
  return xrow_sum(x);
}
__device__ __forceinline__ float wave_max(float x) {
  x = fmaxf(x, dpp<XOR1>(x)); x = fmaxf(x, dpp<XOR2>(x)); x = fmaxf(x, dpp<XOR7>(x)); x = fmaxf(x, dpp<ROR8>(x));
  auto s = __builtin_amdgcn_permlane16_swap(__float_as_uint(x), __float_as_uint(x), false, false);
  x = fmaxf(__uint_as_float(s[0]), __uint_as_float(s[1]));
  auto t = __builtin_amdgcn_permlane32_swap(__float_as_uint(x), __float_as_uint(x), false, false);
  return fmaxf(__uint_as_float(t[0]), __uint_as_float(t[1]));
}
__device__ __forceinline__ float lane_value(float x, int l) { return __builtin_bit_cast(float, __builtin_amdgcn_readlane(__builtin_bit_cast(int, x), l)); }
__device__ __forceinline__ void lds_wait() { asm volatile("s_waitcnt lgkmcnt(0)" ::: "memory"); }
__device__ __forceinline__ void wg_barrier() { __builtin_amdgcn_s_barrier(); asm volatile("" ::: "memory"); }
__device__ __forceinline__ void st_granule(gu64* g, unsigned tag, float v) {
  __hip_atomic_store(g, ((unsigned long long)tag << 32) | (unsigned long long)__float_as_uint(v), RLX_AGENT);
}
__device__ __forceinline__ rsrc_t mk_rsrc(const void* p, int bytes) { return __builtin_amdgcn_make_buffer_rsrc((void*)p, 0, bytes, 0x00020000); }
__device__ __forceinline__ u32x4 ldnt(rsrc_t r, int voff, int soff) { return __builtin_amdgcn_raw_buffer_load_b128(r, voff, soff, 2); }
__device__ __forceinline__ u32x4 lddev(rsrc_t r, int voff, int soff) { return __builtin_amdgcn_raw_buffer_load_b128(r, voff, soff, 16); }
__device__ __forceinline__ unsigned now32() { return (unsigned)__builtin_amdgcn_s_memrealtime(); }
constexpr unsigned SPIN_TICKS = 20000000u;

#define SB() __builtin_amdgcn_sched_barrier(0)
#define VM_WAIT(n) do { asm volatile("s_waitcnt vmcnt(%0)" :: "i"(n)); SB(); } while (0)
#define FRESH() int lane_ = lane0; asm volatile("" : "+v"(lane_)); \
  const int lane = lane_, r = lane >> 4, c = lane & 15, tid = wave * 64 + lane, voff = lane * 16; (void)r; (void)c; (void)tid; (void)voff

struct Args {
  const unsigned short* x; const unsigned short* k; const unsigned short* v; const unsigned short* wq; const unsigned short* wo;
  const unsigned short* w1; const unsigned short* w3; const unsigned short* w2;
  unsigned short* out; unsigned char* ws;
};

__global__ void __launch_bounds__(NTHR, 3) fused_decode(Args a) {
  __shared__ __attribute__((aligned(16))) float lds[L_END];
  const int tid = threadIdx.x, lane = tid & 63, wave = __builtin_amdgcn_readfirstlane(tid >> 6);
  const int r = lane >> 4, c = lane & 15;
  const int xg = blockIdx.x & 7, j = blockIdx.x >> 3;
  const int b = j >> 4, h = xg + ((j >> 2) & 3) * 8, sp = j & 3;
  const int chunk = b * 128 + h * 4 + sp;
  const int voff = lane * 16;
  asm volatile("" :: "s"(a.x), "s"(a.k), "s"(a.v), "s"(a.wq), "s"(a.ws));

  const int g8 = b * 4 + sp;
  const int prow = (g8 >> 2) * 256 + wave * 32 + 2 * (lane >> 2);
  u32x4 xv0, xv1, wq0, wq1;
  unsigned xq0, xq1;
  if (wave < 8) {
    xv0 = *(const GAS u32x4*)(a.x + lane * 8); xv1 = *(const GAS u32x4*)(a.x + DM + lane * 8);
    xq0 = *(const gu32*)(a.x + prow); xq1 = *(const gu32*)(a.x + DM + prow);
    const rsrc_t rq = mk_rsrc(a.wq, DM * NH * DH * 2);
    wq0 = ldnt(rq, prow * (NH * DH * 2) + (lane & 3) * 16, h * (DH * 2) + (g8 & 3) * 64);
    wq1 = ldnt(rq, prow * (NH * DH * 2) + (lane & 3) * 16, h * (DH * 2) + (g8 & 3) * 64 + NH * DH * 2);
    SB();
    wg_barrier();
  }
  int role = wave;
  asm volatile("" : "+s"(role));
  if (__builtin_expect(role >= 8, 1)) {
    const int sw = wave - 8;
    constexpr int HB = SK * DH * 2;
    const unsigned short* kp = a.k + (size_t)(chunk >> 2) * SK * DH;
    const unsigned short* vp = a.v + (size_t)(chunk >> 2) * SK * DH;
    const rsrc_t rk = mk_rsrc(kp, HB), rv = mk_rsrc(vp, HB);
    const int voffh = voff + HB / 2;
    auto window = [&](int g) -> int { g += (j >> 3) * 2; return ((g & 1) << 19) + ((g & 14) << 15) + ((g & 16) << 16) + (sp << 14) + (sw << 12); };
#define ROWVO(n) ((((n) & 1) ? voffh : voff) + (((n) & 7) >> 1) * 1024)
#define ROWSO(grp, n) window((grp) + ((n) >> 3))
#define LDROW(rs, grp, n) ldnt(rs, ROWVO(n), ROWSO(grp, n))
    __builtin_amdgcn_s_setprio(2);
    u32x4 buf[8], pre[NPRE * 8];
#pragma unroll
    for (int u = 0; u < NPRE * 8; ++u) {
      pre[u] = LDROW(rk, 0, u); SB();
      if (u == WIN) {
        if (lane < 4) ((LAS unsigned*)lds)[L_CNT + lane] = lane < 3 ? 0u : now32() + 0x40000000u;
        lds_wait();
        wg_barrier();
        SB();
      }
      if (u >= WIN) VM_WAIT(WIN);
    }
#pragma unroll
    for (int u = 0; u < 8; ++u) {
      buf[u] = LDROW(rk, NPRE, u); SB();
      if (u < 7) VM_WAIT(WIN);
    }
    wg_barrier();
    const u32x4 qp = *(const LAS u32x4*)(lds + L_Q + c * 4);
    LAS float* S = (LAS float*)lds + L_S + sw * 1024;
    float mx = -INFINITY;
#define DOT(w) dot2((w)[3], qp[3], dot2((w)[2], qp[2], dot2((w)[1], qp[1], dot2((w)[0], qp[0], 0.f))))
#define K_FIN(grp) do { \
      _Pragma("unroll") for (int u = 0; u < 4; ++u) { const float lo = x[u] + dpp<ROR8>(x[u]), hi = x[u + 4] + dpp<ROR8>(x[u + 4]); x[u] = (c & 8) ? hi : lo; } \
      _Pragma("unroll") for (int u = 0; u < 2; ++u) { const float lo = x[u] + dpp<XOR7>(x[u]), hi = x[u + 2] + dpp<XOR7>(x[u + 2]); x[u] = (c & 4) ? hi : lo; } \
      { const float lo = x[0] + dpp<XOR2>(x[0]), hi = x[1] + dpp<XOR2>(x[1]); x[0] = (c & 2) ? hi : lo; } \
      float sc = x[0] + dpp<XOR1>(x[0]); \
      sc = rbf(rbf(sc) * SCALE); \
      mx = fmaxf(mx, sc); \
      S[(grp) * 32 + r * 8 + (c >> 1)] = sc; \
      SB(); \
    } while (0)
    static_assert(WIN >= 3 && WIN <= 5, "ring of 8 rows");
    {
      float x[8];
#pragma unroll
      for (int u = 0; u < 8; ++u) {
        if (u > 0 && u <= WIN + 1) { buf[u - 1] = LDROW(rk, NPRE + 1, u - 1); SB(); }
        x[u] = DOT(buf[u]);
        SB();
      }
      K_FIN(NPRE);
    }
#pragma unroll
    for (int pp = 0; pp < NPRE; ++pp) {
      float x[8];
#pragma unroll
      for (int w = 0; w < 8; ++w) x[w] = DOT(pre[pp * 8 + w]);
      K_FIN(pp);
      if (WIN + 1 + pp < 7) { VM_WAIT(WIN); buf[WIN + 1 + pp] = LDROW(rk, NPRE + 1, WIN + 1 + pp); SB(); }
    }
    static_assert(WIN + 1 + NPRE >= 7, "");
    int last = 31;
    asm volatile("" : "+s"(last));
#pragma nounroll
    for (int tt = NPRE + 1; tt <= last; ++tt) {
      const rsrc_t rn = mk_rsrc(tt == last ? vp : kp, HB);
      float x[8];
#pragma unroll
      for (int u = 0; u < 8; ++u) {
        const int n = u + 7;
        const int so = ROWSO(tt, n);
        asm volatile("s_waitcnt vmcnt(%0)" :: "i"(WIN), "s"(so));
        SB();
        buf[n & 7] = ldnt(n >= 8 ? rn : rk, ROWVO(n), so);
        SB();
        x[u] = DOT(buf[u]);
        SB();
      }
      K_FIN(tt);
    }
    mx = wave_max(mx);
    if (lane == 0) lds[L_MX + sw] = mx;
    lds_wait();
    if (lane == 0) __hip_atomic_fetch_add((LAS unsigned*)lds + L_CNT, 1u, RLX_WG);
    f32x2 acc[4] = {{0.f, 0.f}, {0.f, 0.f}, {0.f, 0.f}, {0.f, 0.f}};
    f32x4 s0 = *(const LAS f32x4*)(S + r * 8), s1;
    int open = -1, lag = -1;
#pragma nounroll
    for (int tt = 0; tt <= last; ++tt) {
      const rsrc_t rn = mk_rsrc(vp, tt == last ? 0 : HB);
      unsigned ref, tnow;
#pragma unroll
      for (int u = 0; u < 8; ++u) {
        const int n = u + 7;
        const int so = ROWSO(tt, n);
        asm volatile("s_cmp_gt_i32 %0, 0\n\ts_cbranch_scc1 1f\n\ts_waitcnt vmcnt(%2)\n\ts_cmp_gt_i32 %1, 0\n\ts_cbranch_scc1 1f\n\ts_waitcnt vmcnt(%3)\n1:"
                     :: "s"(open), "s"(lag), "i"(WIN + 2), "i"(WIN), "s"(so) : "scc");
        SB();
        buf[n & 7] = ldnt(n >= 8 ? rn : rv, ROWVO(n), so);
        SB();
        if (u == 0) s1 = *(const LAS f32x4*)(S + tt * 32 + r * 8 + 4);
        if (u == 4) { s0 = *(const LAS f32x4*)(S + ((tt + 1) & 31) * 32 + r * 8); ref = *(volatile LAS unsigned*)(lds + L_REF); tnow = now32(); }
        const float p = __builtin_amdgcn_exp2f(((u < 4 ? s0[u & 3] : s1[u & 3]) - mx) * LOG2E);
#pragma unroll
        for (int k = 0; k < 4; ++k) acc[k] = __builtin_elementwise_fma(bf_unpack(buf[u][k]), f32x2{p, p}, acc[k]);
        asm volatile("" : "+v"(acc[0]), "+v"(acc[1]), "+v"(acc[2]), "+v"(acc[3]));
        if (u == 6) {
          asm volatile("" : "+v"(ref), "+s"(tnow));
          const int el = (int)(tnow - __builtin_amdgcn_readfirstlane(ref));
          lag = el - (LAG_A + tt * LAG_B);
          open = max(el - WIDE_T, lag - LAG_2);
        }
        SB();
      }
    }
    *(LAS f32x4*)(lds + L_O + (sw * 4 + r) * 128 + c * 8) = f32x4{acc[0][0], acc[0][1], acc[1][0], acc[1][1]};
    *(LAS f32x4*)(lds + L_O + (sw * 4 + r) * 128 + c * 8 + 4) = f32x4{acc[2][0], acc[2][1], acc[3][0], acc[3][1]};
    lds_wait();
    wg_barrier();
    wg_barrier();
    if (blockIdx.x < NRED) wg_barrier();
    return;
  }

  unsigned epoch = *(const __attribute__((address_space(4))) unsigned*)(a.ws + WS_EPOCH);
  SB();
  float mb0, mb1, sd0, sd1;
  {
    float f0[8], f1[8];
#pragma unroll
    for (int k = 0; k < 4; ++k) { f0[2 * k] = bf_lo(xv0[k]); f0[2 * k + 1] = bf_hi(xv0[k]); f1[2 * k] = bf_lo(xv1[k]); f1[2 * k + 1] = bf_hi(xv1[k]); }
    float s0 = 0.f, s1 = 0.f;
#pragma unroll
    for (int k = 0; k < 8; ++k) { s0 += f0[k]; s1 += f1[k]; }
    const float m0 = wave_sum(s0) * (1.f / DM), m1 = wave_sum(s1) * (1.f / DM);
    float v0 = 0.f, v1 = 0.f;
#pragma unroll
    for (int k = 0; k < 8; ++k) { const float d0 = f0[k] - m0, d1 = f1[k] - m1; v0 = fmaf(d0, d0, v0); v1 = fmaf(d1, d1, v1); }
    v0 = wave_sum(v0) * (1.f / DM); v1 = wave_sum(v1) * (1.f / DM);
    mb0 = rbf(m0); sd0 = rbf(__builtin_sqrtf(rbf(rbf(v0) + 1e-5f)));
    mb1 = rbf(m1); sd1 = rbf(__builtin_sqrtf(rbf(rbf(v1) + 1e-5f)));
  }
#define XN(xf, mb, sd) rbf(rbf((xf) - (mb)) / (sd))
  asm volatile("" : "+s"(epoch));
  const unsigned tag = epoch + 0x9E3779B9u;
  const float xa0 = XN(bf_lo(xq0), mb0, sd0), xa1 = XN(bf_hi(xq0), mb0, sd0), xb0 = XN(bf_lo(xq1), mb1, sd1), xb1 = XN(bf_hi(xq1), mb1, sd1);
#define PARTIAL(w0_, w1_, n, res) do { \
    float t[8]; \
    _Pragma("unroll") for (int k = 0; k < 4; ++k) { \
      const f32x2 w0 = bf_unpack((w0_)[k]), w1 = bf_unpack((w1_)[k]); \
      const f32x2 qa = __builtin_elementwise_fma(w1, f32x2{xa1, xa1}, w0 * xa0), qb = __builtin_elementwise_fma(w1, f32x2{xb1, xb1}, w0 * xb0); \
      t[2 * k] = tr32(qa[0], qb[0]); t[2 * k + 1] = tr32(qa[1], qb[1]);             \
    } \
    _Pragma("unroll") for (int k = 0; k < 4; ++k) t[k] = tr16(t[k], t[4 + k]);        \
    f32x2 o; \
    _Pragma("unroll") for (int k = 0; k < 2; ++k) { const float lo = t[k] + dpp<ROR8>(t[k]), hi = t[2 + k] + dpp<ROR8>(t[2 + k]); o[k] = (c & 8) ? hi : lo; } \
    const float lo = o[0] + dpp<XOR7>(dpp<XOR3>(o[0])), hi = o[1] + dpp<XOR7>(dpp<XOR3>(o[1]));     \
    lds[L_P + (n) * 512 + wave * 64 + (r >> 1) * 32 + (c & 3) * 8 + (r & 1) * 4 + (c >> 3) * 2 + ((c >> 2) & 1)] = (c & 4) ? hi : lo; \
    lds_wait(); \
    if (lane == 0) __hip_atomic_fetch_add((LAS unsigned*)lds + L_CNT + 1 + (n), 1u, RLX_WG); \
    while (__hip_atomic_load((LAS unsigned*)lds + L_CNT + 1 + (n), RLX_WG) < 8u) __builtin_amdgcn_s_sleep(0); \
    asm volatile("" ::: "memory"); \
    const LAS float* P = (const LAS float*)lds + L_P + (n) * 512 + lane; \
    res = ((P[0] + P[64]) + (P[128] + P[192])) + ((P[256] + P[320]) + (P[384] + P[448])); \
  } while (0)
  {
    float qs;
    PARTIAL(wq0, wq1, 0, qs);
    if (wave == 0) st_granule((gu64*)(a.ws + WS_Q) + (h * 8 + g8) * 64 + lane, tag, qs);
  }
  if (wave == 7) {
    __builtin_amdgcn_s_sleep(32);
    const rsrc_t rg = mk_rsrc(a.ws + WS_Q + (size_t)h * 4096, 4096);
    const unsigned t0 = now32();
    u32x4 g0, g1;
    while (true) {
      g0 = lddev(rg, r * 512 + c * 16, b * 256); g1 = lddev(rg, r * 512 + c * 16, 2048 + b * 256);
      if (__all((g0[1] == tag) & (g0[3] == tag) & (g1[1] == tag) & (g1[3] == tag))) break;
      if (now32() - t0 > SPIN_TICKS) break;
      __builtin_amdgcn_s_sleep(8);
      asm volatile("" ::: "memory");
    }
    ((LAS unsigned*)lds)[L_Q + lane] = pack_bf16(__uint_as_float(g0[0]) + __uint_as_float(g1[0]), __uint_as_float(g0[2]) + __uint_as_float(g1[2]));
    lds_wait();
  }
  wg_barrier();
  const int red = blockIdx.x < NRED ? (xg * 2 + (j & 1)) * 2 + (j >> 1) : -1;
  const unsigned xw = (red >= 0 && tid < 16) ? *(const gu32*)(a.x + (red & 1) * DM + (red >> 1) * 32 + tid * 2) : 0u;
  float gsum = 0.f;
  if (h < 28) {
    const rsrc_t rm = mk_rsrc((g8 & 2) ? a.w3 : a.w1, DM * FF * 2);
    const int so = h * 128 + (g8 & 1) * 64;
    const u32x4 wm0 = ldnt(rm, prow * (FF * 2) + (lane & 3) * 16, so), wm1 = ldnt(rm, prow * (FF * 2) + (lane & 3) * 16, so + FF * 2);
    PARTIAL(wm0, wm1, 1, gsum);
  }
  f32x2 wf[64];
  {
    const rsrc_t ro = mk_rsrc(a.wo + (size_t)h * DH * DM, DH * DM * 2);
#pragma unroll
    for (int i = 0; i < 16; ++i) {
      const u32x4 w = ldnt(ro, voff + (i & 3) * 1024, (wave * 16 + (i & ~3)) * 1024);
      SB();
#pragma unroll
      for (int k = 0; k < 4; ++k) { wf[i * 4 + k] = bf_unpack(w[k]); asm volatile("" : "+v"(wf[i * 4 + k])); }
      SB();
    }
  }
  const int lane0 = lane;
  while (__hip_atomic_load((LAS unsigned*)lds + L_CNT, RLX_WG) < 4u) __builtin_amdgcn_s_sleep(8);
  asm volatile("" ::: "memory");
  gu64* ml = (gu64*)(a.ws + WS_ML);
  gu64* G = (gu64*)(a.ws + WS_G);
  float mw = 0.f;
  const unsigned tk = now32();
  if (wave == 7) {
    FRESH();
    mw = lds[L_MX + (lane & 3)];
    float m = fmaxf(mw, dpp<XOR1>(mw)); m = fmaxf(m, dpp<XOR2>(m));
    float l = 0.f;
#pragma unroll 2
    for (int i = 0; i < 16; ++i) {
      const f32x4 sv = *(const LAS f32x4*)(lds + L_S + i * 256 + lane * 4);
#pragma unroll
      for (int k = 0; k < 4; ++k) l += __builtin_amdgcn_exp2f((sv[k] - m) * LOG2E);
    }
    l = wave_sum(l);
    if (lane < 16) st_granule(ml + chunk * 16 + lane, tag, lane == 0 ? m : lane == 1 ? l : __uint_as_float(tk));
  } else if (wave == 0 && h < 28) {
    FRESH();
    __builtin_amdgcn_s_sleep(16);
    st_granule(G + (h * 8 + g8) * 64 + lane, tag, gsum);
  }
  __builtin_amdgcn_s_sleep(127); __builtin_amdgcn_s_sleep(16);
  f32x2 yacc[4] = {{0.f, 0.f}, {0.f, 0.f}, {0.f, 0.f}, {0.f, 0.f}};
  if (wave < 7) {
    FRESH();
    const int f0 = (h * 4 + sp) * 14 + wave * 2;
    const rsrc_t r2 = mk_rsrc(a.w2 + (size_t)f0 * DM, 2 * DM * 2);
    const u32x4 w20 = ldnt(r2, voff, 0), w21 = ldnt(r2, voff + 1024, 0);
    const int f = f0 + (r & 1);
    gu64* gp = G + ((f >> 6) * 8 + (c & 1) * 4 + (c >> 3) * 2 + ((f >> 5) & 1)) * 64 + b * 32 + (f & 31);
    const unsigned t0 = now32();
    unsigned long long g;
    while (true) {
      g = __hip_atomic_load(gp, RLX_AGENT);
      if (__all((unsigned)(g >> 32) == tag)) break;
      if (now32() - t0 > SPIN_TICKS) break;
      __builtin_amdgcn_s_sleep(127);
    }
    float s = __uint_as_float((unsigned)g);
    s += dpp<XOR1>(s);
    const float gs = rbf(s), us = rbf(dpp<ROR8>(s));
    const float hv = rbf(rbf(gs / (1.f + __expf(-gs))) * us);
    const float h0 = lane_value(hv, 0), h1 = lane_value(hv, 16);
#pragma unroll
    for (int k = 0; k < 4; ++k) yacc[k] = __builtin_elementwise_fma(bf_unpack(w21[k]), f32x2{h1, h1}, bf_unpack(w20[k]) * h0);
  } else {
    FRESH();
    {
      const unsigned long long gt = __hip_atomic_load(ml + (lane * 4 + (lane & 3)) * 16 + 2, RLX_AGENT);
      const bool v = (unsigned)(gt >> 32) == tag;
      const float n = wave_sum(v ? 1.f : 0.f), d = wave_sum(v ? (float)(int)((unsigned)gt - tk) : 0.f);
      if (lane == 0 && n > 0.f) ((LAS unsigned*)lds)[L_REF] = tk + (unsigned)(int)(d / n);
    }
    const unsigned t0 = now32();
    unsigned long long gm, gl;
    while (true) {
      gm = __hip_atomic_load(ml + ((chunk & ~3) + (lane & 3)) * 16, RLX_AGENT);
      gl = __hip_atomic_load(ml + ((chunk & ~3) + (lane & 3)) * 16 + 1, RLX_AGENT);
      if (__all((unsigned)(gm >> 32) == tag && (unsigned)(gl >> 32) == tag)) break;
      if (now32() - t0 > SPIN_TICKS) break;
      __builtin_amdgcn_s_sleep(127);
    }
    const float ms = __uint_as_float((unsigned)gm), ls = __uint_as_float((unsigned)gl);
    float M = fmaxf(ms, dpp<XOR1>(ms)); M = fmaxf(M, dpp<XOR2>(M));
    float L = ls * __builtin_amdgcn_exp2f((ms - M) * LOG2E);
    L += dpp<XOR1>(L); L += dpp<XOR2>(L);
    if (lane < 4) lds[L_FW + lane] = __builtin_amdgcn_exp2f((mw - M) * LOG2E) / L;
    lds_wait();
    {
      const unsigned char* pa = (lane < 32 || red < 0) ? a.ws + WS_Y + chunk * 2048 + (lane & 15) * 128
                                                       : (const unsigned char*)a.out + (red & 1) * (DM * 2) + (red >> 1) * 64;
      const unsigned jk = __hip_atomic_load((gu32*)pa, RLX_AGENT);
      asm volatile("" :: "v"(jk));
    }
    if (blockIdx.x == NWG - 1) {
      while (true) {
        bool ok = true;
#pragma unroll
        for (int i = 0; i < 4; ++i) ok = ok & ((unsigned)(__hip_atomic_load(ml + (i * 64 + lane) * 16 + 2, RLX_AGENT) >> 32) == tag);
        if (__all(ok)) break;
        if (now32() - t0 > SPIN_TICKS) break;
        __builtin_amdgcn_s_sleep(127);
      }
      if (lane == 0) __hip_atomic_store((gu32*)(a.ws + WS_EPOCH), tag, RLX_AGENT);
    }
  }
  {
    FRESH();
    wg_barrier();
    {
      const LAS float* P = (const LAS float*)lds + L_O + (4 * r) * 128 + wave * 16 + c;
      const float o = xrow_sum(((P[0] + P[128]) + (P[256] + P[384])) * lds[L_FW + r]);
#pragma unroll
      for (int i = 0; i < 16; ++i) {
        const float oi = lane_value(o, i);
#pragma unroll
        for (int k = 0; k < 4; ++k) yacc[k] = __builtin_elementwise_fma(wf[i * 4 + k], f32x2{oi, oi}, yacc[k]);
      }
    }
    *(LAS f32x4*)(lds + L_Y + wave * 512 + lane * 8) = f32x4{yacc[0][0], yacc[0][1], yacc[1][0], yacc[1][1]};
    *(LAS f32x4*)(lds + L_Y + wave * 512 + lane * 8 + 4) = f32x4{yacc[2][0], yacc[2][1], yacc[3][0], yacc[3][1]};
    lds_wait();
    wg_barrier();
    const rsrc_t ry = mk_rsrc(a.ws + WS_Y, NWG * 2048);
    {
      const LAS float* P = (const LAS float*)lds + L_Y + tid;
      const float y = ((P[0] + P[512]) + (P[1024] + P[1536])) + ((P[2048] + P[2560]) + (P[3072] + P[3584]));
      const unsigned u = __float_as_uint(y);
      const unsigned w = (((u & 0x7f800000u) == 0x7f800000u ? u | ((u & 0x7fffffu) ? 0x400000u : 0u) : u + 0x400u) & 0xfffff800u) | ((tag >> ((lane & 1) * 11)) & 0x7ffu);
      const unsigned w1 = __builtin_amdgcn_mov_dpp(w, XOR1, 0xf, 0xf, true);
      if (!(lane & 1)) __builtin_amdgcn_raw_buffer_store_b64(u32x2{w, w1}, ry, tid * 4, chunk * 2048, 16);
    }
    if (red >= 0) {
      const unsigned t0 = now32();
      u32x4 g0 = {0u, 0u, 0u, 0u}, g1 = {0u, 0u, 0u, 0u};
      bool ok0 = false, ok1 = false;
      const unsigned ta = tag & 0x7ffu, tb = (tag >> 11) & 0x7ffu;
      while (true) {
        if (!ok0) g0 = lddev(ry, (lane >> 3) * 2048 + (lane & 7) * 16, ((red & 1) * 128 + wave * 16) * 2048 + (red >> 1) * 128);
        if (!ok1) g1 = lddev(ry, (lane >> 3) * 2048 + (lane & 7) * 16, ((red & 1) * 128 + wave * 16 + 8) * 2048 + (red >> 1) * 128);
        ok0 = (((g0[0] ^ ta) | (g0[1] ^ tb) | (g0[2] ^ ta) | (g0[3] ^ tb)) & 0x7ffu) == 0u;
        ok1 = (((g1[0] ^ ta) | (g1[1] ^ tb) | (g1[2] ^ ta) | (g1[3] ^ tb)) & 0x7ffu) == 0u;
        if (__all(ok0 & ok1)) break;
        if (now32() - t0 > SPIN_TICKS) break;
        __builtin_amdgcn_s_sleep(4);
        asm volatile("" ::: "memory");
      }
      float t[4];
#pragma unroll
      for (int k = 0; k < 4; ++k) t[k] = __uint_as_float(g0[k] & 0xfffff800u) + __uint_as_float(g1[k] & 0xfffff800u);
      float s = tr16(tr32(t[0], t[2]), tr32(t[1], t[3]));
      s += dpp<ROR8>(s);
      if (!(lane & 8)) lds[L_FIN + wave * 32 + (lane & 7) * 4 + r] = s;
      lds_wait();
      wg_barrier();
      if (tid < 16) {
        const LAS f32x2* P = (const LAS f32x2*)(lds + L_FIN + tid * 2);
        const f32x2 o = bf_unpack(xw) + (((P[0] + P[16]) + (P[32] + P[48])) + ((P[64] + P[80]) + (P[96] + P[112])));
        *(gu32*)(a.out + (red & 1) * DM + (red >> 1) * 32 + tid * 2) = pack_bf16(o[0], o[1]);
      }
    }
  }
}

}

extern "C" void kernel_launch(void* const* d_in, const int* in_sizes, int n_in, void* d_out, int out_size, void* d_ws, size_t ws_size,
                              hipStream_t stream) {
  Args a;
  a.x = (const unsigned short*)d_in[0]; a.k = (const unsigned short*)d_in[1]; a.v = (const unsigned short*)d_in[2];
  a.wq = (const unsigned short*)d_in[3]; a.wo = (const unsigned short*)d_in[4];
  a.w1 = (const unsigned short*)d_in[6]; a.w3 = (const unsigned short*)d_in[7]; a.w2 = (const unsigned short*)d_in[8];
  a.out = (unsigned short*)d_out; a.ws = (unsigned char*)d_ws;
  fused_decode<<<NWG, NTHR, 0, stream>>>(a);
}
```

```cpp
#include <hip/hip_runtime.h>
#include <hip/hip_bf16.h>
#include <stdint.h>

namespace {

constexpr int NWG = 256, NTHR = 768, NRED = 32;
constexpr int DM = 512, NH = 32, SK = 16384, DH = 128, FF = 1792;
constexpr float LOG2E = 1.4426950408889634f;
constexpr float SCALE = 0.08838834764831845f;
constexpr int WIN = 4;
constexpr int NPRE = 2;
constexpr int LAG_A = 80, LAG_B = 108, LAG_2 = 80, WIDE_T = 3235;
constexpr size_t WS_EPOCH = 0;
constexpr size_t WS_ML = 8192;
constexpr size_t WS_Y = 65536;
constexpr size_t WS_Q = WS_Y + NWG * 2048;
constexpr size_t WS_G = WS_Q + (128u << 10);
constexpr int L_S = 0;
constexpr int L_O = L_S + 4096;
constexpr int L_Y = L_O + 2048;
constexpr int L_P = L_Y + 4096;
constexpr int L_Q = L_P + 1024;
constexpr int L_MX = L_Q + 64;
constexpr int L_FW = L_MX + 4;
constexpr int L_FIN = L_FW + 4;
constexpr int L_CNT = L_FIN + 256;
constexpr int L_REF = L_CNT + 3;
constexpr int L_END = L_REF + 1;

#define GAS __attribute__((address_space(1)))
#define LAS __attribute__((address_space(3)))
#define RLX_AGENT __ATOMIC_RELAXED, __HIP_MEMORY_SCOPE_AGENT
#define RLX_WG __ATOMIC_RELAXED, __HIP_MEMORY_SCOPE_WORKGROUP
typedef unsigned u32x2 __attribute__((ext_vector_type(2)));
typedef unsigned u32x4 __attribute__((ext_vector_type(4)));
typedef float f32x2 __attribute__((ext_vector_type(2)));
typedef float f32x4 __attribute__((ext_vector_type(4)));
typedef __bf16 bf16x2 __attribute__((ext_vector_type(2)));
typedef GAS unsigned gu32;
typedef GAS unsigned long long gu64;
typedef __amdgpu_buffer_rsrc_t rsrc_t;

__device__ __forceinline__ float bf_lo(unsigned u) { return __builtin_bit_cast(float, u << 16); }
__device__ __forceinline__ float bf_hi(unsigned u) { return __builtin_bit_cast(float, u & 0xffff0000u); }
__device__ __forceinline__ f32x2 bf_unpack(unsigned u) { return f32x2{bf_lo(u), bf_hi(u)}; }
__device__ __forceinline__ unsigned pack_bf16(float lo, float hi) {
  bf16x2 r = {(__bf16)lo, (__bf16)hi};
  return __builtin_bit_cast(unsigned, r);
}
__device__ __forceinline__ float rbf(float x) { return (float)(__bf16)x; }
__device__ __forceinline__ float dot2(unsigned a, unsigned b, float acc) {
  return __builtin_amdgcn_fdot2_f32_bf16(__builtin_bit_cast(bf16x2, a), __builtin_bit_cast(bf16x2, b), acc, false);
}
template <int CTRL> __device__ __forceinline__ float dpp(float x) {
  return __builtin_bit_cast(float, __builtin_amdgcn_mov_dpp(__builtin_bit_cast(int, x), CTRL, 0xf, 0xf, true));
}
constexpr int XOR1 = 0xB1, XOR2 = 0x4E, XOR3 = 0x1B, XOR7 = 0x141, ROR8 = 0x128;
__device__ __forceinline__ float tr16(float a, float b) {
  auto s = __builtin_amdgcn_permlane16_swap(__float_as_uint(a), __float_as_uint(b), false, false);
  return __uint_as_float(s[0]) + __uint_as_float(s[1]);
}
__device__ __forceinline__ float tr32(float a, float b) {
  auto s = __builtin_amdgcn_permlane32_swap(__float_as_uint(a), __float_as_uint(b), false, false);
  return __uint_as_float(s[0]) + __uint_as_float(s[1]);
}
__device__ __forceinline__ float xrow_sum(float x) { x = tr16(x, x); return tr32(x, x); }
__device__ __forceinline__ float wave_sum(float x) {
  x += dpp<XOR1>(x); x += dpp<XOR2>(x); x += dpp<XOR7>(x); x += dpp<ROR8>(x);
  return xrow_sum(x);
}
__device__ __forceinline__ float wave_max(float x) {
  x = fmaxf(x, dpp<XOR1>(x)); x = fmaxf(x, dpp<XOR2>(x)); x = fmaxf(x, dpp<XOR7>(x)); x = fmaxf(x, dpp<ROR8>(x));
  auto s = __builtin_amdgcn_permlane16_swap(__float_as_uint(x), __float_as_uint(x), false, false);
  x = fmaxf(__uint_as_float(s[0]), __uint_as_float(s[1]));
  auto t = __builtin_amdgcn_permlane32_swap(__float_as_uint(x), __float_as_uint(x), false, false);
  return fmaxf(__uint_as_float(t[0]), __uint_as_float(t[1]));
}
__device__ __forceinline__ float lane_value(float x, int l) { return __builtin_bit_cast(float, __builtin_amdgcn_readlane(__builtin_bit_cast(int, x), l)); }
__device__ __forceinline__ void lds_wait() { asm volatile("s_waitcnt lgkmcnt(0)" ::: "memory"); }
__device__ __forceinline__ void wg_barrier() { __builtin_amdgcn_s_barrier(); asm volatile("" ::: "memory"); }
__device__ __forceinline__ void st_granule(gu64* g, unsigned tag, float v) {
  __hip_atomic_store(g, ((unsigned long long)tag << 32) | (unsigned long long)__float_as_uint(v), RLX_AGENT);
}
__device__ __forceinline__ rsrc_t mk_rsrc(const void* p, int bytes) { return __builtin_amdgcn_make_buffer_rsrc((void*)p, 0, bytes, 0x00020000); }
__device__ __forceinline__ u32x4 ldnt(rsrc_t r, int voff, int soff) { return __builtin_amdgcn_raw_buffer_load_b128(r, voff, soff, 2); }
__device__ __forceinline__ u32x4 lddev(rsrc_t r, int voff, int soff) { return __builtin_amdgcn_raw_buffer_load_b128(r, voff, soff, 16); }
__device__ __forceinline__ u32x4 lddef(rsrc_t r, int voff, int soff) { return __builtin_amdgcn_raw_buffer_load_b128(r, voff, soff, 0); }
__device__ __forceinline__ unsigned now32() { return (unsigned)__builtin_amdgcn_s_memrealtime(); }
constexpr unsigned SPIN_TICKS = 20000000u;

#define SB() __builtin_amdgcn_sched_barrier(0)
#define VM_WAIT(n) do { asm volatile("s_waitcnt vmcnt(%0)" :: "i"(n)); SB(); } while (0)
#define FRESH() int lane_ = lane0; asm volatile("" : "+v"(lane_)); \
  const int lane = lane_, r = lane >> 4, c = lane & 15, tid = wave * 64 + lane, voff = lane * 16; (void)r; (void)c; (void)tid; (void)voff

struct Args {
  const unsigned short* x; const unsigned short* k; const unsigned short* v; const unsigned short* wq; const unsigned short* wo;
  const unsigned short* w1; const unsigned short* w3; const unsigned short* w2;
  unsigned short* out; unsigned char* ws;
};

__global__ void __launch_bounds__(NTHR, 3) fused_decode(Args a) {
  __shared__ __attribute__((aligned(16))) float lds[L_END];
  const int tid = threadIdx.x, lane = tid & 63, wave = __builtin_amdgcn_readfirstlane(tid >> 6);
  const int r = lane >> 4, c = lane & 15;
  const int xg = blockIdx.x & 7, j = blockIdx.x >> 3;
  const int b = j >> 4, h = xg + ((j >> 2) & 3) * 8, sp = j & 3;
  const int chunk = b * 128 + h * 4 + sp;
  const int voff = lane * 16;
  asm volatile("" :: "s"(a.x), "s"(a.k), "s"(a.v), "s"(a.wq), "s"(a.ws));

  const int g8 = b * 4 + sp;
  const int prow = (g8 >> 2) * 256 + wave * 32 + 2 * (lane >> 2);
  u32x4 xv0, xv1, wq0, wq1;
  unsigned xq0, xq1;
  u32x4 wm0, wm1, pf0, pf1, w20, w21;
  if (wave < 8) {
    xv0 = *(const GAS u32x4*)(a.x + lane * 8); xv1 = *(const GAS u32x4*)(a.x + DM + lane * 8);
    xq0 = *(const gu32*)(a.x + prow); xq1 = *(const gu32*)(a.x + DM + prow);
    const rsrc_t rq = mk_rsrc(a.wq, DM * NH * DH * 2);
    wq0 = ldnt(rq, prow * (NH * DH * 2) + (lane & 3) * 16, h * (DH * 2) + (g8 & 3) * 64);
    wq1 = ldnt(rq, prow * (NH * DH * 2) + (lane & 3) * 16, h * (DH * 2) + (g8 & 3) * 64 + NH * DH * 2);
    SB();
    {
      const rsrc_t rm = mk_rsrc((g8 & 2) ? a.w3 : a.w1, h < 28 ? DM * FF * 2 : 0);
      const int so = h * 128 + (g8 & 1) * 64;
      wm0 = ldnt(rm, prow * (FF * 2) + (lane & 3) * 16, so); wm1 = ldnt(rm, prow * (FF * 2) + (lane & 3) * 16, so + FF * 2);
      const rsrc_t ro = mk_rsrc(a.wo + (size_t)h * DH * DM, DH * DM * 2);
      pf0 = lddef(ro, voff, (g8 * 16 + wave * 2) * 1024); pf1 = lddef(ro, voff + 1024, (g8 * 16 + wave * 2) * 1024);
      const rsrc_t r2 = mk_rsrc(a.w2 + (size_t)((h * 4 + sp) * 14 + wave * 2) * DM, wave < 7 ? 2 * DM * 2 : 0);
      w20 = ldnt(r2, voff, 0); w21 = ldnt(r2, voff + 1024, 0);
    }
    SB();
    wg_barrier();
  }
  int role = wave;
  asm volatile("" : "+s"(role));
  if (__builtin_expect(role >= 8, 1)) {
    const int sw = wave - 8;
    constexpr int HB = SK * DH * 2;
    const unsigned short* kp = a.k + (size_t)(chunk >> 2) * SK * DH;
    const unsigned short* vp = a.v + (size_t)(chunk >> 2) * SK * DH;
    const rsrc_t rk = mk_rsrc(kp, HB), rv = mk_rsrc(vp, HB);
    const int voffh = voff + HB / 2;
    auto window = [&](int g) -> int { g += (j >> 3) * 2; return ((g & 1) << 19) + ((g & 14) << 15) + ((g & 16) << 16) + (sp << 14) + (sw << 12); };
#define ROWVO(n) ((((n) & 1) ? voffh : voff) + (((n) & 7) >> 1) * 1024)
#define ROWSO(grp, n) window((grp) + ((n) >> 3))
#define LDROW(rs, grp, n) ldnt(rs, ROWVO(n), ROWSO(grp, n))
    __builtin_amdgcn_s_setprio(2);
    u32x4 buf[8], pre[NPRE * 8];
    if (lane < 4) ((LAS unsigned*)lds)[L_CNT + lane] = lane < 3 ? 0u : now32() + 0x40000000u;
    lds_wait();
    wg_barrier();
    SB();
#pragma unroll
    for (int u = 0; u < NPRE * 8; ++u) {
      pre[u] = LDROW(rk, 0, u); SB();
      if (u >= WIN) VM_WAIT(WIN);
    }
#pragma unroll
    for (int u = 0; u < 8; ++u) {
      buf[u] = LDROW(rk, NPRE, u); SB();
      if (u < 7) VM_WAIT(WIN);
    }
    wg_barrier();
    const u32x4 qp = *(const LAS u32x4*)(lds + L_Q + c * 4);
    LAS float* S = (LAS float*)lds + L_S + sw * 1024;
    float mx = -INFINITY;
#define DOT(w) dot2((w)[3], qp[3], dot2((w)[2], qp[2], dot2((w)[1], qp[1], dot2((w)[0], qp[0], 0.f))))
#define K_FIN(grp) do { \
      _Pragma("unroll") for (int u = 0; u < 4; ++u) { const float lo = x[u] + dpp<ROR8>(x[u]), hi = x[u + 4] + dpp<ROR8>(x[u + 4]); x[u] = (c & 8) ? hi : lo; } \
      _Pragma("unroll") for (int u = 0; u < 2; ++u) { const float lo = x[u] + dpp<XOR7>(x[u]), hi = x[u + 2] + dpp<XOR7>(x[u + 2]); x[u] = (c & 4) ? hi : lo; } \
      { const float lo = x[0] + dpp<XOR2>(x[0]), hi = x[1] + dpp<XOR2>(x[1]); x[0] = (c & 2) ? hi : lo; } \
      float sc = x[0] + dpp<XOR1>(x[0]); \
      sc = rbf(rbf(sc) * SCALE); \
      mx = fmaxf(mx, sc); \
      S[(grp) * 32 + r * 8 + (c >> 1)] = sc; \
      SB(); \
    } while (0)
    static_assert(WIN >= 3 && WIN <= 5, "ring of 8 rows");
    {
      float x[8];
#pragma unroll
      for (int u = 0; u < 8; ++u) {
        if (u > 0 && u <= WIN + 1) { buf[u - 1] = LDROW(rk, NPRE + 1, u - 1); SB(); }
        x[u] = DOT(buf[u]);
        SB();
      }
      K_FIN(NPRE);
    }
#pragma unroll
    for (int pp = 0; pp < NPRE; ++pp) {
      float x[8];
#pragma unroll
      for (int w = 0; w < 8; ++w) x[w] = DOT(pre[pp * 8 + w]);
      K_FIN(pp);
      if (WIN + 1 + pp < 7) { VM_WAIT(WIN); buf[WIN + 1 + pp] = LDROW(rk, NPRE + 1, WIN + 1 + pp); SB(); }
    }
    static_assert(WIN + 1 + NPRE >= 7, "");
    int last = 31;
    asm volatile("" : "+s"(last));
#pragma nounroll
    for (int tt = NPRE + 1; tt <= last; ++tt) {
      const rsrc_t rn = mk_rsrc(tt == last ? vp : kp, HB);
      float x[8];
#pragma unroll
      for (int u = 0; u < 8; ++u) {
        const int n = u + 7;
        const int so = ROWSO(tt, n);
        asm volatile("s_waitcnt vmcnt(%0)" :: "i"(WIN), "s"(so));
        SB();
        buf[n & 7] = ldnt(n >= 8 ? rn : rk, ROWVO(n), so);
        SB();
        x[u] = DOT(buf[u]);
        SB();
      }
      K_FIN(tt);
    }
    mx = wave_max(mx);
    if (lane == 0) lds[L_MX + sw] = mx;
    lds_wait();
    if (lane == 0) __hip_atomic_fetch_add((LAS unsigned*)lds + L_CNT, 1u, RLX_WG);
    f32x2 acc[4] = {{0.f, 0.f}, {0.f, 0.f}, {0.f, 0.f}, {0.f, 0.f}};
    f32x4 s0 = *(const LAS f32x4*)(S + r * 8), s1;
    int open = -1, lag = -1;
#pragma nounroll
    for (int tt = 0; tt <= last; ++tt) {
      const rsrc_t rn = mk_rsrc(vp, tt == last ? 0 : HB);
      unsigned ref, tnow;
#pragma unroll
      for (int u = 0; u < 8; ++u) {
        const int n = u + 7;
        const int so = ROWSO(tt, n);
        asm volatile("s_cmp_gt_i32 %0, 0\n\ts_cbranch_scc1 1f\n\ts_waitcnt vmcnt(%2)\n\ts_cmp_gt_i32 %1, 0\n\ts_cbranch_scc1 1f\n\ts_waitcnt vmcnt(%3)\n1:"
                     :: "s"(open), "s"(lag), "i"(WIN + 2), "i"(WIN), "s"(so) : "scc");
        SB();
        buf[n & 7] = ldnt(n >= 8 ? rn : rv, ROWVO(n), so);
        SB();
        if (u == 0) s1 = *(const LAS f32x4*)(S + tt * 32 + r * 8 + 4);
        if (u == 4) { s0 = *(const LAS f32x4*)(S + ((tt + 1) & 31) * 32 + r * 8); ref = *(volatile LAS unsigned*)(lds + L_REF); tnow = now32(); }
        const float p = __builtin_amdgcn_exp2f(((u < 4 ? s0[u & 3] : s1[u & 3]) - mx) * LOG2E);
#pragma unroll
        for (int k = 0; k < 4; ++k) acc[k] = __builtin_elementwise_fma(bf_unpack(buf[u][k]), f32x2{p, p}, acc[k]);
        asm volatile("" : "+v"(acc[0]), "+v"(acc[1]), "+v"(acc[2]), "+v"(acc[3]));
        if (u == 6) {
          asm volatile("" : "+v"(ref), "+s"(tnow));
          const int el = (int)(tnow - __builtin_amdgcn_readfirstlane(ref));
          lag = el - (LAG_A + tt * LAG_B);
          open = max(el - WIDE_T, lag - LAG_2);
        }
        SB();
      }
    }
    *(LAS f32x4*)(lds + L_O + (sw * 4 + r) * 128 + c * 8) = f32x4{acc[0][0], acc[0][1], acc[1][0], acc[1][1]};
    *(LAS f32x4*)(lds + L_O + (sw * 4 + r) * 128 + c * 8 + 4) = f32x4{acc[2][0], acc[2][1], acc[3][0], acc[3][1]};
    lds_wait();
    wg_barrier();
    wg_barrier();
    if (blockIdx.x < NRED) wg_barrier();
    return;
  }

  unsigned epoch = *(const __attribute__((address_space(4))) unsigned*)(a.ws + WS_EPOCH);
  SB();
  float mb0, mb1, sd0, sd1;
  {
    float f0[8], f1[8];
#pragma unroll
    for (int k = 0; k < 4; ++k) { f0[2 * k] = bf_lo(xv0[k]); f0[2 * k + 1] = bf_hi(xv0[k]); f1[2 * k] = bf_lo(xv1[k]); f1[2 * k + 1] = bf_hi(xv1[k]); }
    float s0 = 0.f, s1 = 0.f;
#pragma unroll
    for (int k = 0; k < 8; ++k) { s0 += f0[k]; s1 += f1[k]; }
    const float m0 = wave_sum(s0) * (1.f / DM), m1 = wave_sum(s1) * (1.f / DM);
    float v0 = 0.f, v1 = 0.f;
#pragma unroll
    for (int k = 0; k < 8; ++k) { const float d0 = f0[k] - m0, d1 = f1[k] - m1; v0 = fmaf(d0, d0, v0); v1 = fmaf(d1, d1, v1); }
    v0 = wave_sum(v0) * (1.f / DM); v1 = wave_sum(v1) * (1.f / DM);
    mb0 = rbf(m0); sd0 = rbf(__builtin_sqrtf(rbf(rbf(v0) + 1e-5f)));
    mb1 = rbf(m1); sd1 = rbf(__builtin_sqrtf(rbf(rbf(v1) + 1e-5f)));
  }
#define XN(xf, mb, sd) rbf(rbf((xf) - (mb)) / (sd))
  asm volatile("" : "+s"(epoch));
  const unsigned tag = epoch + 0x9E3779B9u;
  const float xa0 = XN(bf_lo(xq0), mb0, sd0), xa1 = XN(bf_hi(xq0), mb0, sd0), xb0 = XN(bf_lo(xq1), mb1, sd1), xb1 = XN(bf_hi(xq1), mb1, sd1);
#define PARTIAL(w0_, w1_, n, res) do { \
    float t[8]; \
    _Pragma("unroll") for (int k = 0; k < 4; ++k) { \
      const f32x2 w0 = bf_unpack((w0_)[k]), w1 = bf_unpack((w1_)[k]); \
      const f32x2 qa = __builtin_elementwise_fma(w1, f32x2{xa1, xa1}, w0 * xa0), qb = __builtin_elementwise_fma(w1, f32x2{xb1, xb1}, w0 * xb0); \
      t[2 * k] = tr32(qa[0], qb[0]); t[2 * k + 1] = tr32(qa[1], qb[1]);             \
    } \
    _Pragma("unroll") for (int k = 0; k < 4; ++k) t[k] = tr16(t[k], t[4 + k]);        \
    f32x2 o; \
    _Pragma("unroll") for (int k = 0; k < 2; ++k) { const float lo = t[k] + dpp<ROR8>(t[k]), hi = t[2 + k] + dpp<ROR8>(t[2 + k]); o[k] = (c & 8) ? hi : lo; } \
    const float lo = o[0] + dpp<XOR7>(dpp<XOR3>(o[0])), hi = o[1] + dpp<XOR7>(dpp<XOR3>(o[1]));     \
    lds[L_P + (n) * 512 + wave * 64 + (r >> 1) * 32 + (c & 3) * 8 + (r & 1) * 4 + (c >> 3) * 2 + ((c >> 2) & 1)] = (c & 4) ? hi : lo; \
    lds_wait(); \
    if (lane == 0) __hip_atomic_fetch_add((LAS unsigned*)lds + L_CNT + 1 + (n), 1u, RLX_WG); \
    while (__hip_atomic_load((LAS unsigned*)lds + L_CNT + 1 + (n), RLX_WG) < 8u) __builtin_amdgcn_s_sleep(0); \
    asm volatile("" ::: "memory"); \
    const LAS float* P = (const LAS float*)lds + L_P + (n) * 512 + lane; \
    res = ((P[0] + P[64]) + (P[128] + P[192])) + ((P[256] + P[320]) + (P[384] + P[448])); \
  } while (0)
  {
    float qs;
    PARTIAL(wq0, wq1, 0, qs);
    if (wave == 0) st_granule((gu64*)(a.ws + WS_Q) + (h * 8 + g8) * 64 + lane, tag, qs);
  }
  if (wave == 7) {
    __builtin_amdgcn_s_sleep(32);
    const rsrc_t rg = mk_rsrc(a.ws + WS_Q + (size_t)h * 4096, 4096);
    const unsigned t0 = now32();
    u32x4 g0, g1;
    while (true) {
      g0 = lddev(rg, r * 512 + c * 16, b * 256); g1 = lddev(rg, r * 512 + c * 16, 2048 + b * 256);
      if (__all((g0[1] == tag) & (g0[3] == tag) & (g1[1] == tag) & (g1[3] == tag))) break;
      if (now32() - t0 > SPIN_TICKS) break;
      __builtin_amdgcn_s_sleep(8);
      asm volatile("" ::: "memory");
    }
    ((LAS unsigned*)lds)[L_Q + lane] = pack_bf16(__uint_as_float(g0[0]) + __uint_as_float(g1[0]), __uint_as_float(g0[2]) + __uint_as_float(g1[2]));
    lds_wait();
  }
  wg_barrier();
  const int red = blockIdx.x < NRED ? (xg * 2 + (j & 1)) * 2 + (j >> 1) : -1;
  const unsigned xw = (red >= 0 && tid < 16) ? *(const gu32*)(a.x + (red & 1) * DM + (red >> 1) * 32 + tid * 2) : 0u;
  asm volatile("" : "+v"(w20), "+v"(w21) : "v"(pf0), "v"(pf1));
  float gsum = 0.f;
  if (h < 28) {
    PARTIAL(wm0, wm1, 1, gsum);
  }
  f32x2 wf[64];
  {
    const rsrc_t ro = mk_rsrc(a.wo + (size_t)h * DH * DM, DH * DM * 2);
#pragma unroll
    for (int i = 0; i < 16; ++i) {
      const u32x4 w = lddef(ro, voff + (i & 3) * 1024, (wave * 16 + (i & ~3)) * 1024);
      SB();
#pragma unroll
      for (int k = 0; k < 4; ++k) { wf[i * 4 + k] = bf_unpack(w[k]); asm volatile("" : "+v"(wf[i * 4 + k])); }
      SB();
    }
  }
  const int lane0 = lane;
  while (__hip_atomic_load((LAS unsigned*)lds + L_CNT, RLX_WG) < 4u) __builtin_amdgcn_s_sleep(8);
  asm volatile("" ::: "memory");
  gu64* ml = (gu64*)(a.ws + WS_ML);
  gu64* G = (gu64*)(a.ws + WS_G);
  float mw = 0.f;
  const unsigned tk = now32();
  if (wave == 7) {
    FRESH();
    mw = lds[L_MX + (lane & 3)];
    float m = fmaxf(mw, dpp<XOR1>(mw)); m = fmaxf(m, dpp<XOR2>(m));
    float l = 0.f;
#pragma unroll 2
    for (int i = 0; i < 16; ++i) {
      const f32x4 sv = *(const LAS f32x4*)(lds + L_S + i * 256 + lane * 4);
#pragma unroll
      for (int k = 0; k < 4; ++k) l += __builtin_amdgcn_exp2f((sv[k] - m) * LOG2E);
    }
    l = wave_sum(l);
    if (lane < 16) st_granule(ml + chunk * 16 + lane, tag, lane == 0 ? m : lane == 1 ? l : __uint_as_float(tk));
  } else if (wave == 0 && h < 28) {
    FRESH();
    __builtin_amdgcn_s_sleep(16);
    st_granule(G + (h * 8 + g8) * 64 + lane, tag, gsum);
  }
  __builtin_amdgcn_s_sleep(127); __builtin_amdgcn_s_sleep(16);
  f32x2 yacc[4] = {{0.f, 0.f}, {0.f, 0.f}, {0.f, 0.f}, {0.f, 0.f}};
  if (wave < 7) {
    FRESH();
    const int f0 = (h * 4 + sp) * 14 + wave * 2;
    const int f = f0 + (r & 1);
    gu64* gp = G + ((f >> 6) * 8 + (c & 1) * 4 + (c >> 3) * 2 + ((f >> 5) & 1)) * 64 + b * 32 + (f & 31);
    const unsigned t0 = now32();
    unsigned long long g;
    while (true) {
      g = __hip_atomic_load(gp, RLX_AGENT);
      if (__all((unsigned)(g >> 32) == tag)) break;
      if (now32() - t0 > SPIN_TICKS) break;
      __builtin_amdgcn_s_sleep(127);
    }
    float s = __uint_as_float((unsigned)g);
    s += dpp<XOR1>(s);
    const float gs = rbf(s), us = rbf(dpp<ROR8>(s));
    const float hv = rbf(rbf(gs / (1.f + __expf(-gs))) * us);
    const float h0 = lane_value(hv, 0), h1 = lane_value(hv, 16);
#pragma unroll
    for (int k = 0; k < 4; ++k) yacc[k] = __builtin_elementwise_fma(bf_unpack(w21[k]), f32x2{h1, h1}, bf_unpack(w20[k]) * h0);
  } else {
    FRESH();
    {
      const unsigned long long gt = __hip_atomic_load(ml + (lane * 4 + (lane & 3)) * 16 + 2, RLX_AGENT);
      const bool v = (unsigned)(gt >> 32) == tag;
      const float n = wave_sum(v ? 1.f : 0.f), d = wave_sum(v ? (float)(int)((unsigned)gt - tk) : 0.f);
      if (lane == 0 && n > 0.f) ((LAS unsigned*)lds)[L_REF] = tk + (unsigned)(int)(d / n);
    }
    const unsigned t0 = now32();
    unsigned long long gm, gl;
    while (true) {
      gm = __hip_atomic_load(ml + ((chunk & ~3) + (lane & 3)) * 16, RLX_AGENT);
      gl = __hip_atomic_load(ml + ((chunk & ~3) + (lane & 3)) * 16 + 1, RLX_AGENT);
      if (__all((unsigned)(gm >> 32) == tag && (unsigned)(gl >> 32) == tag)) break;
      if (now32() - t0 > SPIN_TICKS) break;
      __builtin_amdgcn_s_sleep(127);
    }
    const float ms = __uint_as_float((unsigned)gm), ls = __uint_as_float((unsigned)gl);
    float M = fmaxf(ms, dpp<XOR1>(ms)); M = fmaxf(M, dpp<XOR2>(M));
    float L = ls * __builtin_amdgcn_exp2f((ms - M) * LOG2E);
    L += dpp<XOR1>(L); L += dpp<XOR2>(L);
    if (lane < 4) lds[L_FW + lane] = __builtin_amdgcn_exp2f((mw - M) * LOG2E) / L;
    lds_wait();
    {
      const unsigned char* pa = (lane < 32 || red < 0) ? a.ws + WS_Y + chunk * 2048 + (lane & 15) * 128
                                                       : (const unsigned char*)a.out + (red & 1) * (DM * 2) + (red >> 1) * 64;
      const unsigned jk = __hip_atomic_load((gu32*)pa, RLX_AGENT);
      asm volatile("" :: "v"(jk));
    }
    if (blockIdx.x == NWG - 1) {
      while (true) {
        bool ok = true;
#pragma unroll
        for (int i = 0; i < 4; ++i) ok = ok & ((unsigned)(__hip_atomic_load(ml + (i * 64 + lane) * 16 + 2, RLX_AGENT) >> 32) == tag);
        if (__all(ok)) break;
        if (now32() - t0 > SPIN_TICKS) break;
        __builtin_amdgcn_s_sleep(127);
      }
      if (lane == 0) __hip_atomic_store((gu32*)(a.ws + WS_EPOCH), tag, RLX_AGENT);
    }
  }
  {
    FRESH();
    wg_barrier();
    {
      const LAS float* P = (const LAS float*)lds + L_O + (4 * r) * 128 + wave * 16 + c;
      const float o = xrow_sum(((P[0] + P[128]) + (P[256] + P[384])) * lds[L_FW + r]);
#pragma unroll
      for (int i = 0; i < 16; ++i) {
        const float oi = lane_value(o, i);
#pragma unroll
        for (int k = 0; k < 4; ++k) yacc[k] = __builtin_elementwise_fma(wf[i * 4 + k], f32x2{oi, oi}, yacc[k]);
      }
    }
    *(LAS f32x4*)(lds + L_Y + wave * 512 + lane * 8) = f32x4{yacc[0][0], yacc[0][1], yacc[1][0], yacc[1][1]};
    *(LAS f32x4*)(lds + L_Y + wave * 512 + lane * 8 + 4) = f32x4{yacc[2][0], yacc[2][1], yacc[3][0], yacc[3][1]};
    lds_wait();
    wg_barrier();
    const rsrc_t ry = mk_rsrc(a.ws + WS_Y, NWG * 2048);
    {
      const LAS float* P = (const LAS float*)lds + L_Y + tid;
      const float y = ((P[0] + P[512]) + (P[1024] + P[1536])) + ((P[2048] + P[2560]) + (P[3072] + P[3584]));
      const unsigned u = __float_as_uint(y);
      const unsigned w = (((u & 0x7f800000u) == 0x7f800000u ? u | ((u & 0x7fffffu) ? 0x400000u : 0u) : u + 0x400u) & 0xfffff800u) | ((tag >> ((lane & 1) * 11)) & 0x7ffu);
      const unsigned w1 = __builtin_amdgcn_mov_dpp(w, XOR1, 0xf, 0xf, true);
      if (!(lane & 1)) __builtin_amdgcn_raw_buffer_store_b64(u32x2{w, w1}, ry, tid * 4, chunk * 2048, 16);
    }
    if (red >= 0) {
      const unsigned t0 = now32();
      u32x4 g0 = {0u, 0u, 0u, 0u}, g1 = {0u, 0u, 0u, 0u};
      bool ok0 = false, ok1 = false;
      const unsigned ta = tag & 0x7ffu, tb = (tag >> 11) & 0x7ffu;
      while (true) {
        if (!ok0) g0 = lddev(ry, (lane >> 3) * 2048 + (lane & 7) * 16, ((red & 1) * 128 + wave * 16) * 2048 + (red >> 1) * 128);
        if (!ok1) g1 = lddev(ry, (lane >> 3) * 2048 + (lane & 7) * 16, ((red & 1) * 128 + wave * 16 + 8) * 2048 + (red >> 1) * 128);
        ok0 = (((g0[0] ^ ta) | (g0[1] ^ tb) | (g0[2] ^ ta) | (g0[3] ^ tb)) & 0x7ffu) == 0u;
        ok1 = (((g1[0] ^ ta) | (g1[1] ^ tb) | (g1[2] ^ ta) | (g1[3] ^ tb)) & 0x7ffu) == 0u;
        if (__all(ok0 & ok1)) break;
        if (now32() - t0 > SPIN_TICKS) break;
        __builtin_amdgcn_s_sleep(4);
        asm volatile("" ::: "memory");
      }
      float t[4];
#pragma unroll
      for (int k = 0; k < 4; ++k) t[k] = __uint_as_float(g0[k] & 0xfffff800u) + __uint_as_float(g1[k] & 0xfffff800u);
      float s = tr16(tr32(t[0], t[2]), tr32(t[1], t[3]));
      s += dpp<ROR8>(s);
      if (!(lane & 8)) lds[L_FIN + wave * 32 + (lane & 7) * 4 + r] = s;
      lds_wait();
      wg_barrier();
      if (tid < 16) {
        const LAS f32x2* P = (const LAS f32x2*)(lds + L_FIN + tid * 2);
        const f32x2 o = bf_unpack(xw) + (((P[0] + P[16]) + (P[32] + P[48])) + ((P[64] + P[80]) + (P[96] + P[112])));
        *(gu32*)(a.out + (red & 1) * DM + (red >> 1) * 32 + tid * 2) = pack_bf16(o[0], o[1]);
      }
    }
  }
}

}

extern "C" void kernel_launch(void* const* d_in, const int* in_sizes, int n_in, void* d_out, int out_size, void* d_ws, size_t ws_size,
                              hipStream_t stream) {
  Args a;
  a.x = (const unsigned short*)d_in[0]; a.k = (const unsigned short*)d_in[1]; a.v = (const unsigned short*)d_in[2];
  a.wq = (const unsigned short*)d_in[3]; a.wo = (const unsigned short*)d_in[4];
  a.w1 = (const unsigned short*)d_in[6]; a.w3 = (const unsigned short*)d_in[7]; a.w2 = (const unsigned short*)d_in[8];
  a.out = (unsigned short*)d_out; a.ws = (unsigned char*)d_ws;
  fused_decode<<<NWG, NTHR, 0, stream>>>(a);
}
```

```cpp
#include <hip/hip_runtime.h>
#include <hip/hip_bf16.h>
#include <stdint.h>

namespace {

constexpr int NWG = 256, NTHR = 768, NRED = 32;
constexpr int DM = 512, NH = 32, SK = 16384, DH = 128, FF = 1792;
constexpr float LOG2E = 1.4426950408889634f;
constexpr float SCALE = 0.08838834764831845f;
constexpr int WIN = 4;
constexpr int NPRE = 2;
constexpr int LAG_A = 95, LAG_B = 108, LAG_2 = 80, WIDE_T = 3250;
constexpr size_t WS_EPOCH = 0;
constexpr size_t WS_ML = 8192;
constexpr size_t WS_Y = 65536;
constexpr size_t WS_Q = WS_Y + NWG * 2048;
constexpr size_t WS_G = WS_Q + (128u << 10);
constexpr int L_S = 0;
constexpr int L_O = L_S + 4096;
constexpr int L_Y = L_O + 2048;
constexpr int L_P = L_Y + 4096;
constexpr int L_Q = L_P + 1024;
constexpr int L_MX = L_Q + 64;
constexpr int L_FW = L_MX + 4;
constexpr int L_FIN = L_FW + 4;
constexpr int L_CNT = L_FIN + 256;
constexpr int L_REF = L_CNT + 3;
constexpr int L_TAG = L_REF + 1;
constexpr int L_END = L_TAG + 1;

#define GAS __attribute__((address_space(1)))
#define LAS __attribute__((address_space(3)))
#define RLX_AGENT __ATOMIC_RELAXED, __HIP_MEMORY_SCOPE_AGENT
#define RLX_WG __ATOMIC_RELAXED, __HIP_MEMORY_SCOPE_WORKGROUP
typedef unsigned u32x2 __attribute__((ext_vector_type(2)));
typedef unsigned u32x4 __attribute__((ext_vector_type(4)));
typedef float f32x2 __attribute__((ext_vector_type(2)));
typedef float f32x4 __attribute__((ext_vector_type(4)));
typedef __bf16 bf16x2 __attribute__((ext_vector_type(2)));
typedef GAS unsigned gu32;
typedef GAS unsigned long long gu64;
typedef __amdgpu_buffer_rsrc_t rsrc_t;

__device__ __forceinline__ float bf_lo(unsigned u) { return __builtin_bit_cast(float, u << 16); }
__device__ __forceinline__ float bf_hi(unsigned u) { return __builtin_bit_cast(float, u & 0xffff0000u); }
__device__ __forceinline__ f32x2 bf_unpack(unsigned u) { return f32x2{bf_lo(u), bf_hi(u)}; }
__device__ __forceinline__ unsigned pack_bf16(float lo, float hi) {
  bf16x2 r = {(__bf16)lo, (__bf16)hi};
  return __builtin_bit_cast(unsigned, r);
}
__device__ __forceinline__ float rbf(float x) { return (float)(__bf16)x; }
__device__ __forceinline__ float dot2(unsigned a, unsigned b, float acc) {
  return __builtin_amdgcn_fdot2_f32_bf16(__builtin_bit_cast(bf16x2, a), __builtin_bit_cast(bf16x2, b), acc, false);
}
template <int CTRL> __device__ __forceinline__ float dpp(float x) {
  return __builtin_bit_cast(float, __builtin_amdgcn_mov_dpp(__builtin_bit_cast(int, x), CTRL, 0xf, 0xf, true));
}
constexpr int XOR1 = 0xB1, XOR2 = 0x4E, XOR3 = 0x1B, XOR7 = 0x141, ROR8 = 0x128;
__device__ __forceinline__ float tr16(float a, float b) {
  auto s = __builtin_amdgcn_permlane16_swap(__float_as_uint(a), __float_as_uint(b), false, false);
  return __uint_as_float(s[0]) + __uint_as_float(s[1]);
}
__device__ __forceinline__ float tr32(float a, float b) {
  auto s = __builtin_amdgcn_permlane32_swap(__float_as_uint(a), __float_as_uint(b), false, false);
  return __uint_as_float(s[0]) + __uint_as_float(s[1]);
}
__device__ __forceinline__ float xrow_sum(float x) { x = tr16(x, x); return tr32(x, x); }
__device__ __forceinline__ float wave_sum(float x) {
  x += dpp<XOR1>(x); x += dpp<XOR2>(x); x += dpp<XOR7>(x); x += dpp<ROR8>(x);
  return xrow_sum(x);
}
__device__ __forceinline__ float wave_max(float x) {
  x = fmaxf(x, dpp<XOR1>(x)); x = fmaxf(x, dpp<XOR2>(x)); x = fmaxf(x, dpp<XOR7>(x)); x = fmaxf(x, dpp<ROR8>(x));
  auto s = __builtin_amdgcn_permlane16_swap(__float_as_uint(x), __float_as_uint(x), false, false);
  x = fmaxf(__uint_as_float(s[0]), __uint_as_float(s[1]));
  auto t = __builtin_amdgcn_permlane32_swap(__float_as_uint(x), __float_as_uint(x), false, false);
  return fmaxf(__uint_as_float(t[0]), __uint_as_float(t[1]));
}
__device__ __forceinline__ float lane_value(float x, int l) { return __builtin_bit_cast(float, __builtin_amdgcn_readlane(__builtin_bit_cast(int, x), l)); }
__device__ __forceinline__ void lds_wait() { asm volatile("s_waitcnt lgkmcnt(0)" ::: "memory"); }
__device__ __forceinline__ void wg_barrier() { __builtin_amdgcn_s_barrier(); asm volatile("" ::: "memory"); }
__device__ __forceinline__ void st_granule(gu64* g, unsigned tag, float v) {
  __hip_atomic_store(g, ((unsigned long long)tag << 32) | (unsigned long long)__float_as_uint(v), RLX_AGENT);
}
__device__ __forceinline__ rsrc_t mk_rsrc(const void* p, int bytes) { return __builtin_amdgcn_make_buffer_rsrc((void*)p, 0, bytes, 0x00020000); }
__device__ __forceinline__ u32x4 ldnt(rsrc_t r, int voff, int soff) { return __builtin_amdgcn_raw_buffer_load_b128(r, voff, soff, 2); }
__device__ __forceinline__ u32x4 lddev(rsrc_t r, int voff, int soff) { return __builtin_amdgcn_raw_buffer_load_b128(r, voff, soff, 16); }
__device__ __forceinline__ u32x4 lddef(rsrc_t r, int voff, int soff) { return __builtin_amdgcn_raw_buffer_load_b128(r, voff, soff, 0); }
__device__ __forceinline__ unsigned now32() { return (unsigned)__builtin_amdgcn_s_memrealtime(); }
constexpr unsigned SPIN_TICKS = 20000000u;

#define SB() __builtin_amdgcn_sched_barrier(0)
#define VM_WAIT(n) do { asm volatile("s_waitcnt vmcnt(%0)" :: "i"(n)); SB(); } while (0)
#define FRESH() int lane_ = lane0; asm volatile("" : "+v"(lane_)); \
  const int lane = lane_, r = lane >> 4, c = lane & 15, tid = wave * 64 + lane, voff = lane * 16; (void)r; (void)c; (void)tid; (void)voff

struct Args {
  const unsigned short* x; const unsigned short* k; const unsigned short* v; const unsigned short* wq; const unsigned short* wo;
  const unsigned short* w1; const unsigned short* w3; const unsigned short* w2;
  unsigned short* out; unsigned char* ws;
};

__global__ void __launch_bounds__(NTHR, 3) fused_decode(Args a) {
  __shared__ __attribute__((aligned(16))) float lds[L_END];
  const int tid = threadIdx.x, lane = tid & 63, wave = __builtin_amdgcn_readfirstlane(tid >> 6);
  const int r = lane >> 4, c = lane & 15;
  const int xg = blockIdx.x & 7, j = blockIdx.x >> 3;
  const int b = j >> 4, h = xg + ((j >> 2) & 3) * 8, sp = j & 3;
  const int chunk = b * 128 + h * 4 + sp;
  const int voff = lane * 16;
  asm volatile("" :: "s"(a.x), "s"(a.k), "s"(a.v), "s"(a.wq), "s"(a.ws));

  const int g8 = b * 4 + sp;
  const int prow = (g8 >> 2) * 256 + wave * 32 + 2 * (lane >> 2);
  u32x4 xv0, xv1, wq0, wq1, wm0, wm1, pf0, pf1, w20, w21;
  unsigned xq0, xq1, epoch = 0;
  if (wave < 8) {
    xv0 = *(const GAS u32x4*)(a.x + lane * 8); xv1 = *(const GAS u32x4*)(a.x + DM + lane * 8);
    xq0 = *(const gu32*)(a.x + prow); xq1 = *(const gu32*)(a.x + DM + prow);
    if (wave == 0) epoch = __hip_atomic_load((gu32*)(a.ws + WS_EPOCH), RLX_AGENT);
    const rsrc_t rq = mk_rsrc(a.wq, DM * NH * DH * 2);
    wq0 = ldnt(rq, prow * (NH * DH * 2) + (lane & 3) * 16, h * (DH * 2) + (g8 & 3) * 64);
    wq1 = ldnt(rq, prow * (NH * DH * 2) + (lane & 3) * 16, h * (DH * 2) + (g8 & 3) * 64 + NH * DH * 2);
    SB();
    const rsrc_t rm = mk_rsrc((g8 & 2) ? a.w3 : a.w1, h < 28 ? DM * FF * 2 : 0);
    wm0 = ldnt(rm, prow * (FF * 2) + (lane & 3) * 16, h * 128 + (g8 & 1) * 64);
    wm1 = ldnt(rm, prow * (FF * 2) + (lane & 3) * 16, h * 128 + (g8 & 1) * 64 + FF * 2);
    const rsrc_t ro = mk_rsrc(a.wo + (size_t)h * DH * DM, DH * DM * 2);
    pf0 = lddef(ro, voff, (g8 * 16 + wave * 2) * 1024); pf1 = lddef(ro, voff + 1024, (g8 * 16 + wave * 2) * 1024);
    const rsrc_t r2 = mk_rsrc(a.w2 + (size_t)((h * 4 + sp) * 14 + wave * 2) * DM, wave < 7 ? 2 * DM * 2 : 0);
    w20 = ldnt(r2, voff, 0); w21 = ldnt(r2, voff + 1024, 0);
    SB();
    wg_barrier();
  }
  int role = wave;
  asm volatile("" : "+s"(role));
  if (__builtin_expect(role >= 8, 1)) {
    const int sw = wave - 8;
    constexpr int HB = SK * DH * 2;
    const unsigned short* kp = a.k + (size_t)(chunk >> 2) * SK * DH;
    const unsigned short* vp = a.v + (size_t)(chunk >> 2) * SK * DH;
    const rsrc_t rk = mk_rsrc(kp, HB), rv = mk_rsrc(vp, HB);
    const int voffh = voff + HB / 2;
    auto window = [&](int g) -> int { g += (j >> 3) * 2; return ((g & 1) << 19) + ((g & 14) << 15) + ((g & 16) << 16) + (sp << 14) + (sw << 12); };
#define ROWVO(n) ((((n) & 1) ? voffh : voff) + (((n) & 7) >> 1) * 1024)
#define ROWSO(grp, n) window((grp) + ((n) >> 3))
#define LDROW(rs, grp, n) ldnt(rs, ROWVO(n), ROWSO(grp, n))
    __builtin_amdgcn_s_setprio(2);
    u32x4 buf[8], pre[NPRE * 8];
    if (lane < 4) ((LAS unsigned*)lds)[L_CNT + lane] = lane < 3 ? 0u : now32() + 0x40000000u;
    lds_wait();
    wg_barrier();
    SB();
#pragma unroll
    for (int u = 0; u < NPRE * 8; ++u) {
      pre[u] = LDROW(rk, 0, u); SB();
      if (u >= WIN) VM_WAIT(WIN);
    }
#pragma unroll
    for (int u = 0; u < 8; ++u) {
      buf[u] = LDROW(rk, NPRE, u); SB();
      if (u < 7) VM_WAIT(WIN);
    }
    wg_barrier();
    const u32x4 qp = *(const LAS u32x4*)(lds + L_Q + c * 4);
    LAS float* S = (LAS float*)lds + L_S + sw * 1024;
    float mx = -INFINITY;
#define DOT(w) dot2((w)[3], qp[3], dot2((w)[2], qp[2], dot2((w)[1], qp[1], dot2((w)[0], qp[0], 0.f))))
#define K_FIN(grp) do { \
      _Pragma("unroll") for (int u = 0; u < 4; ++u) { const float lo = x[u] + dpp<ROR8>(x[u]), hi = x[u + 4] + dpp<ROR8>(x[u + 4]); x[u] = (c & 8) ? hi : lo; } \
      _Pragma("unroll") for (int u = 0; u < 2; ++u) { const float lo = x[u] + dpp<XOR7>(x[u]), hi = x[u + 2] + dpp<XOR7>(x[u + 2]); x[u] = (c & 4) ? hi : lo; } \
      { const float lo = x[0] + dpp<XOR2>(x[0]), hi = x[1] + dpp<XOR2>(x[1]); x[0] = (c & 2) ? hi : lo; } \
      float sc = x[0] + dpp<XOR1>(x[0]); \
      sc = rbf(rbf(sc) * SCALE); \
      mx = fmaxf(mx, sc); \
      S[(grp) * 32 + r * 8 + (c >> 1)] = sc; \
      SB(); \
    } while (0)
    static_assert(WIN >= 3 && WIN <= 5, "ring of 8 rows");
    {
      float x[8];
#pragma unroll
      for (int u = 0; u < 8; ++u) {
        if (u > 0 && u <= WIN + 1) { buf[u - 1] = LDROW(rk, NPRE + 1, u - 1); SB(); }
        x[u] = DOT(buf[u]);
        SB();
      }
      K_FIN(NPRE);
    }
#pragma unroll
    for (int pp = 0; pp < NPRE; ++pp) {
      float x[8];
#pragma unroll
      for (int w = 0; w < 8; ++w) x[w] = DOT(pre[pp * 8 + w]);
      K_FIN(pp);
      if (WIN + 1 + pp < 7) { VM_WAIT(WIN); buf[WIN + 1 + pp] = LDROW(rk, NPRE + 1, WIN + 1 + pp); SB(); }
    }
    static_assert(WIN + 1 + NPRE >= 7, "");
    int last = 31;
    asm volatile("" : "+s"(last));
#pragma nounroll
    for (int tt = NPRE + 1; tt <= last; ++tt) {
      const rsrc_t rn = mk_rsrc(tt == last ? vp : kp, HB);
      float x[8];
#pragma unroll
      for (int u = 0; u < 8; ++u) {
        const int n = u + 7;
        const int so = ROWSO(tt, n);
        asm volatile("s_waitcnt vmcnt(%0)" :: "i"(WIN), "s"(so));
        SB();
        buf[n & 7] = ldnt(n >= 8 ? rn : rk, ROWVO(n), so);
        SB();
        x[u] = DOT(buf[u]);
        SB();
      }
      K_FIN(tt);
    }
    mx = wave_max(mx);
    if (lane == 0) lds[L_MX + sw] = mx;
    lds_wait();
    if (lane == 0) __hip_atomic_fetch_add((LAS unsigned*)lds + L_CNT, 1u, RLX_WG);
    f32x2 acc[4] = {{0.f, 0.f}, {0.f, 0.f}, {0.f, 0.f}, {0.f, 0.f}};
    f32x4 s0 = *(const LAS f32x4*)(S + r * 8), s1;
    int open = -1, lag = -1;
#pragma nounroll
    for (int tt = 0; tt <= last; ++tt) {
      const rsrc_t rn = mk_rsrc(vp, tt == last ? 0 : HB);
      unsigned ref, tnow;
#pragma unroll
      for (int u = 0; u < 8; ++u) {
        const int n = u + 7;
        const int so = ROWSO(tt, n);
        asm volatile("s_cmp_gt_i32 %0, 0\n\ts_cbranch_scc1 1f\n\ts_waitcnt vmcnt(%2)\n\ts_cmp_gt_i32 %1, 0\n\ts_cbranch_scc1 1f\n\ts_waitcnt vmcnt(%3)\n1:"
                     :: "s"(open), "s"(lag), "i"(WIN + 2), "i"(WIN), "s"(so) : "scc");
        SB();
        buf[n & 7] = ldnt(n >= 8 ? rn : rv, ROWVO(n), so);
        SB();
        if (u == 0) s1 = *(const LAS f32x4*)(S + tt * 32 + r * 8 + 4);
        if (u == 4) { s0 = *(const LAS f32x4*)(S + ((tt + 1) & 31) * 32 + r * 8); ref = *(volatile LAS unsigned*)(lds + L_REF); tnow = now32(); }
        const float p = __builtin_amdgcn_exp2f(((u < 4 ? s0[u & 3] : s1[u & 3]) - mx) * LOG2E);
#pragma unroll
        for (int k = 0; k < 4; ++k) acc[k] = __builtin_elementwise_fma(bf_unpack(buf[u][k]), f32x2{p, p}, acc[k]);
        asm volatile("" : "+v"(acc[0]), "+v"(acc[1]), "+v"(acc[2]), "+v"(acc[3]));
        if (u == 6) {
          asm volatile("" : "+v"(ref), "+s"(tnow));
          const int el = (int)(tnow - __builtin_amdgcn_readfirstlane(ref));
          lag = el - (LAG_A + tt * LAG_B);
          open = max(el - WIDE_T, lag - LAG_2);
        }
        SB();
      }
    }
    *(LAS f32x4*)(lds + L_O + (sw * 4 + r) * 128 + c * 8) = f32x4{acc[0][0], acc[0][1], acc[1][0], acc[1][1]};
    *(LAS f32x4*)(lds + L_O + (sw * 4 + r) * 128 + c * 8 + 4) = f32x4{acc[2][0], acc[2][1], acc[3][0], acc[3][1]};
    lds_wait();
    wg_barrier();
    wg_barrier();
    if (blockIdx.x < NRED) wg_barrier();
    return;
  }

  float mb0, mb1, sd0, sd1;
  {
    float f0[8], f1[8];
#pragma unroll
    for (int k = 0; k < 4; ++k) { f0[2 * k] = bf_lo(xv0[k]); f0[2 * k + 1] = bf_hi(xv0[k]); f1[2 * k] = bf_lo(xv1[k]); f1[2 * k + 1] = bf_hi(xv1[k]); }
    float s0 = 0.f, s1 = 0.f;
#pragma unroll
    for (int k = 0; k < 8; ++k) { s0 += f0[k]; s1 += f1[k]; }
    const float m0 = wave_sum(s0) * (1.f / DM), m1 = wave_sum(s1) * (1.f / DM);
    float v0 = 0.f, v1 = 0.f;
#pragma unroll
    for (int k = 0; k < 8; ++k) { const float d0 = f0[k] - m0, d1 = f1[k] - m1; v0 = fmaf(d0, d0, v0); v1 = fmaf(d1, d1, v1); }
    v0 = wave_sum(v0) * (1.f / DM); v1 = wave_sum(v1) * (1.f / DM);
    mb0 = rbf(m0); sd0 = rbf(__builtin_sqrtf(rbf(rbf(v0) + 1e-5f)));
    mb1 = rbf(m1); sd1 = rbf(__builtin_sqrtf(rbf(rbf(v1) + 1e-5f)));
  }
#define XN(xf, mb, sd) rbf(rbf((xf) - (mb)) / (sd))
  if (tid == 0) ((LAS unsigned*)lds)[L_TAG] = epoch + 0x9E3779B9u;
  const float xa0 = XN(bf_lo(xq0), mb0, sd0), xa1 = XN(bf_hi(xq0), mb0, sd0), xb0 = XN(bf_lo(xq1), mb1, sd1), xb1 = XN(bf_hi(xq1), mb1, sd1);
#define PARTIAL(w0_, w1_, n, res) do { \
    float t[8]; \
    _Pragma("unroll") for (int k = 0; k < 4; ++k) { \
      const f32x2 w0 = bf_unpack((w0_)[k]), w1 = bf_unpack((w1_)[k]); \
      const f32x2 qa = __builtin_elementwise_fma(w1, f32x2{xa1, xa1}, w0 * xa0), qb = __builtin_elementwise_fma(w1, f32x2{xb1, xb1}, w0 * xb0); \
      t[2 * k] = tr32(qa[0], qb[0]); t[2 * k + 1] = tr32(qa[1], qb[1]);             \
    } \
    _Pragma("unroll") for (int k = 0; k < 4; ++k) t[k] = tr16(t[k], t[4 + k]);        \
    f32x2 o; \
    _Pragma("unroll") for (int k = 0; k < 2; ++k) { const float lo = t[k] + dpp<ROR8>(t[k]), hi = t[2 + k] + dpp<ROR8>(t[2 + k]); o[k] = (c & 8) ? hi : lo; } \
    const float lo = o[0] + dpp<XOR7>(dpp<XOR3>(o[0])), hi = o[1] + dpp<XOR7>(dpp<XOR3>(o[1]));     \
    lds[L_P + (n) * 512 + wave * 64 + (r >> 1) * 32 + (c & 3) * 8 + (r & 1) * 4 + (c >> 3) * 2 + ((c >> 2) & 1)] = (c & 4) ? hi : lo; \
    lds_wait(); \
    if (lane == 0) __hip_atomic_fetch_add((LAS unsigned*)lds + L_CNT + 1 + (n), 1u, RLX_WG); \
    while (__hip_atomic_load((LAS unsigned*)lds + L_CNT + 1 + (n), RLX_WG) < 8u) __builtin_amdgcn_s_sleep(0); \
    asm volatile("" ::: "memory"); \
    const LAS float* P = (const LAS float*)lds + L_P + (n) * 512 + lane; \
    res = ((P[0] + P[64]) + (P[128] + P[192])) + ((P[256] + P[320]) + (P[384] + P[448])); \
  } while (0)
  float qs;
  PARTIAL(wq0, wq1, 0, qs);
  const unsigned tag = __builtin_amdgcn_readfirstlane(((const LAS unsigned*)lds)[L_TAG]);
  if (wave == 0) st_granule((gu64*)(a.ws + WS_Q) + (h * 8 + g8) * 64 + lane, tag, qs);
  if (wave == 7) {
    __builtin_amdgcn_s_sleep(32);
    const rsrc_t rg = mk_rsrc(a.ws + WS_Q + (size_t)h * 4096, 4096);
    const unsigned t0 = now32();
    u32x4 g0, g1;
    while (true) {
      g0 = lddev(rg, r * 512 + c * 16, b * 256); g1 = lddev(rg, r * 512 + c * 16, 2048 + b * 256);
      if (__all((g0[1] == tag) & (g0[3] == tag) & (g1[1] == tag) & (g1[3] == tag))) break;
      if (now32() - t0 > SPIN_TICKS) break;
      __builtin_amdgcn_s_sleep(8);
      asm volatile("" ::: "memory");
    }
    ((LAS unsigned*)lds)[L_Q + lane] = pack_bf16(__uint_as_float(g0[0]) + __uint_as_float(g1[0]), __uint_as_float(g0[2]) + __uint_as_float(g1[2]));
    lds_wait();
  }
  wg_barrier();
  const int red = blockIdx.x < NRED ? (xg * 2 + (j & 1)) * 2 + (j >> 1) : -1;
  const unsigned xw = (red >= 0 && tid < 16) ? *(const gu32*)(a.x + (red & 1) * DM + (red >> 1) * 32 + tid * 2) : 0u;
  asm volatile("" : "+v"(w20), "+v"(w21) : "v"(pf0), "v"(pf1));
  float gsum = 0.f;
  if (h < 28) PARTIAL(wm0, wm1, 1, gsum);
  f32x2 wf[64];
  {
    const rsrc_t ro = mk_rsrc(a.wo + (size_t)h * DH * DM, DH * DM * 2);
#pragma unroll
    for (int i = 0; i < 16; ++i) {
      const u32x4 w = lddef(ro, voff + (i & 3) * 1024, (wave * 16 + (i & ~3)) * 1024);
      SB();
#pragma unroll
      for (int k = 0; k < 4; ++k) { wf[i * 4 + k] = bf_unpack(w[k]); asm volatile("" : "+v"(wf[i * 4 + k])); }
      SB();
    }
  }
  const int lane0 = lane;
  while (__hip_atomic_load((LAS unsigned*)lds + L_CNT, RLX_WG) < 4u) __builtin_amdgcn_s_sleep(8);
  asm volatile("" ::: "memory");
  gu64* ml = (gu64*)(a.ws + WS_ML);
  gu64* G = (gu64*)(a.ws + WS_G);
  float mw = 0.f;
  const unsigned tk = now32();
  if (wave == 7) {
    FRESH();
    mw = lds[L_MX + (lane & 3)];
    float m = fmaxf(mw, dpp<XOR1>(mw)); m = fmaxf(m, dpp<XOR2>(m));
    float l = 0.f;
#pragma unroll 2
    for (int i = 0; i < 16; ++i) {
      const f32x4 sv = *(const LAS f32x4*)(lds + L_S + i * 256 + lane * 4);
#pragma unroll
      for (int k = 0; k < 4; ++k) l += __builtin_amdgcn_exp2f((sv[k] - m) * LOG2E);
    }
    l = wave_sum(l);
    if (lane < 16) st_granule(ml + chunk * 16 + lane, tag, lane == 0 ? m : lane == 1 ? l : __uint_as_float(tk));
  } else if (wave == 0 && h < 28) {
    FRESH();
    __builtin_amdgcn_s_sleep(16);
    st_granule(G + (h * 8 + g8) * 64 + lane, tag, gsum);
  }
  __builtin_amdgcn_s_sleep(127); __builtin_amdgcn_s_sleep(16);
  f32x2 yacc[4] = {{0.f, 0.f}, {0.f, 0.f}, {0.f, 0.f}, {0.f, 0.f}};
  if (wave < 7) {
    FRESH();
    const int f0 = (h * 4 + sp) * 14 + wave * 2;
    const int f = f0 + (r & 1);
    gu64* gp = G + ((f >> 6) * 8 + (c & 1) * 4 + (c >> 3) * 2 + ((f >> 5) & 1)) * 64 + b * 32 + (f & 31);
    const unsigned t0 = now32();
    unsigned long long g;
    while (true) {
      g = __hip_atomic_load(gp, RLX_AGENT);
      if (__all((unsigned)(g >> 32) == tag)) break;
      if (now32() - t0 > SPIN_TICKS) break;
      __builtin_amdgcn_s_sleep(127);
    }
    float s = __uint_as_float((unsigned)g);
    s += dpp<XOR1>(s);
    const float gs = rbf(s), us = rbf(dpp<ROR8>(s));
    const float hv = rbf(rbf(gs / (1.f + __expf(-gs))) * us);
    const float h0 = lane_value(hv, 0), h1 = lane_value(hv, 16);
#pragma unroll
    for (int k = 0; k < 4; ++k) yacc[k] = __builtin_elementwise_fma(bf_unpack(w21[k]), f32x2{h1, h1}, bf_unpack(w20[k]) * h0);
  } else {
    FRESH();
    {
      const unsigned long long gt = __hip_atomic_load(ml + (lane * 4 + (lane & 3)) * 16 + 2, RLX_AGENT);
      const bool v = (unsigned)(gt >> 32) == tag;
      const float n = wave_sum(v ? 1.f : 0.f), d = wave_sum(v ? (float)(int)((unsigned)gt - tk) : 0.f);
      if (lane == 0 && n > 0.f) ((LAS unsigned*)lds)[L_REF] = tk + (unsigned)(int)(d / n);
    }
    const unsigned t0 = now32();
    unsigned long long gm, gl;
    while (true) {
      gm = __hip_atomic_load(ml + ((chunk & ~3) + (lane & 3)) * 16, RLX_AGENT);
      gl = __hip_atomic_load(ml + ((chunk & ~3) + (lane & 3)) * 16 + 1, RLX_AGENT);
      if (__all((unsigned)(gm >> 32) == tag && (unsigned)(gl >> 32) == tag)) break;
      if (now32() - t0 > SPIN_TICKS) break;
      __builtin_amdgcn_s_sleep(127);
    }
    const float ms = __uint_as_float((unsigned)gm), ls = __uint_as_float((unsigned)gl);
    float M = fmaxf(ms, dpp<XOR1>(ms)); M = fmaxf(M, dpp<XOR2>(M));
    float L = ls * __builtin_amdgcn_exp2f((ms - M) * LOG2E);
    L += dpp<XOR1>(L); L += dpp<XOR2>(L);
    if (lane < 4) lds[L_FW + lane] = __builtin_amdgcn_exp2f((mw - M) * LOG2E) / L;
    lds_wait();
    {
      const unsigned char* pa = (lane < 32 || red < 0) ? a.ws + WS_Y + chunk * 2048 + (lane & 15) * 128
                                                       : (const unsigned char*)a.out + (red & 1) * (DM * 2) + (red >> 1) * 64;
      const unsigned jk = __hip_atomic_load((gu32*)pa, RLX_AGENT);
      asm volatile("" :: "v"(jk));
    }
    if (blockIdx.x == NWG - 1) {
      while (true) {
        bool ok = true;
#pragma unroll
        for (int i = 0; i < 4; ++i) ok = ok & ((unsigned)(__hip_atomic_load(ml + (i * 64 + lane) * 16 + 2, RLX_AGENT) >> 32) == tag);
        if (__all(ok)) break;
        if (now32() - t0 > SPIN_TICKS) break;
        __builtin_amdgcn_s_sleep(127);
      }
      if (lane == 0) __hip_atomic_store((gu32*)(a.ws + WS_EPOCH), tag, RLX_AGENT);
    }
  }
  {
    FRESH();
    wg_barrier();
    {
      const LAS float* P = (const LAS float*)lds + L_O + (4 * r) * 128 + wave * 16 + c;
      const float o = xrow_sum(((P[0] + P[128]) + (P[256] + P[384])) * lds[L_FW + r]);
#pragma unroll
      for (int i = 0; i < 16; ++i) {
        const float oi = lane_value(o, i);
#pragma unroll
        for (int k = 0; k < 4; ++k) yacc[k] = __builtin_elementwise_fma(wf[i * 4 + k], f32x2{oi, oi}, yacc[k]);
      }
    }
    *(LAS f32x4*)(lds + L_Y + wave * 512 + lane * 8) = f32x4{yacc[0][0], yacc[0][1], yacc[1][0], yacc[1][1]};
    *(LAS f32x4*)(lds + L_Y + wave * 512 + lane * 8 + 4) = f32x4{yacc[2][0], yacc[2][1], yacc[3][0], yacc[3][1]};
    lds_wait();
    wg_barrier();
    const rsrc_t ry = mk_rsrc(a.ws + WS_Y, NWG * 2048);
    {
      const LAS float* P = (const LAS float*)lds + L_Y + tid;
      const float y = ((P[0] + P[512]) + (P[1024] + P[1536])) + ((P[2048] + P[2560]) + (P[3072] + P[3584]));
      const unsigned u = __float_as_uint(y);
      const unsigned w = (((u & 0x7f800000u) == 0x7f800000u ? u | ((u & 0x7fffffu) ? 0x400000u : 0u) : u + 0x400u) & 0xfffff800u) | ((tag >> ((lane & 1) * 11)) & 0x7ffu);
      const unsigned w1 = __builtin_amdgcn_mov_dpp(w, XOR1, 0xf, 0xf, true);
      if (!(lane & 1)) __builtin_amdgcn_raw_buffer_store_b64(u32x2{w, w1}, ry, tid * 4, chunk * 2048, 16);
    }
    if (red >= 0) {
      const unsigned t0 = now32();
      u32x4 g0 = {0u, 0u, 0u, 0u}, g1 = {0u, 0u, 0u, 0u};
      bool ok0 = false, ok1 = false;
      const unsigned ta = tag & 0x7ffu, tb = (tag >> 11) & 0x7ffu;
      while (true) {
        if (!ok0) g0 = lddev(ry, (lane >> 3) * 2048 + (lane & 7) * 16, ((red & 1) * 128 + wave * 16) * 2048 + (red >> 1) * 128);
        if (!ok1) g1 = lddev(ry, (lane >> 3) * 2048 + (lane & 7) * 16, ((red & 1) * 128 + wave * 16 + 8) * 2048 + (red >> 1) * 128);
        ok0 = (((g0[0] ^ ta) | (g0[1] ^ tb) | (g0[2] ^ ta) | (g0[3] ^ tb)) & 0x7ffu) == 0u;
        ok1 = (((g1[0] ^ ta) | (g1[1] ^ tb) | (g1[2] ^ ta) | (g1[3] ^ tb)) & 0x7ffu) == 0u;
        if (__all(ok0 & ok1)) break;
        if (now32() - t0 > SPIN_TICKS) break;
        __builtin_amdgcn_s_sleep(4);
        asm volatile("" ::: "memory");
      }
      float t[4];
#pragma unroll
      for (int k = 0; k < 4; ++k) t[k] = __uint_as_float(g0[k] & 0xfffff800u) + __uint_as_float(g1[k] & 0xfffff800u);
      float s = tr16(tr32(t[0], t[2]), tr32(t[1], t[3]));
      s += dpp<ROR8>(s);
      if (!(lane & 8)) lds[L_FIN + wave * 32 + (lane & 7) * 4 + r] = s;
      lds_wait();
      wg_barrier();
      if (tid < 16) {
        const LAS f32x2* P = (const LAS f32x2*)(lds + L_FIN + tid * 2);
        const f32x2 o = bf_unpack(xw) + (((P[0] + P[16]) + (P[32] + P[48])) + ((P[64] + P[80]) + (P[96] + P[112])));
        *(gu32*)(a.out + (red & 1) * DM + (red >> 1) * 32 + tid * 2) = pack_bf16(o[0], o[1]);
      }
    }
  }
}

}

extern "C" void kernel_launch(void* const* d_in, const int* in_sizes, int n_in, void* d_out, int out_size, void* d_ws, size_t ws_size,
                              hipStream_t stream) {
  Args a;
  a.x = (const unsigned short*)d_in[0]; a.k = (const unsigned short*)d_in[1]; a.v = (const unsigned short*)d_in[2];
  a.wq = (const unsigned short*)d_in[3]; a.wo = (const unsigned short*)d_in[4];
  a.w1 = (const unsigned short*)d_in[6]; a.w3 = (const unsigned short*)d_in[7]; a.w2 = (const unsigned short*)d_in[8];
  a.out = (unsigned short*)d_out; a.ws = (unsigned char*)d_ws;
  fused_decode<<<NWG, NTHR, 0, stream>>>(a);
}
```

```cpp
#include <hip/hip_runtime.h>
#include <hip/hip_bf16.h>
#include <stdint.h>

namespace {

constexpr int NWG = 256, NTHR = 768, NRED = 32;
constexpr int DM = 512, NH = 32, SK = 16384, DH = 128, FF = 1792;
constexpr float LOG2E = 1.4426950408889634f;
constexpr float SCALE = 0.08838834764831845f;
constexpr int WIN = 4;
constexpr int NPRE = 2;
constexpr int LAG_A = 95, LAG_B = 108, LAG_2 = 80, WIDE_T = 3250;
constexpr size_t WS_EPOCH = 0;
constexpr size_t WS_ML = 8192;
constexpr size_t WS_Y = 65536;
constexpr size_t WS_Q = WS_Y + NWG * 2048;
constexpr size_t WS_G = WS_Q + (128u << 10);
constexpr int L_S = 0;
constexpr int L_O = L_S + 4096;
constexpr int L_Y = L_O + 2048;
constexpr int L_P = L_Y + 4096;
constexpr int L_Q = L_P + 1024;
constexpr int L_MX = L_Q + 64;
constexpr int L_FW = L_MX + 4;
constexpr int L_FIN = L_FW + 4;
constexpr int L_CNT = L_FIN + 256;
constexpr int L_REF = L_CNT + 3;
constexpr int L_EP = L_REF + 1;
constexpr int L_END = L_EP + 1;

#define GAS __attribute__((address_space(1)))
#define LAS __attribute__((address_space(3)))
#define RLX_AGENT __ATOMIC_RELAXED, __HIP_MEMORY_SCOPE_AGENT
#define RLX_WG __ATOMIC_RELAXED, __HIP_MEMORY_SCOPE_WORKGROUP
typedef unsigned u32x2 __attribute__((ext_vector_type(2)));
typedef unsigned u32x4 __attribute__((ext_vector_type(4)));
typedef float f32x2 __attribute__((ext_vector_type(2)));
typedef float f32x4 __attribute__((ext_vector_type(4)));
typedef __bf16 bf16x2 __attribute__((ext_vector_type(2)));
typedef GAS unsigned gu32;
typedef GAS unsigned long long gu64;
typedef __amdgpu_buffer_rsrc_t rsrc_t;

__device__ __forceinline__ float bf_lo(unsigned u) { return __builtin_bit_cast(float, u << 16); }
__device__ __forceinline__ float bf_hi(unsigned u) { return __builtin_bit_cast(float, u & 0xffff0000u); }
__device__ __forceinline__ f32x2 bf_unpack(unsigned u) { return f32x2{bf_lo(u), bf_hi(u)}; }
__device__ __forceinline__ unsigned pack_bf16(float lo, float hi) {
  bf16x2 r = {(__bf16)lo, (__bf16)hi};
  return __builtin_bit_cast(unsigned, r);
}
__device__ __forceinline__ float rbf(float x) { return (float)(__bf16)x; }
__device__ __forceinline__ float dot2(unsigned a, unsigned b, float acc) {
  return __builtin_amdgcn_fdot2_f32_bf16(__builtin_bit_cast(bf16x2, a), __builtin_bit_cast(bf16x2, b), acc, false);
}
template <int CTRL> __device__ __forceinline__ float dpp(float x) {
  return __builtin_bit_cast(float, __builtin_amdgcn_mov_dpp(__builtin_bit_cast(int, x), CTRL, 0xf, 0xf, true));
}
constexpr int XOR1 = 0xB1, XOR2 = 0x4E, XOR3 = 0x1B, XOR7 = 0x141, ROR8 = 0x128;
__device__ __forceinline__ float tr16(float a, float b) {
  auto s = __builtin_amdgcn_permlane16_swap(__float_as_uint(a), __float_as_uint(b), false, false);
  return __uint_as_float(s[0]) + __uint_as_float(s[1]);
}
__device__ __forceinline__ float tr32(float a, float b) {
  auto s = __builtin_amdgcn_permlane32_swap(__float_as_uint(a), __float_as_uint(b), false, false);
  return __uint_as_float(s[0]) + __uint_as_float(s[1]);
}
__device__ __forceinline__ float xrow_sum(float x) { x = tr16(x, x); return tr32(x, x); }
__device__ __forceinline__ float wave_sum(float x) {
  x += dpp<XOR1>(x); x += dpp<XOR2>(x); x += dpp<XOR7>(x); x += dpp<ROR8>(x);
  return xrow_sum(x);
}
__device__ __forceinline__ float wave_max(float x) {
  x = fmaxf(x, dpp<XOR1>(x)); x = fmaxf(x, dpp<XOR2>(x)); x = fmaxf(x, dpp<XOR7>(x)); x = fmaxf(x, dpp<ROR8>(x));
  auto s = __builtin_amdgcn_permlane16_swap(__float_as_uint(x), __float_as_uint(x), false, false);
  x = fmaxf(__uint_as_float(s[0]), __uint_as_float(s[1]));
  auto t = __builtin_amdgcn_permlane32_swap(__float_as_uint(x), __float_as_uint(x), false, false);
  return fmaxf(__uint_as_float(t[0]), __uint_as_float(t[1]));
}
__device__ __forceinline__ float lane_value(float x, int l) { return __builtin_bit_cast(float, __builtin_amdgcn_readlane(__builtin_bit_cast(int, x), l)); }
__device__ __forceinline__ void lds_wait() { asm volatile("s_waitcnt lgkmcnt(0)" ::: "memory"); }
__device__ __forceinline__ void wg_barrier() { __builtin_amdgcn_s_barrier(); asm volatile("" ::: "memory"); }
__device__ __forceinline__ void st_granule(gu64* g, unsigned tag, float v) {
  __hip_atomic_store(g, ((unsigned long long)tag << 32) | (unsigned long long)__float_as_uint(v), RLX_AGENT);
}
__device__ __forceinline__ rsrc_t mk_rsrc(const void* p, int bytes) { return __builtin_amdgcn_make_buffer_rsrc((void*)p, 0, bytes, 0x00020000); }
__device__ __forceinline__ u32x4 ldnt(rsrc_t r, int voff, int soff) { return __builtin_amdgcn_raw_buffer_load_b128(r, voff, soff, 2); }
__device__ __forceinline__ u32x4 lddev(rsrc_t r, int voff, int soff) { return __builtin_amdgcn_raw_buffer_load_b128(r, voff, soff, 16); }
__device__ __forceinline__ u32x4 lddef(rsrc_t r, int voff, int soff) { return __builtin_amdgcn_raw_buffer_load_b128(r, voff, soff, 0); }
__device__ __forceinline__ unsigned now32() { return (unsigned)__builtin_amdgcn_s_memrealtime(); }
constexpr unsigned SPIN_TICKS = 20000000u;

#define SB() __builtin_amdgcn_sched_barrier(0)
#define VM_WAIT(n) do { asm volatile("s_waitcnt vmcnt(%0)" :: "i"(n)); SB(); } while (0)
#define FRESH() int lane_ = lane0; asm volatile("" : "+v"(lane_)); \
  const int lane = lane_, r = lane >> 4, c = lane & 15, tid = wave * 64 + lane, voff = lane * 16; (void)r; (void)c; (void)tid; (void)voff

struct Args {
  const unsigned short* x; const unsigned short* k; const unsigned short* v; const unsigned short* wq; const unsigned short* wo;
  const unsigned short* w1; const unsigned short* w3; const unsigned short* w2;
  unsigned short* out; unsigned char* ws;
};

__global__ void __launch_bounds__(NTHR, 3) fused_decode(Args a) {
  __shared__ __attribute__((aligned(16))) float lds[L_END];
  const int tid = threadIdx.x, lane = tid & 63, wave = __builtin_amdgcn_readfirstlane(tid >> 6);
  const int r = lane >> 4, c = lane & 15;
  const int xg = blockIdx.x & 7, j = blockIdx.x >> 3;
  const int b = j >> 4, h = xg + ((j >> 2) & 3) * 8, sp = j & 3;
  const int chunk = b * 128 + h * 4 + sp;
  const int voff = lane * 16;
  asm volatile("" :: "s"(a.x), "s"(a.k), "s"(a.v), "s"(a.wq), "s"(a.ws));

  const int g8 = b * 4 + sp;
  const int prow = (g8 >> 2) * 256 + wave * 32 + 2 * (lane >> 2);
  u32x4 xv0, xv1, wq0, wq1, wm0, wm1, pf0, pf1, w20, w21;
  unsigned xq0, xq1, epoch;
  if (wave < 8) {
    xv0 = *(const GAS u32x4*)(a.x + lane * 8); xv1 = *(const GAS u32x4*)(a.x + DM + lane * 8);
    xq0 = *(const gu32*)(a.x + prow); xq1 = *(const gu32*)(a.x + DM + prow);
    epoch = __builtin_amdgcn_raw_buffer_load_b32(mk_rsrc(a.ws + WS_EPOCH, wave == 0 ? 4 : 0), 0, 0, 16);
    const rsrc_t rq = mk_rsrc(a.wq, DM * NH * DH * 2);
    wq0 = ldnt(rq, prow * (NH * DH * 2) + (lane & 3) * 16, h * (DH * 2) + (g8 & 3) * 64);
    wq1 = ldnt(rq, prow * (NH * DH * 2) + (lane & 3) * 16, h * (DH * 2) + (g8 & 3) * 64 + NH * DH * 2);
    SB();
    const rsrc_t rm = mk_rsrc((g8 & 2) ? a.w3 : a.w1, h < 28 ? DM * FF * 2 : 0);
    wm0 = ldnt(rm, prow * (FF * 2) + (lane & 3) * 16, h * 128 + (g8 & 1) * 64);
    wm1 = ldnt(rm, prow * (FF * 2) + (lane & 3) * 16, h * 128 + (g8 & 1) * 64 + FF * 2);
    const rsrc_t ro = mk_rsrc(a.wo + (size_t)h * DH * DM, DH * DM * 2);
    pf0 = lddef(ro, voff, (g8 * 16 + wave * 2) * 1024); pf1 = lddef(ro, voff + 1024, (g8 * 16 + wave * 2) * 1024);
    const rsrc_t r2 = mk_rsrc(a.w2 + (size_t)((h * 4 + sp) * 14 + wave * 2) * DM, wave < 7 ? 2 * DM * 2 : 0);
    w20 = ldnt(r2, voff, 0); w21 = ldnt(r2, voff + 1024, 0);
    SB();
    wg_barrier();
  }
  int role = wave;
  asm volatile("" : "+s"(role));
  if (__builtin_expect(role >= 8, 1)) {
    const int sw = wave - 8;
    constexpr int HB = SK * DH * 2;
    const unsigned short* kp = a.k + (size_t)(chunk >> 2) * SK * DH;
    const unsigned short* vp = a.v + (size_t)(chunk >> 2) * SK * DH;
    const rsrc_t rk = mk_rsrc(kp, HB), rv = mk_rsrc(vp, HB);
    const int voffh = voff + HB / 2;
    auto window = [&](int g) -> int { g += (j >> 3) * 2; return ((g & 1) << 19) + ((g & 14) << 15) + ((g & 16) << 16) + (sp << 14) + (sw << 12); };
#define ROWVO(n) ((((n) & 1) ? voffh : voff) + (((n) & 7) >> 1) * 1024)
#define ROWSO(grp, n) window((grp) + ((n) >> 3))
#define LDROW(rs, grp, n) ldnt(rs, ROWVO(n), ROWSO(grp, n))
    __builtin_amdgcn_s_setprio(2);
    u32x4 buf[8], pre[NPRE * 8];
    if (lane < 4) ((LAS unsigned*)lds)[L_CNT + lane] = lane < 3 ? 0u : now32() + 0x40000000u;
    lds_wait();
    wg_barrier();
    SB();
#pragma unroll
    for (int u = 0; u < NPRE * 8; ++u) {
      pre[u] = LDROW(rk, 0, u); SB();
      if (u >= WIN) VM_WAIT(WIN);
    }
#pragma unroll
    for (int u = 0; u < 8; ++u) {
      buf[u] = LDROW(rk, NPRE, u); SB();
      if (u < 7) VM_WAIT(WIN);
    }
    wg_barrier();
    const u32x4 qp = *(const LAS u32x4*)(lds + L_Q + c * 4);
    LAS float* S = (LAS float*)lds + L_S + sw * 1024;
    float mx = -INFINITY;
#define DOT(w) dot2((w)[3], qp[3], dot2((w)[2], qp[2], dot2((w)[1], qp[1], dot2((w)[0], qp[0], 0.f))))
#define K_FIN(grp) do { \
      _Pragma("unroll") for (int u = 0; u < 4; ++u) { const float lo = x[u] + dpp<ROR8>(x[u]), hi = x[u + 4] + dpp<ROR8>(x[u + 4]); x[u] = (c & 8) ? hi : lo; } \
      _Pragma("unroll") for (int u = 0; u < 2; ++u) { const float lo = x[u] + dpp<XOR7>(x[u]), hi = x[u + 2] + dpp<XOR7>(x[u + 2]); x[u] = (c & 4) ? hi : lo; } \
      { const float lo = x[0] + dpp<XOR2>(x[0]), hi = x[1] + dpp<XOR2>(x[1]); x[0] = (c & 2) ? hi : lo; } \
      float sc = x[0] + dpp<XOR1>(x[0]); \
      sc = rbf(rbf(sc) * SCALE); \
      mx = fmaxf(mx, sc); \
      S[(grp) * 32 + r * 8 + (c >> 1)] = sc; \
      SB(); \
    } while (0)
    static_assert(WIN >= 3 && WIN <= 5, "ring of 8 rows");
    {
      float x[8];
#pragma unroll
      for (int u = 0; u < 8; ++u) {
        if (u > 0 && u <= WIN + 1) { buf[u - 1] = LDROW(rk, NPRE + 1, u - 1); SB(); }
        x[u] = DOT(buf[u]);
        SB();
      }
      K_FIN(NPRE);
    }
#pragma unroll
    for (int pp = 0; pp < NPRE; ++pp) {
      float x[8];
#pragma unroll
      for (int w = 0; w < 8; ++w) x[w] = DOT(pre[pp * 8 + w]);
      K_FIN(pp);
      if (WIN + 1 + pp < 7) { VM_WAIT(WIN); buf[WIN + 1 + pp] = LDROW(rk, NPRE + 1, WIN + 1 + pp); SB(); }
    }
    static_assert(WIN + 1 + NPRE >= 7, "");
    int last = 31;
    asm volatile("" : "+s"(last));
#pragma nounroll
    for (int tt = NPRE + 1; tt <= last; ++tt) {
      const rsrc_t rn = mk_rsrc(tt == last ? vp : kp, HB);
      float x[8];
#pragma unroll
      for (int u = 0; u < 8; ++u) {
        const int n = u + 7;
        const int so = ROWSO(tt, n);
        asm volatile("s_waitcnt vmcnt(%0)" :: "i"(WIN), "s"(so));
        SB();
        buf[n & 7] = ldnt(n >= 8 ? rn : rk, ROWVO(n), so);
        SB();
        x[u] = DOT(buf[u]);
        SB();
      }
      K_FIN(tt);
    }
    mx = wave_max(mx);
    if (lane == 0) lds[L_MX + sw] = mx;
    lds_wait();
    if (lane == 0) __hip_atomic_fetch_add((LAS unsigned*)lds + L_CNT, 1u, RLX_WG);
    f32x2 acc[4] = {{0.f, 0.f}, {0.f, 0.f}, {0.f, 0.f}, {0.f, 0.f}};
    f32x4 s0 = *(const LAS f32x4*)(S + r * 8), s1;
    int open = -1, lag = -1;
#pragma nounroll
    for (int tt = 0; tt <= last; ++tt) {
      const rsrc_t rn = mk_rsrc(vp, tt == last ? 0 : HB);
      unsigned ref, tnow;
#pragma unroll
      for (int u = 0; u < 8; ++u) {
        const int n = u + 7;
        const int so = ROWSO(tt, n);
        asm volatile("s_cmp_gt_i32 %0, 0\n\ts_cbranch_scc1 1f\n\ts_waitcnt vmcnt(%2)\n\ts_cmp_gt_i32 %1, 0\n\ts_cbranch_scc1 1f\n\ts_waitcnt vmcnt(%3)\n1:"
                     :: "s"(open), "s"(lag), "i"(WIN + 2), "i"(WIN), "s"(so) : "scc");
        SB();
        buf[n & 7] = ldnt(n >= 8 ? rn : rv, ROWVO(n), so);
        SB();
        if (u == 0) s1 = *(const LAS f32x4*)(S + tt * 32 + r * 8 + 4);
        if (u == 4) { s0 = *(const LAS f32x4*)(S + ((tt + 1) & 31) * 32 + r * 8); ref = *(volatile LAS unsigned*)(lds + L_REF); tnow = now32(); }
        const float p = __builtin_amdgcn_exp2f(((u < 4 ? s0[u & 3] : s1[u & 3]) - mx) * LOG2E);
#pragma unroll
        for (int k = 0; k < 4; ++k) acc[k] = __builtin_elementwise_fma(bf_unpack(buf[u][k]), f32x2{p, p}, acc[k]);
        asm volatile("" : "+v"(acc[0]), "+v"(acc[1]), "+v"(acc[2]), "+v"(acc[3]));
        if (u == 6) {
          asm volatile("" : "+v"(ref), "+s"(tnow));
          const int el = (int)(tnow - __builtin_amdgcn_readfirstlane(ref));
          lag = el - (LAG_A + tt * LAG_B);
          open = max(el - WIDE_T, lag - LAG_2);
        }
        SB();
      }
    }
    *(LAS f32x4*)(lds + L_O + (sw * 4 + r) * 128 + c * 8) = f32x4{acc[0][0], acc[0][1], acc[1][0], acc[1][1]};
    *(LAS f32x4*)(lds + L_O + (sw * 4 + r) * 128 + c * 8 + 4) = f32x4{acc[2][0], acc[2][1], acc[3][0], acc[3][1]};
    lds_wait();
    wg_barrier();
    wg_barrier();
    if (blockIdx.x < NRED) wg_barrier();
    return;
  }

  float mb0, mb1, sd0, sd1;
  {
    float f0[8], f1[8];
#pragma unroll
    for (int k = 0; k < 4; ++k) { f0[2 * k] = bf_lo(xv0[k]); f0[2 * k + 1] = bf_hi(xv0[k]); f1[2 * k] = bf_lo(xv1[k]); f1[2 * k + 1] = bf_hi(xv1[k]); }
    float s0 = 0.f, s1 = 0.f;
#pragma unroll
    for (int k = 0; k < 8; ++k) { s0 += f0[k]; s1 += f1[k]; }
    const float m0 = wave_sum(s0) * (1.f / DM), m1 = wave_sum(s1) * (1.f / DM);
    float v0 = 0.f, v1 = 0.f;
#pragma unroll
    for (int k = 0; k < 8; ++k) { const float d0 = f0[k] - m0, d1 = f1[k] - m1; v0 = fmaf(d0, d0, v0); v1 = fmaf(d1, d1, v1); }
    v0 = wave_sum(v0) * (1.f / DM); v1 = wave_sum(v1) * (1.f / DM);
    mb0 = rbf(m0); sd0 = rbf(__builtin_sqrtf(rbf(rbf(v0) + 1e-5f)));
    mb1 = rbf(m1); sd1 = rbf(__builtin_sqrtf(rbf(rbf(v1) + 1e-5f)));
  }
#define XN(xf, mb, sd) rbf(rbf((xf) - (mb)) / (sd))
  if (tid == 0) ((LAS unsigned*)lds)[L_EP] = epoch;
  const float xa0 = XN(bf_lo(xq0), mb0, sd0), xa1 = XN(bf_hi(xq0), mb0, sd0), xb0 = XN(bf_lo(xq1), mb1, sd1), xb1 = XN(bf_hi(xq1), mb1, sd1);
#define PARTIAL(w0_, w1_, n, res) do { \
    float t[8]; \
    _Pragma("unroll") for (int k = 0; k < 4; ++k) { \
      const f32x2 w0 = bf_unpack((w0_)[k]), w1 = bf_unpack((w1_)[k]); \
      const f32x2 qa = __builtin_elementwise_fma(w1, f32x2{xa1, xa1}, w0 * xa0), qb = __builtin_elementwise_fma(w1, f32x2{xb1, xb1}, w0 * xb0); \
      t[2 * k] = tr32(qa[0], qb[0]); t[2 * k + 1] = tr32(qa[1], qb[1]);             \
    } \
    _Pragma("unroll") for (int k = 0; k < 4; ++k) t[k] = tr16(t[k], t[4 + k]);        \
    f32x2 o; \
    _Pragma("unroll") for (int k = 0; k < 2; ++k) { const float lo = t[k] + dpp<ROR8>(t[k]), hi = t[2 + k] + dpp<ROR8>(t[2 + k]); o[k] = (c & 8) ? hi : lo; } \
    const float lo = o[0] + dpp<XOR7>(dpp<XOR3>(o[0])), hi = o[1] + dpp<XOR7>(dpp<XOR3>(o[1]));     \
    lds[L_P + (n) * 512 + wave * 64 + (r >> 1) * 32 + (c & 3) * 8 + (r & 1) * 4 + (c >> 3) * 2 + ((c >> 2) & 1)] = (c & 4) ? hi : lo; \
    lds_wait(); \
    if (lane == 0) __hip_atomic_fetch_add((LAS unsigned*)lds + L_CNT + 1 + (n), 1u, RLX_WG); \
    while (__hip_atomic_load((LAS unsigned*)lds + L_CNT + 1 + (n), RLX_WG) < 8u) __builtin_amdgcn_s_sleep(0); \
    asm volatile("" ::: "memory"); \
    const LAS float* P = (const LAS float*)lds + L_P + (n) * 512 + lane; \
    res = ((P[0] + P[64]) + (P[128] + P[192])) + ((P[256] + P[320]) + (P[384] + P[448])); \
  } while (0)
  float qs;
  PARTIAL(wq0, wq1, 0, qs);
  const unsigned tag = __builtin_amdgcn_readfirstlane(((const LAS unsigned*)lds)[L_EP]) + 0x9E3779B9u;
  if (wave == 0) st_granule((gu64*)(a.ws + WS_Q) + (h * 8 + g8) * 64 + lane, tag, qs);
  if (wave == 7) {
    __builtin_amdgcn_s_sleep(32);
    const rsrc_t rg = mk_rsrc(a.ws + WS_Q + (size_t)h * 4096, 4096);
    const unsigned t0 = now32();
    u32x4 g0, g1;
    while (true) {
      g0 = lddev(rg, r * 512 + c * 16, b * 256); g1 = lddev(rg, r * 512 + c * 16, 2048 + b * 256);
      if (__all((g0[1] == tag) & (g0[3] == tag) & (g1[1] == tag) & (g1[3] == tag))) break;
      if (now32() - t0 > SPIN_TICKS) break;
      __builtin_amdgcn_s_sleep(8);
      asm volatile("" ::: "memory");
    }
    ((LAS unsigned*)lds)[L_Q + lane] = pack_bf16(__uint_as_float(g0[0]) + __uint_as_float(g1[0]), __uint_as_float(g0[2]) + __uint_as_float(g1[2]));
    lds_wait();
  }
  wg_barrier();
  const int red = blockIdx.x < NRED ? (xg * 2 + (j & 1)) * 2 + (j >> 1) : -1;
  const unsigned xw = (red >= 0 && tid < 16) ? *(const gu32*)(a.x + (red & 1) * DM + (red >> 1) * 32 + tid * 2) : 0u;
  asm volatile("" : "+v"(w20), "+v"(w21) : "v"(pf0), "v"(pf1));
  float gsum = 0.f;
  if (h < 28) PARTIAL(wm0, wm1, 1, gsum);
  f32x2 wf[64];
  {
    const rsrc_t ro = mk_rsrc(a.wo + (size_t)h * DH * DM, DH * DM * 2);
#pragma unroll
    for (int i = 0; i < 16; ++i) {
      const u32x4 w = lddef(ro, voff + (i & 3) * 1024, (wave * 16 + (i & ~3)) * 1024);
      SB();
#pragma unroll
      for (int k = 0; k < 4; ++k) { wf[i * 4 + k] = bf_unpack(w[k]); asm volatile("" : "+v"(wf[i * 4 + k])); }
      SB();
    }
  }
  const int lane0 = lane;
  while (__hip_atomic_load((LAS unsigned*)lds + L_CNT, RLX_WG) < 4u) __builtin_amdgcn_s_sleep(8);
  asm volatile("" ::: "memory");
  gu64* ml = (gu64*)(a.ws + WS_ML);
  gu64* G = (gu64*)(a.ws + WS_G);
  float mw = 0.f;
  const unsigned tk = now32();
  if (wave == 7) {
    FRESH();
    mw = lds[L_MX + (lane & 3)];
    float m = fmaxf(mw, dpp<XOR1>(mw)); m = fmaxf(m, dpp<XOR2>(m));
    float l = 0.f;
#pragma unroll 2
    for (int i = 0; i < 16; ++i) {
      const f32x4 sv = *(const LAS f32x4*)(lds + L_S + i * 256 + lane * 4);
#pragma unroll
      for (int k = 0; k < 4; ++k) l += __builtin_amdgcn_exp2f((sv[k] - m) * LOG2E);
    }
    l = wave_sum(l);
    if (lane < 16) st_granule(ml + chunk * 16 + lane, tag, lane == 0 ? m : lane == 1 ? l : __uint_as_float(tk));
  } else if (wave == 0 && h < 28) {
    FRESH();
    __builtin_amdgcn_s_sleep(16);
    st_granule(G + (h * 8 + g8) * 64 + lane, tag, gsum);
  }
  __builtin_amdgcn_s_sleep(127); __builtin_amdgcn_s_sleep(16);
  f32x2 yacc[4] = {{0.f, 0.f}, {0.f, 0.f}, {0.f, 0.f}, {0.f, 0.f}};
  if (wave < 7) {
    FRESH();
    const int f0 = (h * 4 + sp) * 14 + wave * 2;
    const int f = f0 + (r & 1);
    gu64* gp = G + ((f >> 6) * 8 + (c & 1) * 4 + (c >> 3) * 2 + ((f >> 5) & 1)) * 64 + b * 32 + (f & 31);
    const unsigned t0 = now32();
    unsigned long long g;
    while (true) {
      g = __hip_atomic_load(gp, RLX_AGENT);
      if (__all((unsigned)(g >> 32) == tag)) break;
      if (now32() - t0 > SPIN_TICKS) break;
      __builtin_amdgcn_s_sleep(127);
    }
    float s = __uint_as_float((unsigned)g);
    s += dpp<XOR1>(s);
    const float gs = rbf(s), us = rbf(dpp<ROR8>(s));
    const float hv = rbf(rbf(gs / (1.f + __expf(-gs))) * us);
    const float h0 = lane_value(hv, 0), h1 = lane_value(hv, 16);
#pragma unroll
    for (int k = 0; k < 4; ++k) yacc[k] = __builtin_elementwise_fma(bf_unpack(w21[k]), f32x2{h1, h1}, bf_unpack(w20[k]) * h0);
  } else {
    FRESH();
    {
      const unsigned long long gt = __hip_atomic_load(ml + (lane * 4 + (lane & 3)) * 16 + 2, RLX_AGENT);
      const bool v = (unsigned)(gt >> 32) == tag;
      const float n = wave_sum(v ? 1.f : 0.f), d = wave_sum(v ? (float)(int)((unsigned)gt - tk) : 0.f);
      if (lane == 0 && n > 0.f) ((LAS unsigned*)lds)[L_REF] = tk + (unsigned)(int)(d / n);
    }
    const unsigned t0 = now32();
    unsigned long long gm, gl;
    while (true) {
      gm = __hip_atomic_load(ml + ((chunk & ~3) + (lane & 3)) * 16, RLX_AGENT);
      gl = __hip_atomic_load(ml + ((chunk & ~3) + (lane & 3)) * 16 + 1, RLX_AGENT);
      if (__all((unsigned)(gm >> 32) == tag && (unsigned)(gl >> 32) == tag)) break;
      if (now32() - t0 > SPIN_TICKS) break;
      __builtin_amdgcn_s_sleep(127);
    }
    const float ms = __uint_as_float((unsigned)gm), ls = __uint_as_float((unsigned)gl);
    float M = fmaxf(ms, dpp<XOR1>(ms)); M = fmaxf(M, dpp<XOR2>(M));
    float L = ls * __builtin_amdgcn_exp2f((ms - M) * LOG2E);
    L += dpp<XOR1>(L); L += dpp<XOR2>(L);
    if (lane < 4) lds[L_FW + lane] = __builtin_amdgcn_exp2f((mw - M) * LOG2E) / L;
    lds_wait();
    {
      const unsigned char* pa = (lane < 32 || red < 0) ? a.ws + WS_Y + chunk * 2048 + (lane & 15) * 128
                                                       : (const unsigned char*)a.out + (red & 1) * (DM * 2) + (red >> 1) * 64;
      const unsigned jk = __hip_atomic_load((gu32*)pa, RLX_AGENT);
      asm volatile("" :: "v"(jk));
    }
    if (blockIdx.x == NWG - 1) {
      while (true) {
        bool ok = true;
#pragma unroll
        for (int i = 0; i < 4; ++i) ok = ok & ((unsigned)(__hip_atomic_load(ml + (i * 64 + lane) * 16 + 2, RLX_AGENT) >> 32) == tag);
        if (__all(ok)) break;
        if (now32() - t0 > SPIN_TICKS) break;
        __builtin_amdgcn_s_sleep(127);
      }
      if (lane == 0) __hip_atomic_store((gu32*)(a.ws + WS_EPOCH), tag, RLX_AGENT);
    }
  }
  {
    FRESH();
    wg_barrier();
    {
      const LAS float* P = (const LAS float*)lds + L_O + (4 * r) * 128 + wave * 16 + c;
      const float o = xrow_sum(((P[0] + P[128]) + (P[256] + P[384])) * lds[L_FW + r]);
#pragma unroll
      for (int i = 0; i < 16; ++i) {
        const float oi = lane_value(o, i);
#pragma unroll
        for (int k = 0; k < 4; ++k) yacc[k] = __builtin_elementwise_fma(wf[i * 4 + k], f32x2{oi, oi}, yacc[k]);
      }
    }
    *(LAS f32x4*)(lds + L_Y + wave * 512 + lane * 8) = f32x4{yacc[0][0], yacc[0][1], yacc[1][0], yacc[1][1]};
    *(LAS f32x4*)(lds + L_Y + wave * 512 + lane * 8 + 4) = f32x4{yacc[2][0], yacc[2][1], yacc[3][0], yacc[3][1]};
    lds_wait();
    wg_barrier();
    const rsrc_t ry = mk_rsrc(a.ws + WS_Y, NWG * 2048);
    {
      const LAS float* P = (const LAS float*)lds + L_Y + tid;
      const float y = ((P[0] + P[512]) + (P[1024] + P[1536])) + ((P[2048] + P[2560]) + (P[3072] + P[3584]));
      const unsigned u = __float_as_uint(y);
      const unsigned w = (((u & 0x7f800000u) == 0x7f800000u ? u | ((u & 0x7fffffu) ? 0x400000u : 0u) : u + 0x400u) & 0xfffff800u) | ((tag >> ((lane & 1) * 11)) & 0x7ffu);
      const unsigned w1 = __builtin_amdgcn_mov_dpp(w, XOR1, 0xf, 0xf, true);
      if (!(lane & 1)) __builtin_amdgcn_raw_buffer_store_b64(u32x2{w, w1}, ry, tid * 4, chunk * 2048, 16);
    }
    if (red >= 0) {
      const unsigned t0 = now32();
      u32x4 g0 = {0u, 0u, 0u, 0u}, g1 = {0u, 0u, 0u, 0u};
      bool ok0 = false, ok1 = false;
      const unsigned ta = tag & 0x7ffu, tb = (tag >> 11) & 0x7ffu;
      while (true) {
        if (!ok0) g0 = lddev(ry, (lane >> 3) * 2048 + (lane & 7) * 16, ((red & 1) * 128 + wave * 16) * 2048 + (red >> 1) * 128);
        if (!ok1) g1 = lddev(ry, (lane >> 3) * 2048 + (lane & 7) * 16, ((red & 1) * 128 + wave * 16 + 8) * 2048 + (red >> 1) * 128);
        ok0 = (((g0[0] ^ ta) | (g0[1] ^ tb) | (g0[2] ^ ta) | (g0[3] ^ tb)) & 0x7ffu) == 0u;
        ok1 = (((g1[0] ^ ta) | (g1[1] ^ tb) | (g1[2] ^ ta) | (g1[3] ^ tb)) & 0x7ffu) == 0u;
        if (__all(ok0 & ok1)) break;
        if (now32() - t0 > SPIN_TICKS) break;
        __builtin_amdgcn_s_sleep(4);
        asm volatile("" ::: "memory");
      }
      float t[4];
#pragma unroll
      for (int k = 0; k < 4; ++k) t[k] = __uint_as_float(g0[k] & 0xfffff800u) + __uint_as_float(g1[k] & 0xfffff800u);
      float s = tr16(tr32(t[0], t[2]), tr32(t[1], t[3]));
      s += dpp<ROR8>(s);
      if (!(lane & 8)) lds[L_FIN + wave * 32 + (lane & 7) * 4 + r] = s;
      lds_wait();
      wg_barrier();
      if (tid < 16) {
        const LAS f32x2* P = (const LAS f32x2*)(lds + L_FIN + tid * 2);
        const f32x2 o = bf_unpack(xw) + (((P[0] + P[16]) + (P[32] + P[48])) + ((P[64] + P[80]) + (P[96] + P[112])));
        *(gu32*)(a.out + (red & 1) * DM + (red >> 1) * 32 + tid * 2) = pack_bf16(o[0], o[1]);
      }
    }
  }
}

}

extern "C" void kernel_launch(void* const* d_in, const int* in_sizes, int n_in, void* d_out, int out_size, void* d_ws, size_t ws_size,
                              hipStream_t stream) {
  Args a;
  a.x = (const unsigned short*)d_in[0]; a.k = (const unsigned short*)d_in[1]; a.v = (const unsigned short*)d_in[2];
  a.wq = (const unsigned short*)d_in[3]; a.wo = (const unsigned short*)d_in[4];
  a.w1 = (const unsigned short*)d_in[6]; a.w3 = (const unsigned short*)d_in[7]; a.w2 = (const unsigned short*)d_in[8];
  a.out = (unsigned short*)d_out; a.ws = (unsigned char*)d_ws;
  fused_decode<<<NWG, NTHR, 0, stream>>>(a);
}
```

```cpp
#include <hip/hip_runtime.h>
#include <hip/hip_bf16.h>
#include <stdint.h>

namespace {

constexpr int NWG = 256, NTHR = 768, NRED = 32;
constexpr int DM = 512, NH = 32, SK = 16384, DH = 128, FF = 1792;
constexpr float LOG2E = 1.4426950408889634f;
constexpr float SCALE = 0.08838834764831845f;
constexpr int WIN = 4;
constexpr int NPRE = 2;
constexpr int LAG_A = 95, LAG_B = 108, LAG_2 = 80, WIDE_T = 3250;
constexpr size_t WS_EPOCH = 0;
constexpr size_t WS_ML = 8192;
constexpr size_t WS_Y = 65536;
constexpr size_t WS_Q = WS_Y + NWG * 2048;
constexpr size_t WS_G = WS_Q + (128u << 10);
constexpr int L_S = 0;
constexpr int L_O = L_S + 4096;
constexpr int L_Y = L_O + 2048;
constexpr int L_P = L_Y + 4096;
constexpr int L_Q = L_P + 1024;
constexpr int L_MX = L_Q + 64;
constexpr int L_FW = L_MX + 4;
constexpr int L_FIN = L_FW + 4;
constexpr int L_CNT = L_FIN + 256;
constexpr int L_REF = L_CNT + 3;
constexpr int L_END = L_REF + 1;

#define GAS __attribute__((address_space(1)))
#define LAS __attribute__((address_space(3)))
#define RLX_AGENT __ATOMIC_RELAXED, __HIP_MEMORY_SCOPE_AGENT
#define RLX_WG __ATOMIC_RELAXED, __HIP_MEMORY_SCOPE_WORKGROUP
typedef unsigned u32x2 __attribute__((ext_vector_type(2)));
typedef unsigned u32x4 __attribute__((ext_vector_type(4)));
typedef float f32x2 __attribute__((ext_vector_type(2)));
typedef float f32x4 __attribute__((ext_vector_type(4)));
typedef __bf16 bf16x2 __attribute__((ext_vector_type(2)));
typedef GAS unsigned gu32;
typedef GAS unsigned long long gu64;
typedef __amdgpu_buffer_rsrc_t rsrc_t;

__device__ __forceinline__ float bf_lo(unsigned u) { return __builtin_bit_cast(float, u << 16); }
__device__ __forceinline__ float bf_hi(unsigned u) { return __builtin_bit_cast(float, u & 0xffff0000u); }
__device__ __forceinline__ f32x2 bf_unpack(unsigned u) { return f32x2{bf_lo(u), bf_hi(u)}; }
__device__ __forceinline__ unsigned pack_bf16(float lo, float hi) {
  bf16x2 r = {(__bf16)lo, (__bf16)hi};
  return __builtin_bit_cast(unsigned, r);
}
__device__ __forceinline__ float rbf(float x) { return (float)(__bf16)x; }
__device__ __forceinline__ float dot2(unsigned a, unsigned b, float acc) {
  return __builtin_amdgcn_fdot2_f32_bf16(__builtin_bit_cast(bf16x2, a), __builtin_bit_cast(bf16x2, b), acc, false);
}
template <int CTRL> __device__ __forceinline__ float dpp(float x) {
  return __builtin_bit_cast(float, __builtin_amdgcn_mov_dpp(__builtin_bit_cast(int, x), CTRL, 0xf, 0xf, true));
}
constexpr int XOR1 = 0xB1, XOR2 = 0x4E, XOR3 = 0x1B, XOR7 = 0x141, ROR8 = 0x128;
__device__ __forceinline__ float tr16(float a, float b) {
  auto s = __builtin_amdgcn_permlane16_swap(__float_as_uint(a), __float_as_uint(b), false, false);
  return __uint_as_float(s[0]) + __uint_as_float(s[1]);
}
__device__ __forceinline__ float tr32(float a, float b) {
  auto s = __builtin_amdgcn_permlane32_swap(__float_as_uint(a), __float_as_uint(b), false, false);
  return __uint_as_float(s[0]) + __uint_as_float(s[1]);
}
__device__ __forceinline__ float xrow_sum(float x) { x = tr16(x, x); return tr32(x, x); }
__device__ __forceinline__ float wave_sum(float x) {
  x += dpp<XOR1>(x); x += dpp<XOR2>(x); x += dpp<XOR7>(x); x += dpp<ROR8>(x);
  return xrow_sum(x);
}
__device__ __forceinline__ float wave_max(float x) {
  x = fmaxf(x, dpp<XOR1>(x)); x = fmaxf(x, dpp<XOR2>(x)); x = fmaxf(x, dpp<XOR7>(x)); x = fmaxf(x, dpp<ROR8>(x));
  auto s = __builtin_amdgcn_permlane16_swap(__float_as_uint(x), __float_as_uint(x), false, false);
  x = fmaxf(__uint_as_float(s[0]), __uint_as_float(s[1]));
  auto t = __builtin_amdgcn_permlane32_swap(__float_as_uint(x), __float_as_uint(x), false, false);
  return fmaxf(__uint_as_float(t[0]), __uint_as_float(t[1]));
}
__device__ __forceinline__ float lane_value(float x, int l) { return __builtin_bit_cast(float, __builtin_amdgcn_readlane(__builtin_bit_cast(int, x), l)); }
__device__ __forceinline__ void lds_wait() { asm volatile("s_waitcnt lgkmcnt(0)" ::: "memory"); }
__device__ __forceinline__ void wg_barrier() { __builtin_amdgcn_s_barrier(); asm volatile("" ::: "memory"); }
__device__ __forceinline__ void st_granule(gu64* g, unsigned tag, float v) {
  __hip_atomic_store(g, ((unsigned long long)tag << 32) | (unsigned long long)__float_as_uint(v), RLX_AGENT);
}
__device__ __forceinline__ rsrc_t mk_rsrc(const void* p, int bytes) { return __builtin_amdgcn_make_buffer_rsrc((void*)p, 0, bytes, 0x00020000); }
__device__ __forceinline__ u32x4 ldnt(rsrc_t r, int voff, int soff) { return __builtin_amdgcn_raw_buffer_load_b128(r, voff, soff, 2); }
__device__ __forceinline__ u32x4 lddev(rsrc_t r, int voff, int soff) { return __builtin_amdgcn_raw_buffer_load_b128(r, voff, soff, 16); }
__device__ __forceinline__ u32x4 lddef(rsrc_t r, int voff, int soff) { return __builtin_amdgcn_raw_buffer_load_b128(r, voff, soff, 0); }
__device__ __forceinline__ unsigned now32() { return (unsigned)__builtin_amdgcn_s_memrealtime(); }
constexpr unsigned SPIN_TICKS = 20000000u;

#define SB() __builtin_amdgcn_sched_barrier(0)
#define VM_WAIT(n) do { asm volatile("s_waitcnt vmcnt(%0)" :: "i"(n)); SB(); } while (0)
#define FRESH() int lane_ = lane0; asm volatile("" : "+v"(lane_)); \
  const int lane = lane_, r = lane >> 4, c = lane & 15, tid = wave * 64 + lane, voff = lane * 16; (void)r; (void)c; (void)tid; (void)voff

struct Args {
  const unsigned short* x; const unsigned short* k; const unsigned short* v; const unsigned short* wq; const unsigned short* wo;
  const unsigned short* w1; const unsigned short* w3; const unsigned short* w2;
  unsigned short* out; unsigned char* ws;
};

__global__ void __launch_bounds__(NTHR, 3) fused_decode(Args a) {
  __shared__ __attribute__((aligned(16))) float lds[L_END];
  const int tid = threadIdx.x, lane = tid & 63, wave = __builtin_amdgcn_readfirstlane(tid >> 6);
  const int r = lane >> 4, c = lane & 15;
  const int xg = blockIdx.x & 7, j = blockIdx.x >> 3;
  const int b = j >> 4, h = xg + ((j >> 2) & 3) * 8, sp = j & 3;
  const int chunk = b * 128 + h * 4 + sp;
  const int voff = lane * 16;
  asm volatile("" :: "s"(a.x), "s"(a.k), "s"(a.v), "s"(a.wq), "s"(a.ws));

  const int g8 = b * 4 + sp;
  const int prow = (g8 >> 2) * 256 + wave * 32 + 2 * (lane >> 2);
  u32x4 xv0, xv1, wq0, wq1, wm0, wm1, pf0, pf1, w20, w21;
  unsigned xq0, xq1;
  if (wave < 8) {
    xv0 = *(const GAS u32x4*)(a.x + lane * 8); xv1 = *(const GAS u32x4*)(a.x + DM + lane * 8);
    xq0 = *(const gu32*)(a.x + prow); xq1 = *(const gu32*)(a.x + DM + prow);
    const rsrc_t rq = mk_rsrc(a.wq, DM * NH * DH * 2);
    wq0 = ldnt(rq, prow * (NH * DH * 2) + (lane & 3) * 16, h * (DH * 2) + (g8 & 3) * 64);
    wq1 = ldnt(rq, prow * (NH * DH * 2) + (lane & 3) * 16, h * (DH * 2) + (g8 & 3) * 64 + NH * DH * 2);
    SB();
    const rsrc_t rm = mk_rsrc((g8 & 2) ? a.w3 : a.w1, h < 28 ? DM * FF * 2 : 0);
    wm0 = ldnt(rm, prow * (FF * 2) + (lane & 3) * 16, h * 128 + (g8 & 1) * 64);
    wm1 = ldnt(rm, prow * (FF * 2) + (lane & 3) * 16, h * 128 + (g8 & 1) * 64 + FF * 2);
    const rsrc_t ro = mk_rsrc(a.wo + (size_t)h * DH * DM, DH * DM * 2);
    pf0 = lddef(ro, voff, (g8 * 16 + wave * 2) * 1024); pf1 = lddef(ro, voff + 1024, (g8 * 16 + wave * 2) * 1024);
    const rsrc_t r2 = mk_rsrc(a.w2 + (size_t)((h * 4 + sp) * 14 + wave * 2) * DM, wave < 7 ? 2 * DM * 2 : 0);
    w20 = ldnt(r2, voff, 0); w21 = ldnt(r2, voff + 1024, 0);
    SB();
    wg_barrier();
  }
  int role = wave;
  asm volatile("" : "+s"(role));
  if (__builtin_expect(role >= 8, 1)) {
    const int sw = wave - 8;
    constexpr int HB = SK * DH * 2;
    const unsigned short* kp = a.k + (size_t)(chunk >> 2) * SK * DH;
    const unsigned short* vp = a.v + (size_t)(chunk >> 2) * SK * DH;
    const rsrc_t rk = mk_rsrc(kp, HB), rv = mk_rsrc(vp, HB);
    const int voffh = voff + HB / 2;
    auto window = [&](int g) -> int { g += (j >> 3) * 2; return ((g & 1) << 19) + ((g & 14) << 15) + ((g & 16) << 16) + (sp << 14) + (sw << 12); };
#define ROWVO(n) ((((n) & 1) ? voffh : voff) + (((n) & 7) >> 1) * 1024)
#define ROWSO(grp, n) window((grp) + ((n) >> 3))
#define LDROW(rs, grp, n) ldnt(rs, ROWVO(n), ROWSO(grp, n))
    __builtin_amdgcn_s_setprio(2);
    u32x4 buf[8], pre[NPRE * 8];
    if (lane < 4) ((LAS unsigned*)lds)[L_CNT + lane] = lane < 3 ? 0u : now32() + 0x40000000u;
    lds_wait();
    wg_barrier();
    SB();
#pragma unroll
    for (int u = 0; u < NPRE * 8; ++u) {
      pre[u] = LDROW(rk, 0, u); SB();
      if (u >= WIN) VM_WAIT(WIN);
    }
#pragma unroll
    for (int u = 0; u < 8; ++u) {
      buf[u] = LDROW(rk, NPRE, u); SB();
      if (u < 7) VM_WAIT(WIN);
    }
    wg_barrier();
    const u32x4 qp = *(const LAS u32x4*)(lds + L_Q + c * 4);
    LAS float* S = (LAS float*)lds + L_S + sw * 1024;
    float mx = -INFINITY;
#define DOT(w) dot2((w)[3], qp[3], dot2((w)[2], qp[2], dot2((w)[1], qp[1], dot2((w)[0], qp[0], 0.f))))
#define K_FIN(grp) do { \
      _Pragma("unroll") for (int u = 0; u < 4; ++u) { const float lo = x[u] + dpp<ROR8>(x[u]), hi = x[u + 4] + dpp<ROR8>(x[u + 4]); x[u] = (c & 8) ? hi : lo; } \
      _Pragma("unroll") for (int u = 0; u < 2; ++u) { const float lo = x[u] + dpp<XOR7>(x[u]), hi = x[u + 2] + dpp<XOR7>(x[u + 2]); x[u] = (c & 4) ? hi : lo; } \
      { const float lo = x[0] + dpp<XOR2>(x[0]), hi = x[1] + dpp<XOR2>(x[1]); x[0] = (c & 2) ? hi : lo; } \
      float sc = x[0] + dpp<XOR1>(x[0]); \
      sc = rbf(rbf(sc) * SCALE); \
      mx = fmaxf(mx, sc); \
      S[(grp) * 32 + r * 8 + (c >> 1)] = sc; \
      SB(); \
    } while (0)
    static_assert(WIN >= 3 && WIN <= 5, "ring of 8 rows");
    {
      float x[8];
#pragma unroll
      for (int u = 0; u < 8; ++u) {
        if (u > 0 && u <= WIN + 1) { buf[u - 1] = LDROW(rk, NPRE + 1, u - 1); SB(); }
        x[u] = DOT(buf[u]);
        SB();
      }
      K_FIN(NPRE);
    }
#pragma unroll
    for (int pp = 0; pp < NPRE; ++pp) {
      float x[8];
#pragma unroll
      for (int w = 0; w < 8; ++w) x[w] = DOT(pre[pp * 8 + w]);
      K_FIN(pp);
      if (WIN + 1 + pp < 7) { VM_WAIT(WIN); buf[WIN + 1 + pp] = LDROW(rk, NPRE + 1, WIN + 1 + pp); SB(); }
    }
    static_assert(WIN + 1 + NPRE >= 7, "");
    int last = 31;
    asm volatile("" : "+s"(last));
#pragma nounroll
    for (int tt = NPRE + 1; tt <= last; ++tt) {
      const rsrc_t rn = mk_rsrc(tt == last ? vp : kp, HB);
      float x[8];
#pragma unroll
      for (int u = 0; u < 8; ++u) {
        const int n = u + 7;
        const int so = ROWSO(tt, n);
        asm volatile("s_waitcnt vmcnt(%0)" :: "i"(WIN), "s"(so));
        SB();
        buf[n & 7] = ldnt(n >= 8 ? rn : rk, ROWVO(n), so);
        SB();
        x[u] = DOT(buf[u]);
        SB();
      }
      K_FIN(tt);
    }
    mx = wave_max(mx);
    if (lane == 0) lds[L_MX + sw] = mx;
    lds_wait();
    if (lane == 0) __hip_atomic_fetch_add((LAS unsigned*)lds + L_CNT, 1u, RLX_WG);
    f32x2 acc[4] = {{0.f, 0.f}, {0.f, 0.f}, {0.f, 0.f}, {0.f, 0.f}};
    f32x4 s0 = *(const LAS f32x4*)(S + r * 8), s1;
    int open = -1, lag = -1;
#pragma nounroll
    for (int tt = 0; tt <= last; ++tt) {
      const rsrc_t rn = mk_rsrc(vp, tt == last ? 0 : HB);
      unsigned ref, tnow;
#pragma unroll
      for (int u = 0; u < 8; ++u) {
        const int n = u + 7;
        const int so = ROWSO(tt, n);
        asm volatile("s_cmp_gt_i32 %0, 0\n\ts_cbranch_scc1 1f\n\ts_waitcnt vmcnt(%2)\n\ts_cmp_gt_i32 %1, 0\n\ts_cbranch_scc1 1f\n\ts_waitcnt vmcnt(%3)\n1:"
                     :: "s"(open), "s"(lag), "i"(WIN + 2), "i"(WIN), "s"(so) : "scc");
        SB();
        buf[n & 7] = ldnt(n >= 8 ? rn : rv, ROWVO(n), so);
        SB();
        if (u == 0) s1 = *(const LAS f32x4*)(S + tt * 32 + r * 8 + 4);
        if (u == 4) { s0 = *(const LAS f32x4*)(S + ((tt + 1) & 31) * 32 + r * 8); ref = *(volatile LAS unsigned*)(lds + L_REF); tnow = now32(); }
        const float p = __builtin_amdgcn_exp2f(((u < 4 ? s0[u & 3] : s1[u & 3]) - mx) * LOG2E);
#pragma unroll
        for (int k = 0; k < 4; ++k) acc[k] = __builtin_elementwise_fma(bf_unpack(buf[u][k]), f32x2{p, p}, acc[k]);
        asm volatile("" : "+v"(acc[0]), "+v"(acc[1]), "+v"(acc[2]), "+v"(acc[3]));
        if (u == 6) {
          asm volatile("" : "+v"(ref), "+s"(tnow));
          const int el = (int)(tnow - __builtin_amdgcn_readfirstlane(ref));
          lag = el - (LAG_A + tt * LAG_B);
          open = max(el - WIDE_T, lag - LAG_2);
        }
        SB();
      }
    }
    *(LAS f32x4*)(lds + L_O + (sw * 4 + r) * 128 + c * 8) = f32x4{acc[0][0], acc[0][1], acc[1][0], acc[1][1]};
    *(LAS f32x4*)(lds + L_O + (sw * 4 + r) * 128 + c * 8 + 4) = f32x4{acc[2][0], acc[2][1], acc[3][0], acc[3][1]};
    lds_wait();
    wg_barrier();
    wg_barrier();
    if (blockIdx.x < NRED) wg_barrier();
    return;
  }

  unsigned epoch = *(const __attribute__((address_space(4))) unsigned*)(a.ws + WS_EPOCH);
  SB();
  float mb0, mb1, sd0, sd1;
  {
    float f0[8], f1[8];
#pragma unroll
    for (int k = 0; k < 4; ++k) { f0[2 * k] = bf_lo(xv0[k]); f0[2 * k + 1] = bf_hi(xv0[k]); f1[2 * k] = bf_lo(xv1[k]); f1[2 * k + 1] = bf_hi(xv1[k]); }
    float s0 = 0.f, s1 = 0.f;
#pragma unroll
    for (int k = 0; k < 8; ++k) { s0 += f0[k]; s1 += f1[k]; }
    const float m0 = wave_sum(s0) * (1.f / DM), m1 = wave_sum(s1) * (1.f / DM);
    float v0 = 0.f, v1 = 0.f;
#pragma unroll
    for (int k = 0; k < 8; ++k) { const float d0 = f0[k] - m0, d1 = f1[k] - m1; v0 = fmaf(d0, d0, v0); v1 = fmaf(d1, d1, v1); }
    v0 = wave_sum(v0) * (1.f / DM); v1 = wave_sum(v1) * (1.f / DM);
    mb0 = rbf(m0); sd0 = rbf(__builtin_sqrtf(rbf(rbf(v0) + 1e-5f)));
    mb1 = rbf(m1); sd1 = rbf(__builtin_sqrtf(rbf(rbf(v1) + 1e-5f)));
  }
#define XN(xf, mb, sd) rbf(rbf((xf) - (mb)) / (sd))
  asm volatile("" : "+s"(epoch));
  const unsigned tag = epoch + 0x9E3779B9u;
  const float xa0 = XN(bf_lo(xq0), mb0, sd0), xa1 = XN(bf_hi(xq0), mb0, sd0), xb0 = XN(bf_lo(xq1), mb1, sd1), xb1 = XN(bf_hi(xq1), mb1, sd1);
#define PARTIAL(w0_, w1_, n, res) do { \
    float t[8]; \
    _Pragma("unroll") for (int k = 0; k < 4; ++k) { \
      const f32x2 w0 = bf_unpack((w0_)[k]), w1 = bf_unpack((w1_)[k]); \
      const f32x2 qa = __builtin_elementwise_fma(w1, f32x2{xa1, xa1}, w0 * xa0), qb = __builtin_elementwise_fma(w1, f32x2{xb1, xb1}, w0 * xb0); \
      t[2 * k] = tr32(qa[0], qb[0]); t[2 * k + 1] = tr32(qa[1], qb[1]);             \
    } \
    _Pragma("unroll") for (int k = 0; k < 4; ++k) t[k] = tr16(t[k], t[4 + k]);        \
    f32x2 o; \
    _Pragma("unroll") for (int k = 0; k < 2; ++k) { const float lo = t[k] + dpp<ROR8>(t[k]), hi = t[2 + k] + dpp<ROR8>(t[2 + k]); o[k] = (c & 8) ? hi : lo; } \
    const float lo = o[0] + dpp<XOR7>(dpp<XOR3>(o[0])), hi = o[1] + dpp<XOR7>(dpp<XOR3>(o[1]));     \
    lds[L_P + (n) * 512 + wave * 64 + (r >> 1) * 32 + (c & 3) * 8 + (r & 1) * 4 + (c >> 3) * 2 + ((c >> 2) & 1)] = (c & 4) ? hi : lo; \
    lds_wait(); \
    if (lane == 0) __hip_atomic_fetch_add((LAS unsigned*)lds + L_CNT + 1 + (n), 1u, RLX_WG); \
    while (__hip_atomic_load((LAS unsigned*)lds + L_CNT + 1 + (n), RLX_WG) < 8u) __builtin_amdgcn_s_sleep(0); \
    asm volatile("" ::: "memory"); \
    const LAS float* P = (const LAS float*)lds + L_P + (n) * 512 + lane; \
    res = ((P[0] + P[64]) + (P[128] + P[192])) + ((P[256] + P[320]) + (P[384] + P[448])); \
  } while (0)
  {
    float qs;
    PARTIAL(wq0, wq1, 0, qs);
    if (wave == 0) st_granule((gu64*)(a.ws + WS_Q) + (h * 8 + g8) * 64 + lane, tag, qs);
  }
  if (wave == 7) {
    __builtin_amdgcn_s_sleep(32);
    const rsrc_t rg = mk_rsrc(a.ws + WS_Q + (size_t)h * 4096, 4096);
    const unsigned t0 = now32();
    u32x4 g0, g1;
    while (true) {
      g0 = lddev(rg, r * 512 + c * 16, b * 256); g1 = lddev(rg, r * 512 + c * 16, 2048 + b * 256);
      if (__all((g0[1] == tag) & (g0[3] == tag) & (g1[1] == tag) & (g1[3] == tag))) break;
      if (now32() - t0 > SPIN_TICKS) break;
      __builtin_amdgcn_s_sleep(8);
      asm volatile("" ::: "memory");
    }
    ((LAS unsigned*)lds)[L_Q + lane] = pack_bf16(__uint_as_float(g0[0]) + __uint_as_float(g1[0]), __uint_as_float(g0[2]) + __uint_as_float(g1[2]));
    lds_wait();
  }
  wg_barrier();
  const int red = blockIdx.x < NRED ? (xg * 2 + (j & 1)) * 2 + (j >> 1) : -1;
  const unsigned xw = (red >= 0 && tid < 16) ? *(const gu32*)(a.x + (red & 1) * DM + (red >> 1) * 32 + tid * 2) : 0u;
  asm volatile("" : "+v"(w20), "+v"(w21) : "v"(pf0), "v"(pf1));
  float gsum = 0.f;
  if (h < 28) PARTIAL(wm0, wm1, 1, gsum);
  f32x2 wf[64];
  {
    const rsrc_t ro = mk_rsrc(a.wo + (size_t)h * DH * DM, DH * DM * 2);
#pragma unroll
    for (int i = 0; i < 16; ++i) {
      const u32x4 w = lddef(ro, voff + (i & 3) * 1024, (wave * 16 + (i & ~3)) * 1024);
      SB();
#pragma unroll
      for (int k = 0; k < 4; ++k) { wf[i * 4 + k] = bf_unpack(w[k]); asm volatile("" : "+v"(wf[i * 4 + k])); }
      SB();
    }
  }
  const int lane0 = lane;
  while (__hip_atomic_load((LAS unsigned*)lds + L_CNT, RLX_WG) < 4u) __builtin_amdgcn_s_sleep(8);
  asm volatile("" ::: "memory");
  gu64* ml = (gu64*)(a.ws + WS_ML);
  gu64* G = (gu64*)(a.ws + WS_G);
  float mw = 0.f;
  const unsigned tk = now32();
  if (wave == 7) {
    FRESH();
    mw = lds[L_MX + (lane & 3)];
    float m = fmaxf(mw, dpp<XOR1>(mw)); m = fmaxf(m, dpp<XOR2>(m));
    float l = 0.f;
#pragma unroll 2
    for (int i = 0; i < 16; ++i) {
      const f32x4 sv = *(const LAS f32x4*)(lds + L_S + i * 256 + lane * 4);
#pragma unroll
      for (int k = 0; k < 4; ++k) l += __builtin_amdgcn_exp2f((sv[k] - m) * LOG2E);
    }
    l = wave_sum(l);
    if (lane < 16) st_granule(ml + chunk * 16 + lane, tag, lane == 0 ? m : lane == 1 ? l : __uint_as_float(tk));
  } else if (wave == 0 && h < 28) {
    FRESH();
    __builtin_amdgcn_s_sleep(16);
    st_granule(G + (h * 8 + g8) * 64 + lane, tag, gsum);
  }
  __builtin_amdgcn_s_sleep(127); __builtin_amdgcn_s_sleep(16);
  f32x2 yacc[4] = {{0.f, 0.f}, {0.f, 0.f}, {0.f, 0.f}, {0.f, 0.f}};
  if (wave < 7) {
    FRESH();
    const int f0 = (h * 4 + sp) * 14 + wave * 2;
    const int f = f0 + (r & 1);
    gu64* gp = G + ((f >> 6) * 8 + (c & 1) * 4 + (c >> 3) * 2 + ((f >> 5) & 1)) * 64 + b * 32 + (f & 31);
    const unsigned t0 = now32();
    unsigned long long g;
    while (true) {
      g = __hip_atomic_load(gp, RLX_AGENT);
      if (__all((unsigned)(g >> 32) == tag)) break;
      if (now32() - t0 > SPIN_TICKS) break;
      __builtin_amdgcn_s_sleep(127);
    }
    float s = __uint_as_float((unsigned)g);
    s += dpp<XOR1>(s);
    const float gs = rbf(s), us = rbf(dpp<ROR8>(s));
    const float hv = rbf(rbf(gs / (1.f + __expf(-gs))) * us);
    const float h0 = lane_value(hv, 0), h1 = lane_value(hv, 16);
#pragma unroll
    for (int k = 0; k < 4; ++k) yacc[k] = __builtin_elementwise_fma(bf_unpack(w21[k]), f32x2{h1, h1}, bf_unpack(w20[k]) * h0);
  } else {
    FRESH();
    {
      const unsigned long long gt = __hip_atomic_load(ml + (lane * 4 + (lane & 3)) * 16 + 2, RLX_AGENT);
      const bool v = (unsigned)(gt >> 32) == tag;
      const float n = wave_sum(v ? 1.f : 0.f), d = wave_sum(v ? (float)(int)((unsigned)gt - tk) : 0.f);
      if (lane == 0 && n > 0.f) ((LAS unsigned*)lds)[L_REF] = tk + (unsigned)(int)(d / n);
    }
    const unsigned t0 = now32();
    unsigned long long gm, gl;
    while (true) {
      gm = __hip_atomic_load(ml + ((chunk & ~3) + (lane & 3)) * 16, RLX_AGENT);
      gl = __hip_atomic_load(ml + ((chunk & ~3) + (lane & 3)) * 16 + 1, RLX_AGENT);
      if (__all((unsigned)(gm >> 32) == tag && (unsigned)(gl >> 32) == tag)) break;
      if (now32() - t0 > SPIN_TICKS) break;
      __builtin_amdgcn_s_sleep(127);
    }
    const float ms = __uint_as_float((unsigned)gm), ls = __uint_as_float((unsigned)gl);
    float M = fmaxf(ms, dpp<XOR1>(ms)); M = fmaxf(M, dpp<XOR2>(M));
    float L = ls * __builtin_amdgcn_exp2f((ms - M) * LOG2E);
    L += dpp<XOR1>(L); L += dpp<XOR2>(L);
    if (lane < 4) lds[L_FW + lane] = __builtin_amdgcn_exp2f((mw - M) * LOG2E) / L;
    lds_wait();
    {
      const unsigned char* pa = (lane < 32 || red < 0) ? a.ws + WS_Y + chunk * 2048 + (lane & 15) * 128
                                                       : (const unsigned char*)a.out + (red & 1) * (DM * 2) + (red >> 1) * 64;
      const unsigned jk = __hip_atomic_load((gu32*)pa, RLX_AGENT);
      asm volatile("" :: "v"(jk));
    }
    if (blockIdx.x == NWG - 1) {
      while (true) {
        bool ok = true;
#pragma unroll
        for (int i = 0; i < 4; ++i) ok = ok & ((unsigned)(__hip_atomic_load(ml + (i * 64 + lane) * 16 + 2, RLX_AGENT) >> 32) == tag);
        if (__all(ok)) break;
        if (now32() - t0 > SPIN_TICKS) break;
        __builtin_amdgcn_s_sleep(127);
      }
      if (lane == 0) __hip_atomic_store((gu32*)(a.ws + WS_EPOCH), tag, RLX_AGENT);
    }
  }
  {
    FRESH();
    wg_barrier();
    {
      const LAS float* P = (const LAS float*)lds + L_O + (4 * r) * 128 + wave * 16 + c;
      const float o = xrow_sum(((P[0] + P[128]) + (P[256] + P[384])) * lds[L_FW + r]);
#pragma unroll
      for (int i = 0; i < 16; ++i) {
        const float oi = lane_value(o, i);
#pragma unroll
        for (int k = 0; k < 4; ++k) yacc[k] = __builtin_elementwise_fma(wf[i * 4 + k], f32x2{oi, oi}, yacc[k]);
      }
    }
    *(LAS f32x4*)(lds + L_Y + wave * 512 + lane * 8) = f32x4{yacc[0][0], yacc[0][1], yacc[1][0], yacc[1][1]};
    *(LAS f32x4*)(lds + L_Y + wave * 512 + lane * 8 + 4) = f32x4{yacc[2][0], yacc[2][1], yacc[3][0], yacc[3][1]};
    lds_wait();
    wg_barrier();
    const rsrc_t ry = mk_rsrc(a.ws + WS_Y, NWG * 2048);
    {
      const LAS float* P = (const LAS float*)lds + L_Y + tid;
      const float y = ((P[0] + P[512]) + (P[1024] + P[1536])) + ((P[2048] + P[2560]) + (P[3072] + P[3584]));
      const unsigned u = __float_as_uint(y);
      const unsigned w = (((u & 0x7f800000u) == 0x7f800000u ? u | ((u & 0x7fffffu) ? 0x400000u : 0u) : u + 0x400u) & 0xfffff800u) | ((tag >> ((lane & 1) * 11)) & 0x7ffu);
      const unsigned w1 = __builtin_amdgcn_mov_dpp(w, XOR1, 0xf, 0xf, true);
      if (!(lane & 1)) __builtin_amdgcn_raw_buffer_store_b64(u32x2{w, w1}, ry, tid * 4, chunk * 2048, 16);
    }
    if (red >= 0) {
      const unsigned t0 = now32();
      u32x4 g0 = {0u, 0u, 0u, 0u}, g1 = {0u, 0u, 0u, 0u};
      bool ok0 = false, ok1 = false;
      const unsigned ta = tag & 0x7ffu, tb = (tag >> 11) & 0x7ffu;
      while (true) {
        if (!ok0) g0 = lddev(ry, (lane >> 3) * 2048 + (lane & 7) * 16, ((red & 1) * 128 + wave * 16) * 2048 + (red >> 1) * 128);
        if (!ok1) g1 = lddev(ry, (lane >> 3) * 2048 + (lane & 7) * 16, ((red & 1) * 128 + wave * 16 + 8) * 2048 + (red >> 1) * 128);
        ok0 = (((g0[0] ^ ta) | (g0[1] ^ tb) | (g0[2] ^ ta) | (g0[3] ^ tb)) & 0x7ffu) == 0u;
        ok1 = (((g1[0] ^ ta) | (g1[1] ^ tb) | (g1[2] ^ ta) | (g1[3] ^ tb)) & 0x7ffu) == 0u;
        if (__all(ok0 & ok1)) break;
        if (now32() - t0 > SPIN_TICKS) break;
        __builtin_amdgcn_s_sleep(4);
        asm volatile("" ::: "memory");
      }
      float t[4];
#pragma unroll
      for (int k = 0; k < 4; ++k) t[k] = __uint_as_float(g0[k] & 0xfffff800u) + __uint_as_float(g1[k] & 0xfffff800u);
      float s = tr16(tr32(t[0], t[2]), tr32(t[1], t[3]));
      s += dpp<ROR8>(s);
      if (!(lane & 8)) lds[L_FIN + wave * 32 + (lane & 7) * 4 + r] = s;
      lds_wait();
      wg_barrier();
      if (tid < 16) {
        const LAS f32x2* P = (const LAS f32x2*)(lds + L_FIN + tid * 2);
        const f32x2 o = bf_unpack(xw) + (((P[0] + P[16]) + (P[32] + P[48])) + ((P[64] + P[80]) + (P[96] + P[112])));
        *(gu32*)(a.out + (red & 1) * DM + (red >> 1) * 32 + tid * 2) = pack_bf16(o[0], o[1]);
      }
    }
  }
}

}

extern "C" void kernel_launch(void* const* d_in, const int* in_sizes, int n_in, void* d_out, int out_size, void* d_ws, size_t ws_size,
                              hipStream_t stream) {
  Args a;
  a.x = (const unsigned short*)d_in[0]; a.k = (const unsigned short*)d_in[1]; a.v = (const unsigned short*)d_in[2];
  a.wq = (const unsigned short*)d_in[3]; a.wo = (const unsigned short*)d_in[4];
  a.w1 = (const unsigned short*)d_in[6]; a.w3 = (const unsigned short*)d_in[7]; a.w2 = (const unsigned short*)d_in[8];
  a.out = (unsigned short*)d_out; a.ws = (unsigned char*)d_ws;
  fused_decode<<<NWG, NTHR, 0, stream>>>(a);
}
```

```cpp
#include <hip/hip_runtime.h>
#include <hip/hip_bf16.h>
#include <stdint.h>

namespace {

constexpr int NWG = 256, NTHR = 768, NRED = 32;
constexpr int DM = 512, NH = 32, SK = 16384, DH = 128, FF = 1792;
constexpr float LOG2E = 1.4426950408889634f;
constexpr float SCALE = 0.08838834764831845f;
constexpr int WIN = 4;
constexpr int NPRE = 2;
constexpr int LAG_A = 95, LAG_B = 108, LAG_2 = 80, WIDE_T = 3250;
constexpr size_t WS_EPOCH = 0;
constexpr size_t WS_ML = 8192;
constexpr size_t WS_Y = 65536;
constexpr size_t WS_Q = WS_Y + NWG * 2048;
constexpr size_t WS_G = WS_Q + (128u << 10);
constexpr int L_S = 0;
constexpr int L_O = L_S + 4096;
constexpr int L_Y = L_O + 2048;
constexpr int L_P = L_Y + 4096;
constexpr int L_Q = L_P + 1024;
constexpr int L_MX = L_Q + 64;
constexpr int L_FW = L_MX + 4;
constexpr int L_FIN = L_FW + 4;
constexpr int L_CNT = L_FIN + 256;
constexpr int L_REF = L_CNT + 3;
constexpr int L_EP = L_REF + 1;
constexpr int L_END = L_EP + 1;

#define GAS __attribute__((address_space(1)))
#define LAS __attribute__((address_space(3)))
#define RLX_AGENT __ATOMIC_RELAXED, __HIP_MEMORY_SCOPE_AGENT
#define RLX_WG __ATOMIC_RELAXED, __HIP_MEMORY_SCOPE_WORKGROUP
typedef unsigned u32x2 __attribute__((ext_vector_type(2)));
typedef unsigned u32x4 __attribute__((ext_vector_type(4)));
typedef float f32x2 __attribute__((ext_vector_type(2)));
typedef float f32x4 __attribute__((ext_vector_type(4)));
typedef __bf16 bf16x2 __attribute__((ext_vector_type(2)));
typedef GAS unsigned gu32;
typedef GAS unsigned long long gu64;
typedef __amdgpu_buffer_rsrc_t rsrc_t;

__device__ __forceinline__ float bf_lo(unsigned u) { return __builtin_bit_cast(float, u << 16); }
__device__ __forceinline__ float bf_hi(unsigned u) { return __builtin_bit_cast(float, u & 0xffff0000u); }
__device__ __forceinline__ f32x2 bf_unpack(unsigned u) { return f32x2{bf_lo(u), bf_hi(u)}; }
__device__ __forceinline__ unsigned pack_bf16(float lo, float hi) {
  bf16x2 r = {(__bf16)lo, (__bf16)hi};
  return __builtin_bit_cast(unsigned, r);
}
__device__ __forceinline__ float rbf(float x) { return (float)(__bf16)x; }
__device__ __forceinline__ float dot2(unsigned a, unsigned b, float acc) {
  return __builtin_amdgcn_fdot2_f32_bf16(__builtin_bit_cast(bf16x2, a), __builtin_bit_cast(bf16x2, b), acc, false);
}
template <int CTRL> __device__ __forceinline__ float dpp(float x) {
  return __builtin_bit_cast(float, __builtin_amdgcn_mov_dpp(__builtin_bit_cast(int, x), CTRL, 0xf, 0xf, true));
}
constexpr int XOR1 = 0xB1, XOR2 = 0x4E, XOR3 = 0x1B, XOR7 = 0x141, ROR8 = 0x128;
__device__ __forceinline__ float tr16(float a, float b) {
  auto s = __builtin_amdgcn_permlane16_swap(__float_as_uint(a), __float_as_uint(b), false, false);
  return __uint_as_float(s[0]) + __uint_as_float(s[1]);
}
__device__ __forceinline__ float tr32(float a, float b) {
  auto s = __builtin_amdgcn_permlane32_swap(__float_as_uint(a), __float_as_uint(b), false, false);
  return __uint_as_float(s[0]) + __uint_as_float(s[1]);
}
__device__ __forceinline__ float xrow_sum(float x) { x = tr16(x, x); return tr32(x, x); }
__device__ __forceinline__ float wave_sum(float x) {
  x += dpp<XOR1>(x); x += dpp<XOR2>(x); x += dpp<XOR7>(x); x += dpp<ROR8>(x);
  return xrow_sum(x);
}
__device__ __forceinline__ float wave_max(float x) {
  x = fmaxf(x, dpp<XOR1>(x)); x = fmaxf(x, dpp<XOR2>(x)); x = fmaxf(x, dpp<XOR7>(x)); x = fmaxf(x, dpp<ROR8>(x));
  auto s = __builtin_amdgcn_permlane16_swap(__float_as_uint(x), __float_as_uint(x), false, false);
  x = fmaxf(__uint_as_float(s[0]), __uint_as_float(s[1]));
  auto t = __builtin_amdgcn_permlane32_swap(__float_as_uint(x), __float_as_uint(x), false, false);
  return fmaxf(__uint_as_float(t[0]), __uint_as_float(t[1]));
}
__device__ __forceinline__ float lane_value(float x, int l) { return __builtin_bit_cast(float, __builtin_amdgcn_readlane(__builtin_bit_cast(int, x), l)); }
__device__ __forceinline__ void lds_wait() { asm volatile("s_waitcnt lgkmcnt(0)" ::: "memory"); }
__device__ __forceinline__ void wg_barrier() { __builtin_amdgcn_s_barrier(); asm volatile("" ::: "memory"); }
__device__ __forceinline__ void st_granule(gu64* g, unsigned tag, float v) {
  __hip_atomic_store(g, ((unsigned long long)tag << 32) | (unsigned long long)__float_as_uint(v), RLX_AGENT);
}
__device__ __forceinline__ rsrc_t mk_rsrc(const void* p, int bytes) { return __builtin_amdgcn_make_buffer_rsrc((void*)p, 0, bytes, 0x00020000); }
__device__ __forceinline__ u32x4 ldnt(rsrc_t r, int voff, int soff) { return __builtin_amdgcn_raw_buffer_load_b128(r, voff, soff, 2); }
__device__ __forceinline__ u32x4 lddev(rsrc_t r, int voff, int soff) { return __builtin_amdgcn_raw_buffer_load_b128(r, voff, soff, 16); }
__device__ __forceinline__ u32x4 lddef(rsrc_t r, int voff, int soff) { return __builtin_amdgcn_raw_buffer_load_b128(r, voff, soff, 0); }
__device__ __forceinline__ unsigned now32() { return (unsigned)__builtin_amdgcn_s_memrealtime(); }
constexpr unsigned SPIN_TICKS = 20000000u;

#define SB() __builtin_amdgcn_sched_barrier(0)
#define VM_WAIT(n) do { asm volatile("s_waitcnt vmcnt(%0)" :: "i"(n)); SB(); } while (0)
#define FRESH() int lane_ = lane0; asm volatile("" : "+v"(lane_)); \
  const int lane = lane_, r = lane >> 4, c = lane & 15, tid = wave * 64 + lane, voff = lane * 16; (void)r; (void)c; (void)tid; (void)voff

struct Args {
  const unsigned short* x; const unsigned short* k; const unsigned short* v; const unsigned short* wq; const unsigned short* wo;
  const unsigned short* w1; const unsigned short* w3; const unsigned short* w2;
  unsigned short* out; unsigned char* ws;
};

__global__ void __launch_bounds__(NTHR, 3) fused_decode(Args a) {
  __shared__ __attribute__((aligned(16))) float lds[L_END];
  const int tid = threadIdx.x, lane = tid & 63, wave = __builtin_amdgcn_readfirstlane(tid >> 6);
  const int r = lane >> 4, c = lane & 15;
  const int xg = blockIdx.x & 7, j = blockIdx.x >> 3;
  const int b = j >> 4, h = xg + ((j >> 2) & 3) * 8, sp = j & 3;
  const int chunk = b * 128 + h * 4 + sp;
  const int voff = lane * 16;
  asm volatile("" :: "s"(a.x), "s"(a.k), "s"(a.v), "s"(a.wq), "s"(a.ws));

  const int g8 = b * 4 + sp;
  const int prow = (g8 >> 2) * 256 + wave * 32 + 2 * (lane >> 2);
  u32x4 xv0, xv1, wq0, wq1, wm0, wm1, pf0, pf1, w20, w21;
  unsigned xq0, xq1, warm;
  if (wave < 8) {
    asm volatile("s_load_dword %0, %1, 0x400" : "=s"(warm) : "s"(a.ws));
    xv0 = *(const GAS u32x4*)(a.x + lane * 8); xv1 = *(const GAS u32x4*)(a.x + DM + lane * 8);
    xq0 = *(const gu32*)(a.x + prow); xq1 = *(const gu32*)(a.x + DM + prow);
    const rsrc_t rq = mk_rsrc(a.wq, DM * NH * DH * 2);
    wq0 = ldnt(rq, prow * (NH * DH * 2) + (lane & 3) * 16, h * (DH * 2) + (g8 & 3) * 64);
    wq1 = ldnt(rq, prow * (NH * DH * 2) + (lane & 3) * 16, h * (DH * 2) + (g8 & 3) * 64 + NH * DH * 2);
    SB();
    const rsrc_t rm = mk_rsrc((g8 & 2) ? a.w3 : a.w1, h < 28 ? DM * FF * 2 : 0);
    wm0 = ldnt(rm, prow * (FF * 2) + (lane & 3) * 16, h * 128 + (g8 & 1) * 64);
    wm1 = ldnt(rm, prow * (FF * 2) + (lane & 3) * 16, h * 128 + (g8 & 1) * 64 + FF * 2);
    const rsrc_t ro = mk_rsrc(a.wo + (size_t)h * DH * DM, DH * DM * 2);
    pf0 = lddef(ro, voff, (g8 * 16 + wave * 2) * 1024); pf1 = lddef(ro, voff + 1024, (g8 * 16 + wave * 2) * 1024);
    const rsrc_t r2 = mk_rsrc(a.w2 + (size_t)((h * 4 + sp) * 14 + wave * 2) * DM, wave < 7 ? 2 * DM * 2 : 0);
    w20 = ldnt(r2, voff, 0); w21 = ldnt(r2, voff + 1024, 0);
    SB();
    wg_barrier();
  }
  int role = wave;
  asm volatile("" : "+s"(role));
  if (__builtin_expect(role >= 8, 1)) {
    const int sw = wave - 8;
    constexpr int HB = SK * DH * 2;
    const unsigned short* kp = a.k + (size_t)(chunk >> 2) * SK * DH;
    const unsigned short* vp = a.v + (size_t)(chunk >> 2) * SK * DH;
    const rsrc_t rk = mk_rsrc(kp, HB), rv = mk_rsrc(vp, HB);
    const int voffh = voff + HB / 2;
    auto window = [&](int g) -> int { g += (j >> 3) * 2; return ((g & 1) << 19) + ((g & 14) << 15) + ((g & 16) << 16) + (sp << 14) + (sw << 12); };
#define ROWVO(n) ((((n) & 1) ? voffh : voff) + (((n) & 7) >> 1) * 1024)
#define ROWSO(grp, n) window((grp) + ((n) >> 3))
#define LDROW(rs, grp, n) ldnt(rs, ROWVO(n), ROWSO(grp, n))
    __builtin_amdgcn_s_setprio(2);
    u32x4 buf[8], pre[NPRE * 8];
    if (lane < 4) ((LAS unsigned*)lds)[L_CNT + lane] = lane < 3 ? 0u : now32() + 0x40000000u;
    lds_wait();
    wg_barrier();
    SB();
#pragma unroll
    for (int u = 0; u < NPRE * 8; ++u) {
      pre[u] = LDROW(rk, 0, u); SB();
      if (u >= WIN) VM_WAIT(WIN);
    }
#pragma unroll
    for (int u = 0; u < 8; ++u) {
      buf[u] = LDROW(rk, NPRE, u); SB();
      if (u < 7) VM_WAIT(WIN);
    }
    wg_barrier();
    const u32x4 qp = *(const LAS u32x4*)(lds + L_Q + c * 4);
    LAS float* S = (LAS float*)lds + L_S + sw * 1024;
    float mx = -INFINITY;
#define DOT(w) dot2((w)[3], qp[3], dot2((w)[2], qp[2], dot2((w)[1], qp[1], dot2((w)[0], qp[0], 0.f))))
#define K_FIN(grp) do { \
      _Pragma("unroll") for (int u = 0; u < 4; ++u) { const float lo = x[u] + dpp<ROR8>(x[u]), hi = x[u + 4] + dpp<ROR8>(x[u + 4]); x[u] = (c & 8) ? hi : lo; } \
      _Pragma("unroll") for (int u = 0; u < 2; ++u) { const float lo = x[u] + dpp<XOR7>(x[u]), hi = x[u + 2] + dpp<XOR7>(x[u + 2]); x[u] = (c & 4) ? hi : lo; } \
      { const float lo = x[0] + dpp<XOR2>(x[0]), hi = x[1] + dpp<XOR2>(x[1]); x[0] = (c & 2) ? hi : lo; } \
      float sc = x[0] + dpp<XOR1>(x[0]); \
      sc = rbf(rbf(sc) * SCALE); \
      mx = fmaxf(mx, sc); \
      S[(grp) * 32 + r * 8 + (c >> 1)] = sc; \
      SB(); \
    } while (0)
    static_assert(WIN >= 3 && WIN <= 5, "ring of 8 rows");
    {
      float x[8];
#pragma unroll
      for (int u = 0; u < 8; ++u) {
        if (u > 0 && u <= WIN + 1) { buf[u - 1] = LDROW(rk, NPRE + 1, u - 1); SB(); }
        x[u] = DOT(buf[u]);
        SB();
      }
      K_FIN(NPRE);
    }
#pragma unroll
    for (int pp = 0; pp < NPRE; ++pp) {
      float x[8];
#pragma unroll
      for (int w = 0; w < 8; ++w) x[w] = DOT(pre[pp * 8 + w]);
      K_FIN(pp);
      if (WIN + 1 + pp < 7) { VM_WAIT(WIN); buf[WIN + 1 + pp] = LDROW(rk, NPRE + 1, WIN + 1 + pp); SB(); }
    }
    static_assert(WIN + 1 + NPRE >= 7, "");
    int last = 31;
    asm volatile("" : "+s"(last));
#pragma nounroll
    for (int tt = NPRE + 1; tt <= last; ++tt) {
      const rsrc_t rn = mk_rsrc(tt == last ? vp : kp, HB);
      float x[8];
#pragma unroll
      for (int u = 0; u < 8; ++u) {
        const int n = u + 7;
        const int so = ROWSO(tt, n);
        asm volatile("s_waitcnt vmcnt(%0)" :: "i"(WIN), "s"(so));
        SB();
        buf[n & 7] = ldnt(n >= 8 ? rn : rk, ROWVO(n), so);
        SB();
        x[u] = DOT(buf[u]);
        SB();
      }
      K_FIN(tt);
    }
    mx = wave_max(mx);
    if (lane == 0) lds[L_MX + sw] = mx;
    lds_wait();
    if (lane == 0) __hip_atomic_fetch_add((LAS unsigned*)lds + L_CNT, 1u, RLX_WG);
    f32x2 acc[4] = {{0.f, 0.f}, {0.f, 0.f}, {0.f, 0.f}, {0.f, 0.f}};
    f32x4 s0 = *(const LAS f32x4*)(S + r * 8), s1;
    int open = -1, lag = -1;
#pragma nounroll
    for (int tt = 0; tt <= last; ++tt) {
      const rsrc_t rn = mk_rsrc(vp, tt == last ? 0 : HB);
      unsigned ref, tnow;
#pragma unroll
      for (int u = 0; u < 8; ++u) {
        const int n = u + 7;
        const int so = ROWSO(tt, n);
        asm volatile("s_cmp_gt_i32 %0, 0\n\ts_cbranch_scc1 1f\n\ts_waitcnt vmcnt(%2)\n\ts_cmp_gt_i32 %1, 0\n\ts_cbranch_scc1 1f\n\ts_waitcnt vmcnt(%3)\n1:"
                     :: "s"(open), "s"(lag), "i"(WIN + 2), "i"(WIN), "s"(so) : "scc");
        SB();
        buf[n & 7] = ldnt(n >= 8 ? rn : rv, ROWVO(n), so);
        SB();
        if (u == 0) s1 = *(const LAS f32x4*)(S + tt * 32 + r * 8 + 4);
        if (u == 4) { s0 = *(const LAS f32x4*)(S + ((tt + 1) & 31) * 32 + r * 8); ref = *(volatile LAS unsigned*)(lds + L_REF); tnow = now32(); }
        const float p = __builtin_amdgcn_exp2f(((u < 4 ? s0[u & 3] : s1[u & 3]) - mx) * LOG2E);
#pragma unroll
        for (int k = 0; k < 4; ++k) acc[k] = __builtin_elementwise_fma(bf_unpack(buf[u][k]), f32x2{p, p}, acc[k]);
        asm volatile("" : "+v"(acc[0]), "+v"(acc[1]), "+v"(acc[2]), "+v"(acc[3]));
        if (u == 6) {
          asm volatile("" : "+v"(ref), "+s"(tnow));
          const int el = (int)(tnow - __builtin_amdgcn_readfirstlane(ref));
          lag = el - (LAG_A + tt * LAG_B);
          open = max(el - WIDE_T, lag - LAG_2);
        }
        SB();
      }
    }
    *(LAS f32x4*)(lds + L_O + (sw * 4 + r) * 128 + c * 8) = f32x4{acc[0][0], acc[0][1], acc[1][0], acc[1][1]};
    *(LAS f32x4*)(lds + L_O + (sw * 4 + r) * 128 + c * 8 + 4) = f32x4{acc[2][0], acc[2][1], acc[3][0], acc[3][1]};
    lds_wait();
    wg_barrier();
    wg_barrier();
    if (blockIdx.x < NRED) wg_barrier();
    return;
  }

  asm volatile("s_waitcnt lgkmcnt(0)" : "+s"(warm));
  const unsigned epoch = __builtin_amdgcn_raw_buffer_load_b32(mk_rsrc(a.ws + WS_EPOCH, wave == 0 ? 4 : 0), 0, 0, 16);
  SB();
  float mb0, mb1, sd0, sd1;
  {
    float f0[8], f1[8];
#pragma unroll
    for (int k = 0; k < 4; ++k) { f0[2 * k] = bf_lo(xv0[k]); f0[2 * k + 1] = bf_hi(xv0[k]); f1[2 * k] = bf_lo(xv1[k]); f1[2 * k + 1] = bf_hi(xv1[k]); }
    float s0 = 0.f, s1 = 0.f;
#pragma unroll
    for (int k = 0; k < 8; ++k) { s0 += f0[k]; s1 += f1[k]; }
    const float m0 = wave_sum(s0) * (1.f / DM), m1 = wave_sum(s1) * (1.f / DM);
    float v0 = 0.f, v1 = 0.f;
#pragma unroll
    for (int k = 0; k < 8; ++k) { const float d0 = f0[k] - m0, d1 = f1[k] - m1; v0 = fmaf(d0, d0, v0); v1 = fmaf(d1, d1, v1); }
    v0 = wave_sum(v0) * (1.f / DM); v1 = wave_sum(v1) * (1.f / DM);
    mb0 = rbf(m0); sd0 = rbf(__builtin_sqrtf(rbf(rbf(v0) + 1e-5f)));
    mb1 = rbf(m1); sd1 = rbf(__builtin_sqrtf(rbf(rbf(v1) + 1e-5f)));
  }
#define XN(xf, mb, sd) rbf(rbf((xf) - (mb)) / (sd))
  const float xa0 = XN(bf_lo(xq0), mb0, sd0), xa1 = XN(bf_hi(xq0), mb0, sd0), xb0 = XN(bf_lo(xq1), mb1, sd1), xb1 = XN(bf_hi(xq1), mb1, sd1);
#define PARTIAL(w0_, w1_, n, res) do { \
    float t[8]; \
    _Pragma("unroll") for (int k = 0; k < 4; ++k) { \
      const f32x2 w0 = bf_unpack((w0_)[k]), w1 = bf_unpack((w1_)[k]); \
      const f32x2 qa = __builtin_elementwise_fma(w1, f32x2{xa1, xa1}, w0 * xa0), qb = __builtin_elementwise_fma(w1, f32x2{xb1, xb1}, w0 * xb0); \
      t[2 * k] = tr32(qa[0], qb[0]); t[2 * k + 1] = tr32(qa[1], qb[1]);             \
    } \
    _Pragma("unroll") for (int k = 0; k < 4; ++k) t[k] = tr16(t[k], t[4 + k]);        \
    f32x2 o; \
    _Pragma("unroll") for (int k = 0; k < 2; ++k) { const float lo = t[k] + dpp<ROR8>(t[k]), hi = t[2 + k] + dpp<ROR8>(t[2 + k]); o[k] = (c & 8) ? hi : lo; } \
    const float lo = o[0] + dpp<XOR7>(dpp<XOR3>(o[0])), hi = o[1] + dpp<XOR7>(dpp<XOR3>(o[1]));     \
    lds[L_P + (n) * 512 + wave * 64 + (r >> 1) * 32 + (c & 3) * 8 + (r & 1) * 4 + (c >> 3) * 2 + ((c >> 2) & 1)] = (c & 4) ? hi : lo; \
    if ((n) == 0 && tid == 0) ((LAS unsigned*)lds)[L_EP] = epoch; \
    lds_wait(); \
    if (lane == 0) __hip_atomic_fetch_add((LAS unsigned*)lds + L_CNT + 1 + (n), 1u, RLX_WG); \
    while (__hip_atomic_load((LAS unsigned*)lds + L_CNT + 1 + (n), RLX_WG) < 8u) __builtin_amdgcn_s_sleep(0); \
    asm volatile("" ::: "memory"); \
    const LAS float* P = (const LAS float*)lds + L_P + (n) * 512 + lane; \
    res = ((P[0] + P[64]) + (P[128] + P[192])) + ((P[256] + P[320]) + (P[384] + P[448])); \
  } while (0)
  float qs;
  PARTIAL(wq0, wq1, 0, qs);
  const unsigned tag = __builtin_amdgcn_readfirstlane(((const LAS unsigned*)lds)[L_EP]) + 0x9E3779B9u;
  if (wave == 0) st_granule((gu64*)(a.ws + WS_Q) + (h * 8 + g8) * 64 + lane, tag, qs);
  if (wave == 7) {
    __builtin_amdgcn_s_sleep(32);
    const rsrc_t rg = mk_rsrc(a.ws + WS_Q + (size_t)h * 4096, 4096);
    const unsigned t0 = now32();
    u32x4 g0, g1;
    while (true) {
      g0 = lddev(rg, r * 512 + c * 16, b * 256); g1 = lddev(rg, r * 512 + c * 16, 2048 + b * 256);
      if (__all((g0[1] == tag) & (g0[3] == tag) & (g1[1] == tag) & (g1[3] == tag))) break;
      if (now32() - t0 > SPIN_TICKS) break;
      __builtin_amdgcn_s_sleep(8);
      asm volatile("" ::: "memory");
    }
    ((LAS unsigned*)lds)[L_Q + lane] = pack_bf16(__uint_as_float(g0[0]) + __uint_as_float(g1[0]), __uint_as_float(g0[2]) + __uint_as_float(g1[2]));
    lds_wait();
  }
  wg_barrier();
  const int red = blockIdx.x < NRED ? (xg * 2 + (j & 1)) * 2 + (j >> 1) : -1;
  const unsigned xw = (red >= 0 && tid < 16) ? *(const gu32*)(a.x + (red & 1) * DM + (red >> 1) * 32 + tid * 2) : 0u;
  asm volatile("" : "+v"(w20), "+v"(w21) : "v"(pf0), "v"(pf1));
  if (wave == 6) {
    typedef const __attribute__((address_space(4))) unsigned* cptr;
    const size_t hb = (size_t)(chunk >> 2) * SK * DH;
    unsigned acc = 0;
#pragma unroll
    for (int i = 0; i < 32; ++i) acc ^= *(cptr)(a.v + hb + (size_t)i * (SK * DH / 32)) ^ *(cptr)(a.k + hb + (size_t)i * (SK * DH / 32));
    asm volatile("" :: "s"(acc));
  }
  float gsum = 0.f;
  if (h < 28) PARTIAL(wm0, wm1, 1, gsum);
  f32x2 wf[64];
  {
    const rsrc_t ro = mk_rsrc(a.wo + (size_t)h * DH * DM, DH * DM * 2);
#pragma unroll
    for (int i = 0; i < 16; ++i) {
      const u32x4 w = lddef(ro, voff + (i & 3) * 1024, (wave * 16 + (i & ~3)) * 1024);
      SB();
#pragma unroll
      for (int k = 0; k < 4; ++k) { wf[i * 4 + k] = bf_unpack(w[k]); asm volatile("" : "+v"(wf[i * 4 + k])); }
      SB();
    }
  }
  const int lane0 = lane;
  while (__hip_atomic_load((LAS unsigned*)lds + L_CNT, RLX_WG) < 4u) __builtin_amdgcn_s_sleep(8);
  asm volatile("" ::: "memory");
  gu64* ml = (gu64*)(a.ws + WS_ML);
  gu64* G = (gu64*)(a.ws + WS_G);
  float mw = 0.f;
  const unsigned tk = now32();
  if (wave == 7) {
    FRESH();
    mw = lds[L_MX + (lane & 3)];
    float m = fmaxf(mw, dpp<XOR1>(mw)); m = fmaxf(m, dpp<XOR2>(m));
    float l = 0.f;
#pragma unroll 2
    for (int i = 0; i < 16; ++i) {
      const f32x4 sv = *(const LAS f32x4*)(lds + L_S + i * 256 + lane * 4);
#pragma unroll
      for (int k = 0; k < 4; ++k) l += __builtin_amdgcn_exp2f((sv[k] - m) * LOG2E);
    }
    l = wave_sum(l);
    if (lane < 16) st_granule(ml + chunk * 16 + lane, tag, lane == 0 ? m : lane == 1 ? l : __uint_as_float(tk));
  } else if (wave == 0 && h < 28) {
    FRESH();
    __builtin_amdgcn_s_sleep(16);
    st_granule(G + (h * 8 + g8) * 64 + lane, tag, gsum);
  }
  __builtin_amdgcn_s_sleep(127); __builtin_amdgcn_s_sleep(16);
  f32x2 yacc[4] = {{0.f, 0.f}, {0.f, 0.f}, {0.f, 0.f}, {0.f, 0.f}};
  if (wave < 7) {
    FRESH();
    const int f0 = (h * 4 + sp) * 14 + wave * 2;
    const int f = f0 + (r & 1);
    gu64* gp = G + ((f >> 6) * 8 + (c & 1) * 4 + (c >> 3) * 2 + ((f >> 5) & 1)) * 64 + b * 32 + (f & 31);
    const unsigned t0 = now32();
    unsigned long long g;
    while (true) {
      g = __hip_atomic_load(gp, RLX_AGENT);
      if (__all((unsigned)(g >> 32) == tag)) break;
      if (now32() - t0 > SPIN_TICKS) break;
      __builtin_amdgcn_s_sleep(127);
    }
    float s = __uint_as_float((unsigned)g);
    s += dpp<XOR1>(s);
    const float gs = rbf(s), us = rbf(dpp<ROR8>(s));
    const float hv = rbf(rbf(gs / (1.f + __expf(-gs))) * us);
    const float h0 = lane_value(hv, 0), h1 = lane_value(hv, 16);
#pragma unroll
    for (int k = 0; k < 4; ++k) yacc[k] = __builtin_elementwise_fma(bf_unpack(w21[k]), f32x2{h1, h1}, bf_unpack(w20[k]) * h0);
  } else {
    FRESH();
    {
      const unsigned long long gt = __hip_atomic_load(ml + (lane * 4 + (lane & 3)) * 16 + 2, RLX_AGENT);
      const bool v = (unsigned)(gt >> 32) == tag;
      const float n = wave_sum(v ? 1.f : 0.f), d = wave_sum(v ? (float)(int)((unsigned)gt - tk) : 0.f);
      if (lane == 0 && n > 0.f) ((LAS unsigned*)lds)[L_REF] = tk + (unsigned)(int)(d / n);
    }
    const unsigned t0 = now32();
    unsigned long long gm, gl;
    while (true) {
      gm = __hip_atomic_load(ml + ((chunk & ~3) + (lane & 3)) * 16, RLX_AGENT);
      gl = __hip_atomic_load(ml + ((chunk & ~3) + (lane & 3)) * 16 + 1, RLX_AGENT);
      if (__all((unsigned)(gm >> 32) == tag && (unsigned)(gl >> 32) == tag)) break;
      if (now32() - t0 > SPIN_TICKS) break;
      __builtin_amdgcn_s_sleep(127);
    }
    const float ms = __uint_as_float((unsigned)gm), ls = __uint_as_float((unsigned)gl);
    float M = fmaxf(ms, dpp<XOR1>(ms)); M = fmaxf(M, dpp<XOR2>(M));
    float L = ls * __builtin_amdgcn_exp2f((ms - M) * LOG2E);
    L += dpp<XOR1>(L); L += dpp<XOR2>(L);
    if (lane < 4) lds[L_FW + lane] = __builtin_amdgcn_exp2f((mw - M) * LOG2E) / L;
    lds_wait();
    {
      const unsigned char* pa = (lane < 32 || red < 0) ? a.ws + WS_Y + chunk * 2048 + (lane & 15) * 128
                                                       : (const unsigned char*)a.out + (red & 1) * (DM * 2) + (red >> 1) * 64;
      const unsigned jk = __hip_atomic_load((gu32*)pa, RLX_AGENT);
      asm volatile("" :: "v"(jk));
    }
    if (blockIdx.x == NWG - 1) {
      while (true) {
        bool ok = true;
#pragma unroll
        for (int i = 0; i < 4; ++i) ok = ok & ((unsigned)(__hip_atomic_load(ml + (i * 64 + lane) * 16 + 2, RLX_AGENT) >> 32) == tag);
        if (__all(ok)) break;
        if (now32() - t0 > SPIN_TICKS) break;
        __builtin_amdgcn_s_sleep(127);
      }
      if (lane == 0) __hip_atomic_store((gu32*)(a.ws + WS_EPOCH), tag, RLX_AGENT);
    }
  }
  {
    FRESH();
    wg_barrier();
    {
      const LAS float* P = (const LAS float*)lds + L_O + (4 * r) * 128 + wave * 16 + c;
      const float o = xrow_sum(((P[0] + P[128]) + (P[256] + P[384])) * lds[L_FW + r]);
#pragma unroll
      for (int i = 0; i < 16; ++i) {
        const float oi = lane_value(o, i);
#pragma unroll
        for (int k = 0; k < 4; ++k) yacc[k] = __builtin_elementwise_fma(wf[i * 4 + k], f32x2{oi, oi}, yacc[k]);
      }
    }
    *(LAS f32x4*)(lds + L_Y + wave * 512 + lane * 8) = f32x4{yacc[0][0], yacc[0][1], yacc[1][0], yacc[1][1]};
    *(LAS f32x4*)(lds + L_Y + wave * 512 + lane * 8 + 4) = f32x4{yacc[2][0], yacc[2][1], yacc[3][0], yacc[3][1]};
    lds_wait();
    wg_barrier();
    const rsrc_t ry = mk_rsrc(a.ws + WS_Y, NWG * 2048);
    {
      const LAS float* P = (const LAS float*)lds + L_Y + tid;
      const float y = ((P[0] + P[512]) + (P[1024] + P[1536])) + ((P[2048] + P[2560]) + (P[3072] + P[3584]));
      const unsigned u = __float_as_uint(y);
      const unsigned w = (((u & 0x7f800000u) == 0x7f800000u ? u | ((u & 0x7fffffu) ? 0x400000u : 0u) : u + 0x400u) & 0xfffff800u) | ((tag >> ((lane & 1) * 11)) & 0x7ffu);
      const unsigned w1 = __builtin_amdgcn_mov_dpp(w, XOR1, 0xf, 0xf, true);
      if (!(lane & 1)) __builtin_amdgcn_raw_buffer_store_b64(u32x2{w, w1}, ry, tid * 4, chunk * 2048, 16);
    }
    if (red >= 0) {
      const unsigned t0 = now32();
      u32x4 g0 = {0u, 0u, 0u, 0u}, g1 = {0u, 0u, 0u, 0u};
      bool ok0 = false, ok1 = false;
      const unsigned ta = tag & 0x7ffu, tb = (tag >> 11) & 0x7ffu;
      while (true) {
        if (!ok0) g0 = lddev(ry, (lane >> 3) * 2048 + (lane & 7) * 16, ((red & 1) * 128 + wave * 16) * 2048 + (red >> 1) * 128);
        if (!ok1) g1 = lddev(ry, (lane >> 3) * 2048 + (lane & 7) * 16, ((red & 1) * 128 + wave * 16 + 8) * 2048 + (red >> 1) * 128);
        ok0 = (((g0[0] ^ ta) | (g0[1] ^ tb) | (g0[2] ^ ta) | (g0[3] ^ tb)) & 0x7ffu) == 0u;
        ok1 = (((g1[0] ^ ta) | (g1[1] ^ tb) | (g1[2] ^ ta) | (g1[3] ^ tb)) & 0x7ffu) == 0u;
        if (__all(ok0 & ok1)) break;
        if (now32() - t0 > SPIN_TICKS) break;
        __builtin_amdgcn_s_sleep(4);
        asm volatile("" ::: "memory");
      }
      float t[4];
#pragma unroll
      for (int k = 0; k < 4; ++k) t[k] = __uint_as_float(g0[k] & 0xfffff800u) + __uint_as_float(g1[k] & 0xfffff800u);
      float s = tr16(tr32(t[0], t[2]), tr32(t[1], t[3]));
      s += dpp<ROR8>(s);
      if (!(lane & 8)) lds[L_FIN + wave * 32 + (lane & 7) * 4 + r] = s;
      lds_wait();
      wg_barrier();
      if (tid < 16) {
        const LAS f32x2* P = (const LAS f32x2*)(lds + L_FIN + tid * 2);
        const f32x2 o = bf_unpack(xw) + (((P[0] + P[16]) + (P[32] + P[48])) + ((P[64] + P[80]) + (P[96] + P[112])));
        *(gu32*)(a.out + (red & 1) * DM + (red >> 1) * 32 + tid * 2) = pack_bf16(o[0], o[1]);
      }
    }
  }
}

}

extern "C" void kernel_launch(void* const* d_in, const int* in_sizes, int n_in, void* d_out, int out_size, void* d_ws, size_t ws_size,
                              hipStream_t stream) {
  Args a;
  a.x = (const unsigned short*)d_in[0]; a.k = (const unsigned short*)d_in[1]; a.v = (const unsigned short*)d_in[2];
  a.wq = (const unsigned short*)d_in[3]; a.wo = (const unsigned short*)d_in[4];
  a.w1 = (const unsigned short*)d_in[6]; a.w3 = (const unsigned short*)d_in[7]; a.w2 = (const unsigned short*)d_in[8];
  a.out = (unsigned short*)d_out; a.ws = (unsigned char*)d_ws;
  fused_decode<<<NWG, NTHR, 0, stream>>>(a);
}
```

```cpp
#include <hip/hip_runtime.h>
#include <hip/hip_bf16.h>
#include <stdint.h>

namespace {

constexpr int NWG = 256, NTHR = 768, NRED = 32;
constexpr int DM = 512, NH = 32, SK = 16384, DH = 128, FF = 1792;
constexpr float LOG2E = 1.4426950408889634f;
constexpr float SCALE = 0.08838834764831845f;
constexpr int WIN = 4;
constexpr int NPRE = 2;
constexpr int LAG_A = 95, LAG_B = 108, LAG_2 = 80, WIDE_T = 3250;
constexpr size_t WS_EPOCH = 0;
constexpr size_t WS_ML = 8192;
constexpr size_t WS_Y = 65536;
constexpr size_t WS_Q = WS_Y + NWG * 2048;
constexpr size_t WS_G = WS_Q + (128u << 10);
constexpr int L_S = 0;
constexpr int L_O = L_S + 4096;
constexpr int L_Y = L_O + 2048;
constexpr int L_P = L_Y + 4096;
constexpr int L_Q = L_P + 1024;
constexpr int L_MX = L_Q + 64;
constexpr int L_FW = L_MX + 4;
constexpr int L_FIN = L_FW + 4;
constexpr int L_CNT = L_FIN + 256;
constexpr int L_REF = L_CNT + 3;
constexpr int L_EP = L_REF + 1;
constexpr int L_END = L_EP + 1;

#define GAS __attribute__((address_space(1)))
#define LAS __attribute__((address_space(3)))
#define RLX_AGENT __ATOMIC_RELAXED, __HIP_MEMORY_SCOPE_AGENT
#define RLX_WG __ATOMIC_RELAXED, __HIP_MEMORY_SCOPE_WORKGROUP
typedef unsigned u32x2 __attribute__((ext_vector_type(2)));
typedef unsigned u32x4 __attribute__((ext_vector_type(4)));
typedef float f32x2 __attribute__((ext_vector_type(2)));
typedef float f32x4 __attribute__((ext_vector_type(4)));
typedef __bf16 bf16x2 __attribute__((ext_vector_type(2)));
typedef GAS unsigned gu32;
typedef GAS unsigned long long gu64;
typedef __amdgpu_buffer_rsrc_t rsrc_t;

__device__ __forceinline__ float bf_lo(unsigned u) { return __builtin_bit_cast(float, u << 16); }
__device__ __forceinline__ float bf_hi(unsigned u) { return __builtin_bit_cast(float, u & 0xffff0000u); }
__device__ __forceinline__ f32x2 bf_unpack(unsigned u) { return f32x2{bf_lo(u), bf_hi(u)}; }
__device__ __forceinline__ unsigned pack_bf16(float lo, float hi) {
  bf16x2 r = {(__bf16)lo, (__bf16)hi};
  return __builtin_bit_cast(unsigned, r);
}
__device__ __forceinline__ float rbf(float x) { return (float)(__bf16)x; }
__device__ __forceinline__ float dot2(unsigned a, unsigned b, float acc) {
  return __builtin_amdgcn_fdot2_f32_bf16(__builtin_bit_cast(bf16x2, a), __builtin_bit_cast(bf16x2, b), acc, false);
}
template <int CTRL> __device__ __forceinline__ float dpp(float x) {
  return __builtin_bit_cast(float, __builtin_amdgcn_mov_dpp(__builtin_bit_cast(int, x), CTRL, 0xf, 0xf, true));
}
constexpr int XOR1 = 0xB1, XOR2 = 0x4E, XOR3 = 0x1B, XOR7 = 0x141, ROR8 = 0x128;
__device__ __forceinline__ float tr16(float a, float b) {
  auto s = __builtin_amdgcn_permlane16_swap(__float_as_uint(a), __float_as_uint(b), false, false);
  return __uint_as_float(s[0]) + __uint_as_float(s[1]);
}
__device__ __forceinline__ float tr32(float a, float b) {
  auto s = __builtin_amdgcn_permlane32_swap(__float_as_uint(a), __float_as_uint(b), false, false);
  return __uint_as_float(s[0]) + __uint_as_float(s[1]);
}
__device__ __forceinline__ float xrow_sum(float x) { x = tr16(x, x); return tr32(x, x); }
__device__ __forceinline__ float wave_sum(float x) {
  x += dpp<XOR1>(x); x += dpp<XOR2>(x); x += dpp<XOR7>(x); x += dpp<ROR8>(x);
  return xrow_sum(x);
}
__device__ __forceinline__ float wave_max(float x) {
  x = fmaxf(x, dpp<XOR1>(x)); x = fmaxf(x, dpp<XOR2>(x)); x = fmaxf(x, dpp<XOR7>(x)); x = fmaxf(x, dpp<ROR8>(x));
  auto s = __builtin_amdgcn_permlane16_swap(__float_as_uint(x), __float_as_uint(x), false, false);
  x = fmaxf(__uint_as_float(s[0]), __uint_as_float(s[1]));
  auto t = __builtin_amdgcn_permlane32_swap(__float_as_uint(x), __float_as_uint(x), false, false);
  return fmaxf(__uint_as_float(t[0]), __uint_as_float(t[1]));
}
__device__ __forceinline__ float lane_value(float x, int l) { return __builtin_bit_cast(float, __builtin_amdgcn_readlane(__builtin_bit_cast(int, x), l)); }
__device__ __forceinline__ void lds_wait() { asm volatile("s_waitcnt lgkmcnt(0)" ::: "memory"); }
__device__ __forceinline__ void wg_barrier() { __builtin_amdgcn_s_barrier(); asm volatile("" ::: "memory"); }
__device__ __forceinline__ void st_granule(gu64* g, unsigned tag, float v) {
  __hip_atomic_store(g, ((unsigned long long)tag << 32) | (unsigned long long)__float_as_uint(v), RLX_AGENT);
}
__device__ __forceinline__ rsrc_t mk_rsrc(const void* p, int bytes) { return __builtin_amdgcn_make_buffer_rsrc((void*)p, 0, bytes, 0x00020000); }
__device__ __forceinline__ u32x4 ldnt(rsrc_t r, int voff, int soff) { return __builtin_amdgcn_raw_buffer_load_b128(r, voff, soff, 2); }
__device__ __forceinline__ u32x4 lddev(rsrc_t r, int voff, int soff) { return __builtin_amdgcn_raw_buffer_load_b128(r, voff, soff, 16); }
__device__ __forceinline__ u32x4 lddef(rsrc_t r, int voff, int soff) { return __builtin_amdgcn_raw_buffer_load_b128(r, voff, soff, 0); }
__device__ __forceinline__ unsigned now32() { return (unsigned)__builtin_amdgcn_s_memrealtime(); }
constexpr unsigned SPIN_TICKS = 20000000u;

#define SB() __builtin_amdgcn_sched_barrier(0)
#define VM_WAIT(n) do { asm volatile("s_waitcnt vmcnt(%0)" :: "i"(n)); SB(); } while (0)
#define FRESH() int lane_ = lane0; asm volatile("" : "+v"(lane_)); \
  const int lane = lane_, r = lane >> 4, c = lane & 15, tid = wave * 64 + lane, voff = lane * 16; (void)r; (void)c; (void)tid; (void)voff

struct Args {
  const unsigned short* x; const unsigned short* k; const unsigned short* v; const unsigned short* wq; const unsigned short* wo;
  const unsigned short* w1; const unsigned short* w3; const unsigned short* w2;
  unsigned short* out; unsigned char* ws;
};

__global__ void __launch_bounds__(NTHR, 3) fused_decode(Args a) {
  __shared__ __attribute__((aligned(16))) float lds[L_END];
  const int tid = threadIdx.x, lane = tid & 63, wave = __builtin_amdgcn_readfirstlane(tid >> 6);
  const int r = lane >> 4, c = lane & 15;
  const int xg = blockIdx.x & 7, j = blockIdx.x >> 3;
  const int b = j >> 4, h = xg + ((j >> 2) & 3) * 8, sp = j & 3;
  const int chunk = b * 128 + h * 4 + sp;
  const int voff = lane * 16;
  asm volatile("" :: "s"(a.x), "s"(a.k), "s"(a.v), "s"(a.wq), "s"(a.ws));

  const int g8 = b * 4 + sp;
  const int prow = (g8 >> 2) * 256 + wave * 32 + 2 * (lane >> 2);
  u32x4 xv0, xv1, wq0, wq1, wm0, wm1, pf0, pf1, w20, w21;
  unsigned xq0, xq1, warm;
  if (wave < 8) {
    asm volatile("s_load_dword %0, %1, 0x400" : "=s"(warm) : "s"(a.ws));
    xv0 = *(const GAS u32x4*)(a.x + lane * 8); xv1 = *(const GAS u32x4*)(a.x + DM + lane * 8);
    xq0 = *(const gu32*)(a.x + prow); xq1 = *(const gu32*)(a.x + DM + prow);
    const rsrc_t rq = mk_rsrc(a.wq, DM * NH * DH * 2);
    wq0 = ldnt(rq, prow * (NH * DH * 2) + (lane & 3) * 16, h * (DH * 2) + (g8 & 3) * 64);
    wq1 = ldnt(rq, prow * (NH * DH * 2) + (lane & 3) * 16, h * (DH * 2) + (g8 & 3) * 64 + NH * DH * 2);
    SB();
    const rsrc_t rm = mk_rsrc((g8 & 2) ? a.w3 : a.w1, h < 28 ? DM * FF * 2 : 0);
    wm0 = ldnt(rm, prow * (FF * 2) + (lane & 3) * 16, h * 128 + (g8 & 1) * 64);
    wm1 = ldnt(rm, prow * (FF * 2) + (lane & 3) * 16, h * 128 + (g8 & 1) * 64 + FF * 2);
    const rsrc_t ro = mk_rsrc(a.wo + (size_t)h * DH * DM, DH * DM * 2);
    pf0 = lddef(ro, voff, (g8 * 16 + wave * 2) * 1024); pf1 = lddef(ro, voff + 1024, (g8 * 16 + wave * 2) * 1024);
    const rsrc_t r2 = mk_rsrc(a.w2 + (size_t)((h * 4 + sp) * 14 + wave * 2) * DM, wave < 7 ? 2 * DM * 2 : 0);
    w20 = ldnt(r2, voff, 0); w21 = ldnt(r2, voff + 1024, 0);
    SB();
    wg_barrier();
  }
  int role = wave;
  asm volatile("" : "+s"(role));
  if (__builtin_expect(role >= 8, 1)) {
    const int sw = wave - 8;
    constexpr int HB = SK * DH * 2;
    const unsigned short* kp = a.k + (size_t)(chunk >> 2) * SK * DH;
    const unsigned short* vp = a.v + (size_t)(chunk >> 2) * SK * DH;
    const rsrc_t rk = mk_rsrc(kp, HB), rv = mk_rsrc(vp, HB);
    const int voffh = voff + HB / 2;
    auto window = [&](int g) -> int { g += (j >> 3) * 2; return ((g & 1) << 19) + ((g & 14) << 15) + ((g & 16) << 16) + (sp << 14) + (sw << 12); };
#define ROWVO(n) ((((n) & 1) ? voffh : voff) + (((n) & 7) >> 1) * 1024)
#define ROWSO(grp, n) window((grp) + ((n) >> 3))
#define LDROW(rs, grp, n) ldnt(rs, ROWVO(n), ROWSO(grp, n))
    __builtin_amdgcn_s_setprio(2);
    u32x4 buf[8], pre[NPRE * 8];
    if (lane < 4) ((LAS unsigned*)lds)[L_CNT + lane] = lane < 3 ? 0u : now32() + 0x40000000u;
    lds_wait();
    wg_barrier();
    SB();
#pragma unroll
    for (int u = 0; u < NPRE * 8; ++u) {
      pre[u] = LDROW(rk, 0, u); SB();
      if (u >= WIN) VM_WAIT(WIN);
    }
#pragma unroll
    for (int u = 0; u < 8; ++u) {
      buf[u] = LDROW(rk, NPRE, u); SB();
      if (u < 7) VM_WAIT(WIN);
    }
    wg_barrier();
    const u32x4 qp = *(const LAS u32x4*)(lds + L_Q + c * 4);
    LAS float* S = (LAS float*)lds + L_S + sw * 1024;
    float mx = -INFINITY;
#define DOT(w) dot2((w)[3], qp[3], dot2((w)[2], qp[2], dot2((w)[1], qp[1], dot2((w)[0], qp[0], 0.f))))
#define K_FIN(grp) do { \
      _Pragma("unroll") for (int u = 0; u < 4; ++u) { const float lo = x[u] + dpp<ROR8>(x[u]), hi = x[u + 4] + dpp<ROR8>(x[u + 4]); x[u] = (c & 8) ? hi : lo; } \
      _Pragma("unroll") for (int u = 0; u < 2; ++u) { const float lo = x[u] + dpp<XOR7>(x[u]), hi = x[u + 2] + dpp<XOR7>(x[u + 2]); x[u] = (c & 4) ? hi : lo; } \
      { const float lo = x[0] + dpp<XOR2>(x[0]), hi = x[1] + dpp<XOR2>(x[1]); x[0] = (c & 2) ? hi : lo; } \
      float sc = x[0] + dpp<XOR1>(x[0]); \
      sc = rbf(rbf(sc) * SCALE); \
      mx = fmaxf(mx, sc); \
      S[(grp) * 32 + r * 8 + (c >> 1)] = sc; \
      SB(); \
    } while (0)
    static_assert(WIN >= 3 && WIN <= 5, "ring of 8 rows");
    {
      float x[8];
#pragma unroll
      for (int u = 0; u < 8; ++u) {
        if (u > 0 && u <= WIN + 1) { buf[u - 1] = LDROW(rk, NPRE + 1, u - 1); SB(); }
        x[u] = DOT(buf[u]);
        SB();
      }
      K_FIN(NPRE);
    }
#pragma unroll
    for (int pp = 0; pp < NPRE; ++pp) {
      float x[8];
#pragma unroll
      for (int w = 0; w < 8; ++w) x[w] = DOT(pre[pp * 8 + w]);
      K_FIN(pp);
      if (WIN + 1 + pp < 7) { VM_WAIT(WIN); buf[WIN + 1 + pp] = LDROW(rk, NPRE + 1, WIN + 1 + pp); SB(); }
    }
    static_assert(WIN + 1 + NPRE >= 7, "");
    int last = 31;
    asm volatile("" : "+s"(last));
#pragma nounroll
    for (int tt = NPRE + 1; tt <= last; ++tt) {
      const rsrc_t rn = mk_rsrc(tt == last ? vp : kp, HB);
      float x[8];
#pragma unroll
      for (int u = 0; u < 8; ++u) {
        const int n = u + 7;
        const int so = ROWSO(tt, n);
        asm volatile("s_waitcnt vmcnt(%0)" :: "i"(WIN), "s"(so));
        SB();
        buf[n & 7] = ldnt(n >= 8 ? rn : rk, ROWVO(n), so);
        SB();
        x[u] = DOT(buf[u]);
        SB();
      }
      K_FIN(tt);
    }
    mx = wave_max(mx);
    if (lane == 0) lds[L_MX + sw] = mx;
    lds_wait();
    if (lane == 0) __hip_atomic_fetch_add((LAS unsigned*)lds + L_CNT, 1u, RLX_WG);
    f32x2 acc[4] = {{0.f, 0.f}, {0.f, 0.f}, {0.f, 0.f}, {0.f, 0.f}};
    f32x4 s0 = *(const LAS f32x4*)(S + r * 8), s1;
    int open = -1, lag = -1;
#pragma nounroll
    for (int tt = 0; tt <= last; ++tt) {
      const rsrc_t rn = mk_rsrc(vp, tt == last ? 0 : HB);
      unsigned ref, tnow;
#pragma unroll
      for (int u = 0; u < 8; ++u) {
        const int n = u + 7;
        const int so = ROWSO(tt, n);
        asm volatile("s_cmp_gt_i32 %0, 0\n\ts_cbranch_scc1 1f\n\ts_waitcnt vmcnt(%2)\n\ts_cmp_gt_i32 %1, 0\n\ts_cbranch_scc1 1f\n\ts_waitcnt vmcnt(%3)\n1:"
                     :: "s"(open), "s"(lag), "i"(WIN + 2), "i"(WIN), "s"(so) : "scc");
        SB();
        buf[n & 7] = ldnt(n >= 8 ? rn : rv, ROWVO(n), so);
        SB();
        if (u == 0) s1 = *(const LAS f32x4*)(S + tt * 32 + r * 8 + 4);
        if (u == 4) { s0 = *(const LAS f32x4*)(S + ((tt + 1) & 31) * 32 + r * 8); ref = *(volatile LAS unsigned*)(lds + L_REF); tnow = now32(); }
        const float p = __builtin_amdgcn_exp2f(((u < 4 ? s0[u & 3] : s1[u & 3]) - mx) * LOG2E);
#pragma unroll
        for (int k = 0; k < 4; ++k) acc[k] = __builtin_elementwise_fma(bf_unpack(buf[u][k]), f32x2{p, p}, acc[k]);
        asm volatile("" : "+v"(acc[0]), "+v"(acc[1]), "+v"(acc[2]), "+v"(acc[3]));
        if (u == 6) {
          asm volatile("" : "+v"(ref), "+s"(tnow));
          const int el = (int)(tnow - __builtin_amdgcn_readfirstlane(ref));
          lag = el - (LAG_A + tt * LAG_B);
          open = max(el - WIDE_T, lag - LAG_2);
        }
        SB();
      }
    }
    *(LAS f32x4*)(lds + L_O + (sw * 4 + r) * 128 + c * 8) = f32x4{acc[0][0], acc[0][1], acc[1][0], acc[1][1]};
    *(LAS f32x4*)(lds + L_O + (sw * 4 + r) * 128 + c * 8 + 4) = f32x4{acc[2][0], acc[2][1], acc[3][0], acc[3][1]};
    lds_wait();
    wg_barrier();
    wg_barrier();
    if (blockIdx.x < NRED) wg_barrier();
    return;
  }

  asm volatile("s_waitcnt lgkmcnt(0)" : "+s"(warm));
  const unsigned epoch = __builtin_amdgcn_raw_buffer_load_b32(mk_rsrc(a.ws + WS_EPOCH, wave == 0 ? 4 : 0), 0, 0, 16);
  SB();
  float mb0, mb1, sd0, sd1;
  {
    float f0[8], f1[8];
#pragma unroll
    for (int k = 0; k < 4; ++k) { f0[2 * k] = bf_lo(xv0[k]); f0[2 * k + 1] = bf_hi(xv0[k]); f1[2 * k] = bf_lo(xv1[k]); f1[2 * k + 1] = bf_hi(xv1[k]); }
    float s0 = 0.f, s1 = 0.f;
#pragma unroll
    for (int k = 0; k < 8; ++k) { s0 += f0[k]; s1 += f1[k]; }
    const float m0 = wave_sum(s0) * (1.f / DM), m1 = wave_sum(s1) * (1.f / DM);
    float v0 = 0.f, v1 = 0.f;
#pragma unroll
    for (int k = 0; k < 8; ++k) { const float d0 = f0[k] - m0, d1 = f1[k] - m1; v0 = fmaf(d0, d0, v0); v1 = fmaf(d1, d1, v1); }
    v0 = wave_sum(v0) * (1.f / DM); v1 = wave_sum(v1) * (1.f / DM);
    mb0 = rbf(m0); sd0 = rbf(__builtin_sqrtf(rbf(rbf(v0) + 1e-5f)));
    mb1 = rbf(m1); sd1 = rbf(__builtin_sqrtf(rbf(rbf(v1) + 1e-5f)));
  }
#define XN(xf, mb, sd) rbf(rbf((xf) - (mb)) / (sd))
  const float xa0 = XN(bf_lo(xq0), mb0, sd0), xa1 = XN(bf_hi(xq0), mb0, sd0), xb0 = XN(bf_lo(xq1), mb1, sd1), xb1 = XN(bf_hi(xq1), mb1, sd1);
#define PARTIAL(w0_, w1_, n, res) do { \
    float t[8]; \
    _Pragma("unroll") for (int k = 0; k < 4; ++k) { \
      const f32x2 w0 = bf_unpack((w0_)[k]), w1 = bf_unpack((w1_)[k]); \
      const f32x2 qa = __builtin_elementwise_fma(w1, f32x2{xa1, xa1}, w0 * xa0), qb = __builtin_elementwise_fma(w1, f32x2{xb1, xb1}, w0 * xb0); \
      t[2 * k] = tr32(qa[0], qb[0]); t[2 * k + 1] = tr32(qa[1], qb[1]);             \
    } \
    _Pragma("unroll") for (int k = 0; k < 4; ++k) t[k] = tr16(t[k], t[4 + k]);        \
    f32x2 o; \
    _Pragma("unroll") for (int k = 0; k < 2; ++k) { const float lo = t[k] + dpp<ROR8>(t[k]), hi = t[2 + k] + dpp<ROR8>(t[2 + k]); o[k] = (c & 8) ? hi : lo; } \
    const float lo = o[0] + dpp<XOR7>(dpp<XOR3>(o[0])), hi = o[1] + dpp<XOR7>(dpp<XOR3>(o[1]));     \
    lds[L_P + (n) * 512 + wave * 64 + (r >> 1) * 32 + (c & 3) * 8 + (r & 1) * 4 + (c >> 3) * 2 + ((c >> 2) & 1)] = (c & 4) ? hi : lo; \
    if ((n) == 0 && tid == 0) ((LAS unsigned*)lds)[L_EP] = epoch; \
    lds_wait(); \
    if (lane == 0) __hip_atomic_fetch_add((LAS unsigned*)lds + L_CNT + 1 + (n), 1u, RLX_WG); \
    while (__hip_atomic_load((LAS unsigned*)lds + L_CNT + 1 + (n), RLX_WG) < 8u) __builtin_amdgcn_s_sleep(0); \
    asm volatile("" ::: "memory"); \
    const LAS float* P = (const LAS float*)lds + L_P + (n) * 512 + lane; \
    res = ((P[0] + P[64]) + (P[128] + P[192])) + ((P[256] + P[320]) + (P[384] + P[448])); \
  } while (0)
  float qs;
  PARTIAL(wq0, wq1, 0, qs);
  const unsigned tag = __builtin_amdgcn_readfirstlane(((const LAS unsigned*)lds)[L_EP]) + 0x9E3779B9u;
  if (wave == 0) st_granule((gu64*)(a.ws + WS_Q) + (h * 8 + g8) * 64 + lane, tag, qs);
  if (wave == 7) {
    __builtin_amdgcn_s_sleep(32);
    const rsrc_t rg = mk_rsrc(a.ws + WS_Q + (size_t)h * 4096, 4096);
    const unsigned t0 = now32();
    u32x4 g0, g1;
    while (true) {
      g0 = lddev(rg, r * 512 + c * 16, b * 256); g1 = lddev(rg, r * 512 + c * 16, 2048 + b * 256);
      if (__all((g0[1] == tag) & (g0[3] == tag) & (g1[1] == tag) & (g1[3] == tag))) break;
      if (now32() - t0 > SPIN_TICKS) break;
      __builtin_amdgcn_s_sleep(8);
      asm volatile("" ::: "memory");
    }
    ((LAS unsigned*)lds)[L_Q + lane] = pack_bf16(__uint_as_float(g0[0]) + __uint_as_float(g1[0]), __uint_as_float(g0[2]) + __uint_as_float(g1[2]));
    lds_wait();
  }
  wg_barrier();
  const int red = blockIdx.x < NRED ? (xg * 2 + (j & 1)) * 2 + (j >> 1) : -1;
  const unsigned xw = (red >= 0 && tid < 16) ? *(const gu32*)(a.x + (red & 1) * DM + (red >> 1) * 32 + tid * 2) : 0u;
  asm volatile("" : "+v"(w20), "+v"(w21) : "v"(pf0), "v"(pf1));
  float gsum = 0.f;
  if (h < 28) PARTIAL(wm0, wm1, 1, gsum);
  f32x2 wf[64];
  {
    const rsrc_t ro = mk_rsrc(a.wo + (size_t)h * DH * DM, DH * DM * 2);
#pragma unroll
    for (int i = 0; i < 16; ++i) {
      const u32x4 w = lddef(ro, voff + (i & 3) * 1024, (wave * 16 + (i & ~3)) * 1024);
      SB();
#pragma unroll
      for (int k = 0; k < 4; ++k) { wf[i * 4 + k] = bf_unpack(w[k]); asm volatile("" : "+v"(wf[i * 4 + k])); }
      SB();
    }
  }
  const int lane0 = lane;
  while (__hip_atomic_load((LAS unsigned*)lds + L_CNT, RLX_WG) < 4u) __builtin_amdgcn_s_sleep(8);
  asm volatile("" ::: "memory");
  gu64* ml = (gu64*)(a.ws + WS_ML);
  gu64* G = (gu64*)(a.ws + WS_G);
  float mw = 0.f;
  const unsigned tk = now32();
  if (wave == 7) {
    FRESH();
    mw = lds[L_MX + (lane & 3)];
    float m = fmaxf(mw, dpp<XOR1>(mw)); m = fmaxf(m, dpp<XOR2>(m));
    float l = 0.f;
#pragma unroll 2
    for (int i = 0; i < 16; ++i) {
      const f32x4 sv = *(const LAS f32x4*)(lds + L_S + i * 256 + lane * 4);
#pragma unroll
      for (int k = 0; k < 4; ++k) l += __builtin_amdgcn_exp2f((sv[k] - m) * LOG2E);
    }
    l = wave_sum(l);
    if (lane < 16) st_granule(ml + chunk * 16 + lane, tag, lane == 0 ? m : lane == 1 ? l : __uint_as_float(tk));
  } else if (wave == 0 && h < 28) {
    FRESH();
    __builtin_amdgcn_s_sleep(16);
    st_granule(G + (h * 8 + g8) * 64 + lane, tag, gsum);
  }
  __builtin_amdgcn_s_sleep(127); __builtin_amdgcn_s_sleep(16);
  f32x2 yacc[4] = {{0.f, 0.f}, {0.f, 0.f}, {0.f, 0.f}, {0.f, 0.f}};
  if (wave < 7) {
    FRESH();
    const int f0 = (h * 4 + sp) * 14 + wave * 2;
    const int f = f0 + (r & 1);
    gu64* gp = G + ((f >> 6) * 8 + (c & 1) * 4 + (c >> 3) * 2 + ((f >> 5) & 1)) * 64 + b * 32 + (f & 31);
    const unsigned t0 = now32();
    unsigned long long g;
    while (true) {
      g = __hip_atomic_load(gp, RLX_AGENT);
      if (__all((unsigned)(g >> 32) == tag)) break;
      if (now32() - t0 > SPIN_TICKS) break;
      __builtin_amdgcn_s_sleep(127);
    }
    float s = __uint_as_float((unsigned)g);
    s += dpp<XOR1>(s);
    const float gs = rbf(s), us = rbf(dpp<ROR8>(s));
    const float hv = rbf(rbf(gs / (1.f + __expf(-gs))) * us);
    const float h0 = lane_value(hv, 0), h1 = lane_value(hv, 16);
#pragma unroll
    for (int k = 0; k < 4; ++k) yacc[k] = __builtin_elementwise_fma(bf_unpack(w21[k]), f32x2{h1, h1}, bf_unpack(w20[k]) * h0);
  } else {
    FRESH();
    {
      const unsigned long long gt = __hip_atomic_load(ml + (lane * 4 + (lane & 3)) * 16 + 2, RLX_AGENT);
      const bool v = (unsigned)(gt >> 32) == tag;
      const float n = wave_sum(v ? 1.f : 0.f), d = wave_sum(v ? (float)(int)((unsigned)gt - tk) : 0.f);
      if (lane == 0 && n > 0.f) ((LAS unsigned*)lds)[L_REF] = tk + (unsigned)(int)(d / n);
    }
    const unsigned t0 = now32();
    unsigned long long gm, gl;
    while (true) {
      gm = __hip_atomic_load(ml + ((chunk & ~3) + (lane & 3)) * 16, RLX_AGENT);
      gl = __hip_atomic_load(ml + ((chunk & ~3) + (lane & 3)) * 16 + 1, RLX_AGENT);
      if (__all((unsigned)(gm >> 32) == tag && (unsigned)(gl >> 32) == tag)) break;
      if (now32() - t0 > SPIN_TICKS) break;
      __builtin_amdgcn_s_sleep(127);
    }
    const float ms = __uint_as_float((unsigned)gm), ls = __uint_as_float((unsigned)gl);
    float M = fmaxf(ms, dpp<XOR1>(ms)); M = fmaxf(M, dpp<XOR2>(M));
    float L = ls * __builtin_amdgcn_exp2f((ms - M) * LOG2E);
    L += dpp<XOR1>(L); L += dpp<XOR2>(L);
    if (lane < 4) lds[L_FW + lane] = __builtin_amdgcn_exp2f((mw - M) * LOG2E) / L;
    lds_wait();
    {
      const unsigned char* pa = (lane < 32 || red < 0) ? a.ws + WS_Y + chunk * 2048 + (lane & 15) * 128
                                                       : (const unsigned char*)a.out + (red & 1) * (DM * 2) + (red >> 1) * 64;
      const unsigned jk = __hip_atomic_load((gu32*)pa, RLX_AGENT);
      asm volatile("" :: "v"(jk));
    }
    if (blockIdx.x == NWG - 1) {
      while (true) {
        bool ok = true;
#pragma unroll
        for (int i = 0; i < 4; ++i) ok = ok & ((unsigned)(__hip_atomic_load(ml + (i * 64 + lane) * 16 + 2, RLX_AGENT) >> 32) == tag);
        if (__all(ok)) break;
        if (now32() - t0 > SPIN_TICKS) break;
        __builtin_amdgcn_s_sleep(127);
      }
      if (lane == 0) __hip_atomic_store((gu32*)(a.ws + WS_EPOCH), tag, RLX_AGENT);
    }
  }
  {
    FRESH();
    wg_barrier();
    {
      const LAS float* P = (const LAS float*)lds + L_O + (4 * r) * 128 + wave * 16 + c;
      const float o = xrow_sum(((P[0] + P[128]) + (P[256] + P[384])) * lds[L_FW + r]);
#pragma unroll
      for (int i = 0; i < 16; ++i) {
        const float oi = lane_value(o, i);
#pragma unroll
        for (int k = 0; k < 4; ++k) yacc[k] = __builtin_elementwise_fma(wf[i * 4 + k], f32x2{oi, oi}, yacc[k]);
      }
    }
    *(LAS f32x4*)(lds + L_Y + wave * 512 + lane * 8) = f32x4{yacc[0][0], yacc[0][1], yacc[1][0], yacc[1][1]};
    *(LAS f32x4*)(lds + L_Y + wave * 512 + lane * 8 + 4) = f32x4{yacc[2][0], yacc[2][1], yacc[3][0], yacc[3][1]};
    lds_wait();
    wg_barrier();
    const rsrc_t ry = mk_rsrc(a.ws + WS_Y, NWG * 2048);
    {
      const LAS float* P = (const LAS float*)lds + L_Y + tid;
      const float y = ((P[0] + P[512]) + (P[1024] + P[1536])) + ((P[2048] + P[2560]) + (P[3072] + P[3584]));
      const unsigned u = __float_as_uint(y);
      const unsigned w = (((u & 0x7f800000u) == 0x7f800000u ? u | ((u & 0x7fffffu) ? 0x400000u : 0u) : u + 0x400u) & 0xfffff800u) | ((tag >> ((lane & 1) * 11)) & 0x7ffu);
      const unsigned w1 = __builtin_amdgcn_mov_dpp(w, XOR1, 0xf, 0xf, true);
      if (!(lane & 1)) __builtin_amdgcn_raw_buffer_store_b64(u32x2{w, w1}, ry, tid * 4, chunk * 2048, 16);
    }
    if (red >= 0) {
      const unsigned t0 = now32();
      u32x4 g0 = {0u, 0u, 0u, 0u}, g1 = {0u, 0u, 0u, 0u};
      bool ok0 = false, ok1 = false;
      const unsigned ta = tag & 0x7ffu, tb = (tag >> 11) & 0x7ffu;
      while (true) {
        if (!ok0) g0 = lddev(ry, (lane >> 3) * 2048 + (lane & 7) * 16, ((red & 1) * 128 + wave * 16) * 2048 + (red >> 1) * 128);
        if (!ok1) g1 = lddev(ry, (lane >> 3) * 2048 + (lane & 7) * 16, ((red & 1) * 128 + wave * 16 + 8) * 2048 + (red >> 1) * 128);
        ok0 = (((g0[0] ^ ta) | (g0[1] ^ tb) | (g0[2] ^ ta) | (g0[3] ^ tb)) & 0x7ffu) == 0u;
        ok1 = (((g1[0] ^ ta) | (g1[1] ^ tb) | (g1[2] ^ ta) | (g1[3] ^ tb)) & 0x7ffu) == 0u;
        if (__all(ok0 & ok1)) break;
        if (now32() - t0 > SPIN_TICKS) break;
        __builtin_amdgcn_s_sleep(4);
        asm volatile("" ::: "memory");
      }
      float t[4];
#pragma unroll
      for (int k = 0; k < 4; ++k) t[k] = __uint_as_float(g0[k] & 0xfffff800u) + __uint_as_float(g1[k] & 0xfffff800u);
      float s = tr16(tr32(t[0], t[2]), tr32(t[1], t[3]));
      s += dpp<ROR8>(s);
      if (!(lane & 8)) lds[L_FIN + wave * 32 + (lane & 7) * 4 + r] = s;
      lds_wait();
      wg_barrier();
      if (tid < 16) {
        const LAS f32x2* P = (const LAS f32x2*)(lds + L_FIN + tid * 2);
        const f32x2 o = bf_unpack(xw) + (((P[0] + P[16]) + (P[32] + P[48])) + ((P[64] + P[80]) + (P[96] + P[112])));
        *(gu32*)(a.out + (red & 1) * DM + (red >> 1) * 32 + tid * 2) = pack_bf16(o[0], o[1]);
      }
    }
  }
}

}

extern "C" void kernel_launch(void* const* d_in, const int* in_sizes, int n_in, void* d_out, int out_size, void* d_ws, size_t ws_size,
                              hipStream_t stream) {
  Args a;
  a.x = (const unsigned short*)d_in[0]; a.k = (const unsigned short*)d_in[1]; a.v = (const unsigned short*)d_in[2];
  a.wq = (const unsigned short*)d_in[3]; a.wo = (const unsigned short*)d_in[4];
  a.w1 = (const unsigned short*)d_in[6]; a.w3 = (const unsigned short*)d_in[7]; a.w2 = (const unsigned short*)d_in[8];
  a.out = (unsigned short*)d_out; a.ws = (unsigned char*)d_ws;
  fused_decode<<<NWG, NTHR, 0, stream>>>(a);
}
```
